```python
import torch

def _softmax(x, axis=-1):
    return torch.softmax(x, dim=axis)
def _gelu(x):
    return torch.nn.functional.gelu(x, approximate="tanh")
def _silu(x):
    return torch.nn.functional.silu(x)
def _sigmoid(x):
    return torch.sigmoid(x)
def _layernorm(x, axis=-1, eps=1e-5):
    m = x.mean(dim=axis, keepdim=True)
    v = x.var(dim=axis, keepdim=True, unbiased=False)
    return (x - m) / torch.sqrt(v + eps)
def _scatter_add(tgt, idx, src, axis=0):
    return tgt.clone().index_add_(axis, idx, src)
def _segment_sum(src, seg, n):
    out = torch.zeros((n,) + src.shape[1:], dtype=src.dtype)
    return out.index_add_(0, seg, src)
def _pad(x, pw):
    flat = tuple(v for pair in reversed(pw) for v in pair)
    return torch.nn.functional.pad(x, flat)

kernel_name = 'sequential_preLN__softmax_full_mha__glu_gated_reglu__prompt'

def setup_inputs():

    _w = (lambda a: (lambda t: t.to(torch.bfloat16) if t.is_floating_point() else t)(torch.as_tensor(a)))
    def _A_setup():
        B, S, D = 1, 2048, 448
        return {
            "x": _w((lambda *s: torch.randn(*s, dtype=torch.bfloat16) * 0.05)(B, S, D)),
            "wqkv": _w((lambda *s: torch.randn(*s, dtype=torch.bfloat16) * 0.05)(D, 3 * D)),
            "wo": _w((lambda *s: torch.randn(*s, dtype=torch.bfloat16) * 0.05)(D, D)),
        }
    _A_in = _A_setup()

    def _B_setup():
        B, S, D, F = 1, 2048, 448, 1792
        return {
            "x": _w((lambda *s: torch.randn(*s, dtype=torch.bfloat16) * 0.05)(B, S, D)),
            "W_gate": _w((lambda *s: torch.randn(*s, dtype=torch.bfloat16) * 0.4)(D, F) * (2.0 / D) ** 0.5),
            "W_up": _w((lambda *s: torch.randn(*s, dtype=torch.bfloat16) * 0.4)(D, F) * (2.0 / D) ** 0.5),
            "W_down": _w((lambda *s: torch.randn(*s, dtype=torch.bfloat16) * 0.4)(F, D) * (1.0 / F) ** 0.5),
        }
    _B_in = _B_setup()

    return {
        "A_x": _A_in["x"],
        "A_wqkv": _A_in["wqkv"],
        "A_wo": _A_in["wo"],
        "B_x": _B_in["x"],
        "B_W_gate": _B_in["W_gate"],
        "B_W_up": _B_in["W_up"],
        "B_W_down": _B_in["W_down"],
    }

def reference(A_x, A_wqkv, A_wo, B_x, B_W_gate, B_W_up, B_W_down):
    def _A_ref(x, wqkv, wo):
        B, S, D = x.shape
        H = 16; Dh = D // H
        qkv = (lambda x, *d: x.permute(*d))((x @ wqkv).reshape(B, S, 3, H, Dh), 2, 0, 3, 1, 4)
        q, k, v = qkv[0], qkv[1], qkv[2]
        scale = Dh ** -0.5
        scores = torch.einsum("bhqd,bhkd->bhqk", q, k) * scale
        p = _softmax(scores, axis=-1)
        o = torch.einsum("bhqk,bhkd->bhqd", p, v)
        return (lambda x, *d: x.permute(*d))(o, 0, 2, 1, 3).reshape(B, S, D) @ wo

    def _B_ref(x, W_gate, W_up, W_down):
        gate = torch.einsum("bsd,df->bsf", x, W_gate)
        up = torch.einsum("bsd,df->bsf", x, W_up)
        gate = gate * (lambda x, dt: x.to(dt))(gate > 0, gate.dtype)
        h = gate * up
        return torch.einsum("bsf,fd->bsd", h, W_down)

    x = A_x
    h = x + _A_ref(_layernorm(x, axis=-1), A_wqkv, A_wo)
    h2 = h + _B_ref(_layernorm(h, axis=-1), B_W_gate, B_W_up, B_W_down)
    return h2
```

```cpp
#include <hip/hip_runtime.h>
#include <hip/hip_bf16.h>
#include <stdint.h>

typedef __attribute__((ext_vector_type(8))) short bf16x8;
typedef __attribute__((ext_vector_type(4))) short s16x4;
typedef __attribute__((ext_vector_type(2))) __bf16 bf16x2;
typedef __attribute__((ext_vector_type(16))) float f32x16;
typedef __attribute__((ext_vector_type(4))) float f32x4;
typedef __attribute__((ext_vector_type(2))) float f32x2;
typedef __attribute__((ext_vector_type(4))) unsigned u32x4;
typedef __attribute__((ext_vector_type(2))) unsigned u32x2;
typedef short v4i16_t __attribute__((ext_vector_type(4)));
#define LAS __attribute__((address_space(3)))
typedef LAS char* lds_ptr;
#define DI __device__ __forceinline__
#define MFMA(a, b, c) __builtin_amdgcn_mfma_f32_32x32x16_bf16(a, b, c, 0, 0, 0)

constexpr int S = 2048, D = 448, H = 16, DH = 28, F = 1792;
constexpr float EPS = 1e-5f;
constexpr size_t WS_QF = 0;
constexpr size_t WS_KV = WS_QF + 2097152;
constexpr size_t WS_OF = WS_KV + 4194304;
constexpr size_t WS_HF = WS_OF + 2097152;
constexpr size_t WS_GF = WS_HF + 64 * 28 * 1024;
constexpr size_t WS_HS = WS_GF + 64 * 112 * 1024;
constexpr size_t WS_NRM = WS_HS + 14 * 2048 * 8;

DI unsigned cvtpk(float lo, float hi) { unsigned r; asm("v_cvt_pk_bf16_f32 %0, %1, %2" : "=v"(r) : "v"(lo), "v"(hi)); return r; }
DI float bflo(unsigned u) { return __uint_as_float(u << 16); }
DI float bfhi(unsigned u) { return __uint_as_float(u & 0xffff0000u); }
DI void glds16(const void* g, unsigned lds_base) {
    unsigned sv;
    asm volatile("s_mov_b32 %0, m0\n\ts_mov_b32 m0, %2\n\ts_nop 0\n\tglobal_load_lds_dwordx4 %1, off\n\ts_mov_b32 m0, %0"
                 : "=&s"(sv) : "v"(g), "s"(lds_base) : "memory");
}
DI s16x4 trd(lds_ptr p) { return __builtin_bit_cast(s16x4, __builtin_amdgcn_ds_read_tr16_b64_v4i16((LAS v4i16_t*)p)); }
DI bf16x8 cat(s16x4 a, s16x4 b) { return __builtin_shufflevector(a, b, 0, 1, 2, 3, 4, 5, 6, 7); }
DI float swap_add(float v) { auto r = __builtin_amdgcn_permlane32_swap(__float_as_uint(v), __float_as_uint(v), false, false); return __uint_as_float(r[0]) + __uint_as_float(r[1]); }
DI float swap_max(float v) { auto r = __builtin_amdgcn_permlane32_swap(__float_as_uint(v), __float_as_uint(v), false, false); return fmaxf(__uint_as_float(r[0]), __uint_as_float(r[1])); }
DI u32x4 pack8(const f32x16& a, int s) { return (u32x4){cvtpk(a[8 * s], a[8 * s + 1]), cvtpk(a[8 * s + 2], a[8 * s + 3]), cvtpk(a[8 * s + 4], a[8 * s + 5]), cvtpk(a[8 * s + 6], a[8 * s + 7])}; }
DI float dot2(unsigned a, unsigned b, float c) { return __builtin_amdgcn_fdot2_f32_bf16(__builtin_bit_cast(bf16x2, a), __builtin_bit_cast(bf16x2, b), c, false); }
#define WAITV(n) asm volatile("s_waitcnt vmcnt(" #n ")" ::: "memory")
#define BAR() asm volatile("s_waitcnt lgkmcnt(0)\n\ts_barrier" ::: "memory")

constexpr int K1_X = 0, K1_W = 114688, K1_LDS = 163840;
__global__ __launch_bounds__(256, 1) void k1_qkv(const char* __restrict__ x, const char* __restrict__ wqkv, char* __restrict__ ws) {
    extern __shared__ __attribute__((aligned(16))) char smem[];
    lds_ptr lds = (lds_ptr)smem;
    const int tid = threadIdx.x, lane = tid & 63, w = __builtin_amdgcn_readfirstlane(tid >> 6);
    const int bid = blockIdx.x, head = (bid & 7) * 2 + ((bid >> 3) & 1), rb = bid >> 4;
    const int r = lane & 31, hf = lane >> 5;
    const unsigned lds0 = (unsigned)(uintptr_t)smem;
    {
        const int i = lane >> 3, pc = lane & 7;
#pragma unroll
        for (int b = 0; b < 4; ++b) {
            const int rr = 8 * b + i, c = pc ^ ((rr >> 1) & 7);
            const char* src = x + (size_t)(rb * 128 + w * 32 + rr) * 896 + c * 16;
#pragma unroll
            for (int L = 0; L < 7; ++L) glds16(src + L * 128, lds0 + K1_X + w * 28672 + L * 4096 + b * 1024);
        }
    }
    const char* wsrc = wqkv + head * 56 + (lane >> 2) * 2688 + (lane & 3) * 16;
#pragma unroll
    for (int j = 0; j < 12; ++j) {
        const int idx = w * 12 + j, sec = idx >> 4, kg = idx & 15;
        glds16(wsrc + sec * 896 + kg * 16 * 2688, lds0 + K1_W + sec * 16384 + kg * 1024);
    }
    u32x4 w2[9];
    {
        auto rs = __builtin_amdgcn_make_buffer_rsrc((void*)wqkv, 0, 448 * 1344 * 2, 0x00020000);
#pragma unroll
        for (int j = 0; j < 9; ++j) {
            const int idx = w * 9 + j, sec = idx / 12, kg = idx % 12;
            const int off = head * 56 + (256 + kg * 16 + (lane >> 2)) * 2688 + sec * 896 + (lane & 3) * 16;
            w2[j] = __builtin_bit_cast(u32x4, __builtin_amdgcn_raw_buffer_load_b128(rs, off, 0, 0));
        }
    }
    lds_ptr xb[4];
#pragma unroll
    for (int q = 0; q < 4; ++q) xb[q] = lds + K1_X + w * 28672 + (r >> 3) * 1024 + (r & 7) * 128 + (((2 * q + hf) ^ ((r >> 1) & 7)) * 16);
    lds_ptr wb = lds + K1_W + (8 * hf + ((lane & 15) >> 2)) * 64 + ((lane >> 4) & 1) * 32 + (lane & 3) * 8;
    f32x16 aq = {}, ak = {}, av = {};
    float sx = 0.f, sxx = 0.f, cs0 = 0.f, cs1 = 0.f, cs2 = 0.f;
    const unsigned ones = 0x3f803f80u;
    WAITV(9);
    BAR();
#pragma unroll
    for (int s = 0; s < 28; ++s) {
        if (s == 16) {
            BAR();
#pragma unroll
            for (int j = 0; j < 9; ++j) {
                const int idx = w * 9 + j, sec = idx / 12, kg = idx % 12;
                *(LAS u32x4*)(lds + K1_W + sec * 16384 + kg * 1024 + lane * 16) = w2[j];
            }
            BAR();
        }
        const bf16x8 xf = *(LAS bf16x8*)(xb[s & 3] + (s >> 2) * 4096);
        const int ko = (s & 15) * 1024;
        const bf16x8 f0 = cat(trd(wb + ko), trd(wb + ko + 256));
        const bf16x8 f1 = cat(trd(wb + 16384 + ko), trd(wb + 16384 + ko + 256));
        const bf16x8 f2 = cat(trd(wb + 32768 + ko), trd(wb + 32768 + ko + 256));
        aq = MFMA(f0, xf, aq);
        ak = MFMA(f1, xf, ak);
        av = MFMA(xf, f2, av);
        const u32x4 xu = __builtin_bit_cast(u32x4, xf);
#pragma unroll
        for (int j = 0; j < 4; ++j) { sx = dot2(xu[j], ones, sx); sxx = dot2(xu[j], xu[j], sxx); }
        if ((s & 3) == w) {
            const u32x4 u0 = __builtin_bit_cast(u32x4, f0), u1 = __builtin_bit_cast(u32x4, f1), u2 = __builtin_bit_cast(u32x4, f2);
#pragma unroll
            for (int j = 0; j < 4; ++j) { cs0 = dot2(u0[j], ones, cs0); cs1 = dot2(u1[j], ones, cs1); cs2 = dot2(u2[j], ones, cs2); }
        }
    }
    sx = swap_add(sx); sxx = swap_add(sxx);
    const float mean = sx * (1.f / D);
    const float var = fmaxf(sxx * (1.f / D) - mean * mean, 0.f);
    const float rstd = __builtin_amdgcn_rsqf(var + EPS);
    cs0 = swap_add(cs0); cs1 = swap_add(cs1); cs2 = swap_add(cs2);
    BAR();
    LAS float* tab = (LAS float*)lds;
    LAS float* st = (LAS float*)(lds + 2048) + w * 64;
    if (hf == 0) { tab[(w * 3 + 0) * 32 + r] = cs0; tab[(w * 3 + 1) * 32 + r] = cs1; tab[(w * 3 + 2) * 32 + r] = cs2; st[r] = mean; st[32 + r] = rstd; }
    BAR();
    const float QS = 0.18898223650461362f * 1.4426950408889634f;
    float qn = 0.f, kn = 0.f;
#pragma unroll
    for (int g = 0; g < 4; ++g) {
        f32x4 cq = {}, ck = {};
#pragma unroll
        for (int ww = 0; ww < 4; ++ww) { cq += *(LAS f32x4*)(tab + (ww * 3 + 0) * 32 + 8 * g + 4 * hf); ck += *(LAS f32x4*)(tab + (ww * 3 + 1) * 32 + 8 * g + 4 * hf); }
        const f32x4 mv = *(LAS f32x4*)(st + 8 * g + 4 * hf), rv = *(LAS f32x4*)(st + 32 + 8 * g + 4 * hf);
        const float cv = tab[2 * 32 + r] + tab[5 * 32 + r] + tab[8 * 32 + r] + tab[11 * 32 + r];
#pragma unroll
        for (int j = 0; j < 4; ++j) {
            const int reg = 4 * g + j;
            float q = (aq[reg] - mean * cq[j]) * (rstd * QS), k = (ak[reg] - mean * ck[j]) * rstd, v = (av[reg] - mv[j] * cv) * rv[j];
            if (g == 3 && hf == 1) { q = 0.f; k = 0.f; }
            if (r >= DH) v = (r == DH) ? 1.f : 0.f;
            aq[reg] = q; ak[reg] = k; av[reg] = v;
            qn += q * q; kn += k * k;
        }
    }
    const int tt = rb * 4 + w;
    char* qf = ws + WS_QF + ((size_t)(head * 64 + tt) * 2) * 1024 + lane * 16;
    char* kv = ws + WS_KV + ((size_t)(head * 64 + tt) * 4) * 1024 + lane * 16;
    *(u32x4*)(qf) = pack8(aq, 0); *(u32x4*)(qf + 1024) = pack8(aq, 1);
    *(u32x4*)(kv) = pack8(ak, 0); *(u32x4*)(kv + 1024) = pack8(ak, 1);
    *(u32x4*)(kv + 2048) = pack8(av, 0); *(u32x4*)(kv + 3072) = pack8(av, 1);
    qn = swap_add(qn); kn = swap_add(kn);
#pragma unroll
    for (int o = 16; o >= 1; o >>= 1) { qn = fmaxf(qn, __shfl_xor(qn, o)); kn = fmaxf(kn, __shfl_xor(kn, o)); }
    if (lane == 0) *(f32x2*)(ws + WS_NRM + (size_t)(head * 64 + tt) * 8) = (f32x2){qn, kn};
}

constexpr int K2_LDS = 131072;
__global__ __launch_bounds__(512, 2) void k2_attn(char* __restrict__ ws) {
    extern __shared__ __attribute__((aligned(16))) char smem[];
    lds_ptr lds = (lds_ptr)smem;
    const int tid = threadIdx.x, lane = tid & 63, w = __builtin_amdgcn_readfirstlane(tid >> 6);
    const int bid = blockIdx.x, head = (bid & 7) * 2 + ((bid >> 3) & 1), rb = bid >> 4;
    const int r = lane & 31, hf = lane >> 5;
    const unsigned lds0 = (unsigned)(uintptr_t)smem;
    const int qt = w & 3, kh = w >> 2;
    const char* kvsrc = ws + WS_KV + (size_t)head * 262144 + (size_t)kh * 131072 + (w & 3) * 2048 + lane * 16;
    const unsigned dst = lds0 + kh * 8192 + (w & 3) * 2048;
    const char* qf = ws + WS_QF + ((size_t)(head * 64 + rb * 4 + qt) * 2) * 1024 + lane * 16;
    const bf16x8 q0 = *(const bf16x8*)qf, q1 = *(const bf16x8*)(qf + 1024);
    const f32x2 nr = *(const f32x2*)(ws + WS_NRM + (size_t)(head * 64 + lane) * 8);
#pragma unroll
    for (int t = 0; t < 7; ++t) { glds16(kvsrc + t * 8192, dst + t * 16384); glds16(kvsrc + t * 8192 + 1024, dst + t * 16384 + 1024); }
    float kmax = nr[1], qmax = ((lane >> 2) == rb) ? nr[0] : 0.f;
#pragma unroll
    for (int o = 32; o >= 1; o >>= 1) { kmax = fmaxf(kmax, __shfl_xor(kmax, o)); qmax = fmaxf(qmax, __shfl_xor(qmax, o)); }
    const bool fast = __builtin_amdgcn_readfirstlane((kmax * qmax <= 3600.f) ? 1 : 0) != 0;
    f32x16 o = {};
    float m = -INFINITY;
    lds_ptr base = lds + kh * 8192 + lane * 16;
    if (fast) {
        for (int t = 0; t < 16; ++t) {
            if (t < 10) { WAITV(12); } else { WAITV(0); }
            BAR();
            if (t + 7 < 16) { const int tn = t + 7; glds16(kvsrc + tn * 8192, dst + (tn & 7) * 16384); glds16(kvsrc + tn * 8192 + 1024, dst + (tn & 7) * 16384 + 1024); }
            lds_ptr p = base + (t & 7) * 16384;
#pragma unroll
            for (int u = 0; u < 2; ++u) {
                const bf16x8 k0 = *(LAS bf16x8*)(p + u * 4096), k1 = *(LAS bf16x8*)(p + u * 4096 + 1024);
                const bf16x8 v0 = *(LAS bf16x8*)(p + u * 4096 + 2048), v1 = *(LAS bf16x8*)(p + u * 4096 + 3072);
                f32x16 s = {};
                s = MFMA(k0, q0, s); s = MFMA(k1, q1, s);
#pragma unroll
                for (int i = 0; i < 16; ++i) s[i] = __builtin_amdgcn_exp2f(s[i]);
                o = MFMA(v0, __builtin_bit_cast(bf16x8, pack8(s, 0)), o);
                o = MFMA(v1, __builtin_bit_cast(bf16x8, pack8(s, 1)), o);
            }
        }
    } else {
        for (int t = 0; t < 16; ++t) {
            if (t < 10) { WAITV(12); } else { WAITV(0); }
            BAR();
            if (t + 7 < 16) { const int tn = t + 7; glds16(kvsrc + tn * 8192, dst + (tn & 7) * 16384); glds16(kvsrc + tn * 8192 + 1024, dst + (tn & 7) * 16384 + 1024); }
            lds_ptr p = base + (t & 7) * 16384;
            for (int u = 0; u < 2; ++u) {
                const bf16x8 k0 = *(LAS bf16x8*)(p + u * 4096), k1 = *(LAS bf16x8*)(p + u * 4096 + 1024);
                const bf16x8 v0 = *(LAS bf16x8*)(p + u * 4096 + 2048), v1 = *(LAS bf16x8*)(p + u * 4096 + 3072);
                f32x16 s = {};
                s = MFMA(k0, q0, s); s = MFMA(k1, q1, s);
                float mx = s[0];
#pragma unroll
                for (int i = 1; i < 16; ++i) mx = fmaxf(mx, s[i]);
                mx = fmaxf(swap_max(mx), m);
                const float alpha = __builtin_amdgcn_exp2f(m - mx);
                m = mx;
#pragma unroll
                for (int i = 0; i < 16; ++i) { s[i] = __builtin_amdgcn_exp2f(s[i] - mx); o[i] *= alpha; }
                o = MFMA(v0, __builtin_bit_cast(bf16x8, pack8(s, 0)), o);
                o = MFMA(v1, __builtin_bit_cast(bf16x8, pack8(s, 1)), o);
            }
        }
    }
    BAR();
    LAS float* xo = (LAS float*)lds + qt * (17 * 64);
    if (kh == 1) {
#pragma unroll
        for (int i = 0; i < 16; ++i) xo[i * 64 + lane] = o[i];
        xo[16 * 64 + lane] = m;
    }
    BAR();
    if (kh == 0) {
        if (fast) {
#pragma unroll
            for (int i = 0; i < 16; ++i) o[i] += xo[i * 64 + lane];
        } else {
            const float m2 = xo[16 * 64 + lane], mm = fmaxf(m, m2);
            const float a1 = __builtin_amdgcn_exp2f(m - mm), a2 = __builtin_amdgcn_exp2f(m2 - mm);
#pragma unroll
            for (int i = 0; i < 16; ++i) o[i] = o[i] * a1 + xo[i * 64 + lane] * a2;
        }
        auto lr = __builtin_amdgcn_permlane32_swap(__float_as_uint(o[12]), __float_as_uint(o[12]), false, false);
        const float inv = __builtin_amdgcn_rcpf(__uint_as_float(lr[1]));
#pragma unroll
        for (int i = 0; i < 16; ++i) o[i] = (i >= 12 && hf == 1) ? 0.f : o[i] * inv;
        char* of = ws + WS_OF + ((size_t)(rb * 4 + qt) * 32 + head * 2) * 1024 + lane * 16;
        *(u32x4*)(of) = pack8(o, 0); *(u32x4*)(of + 1024) = pack8(o, 1);
    }
}

constexpr int K3_LDS = 456 * 128;
__global__ __launch_bounds__(256, 1) void k3_wo(const char* __restrict__ x, const char* __restrict__ wo, char* __restrict__ ws) {
    extern __shared__ __attribute__((aligned(16))) char smem[];
    lds_ptr lds = (lds_ptr)smem;
    const int tid = threadIdx.x, lane = tid & 63, w = __builtin_amdgcn_readfirstlane(tid >> 6);
    const int bid = blockIdx.x, nb = bid % 7, tb = bid / 7;
    const int r = lane & 31, hf = lane >> 5;
    const unsigned lds0 = (unsigned)(uintptr_t)smem;
    const int tt = tb * 2 + (w & 1), nt = nb * 2 + (w >> 1);
    {
        const int row = lane >> 3, c = (lane & 7) ^ (((row >> 1) & 1) << 2);
        const char* src = wo + (size_t)row * 896 + nb * 128 + c * 16;
#pragma unroll
        for (int j = 0; j < 14; ++j) glds16(src + (size_t)(w * 14 + j) * 8 * 896, lds0 + (w * 14 + j) * 1024);
    }
    if (tid < 64) *(LAS u32x4*)(lds + 448 * 128 + tid * 16) = (u32x4){0, 0, 0, 0};
    const char* of = ws + WS_OF + (size_t)tt * 32768 + lane * 16;
    bf16x8 ofr[32];
#pragma unroll
    for (int s = 0; s < 32; ++s) ofr[s] = *(const bf16x8*)(of + s * 1024);
    f32x16 acc;
    const char* xr = x + (size_t)(tt * 32 + r) * 896 + nt * 64 + hf * 8;
#pragma unroll
    for (int g = 0; g < 4; ++g) { const u32x2 u = *(const u32x2*)(xr + g * 16); acc[4 * g] = bflo(u[0]); acc[4 * g + 1] = bfhi(u[0]); acc[4 * g + 2] = bflo(u[1]); acc[4 * g + 3] = bfhi(u[1]); }
    f32x16 a2 = {};
    const int q = (lane & 15) >> 2;
    lds_ptr wb = lds + (4 * hf + q) * 128 + (((w >> 1) ^ (q >> 1)) * 64) + ((lane >> 4) & 1) * 32 + (lane & 3) * 8;
    WAITV(0);
    BAR();
#pragma unroll
    for (int s = 0; s < 32; ++s) {
        const int k0 = (s >> 1) * 28 + (s & 1) * 16;
        const bf16x8 f = cat(trd(wb + k0 * 128), trd(wb + (k0 + 8) * 128));
        a2 = MFMA(f, ofr[s], a2);
    }
    float s1 = 0.f, s2 = 0.f;
    u32x4 p[2];
#pragma unroll
    for (int i = 0; i < 16; i += 2) {
        const unsigned a = cvtpk(a2[i], a2[i + 1]);
        const unsigned hh = cvtpk(acc[i] + bflo(a), acc[i + 1] + bfhi(a));
        p[i >> 3][(i >> 1) & 3] = hh;
        const float h0 = bflo(hh), h1 = bfhi(hh);
        s1 += h0 + h1; s2 += h0 * h0 + h1 * h1;
    }
    char* hfp = ws + WS_HF + ((size_t)tt * 28 + nt * 2) * 1024 + lane * 16;
    *(u32x4*)hfp = p[0]; *(u32x4*)(hfp + 1024) = p[1];
    s1 = swap_add(s1); s2 = swap_add(s2);
    if (hf == 0) *(f32x2*)(ws + WS_HS + ((size_t)nt * 2048 + tt * 32 + r) * 8) = (f32x2){s1, s2};
}

constexpr int K4_LDS = 163840;
__global__ __launch_bounds__(512, 2) void k4_glu(const char* __restrict__ wg, const char* __restrict__ wu, char* __restrict__ ws) {
    extern __shared__ __attribute__((aligned(16))) char smem[];
    lds_ptr lds = (lds_ptr)smem;
    const int tid = threadIdx.x, lane = tid & 63, w = __builtin_amdgcn_readfirstlane(tid >> 6);
    const int bid = blockIdx.x, fb = bid % 14, tb = bid / 14;
    const int r = lane & 31, hf = lane >> 5;
    const unsigned lds0 = (unsigned)(uintptr_t)smem;
    const int tt = tb * 4 + (w & 3), fh = w >> 2;
    const int row = lane >> 3, cc = (lane & 7) ^ (((row >> 1) & 1) << 2);
    const char* wsrc = ((w >> 2) ? wu : wg) + (size_t)((w & 1) * 32 + row) * 3584 + fb * 256 + ((w >> 1) & 1) * 128 + cc * 16;
    const unsigned wdst = lds0 + (w >> 1) * 8192 + (w & 1) * 4096;
    const char* hp = ws + WS_HF + (size_t)tt * 28672 + lane * 16;
    u32x4 hfr[28];
#pragma unroll
    for (int s = 0; s < 28; ++s) hfr[s] = *(const u32x4*)(hp + s * 1024);
    float s1 = 0.f, s2 = 0.f;
#pragma unroll
    for (int j = 0; j < 7; ++j) { const f32x2 v = *(const f32x2*)(ws + WS_HS + ((size_t)(2 * j + hf) * 2048 + tt * 32 + r) * 8); s1 += v[0]; s2 += v[1]; }
#pragma unroll
    for (int c = 0; c < 5; ++c)
#pragma unroll
        for (int j = 0; j < 4; ++j) glds16(wsrc + (size_t)(c * 64 + j * 8) * 3584, wdst + c * 32768 + j * 1024);
    s1 = swap_add(s1); s2 = swap_add(s2);
    const float mean = s1 * (1.f / D);
    const float rstd = __builtin_amdgcn_rsqf(fmaxf(s2 * (1.f / D) - mean * mean, 0.f) + EPS);
    const float nm = -mean * rstd;
    f32x16 g0 = {}, g1 = {}, u0 = {}, u1 = {};
    const int q = (lane & 15) >> 2;
    lds_ptr wb0 = lds + fh * 8192 + (4 * hf + q) * 128 + ((q >> 1) * 64) + ((lane >> 4) & 1) * 32 + (lane & 3) * 8;
    lds_ptr wb1 = lds + fh * 8192 + (4 * hf + q) * 128 + (((q >> 1) ^ 1) * 64) + ((lane >> 4) & 1) * 32 + (lane & 3) * 8;
#pragma unroll
    for (int c = 0; c < 7; ++c) {
        if (c == 0) { WAITV(16); } else if (c == 1) { WAITV(12); } else if (c == 2) { WAITV(12); } else if (c == 3) { WAITV(12); } else if (c == 4) { WAITV(8); } else if (c == 5) { WAITV(4); } else { WAITV(0); }
        BAR();
        if (c == 1 || c == 2) {
#pragma unroll
            for (int j = 0; j < 4; ++j) glds16(wsrc + (size_t)((c + 4) * 64 + j * 8) * 3584, wdst + (c - 1) * 32768 + j * 1024);
        }
        const int so = (c % 5) * 32768;
#pragma unroll
        for (int ks = 0; ks < 4; ++ks) {
            const u32x4 hu = hfr[c * 4 + ks];
            u32x4 hn;
#pragma unroll
            for (int j = 0; j < 4; ++j) hn[j] = cvtpk(fmaf(bflo(hu[j]), rstd, nm), fmaf(bfhi(hu[j]), rstd, nm));
            const bf16x8 hb = __builtin_bit_cast(bf16x8, hn);
            const int ko = so + ks * 2048;
            g0 = MFMA(cat(trd(wb0 + ko), trd(wb0 + ko + 1024)), hb, g0);
            g1 = MFMA(cat(trd(wb1 + ko), trd(wb1 + ko + 1024)), hb, g1);
            u0 = MFMA(cat(trd(wb0 + ko + 16384), trd(wb0 + ko + 16384 + 1024)), hb, u0);
            u1 = MFMA(cat(trd(wb1 + ko + 16384), trd(wb1 + ko + 16384 + 1024)), hb, u1);
        }
    }
#pragma unroll
    for (int i = 0; i < 16; ++i) { g0[i] = fmaxf(g0[i], 0.f) * u0[i]; g1[i] = fmaxf(g1[i], 0.f) * u1[i]; }
    char* gp = ws + WS_GF + ((size_t)tt * 112 + fb * 8 + fh * 4) * 1024 + lane * 16;
    *(u32x4*)(gp) = pack8(g0, 0); *(u32x4*)(gp + 1024) = pack8(g0, 1);
    *(u32x4*)(gp + 2048) = pack8(g1, 0); *(u32x4*)(gp + 3072) = pack8(g1, 1);
}

constexpr int K5_LDS = 163840;
__global__ __launch_bounds__(512, 2) void k5_down(const char* __restrict__ wd, char* __restrict__ ws, char* __restrict__ out) {
    extern __shared__ __attribute__((aligned(16))) char smem[];
    lds_ptr lds = (lds_ptr)smem;
    const int tid = threadIdx.x, lane = tid & 63, w = __builtin_amdgcn_readfirstlane(tid >> 6);
    const int bid = blockIdx.x, nb = bid % 7, tb = bid / 7;
    const int r = lane & 31, hf = lane >> 5;
    const unsigned lds0 = (unsigned)(uintptr_t)smem;
    const int row = lane >> 3, cc = (lane & 7) ^ (((row >> 1) & 1) << 2);
    const char* wsrc = wd + (size_t)(w * 224 + row) * 896 + nb * 128 + cc * 16;
    const unsigned wdst = lds0 + w * 20480;
#pragma unroll
    for (int j = 0; j < 20; ++j) glds16(wsrc + (size_t)j * 8 * 896, wdst + j * 1024);
    const char* gp = ws + WS_GF + ((size_t)(tb * 2) * 112 + w * 14) * 1024 + lane * 16;
    bf16x8 ga[14], gb[14];
#pragma unroll
    for (int s = 0; s < 14; ++s) { ga[s] = *(const bf16x8*)(gp + s * 1024); gb[s] = *(const bf16x8*)(gp + 112 * 1024 + s * 1024); }
    u32x4 w2[8];
#pragma unroll
    for (int j = 0; j < 8; ++j) w2[j] = *(const u32x4*)(wsrc + (size_t)(20 + j) * 8 * 896);
    f32x16 c00 = {}, c01 = {}, c10 = {}, c11 = {};
    const int q = (lane & 15) >> 2;
    lds_ptr wb0 = lds + w * 20480 + (4 * hf + q) * 128 + ((q >> 1) * 64) + ((lane >> 4) & 1) * 32 + (lane & 3) * 8;
    lds_ptr wb1 = lds + w * 20480 + (4 * hf + q) * 128 + (((q >> 1) ^ 1) * 64) + ((lane >> 4) & 1) * 32 + (lane & 3) * 8;
    WAITV(36);
#pragma unroll
    for (int s = 0; s < 14; ++s) {
        if (s == 10) {
            asm volatile("s_waitcnt lgkmcnt(0)" ::: "memory");
#pragma unroll
            for (int j = 0; j < 8; ++j) *(LAS u32x4*)(lds + w * 20480 + j * 1024 + lane * 16) = w2[j];
            asm volatile("s_waitcnt lgkmcnt(0)" ::: "memory");
        }
        const int ko = (s % 10) * 2048;
        const bf16x8 f0 = cat(trd(wb0 + ko), trd(wb0 + ko + 1024)), f1 = cat(trd(wb1 + ko), trd(wb1 + ko + 1024));
        c00 = MFMA(f0, ga[s], c00); c01 = MFMA(f0, gb[s], c01);
        c10 = MFMA(f1, ga[s], c10); c11 = MFMA(f1, gb[s], c11);
    }
    BAR();
    LAS f32x4* red = (LAS f32x4*)lds;
#pragma unroll
    for (int g = 0; g < 4; ++g) {
        red[((w * 4 + 0) * 4 + g) * 64 + lane] = (f32x4){c00[4 * g], c00[4 * g + 1], c00[4 * g + 2], c00[4 * g + 3]};
        red[((w * 4 + 1) * 4 + g) * 64 + lane] = (f32x4){c01[4 * g], c01[4 * g + 1], c01[4 * g + 2], c01[4 * g + 3]};
        red[((w * 4 + 2) * 4 + g) * 64 + lane] = (f32x4){c10[4 * g], c10[4 * g + 1], c10[4 * g + 2], c10[4 * g + 3]};
        red[((w * 4 + 3) * 4 + g) * 64 + lane] = (f32x4){c11[4 * g], c11[4 * g + 1], c11[4 * g + 2], c11[4 * g + 3]};
    }
    const int tile = w >> 1, sh = w & 1, tt = tb * 2 + (tile & 1), nt = nb * 2 + (tile >> 1);
    const u32x4 hu = *(const u32x4*)(ws + WS_HF + ((size_t)tt * 28 + nt * 2 + sh) * 1024 + lane * 16);
    BAR();
    f32x4 a = {}, b = {};
#pragma unroll
    for (int ww = 0; ww < 8; ++ww) { a += red[((ww * 4 + tile) * 4 + 2 * sh) * 64 + lane]; b += red[((ww * 4 + tile) * 4 + 2 * sh + 1) * 64 + lane]; }
    const unsigned a0 = cvtpk(a[0], a[1]), a1 = cvtpk(a[2], a[3]), b0 = cvtpk(b[0], b[1]), b1 = cvtpk(b[2], b[3]);
    u32x2 oa = {cvtpk(bflo(hu[0]) + bflo(a0), bfhi(hu[0]) + bfhi(a0)), cvtpk(bflo(hu[1]) + bflo(a1), bfhi(hu[1]) + bfhi(a1))};
    u32x2 ob = {cvtpk(bflo(hu[2]) + bflo(b0), bfhi(hu[2]) + bfhi(b0)), cvtpk(bflo(hu[3]) + bflo(b1), bfhi(hu[3]) + bfhi(b1))};
    char* op = out + (size_t)(tt * 32 + r) * 896 + nt * 64 + sh * 32 + hf * 8;
    *(u32x2*)op = oa; *(u32x2*)(op + 16) = ob;
}

extern "C" void kernel_launch(void* const* d_in, const int* in_sizes, int n_in, void* d_out, int out_size, void* d_ws, size_t ws_size, hipStream_t stream) {
    const char* x = (const char*)d_in[0];
    const char* wqkv = (const char*)d_in[1];
    const char* wo = (const char*)d_in[2];
    const char* wg = (const char*)d_in[4];
    const char* wu = (const char*)d_in[5];
    const char* wd = (const char*)d_in[6];
    char* ws = (char*)d_ws;
    (void)hipFuncSetAttribute((const void*)k1_qkv, hipFuncAttributeMaxDynamicSharedMemorySize, K1_LDS);
    (void)hipFuncSetAttribute((const void*)k2_attn, hipFuncAttributeMaxDynamicSharedMemorySize, K2_LDS);
    (void)hipFuncSetAttribute((const void*)k4_glu, hipFuncAttributeMaxDynamicSharedMemorySize, K4_LDS);
    (void)hipFuncSetAttribute((const void*)k5_down, hipFuncAttributeMaxDynamicSharedMemorySize, K5_LDS);
    k1_qkv<<<256, 256, K1_LDS, stream>>>(x, wqkv, ws);
    k2_attn<<<256, 512, K2_LDS, stream>>>(ws);
    k3_wo<<<224, 256, K3_LDS, stream>>>(x, wo, ws);
    k4_glu<<<224, 512, K4_LDS, stream>>>(wg, wu, ws);
    k5_down<<<224, 512, K5_LDS, stream>>>(wd, ws, (char*)d_out);
}
```

```cpp
#include <hip/hip_runtime.h>
#include <hip/hip_bf16.h>
#include <stdint.h>

typedef __attribute__((ext_vector_type(8))) short bf16x8;
typedef __attribute__((ext_vector_type(4))) short s16x4;
typedef __attribute__((ext_vector_type(2))) __bf16 bf16x2;
typedef __attribute__((ext_vector_type(16))) float f32x16;
typedef __attribute__((ext_vector_type(4))) float f32x4;
typedef __attribute__((ext_vector_type(2))) float f32x2;
typedef __attribute__((ext_vector_type(4))) unsigned u32x4;
typedef __attribute__((ext_vector_type(2))) unsigned u32x2;
typedef short v4i16_t __attribute__((ext_vector_type(4)));
typedef unsigned long long u64;
#define LAS __attribute__((address_space(3)))
#define GAS __attribute__((address_space(1)))
typedef LAS char* lds_ptr;
#define DI __device__ __forceinline__
#define MFMA(a, b, c) __builtin_amdgcn_mfma_f32_32x32x16_bf16(a, b, c, 0, 0, 0)
#define MFMA16(a, b, c) __builtin_amdgcn_mfma_f32_16x16x32_bf16(a, b, c, 0, 0, 0)
#define BF8(x) __builtin_bit_cast(bf16x8, x)
#define U4(x) __builtin_bit_cast(u32x4, x)

constexpr int D = 448;
constexpr float EPS = 1e-5f;
constexpr unsigned WS_FLAG = 0;
constexpr unsigned FL_EPOCH = 0, FL_KV = 1, FL_RT = FL_KV + 256, FL_RD = FL_RT + 32, FL_O = FL_RD + 32, FL_H = FL_O + 256, FL_G = FL_H + 256;
constexpr unsigned WS_KV = 1u << 20;
constexpr unsigned WS_QF = WS_KV + 4194304;
constexpr unsigned WS_OF = WS_QF + 2097152;
constexpr unsigned WS_HF = WS_OF + 2097152;
constexpr unsigned WS_GF = WS_HF + 64 * 28 * 1024;
constexpr unsigned WS_WGU = WS_GF + 64 * 112 * 1024;
constexpr unsigned WS_WD = WS_WGU + 112 * 28 * 1024;
constexpr unsigned WS_HS = WS_WD + 7 * 112 * 2 * 1024;
constexpr unsigned WS_END = WS_HS + 64 * 28 * 256 + 1024;
constexpr unsigned OOB = 0x40000000u;
constexpr int LDS_BYTES = 163840;

DI unsigned cvtpk(float lo, float hi) { return __builtin_bit_cast(unsigned, __builtin_convertvector((f32x2){lo, hi}, bf16x2)); }
DI float bflo(unsigned u) { return __uint_as_float(u << 16); }
DI float bfhi(unsigned u) { return __uint_as_float(u & 0xffff0000u); }
DI void glds(unsigned voff, const void* sbase, unsigned dst) {
    asm volatile("s_mov_b32 m0, %2\n\ts_nop 0\n\tglobal_load_lds_dwordx4 %0, %1" :: "v"(voff), "s"(sbase), "s"(dst) : "memory");
}
DI void glds_dev(unsigned voff, const void* sbase, unsigned dst) {
    asm volatile("s_mov_b32 m0, %2\n\ts_nop 0\n\tglobal_load_lds_dwordx4 %0, %1 sc1" :: "v"(voff), "s"(sbase), "s"(dst) : "memory");
}
DI void glds2_dev(unsigned voff, const void* sbase, unsigned dst) {
    asm volatile("s_mov_b32 m0, %2\n\ts_nop 0\n\tglobal_load_lds_dwordx4 %0, %1 sc1\n\tglobal_load_lds_dwordx4 %0, %1 offset:1024 sc1" :: "v"(voff), "s"(sbase), "s"(dst) : "memory");
}
DI void glds4_dev(unsigned voff, const void* sbase, unsigned dst) {
    asm volatile("s_mov_b32 m0, %2\n\ts_nop 0\n\tglobal_load_lds_dwordx4 %0, %1 sc1\n\tglobal_load_lds_dwordx4 %0, %1 offset:1024 sc1\n\t"
                 "global_load_lds_dwordx4 %0, %1 offset:2048 sc1\n\tglobal_load_lds_dwordx4 %0, %1 offset:3072 sc1" :: "v"(voff), "s"(sbase), "s"(dst) : "memory");
}
DI s16x4 trd(lds_ptr p) { return __builtin_bit_cast(s16x4, __builtin_amdgcn_ds_read_tr16_b64_v4i16((LAS v4i16_t*)p)); }
DI bf16x8 cat(s16x4 a, s16x4 b) { return __builtin_shufflevector(a, b, 0, 1, 2, 3, 4, 5, 6, 7); }
DI bf16x8 lds8(lds_ptr p) { return *(LAS bf16x8*)p; }
DI float swap_add(float v) { auto r = __builtin_amdgcn_permlane32_swap(__float_as_uint(v), __float_as_uint(v), false, false); return __uint_as_float(r[0]) + __uint_as_float(r[1]); }
DI float swap_max(float v) { auto r = __builtin_amdgcn_permlane32_swap(__float_as_uint(v), __float_as_uint(v), false, false); return fmaxf(__uint_as_float(r[0]), __uint_as_float(r[1])); }
DI u32x4 pack8(const f32x16& a, int s) { return (u32x4){cvtpk(a[8 * s], a[8 * s + 1]), cvtpk(a[8 * s + 2], a[8 * s + 3]), cvtpk(a[8 * s + 4], a[8 * s + 5]), cvtpk(a[8 * s + 6], a[8 * s + 7])}; }
DI float dot2(unsigned a, unsigned b, float c) { return __builtin_amdgcn_fdot2_f32_bf16(__builtin_bit_cast(bf16x2, a), __builtin_bit_cast(bf16x2, b), c, false); }
DI void acc_stats(u32x4 u, float& s1, float& s2) {
#pragma unroll
    for (int i = 0; i < 4; ++i) { const float a = bflo(u[i]), b = bfhi(u[i]); s1 += a; s2 = fmaf(a, a, s2); s1 += b; s2 = fmaf(b, b, s2); }
}
template <int N> struct IC { static constexpr int v = N; };
template <int B, int E, class F> DI void sfor(F&& f) { if constexpr (B < E) { f(IC<B>{}); sfor<B + 1, E>(f); } }
template <int N> DI void waitv() { asm volatile("s_waitcnt vmcnt(%0)" :: "i"(N) : "memory"); }
template <int N> DI void waitv_bar() { asm volatile("s_waitcnt vmcnt(%0) lgkmcnt(0)\n\ts_barrier" :: "i"(N) : "memory"); }
#define BAR() asm volatile("s_waitcnt lgkmcnt(0)\n\ts_barrier" ::: "memory")
#define FENCE() asm volatile("" ::: "memory")
typedef __amdgpu_buffer_rsrc_t rsrc_t;
DI u32x4 ld16(rsrc_t rs, unsigned voff, unsigned soff) { return U4(__builtin_amdgcn_raw_buffer_load_b128(rs, voff, soff, 0)); }
DI u32x4 ld16_dev(rsrc_t rs, unsigned voff, unsigned soff) { return U4(__builtin_amdgcn_raw_buffer_load_b128(rs, voff, soff, 16)); }
DI u32x2 ld8(rsrc_t rs, unsigned voff, unsigned soff) { return __builtin_bit_cast(u32x2, __builtin_amdgcn_raw_buffer_load_b64(rs, voff, soff, 0)); }
DI u32x2 ld8_dev(rsrc_t rs, unsigned voff, unsigned soff) { return __builtin_bit_cast(u32x2, __builtin_amdgcn_raw_buffer_load_b64(rs, voff, soff, 16)); }
DI void st16_dev(u32x4 v, rsrc_t rs, unsigned voff, unsigned soff) { __builtin_amdgcn_raw_buffer_store_b128(v, rs, voff + soff, 0, 16); }
DI void st8_dev(u32x2 v, rsrc_t rs, unsigned voff, unsigned soff) { __builtin_amdgcn_raw_buffer_store_b64(v, rs, voff, soff, 16); }
DI void set_flag(char* ws, unsigned idx, u64 tag) { __hip_atomic_store((GAS u64*)(ws + WS_FLAG + (size_t)idx * 128), tag, __ATOMIC_RELAXED, __HIP_MEMORY_SCOPE_AGENT); }
DI void poll_flag(char* ws, unsigned idx, u64 tag) {
    GAS u64* p = (GAS u64*)(ws + WS_FLAG + (size_t)idx * 128);
#pragma unroll 1
    for (int spin = 0; spin < (1 << 16); ++spin) {
        const u64 v = __hip_atomic_load(p, __ATOMIC_RELAXED, __HIP_MEMORY_SCOPE_AGENT);
        if (__all(v == tag)) break;
        __builtin_amdgcn_s_sleep(1);
    }
}
DI u64 peek_flag(char* ws, unsigned idx) { return __hip_atomic_load((GAS u64*)(ws + WS_FLAG + (size_t)idx * 128), __ATOMIC_RELAXED, __HIP_MEMORY_SCOPE_AGENT); }
#define SB() __builtin_amdgcn_sched_barrier(0)
#define PIN(a) asm volatile("" : "+v"(a))
#define PIN2(a, b) asm volatile("" : "+v"(a), "+v"(b))
DI lds_ptr opq(lds_ptr p) { unsigned u = (unsigned)(uintptr_t)p; asm("" : "+v"(u)); return (lds_ptr)(uintptr_t)u; }
#define TS(i) do {} while (0)

constexpr int wd_slot(int s) { return (s * 17 + 27) / 28 != ((s + 1) * 17 + 27) / 28 ? (s * 17 + 27) / 28 : -1; }
constexpr int phys(int t) { return ((t % 9) + 2) % 9 * 16384; }

__global__ __launch_bounds__(512, 2) void block_fwd(const char* x, const char* wqkv, const char* wo, const char* wg, const char* wu, const char* wd, char* out, char* ws) {
    extern __shared__ __attribute__((aligned(16))) char smem[];
    lds_ptr lds = (lds_ptr)smem;
    const int tid = threadIdx.x, lane = tid & 63, w = __builtin_amdgcn_readfirstlane(tid >> 6);
    const int bid = blockIdx.x, xcd = bid & 7, loc = bid >> 3;
    const int r = lane & 31, hf = lane >> 5, q4 = (lane & 15) >> 2, g1 = (lane >> 4) & 1, p4 = lane & 3;
    const unsigned lds0 = (unsigned)(uintptr_t)smem;
    const unsigned l16 = lane * 16;
    TS(0);
    const rsrc_t rws = __builtin_amdgcn_make_buffer_rsrc((void*)ws, 0, WS_END, 0x00020000);
    const u64 epoch = peek_flag(ws, FL_EPOCH);
    const unsigned ones = 0x3f803f80u;
    const bool ffn = loc < 28;
    const int l7 = loc / 7, m7 = loc % 7;
    const int tb = xcd * 4 + l7;
    const int tt = w & 1, ct = (w >> 1) & 1, kh = w >> 2;
    const int row8 = lane >> 3;
    const unsigned swz = (unsigned)(((lane & 7) ^ (((row8 >> 1) & 1) << 2)) * 16);

    if (ffn) {
        constexpr int A_W = 57344, A_WS = 24576, A_STAT = 155648, A_CS = A_STAT + 1024;
        const int xr = (w & 3) * 8 + row8;
        const unsigned xo = (unsigned)(xr * 896 + (((lane & 7) ^ ((xr >> 1) & 7)) * 16));
        const char* xs = x + (size_t)(tb * 64 + kh * 32) * 896;
        const unsigned xd = lds0 + kh * 28672 + (w & 3) * 1024;
        const unsigned wvo = (unsigned)(row8 * 2688) + swz;
        const char* wsrc = wqkv + m7 * 128 + (size_t)w * 8 * 2688;
        const unsigned wdst = lds0 + A_W + w * 1024;
#define A_PIECE(c, i) do { if ((i) == 0) { if ((c) < 6) glds(xo, xs + ((c) + 1) * 128, xd + ((c) + 1) * 4096); } \
                           else glds(wvo, wsrc + (size_t)(c) * 64 * 2688 + ((i) % 3) * 896, wdst + ((c) & 3) * A_WS + ((i) % 3) * 8192); } while (0)
        glds(xo, xs, xd);
#pragma unroll
        for (int c = 0; c < 3; ++c) { A_PIECE(c, 0); A_PIECE(c, 1); A_PIECE(c, 2); A_PIECE(c, 3); }
        lds_ptr xb[4], wb1[4], wb1h[4];
#pragma unroll
        for (int j = 0; j < 4; ++j) {
            const int ks = j ^ (w & 3);
            xb[j] = lds + tt * 28672 + (r >> 3) * 1024 + (r & 7) * 128 + (((2 * ks + hf) ^ ((r >> 1) & 7)) * 16);
            wb1[j] = opq(lds + A_W + (kh ? 0 : 8192) + ks * 2048 + (8 * hf + q4) * 128 + ((ct ^ (q4 >> 1)) * 64) + g1 * 32 + p4 * 8);
            wb1h[j] = opq(wb1[j] + 2 * A_WS);
        }
        LAS f32x2* sxp = (LAS f32x2*)(lds + A_STAT) + tt * 64;
        LAS float* cst = (LAS float*)(lds + A_CS) + ct * 32;
        LAS float* stat = (LAS float*)(lds + A_CS + 1536) + w * 64;
        const unsigned sel = ((lane & 15) == g1) ? ones : 0u;
        const bf16x8 self = BF8(((u32x4){sel, sel, sel, sel}));
        unsigned go1[2], go2[2]; int gi[2];
#pragma unroll
        for (int p = 0; p < 2; ++p) {
            const int G = m7 * 8 + ct * 4 + 2 * p + hf, hp = G / 7, i = G - hp * 7;
            const unsigned hs = kh ? 131072u : 262144u;
            const unsigned to = (kh ? WS_QF + (tb * 2 + tt) * 2048 : WS_KV + (tb * 2 + tt) * 4096) + r * 16;
            go1[p] = (unsigned)(2 * hp + (i > 3)) * hs + (unsigned)(i > 3 ? i - 3 : i) * 512 + to;
            go2[p] = i == 3 ? (unsigned)(2 * hp + 1) * hs + to : OOB;
            gi[p] = i;
        }
        const int vcol = m7 * 64 + ct * 32 + r, vh = vcol / 28, vd = vcol - vh * 28;
        unsigned vo = WS_KV + (tb * 2 + tt) * 4096 + (unsigned)vh * 262144 + 2048 + hf * 512 + vd * 16;
        if (w < 4) {
            const unsigned pv = vd == 0 ? ones : 0u, po = vd < 4 ? vo + 448 : OOB;
            st16_dev((u32x4){pv, pv, pv, pv}, rws, po, 0); st16_dev((u32x4){pv, pv, pv, pv}, rws, po + 1024, 0);
        }
        PIN2(go1[0], go1[1]); PIN2(go2[0], go2[1]); PIN(vo);
        bf16x8 fx[4], f1[4], f2[4];
#define A_WAIT(c) do { if ((c) < 4) waitv_bar<8>(); else if ((c) == 4) waitv_bar<7>(); else if ((c) == 5) waitv_bar<3>(); else waitv_bar<0>(); if ((c) == 0) TS(1); if ((c) == 6) TS(2); } while (0)
#define A_WP(c, ks) ((((c) & 2) ? wb1h[ks] : wb1[ks]) + ((c) & 1) * A_WS)
#define A_ROWSTAT() const f32x2 sv = sxp[r] + sxp[32 + r]; const float mean = sv[0] * (1.f / D); \
            rs = __builtin_amdgcn_rsqf(fmaxf(sv[1] * (1.f / D) - mean * mean, 0.f) + EPS); mr = mean * rs;
        float rs = 0.f, mr = 0.f;
        if (w < 4) {
            f32x16 ak = {}, av = {};
            f32x4 ck4 = {}, cv4 = {}, rv[4], mv[4];
#pragma unroll
            for (int n = 0; n < 30; ++n) {
                const int c = n >> 2, ks = n & 3, km = (n - 2) & 3;
                if (n < 28 && ks == 0) A_WAIT(c);
                if (n >= 2) { ak = MFMA(f1[km], fx[km], ak); PIN(ak); } SB();
                if (n < 28) { fx[ks] = lds8(xb[ks] + c * 4096); f1[ks] = cat(trd(A_WP(c, ks)), trd(A_WP(c, ks) + 512)); } SB();
                if (n >= 2) { av = MFMA(fx[km], f2[km], av); PIN(av); } SB();
                if (n < 28) f2[ks] = cat(trd(A_WP(c, ks) + 8192), trd(A_WP(c, ks) + 8192 + 512)); SB();
                if (n >= 2 && !(km & 1)) { ck4 = MFMA16(self, f1[km], ck4); PIN(ck4); } SB();
                if (c < 4) A_PIECE(c + 3, ks);
                if (n == 25) { A_ROWSTAT(); stat[lane] = hf ? mr : rs; PIN2(rs, mr); }
                if (n == 27) {
#pragma unroll
                    for (int g = 0; g < 4; ++g) { rv[g] = *(LAS f32x4*)(stat + 8 * g + 4 * hf); mv[g] = *(LAS f32x4*)(stat + 32 + 8 * g + 4 * hf); }
                }
                SB();
                if (n >= 2 && !(km & 1)) { cv4 = MFMA16(self, f2[km], cv4); PIN(cv4); } SB();
            }
            if (lane < 16) { cst[tt * 192 + 64 + lane] = ck4[0]; cst[tt * 192 + 80 + lane] = ck4[1]; cst[tt * 192 + 128 + lane] = cv4[0]; cst[tt * 192 + 144 + lane] = cv4[1]; }
#pragma unroll
            for (int i = 0; i < 16; ++i) { ak[i] *= rs; av[i] *= rv[i >> 2][i & 3]; }
            PIN2(ak, av);
            BAR();
            TS(3);
            f32x4 ca[4], cb[4];
#pragma unroll
            for (int g = 0; g < 4; ++g) { ca[g] = *(LAS f32x4*)(cst + 64 + 8 * g + 4 * hf); cb[g] = *(LAS f32x4*)(cst + 256 + 8 * g + 4 * hf); }
            const float cv = cst[128 + r] + cst[320 + r];
            SB();
#pragma unroll
            for (int i = 0; i < 16; ++i) { ak[i] -= mr * (ca[i >> 2][i & 3] + cb[i >> 2][i & 3]); av[i] -= mv[i >> 2][i & 3] * cv; }
#pragma unroll
            for (int p = 0; p < 2; ++p) {
                const u32x4 f = pack8(ak, p);
                const auto s0 = __builtin_amdgcn_permlane32_swap(f[0], f[2], false, false), s1 = __builtin_amdgcn_permlane32_swap(f[1], f[3], false, false);
                const u32x4 c = {s0[0], s1[0], s0[1], s1[1]};
                st16_dev(c, rws, go1[p], 0); st16_dev(c, rws, go2[p], 0);
            }
            st16_dev(pack8(av, 0), rws, vo, 0); st16_dev(pack8(av, 1), rws, vo + 1024, 0);
        } else {
            f32x16 aq = {};
            f32x4 cq4 = {};
            float sx = 0.f, sxx = 0.f;
            u32x4 su[2];
#pragma unroll
            for (int n = 0; n < 30; ++n) {
                const int c = n >> 2, ks = n & 3, km = (n - 2) & 3;
                if (n < 28 && ks == 0) A_WAIT(c);
                if (n >= 2) { aq = MFMA(f1[km], fx[km], aq); PIN(aq); } SB();
                if (n < 28) { fx[ks] = lds8(xb[ks] + c * 4096); f1[ks] = cat(trd(A_WP(c, ks)), trd(A_WP(c, ks) + 512)); }
                if (c < 6 && ks < 2) su[ks] = U4(lds8(xb[ks] + (c + 1) * 4096));
                SB();
                if (n >= 2 && !(km & 1)) { cq4 = MFMA16(self, f1[km], cq4); PIN(cq4); } SB();
                if (c < 4) A_PIECE(c + 3, ks);
                if (n == 2 || n == 3) { acc_stats(U4(fx[km]), sx, sxx); PIN2(sx, sxx); }
                if (c < 6 && ks >= 2) { acc_stats(su[ks - 2], sx, sxx); PIN2(sx, sxx); }
                if (n == 23) sxp[ct * 32 + r] = (f32x2){swap_add(sx), swap_add(sxx)};
                if (n == 25) { A_ROWSTAT(); PIN2(rs, mr); }
                SB();
            }
            if (lane < 16) { cst[tt * 192 + lane] = cq4[0]; cst[tt * 192 + 16 + lane] = cq4[1]; }
            const float QS = 0.18898223650461362f * 1.4426950408889634f;
            const float rq = rs * QS, mq = mr * QS;
#pragma unroll
            for (int i = 0; i < 16; ++i) aq[i] *= rq;
            PIN(aq);
            BAR();
#pragma unroll
            for (int g = 0; g < 4; ++g) {
                const f32x4 cc = *(LAS f32x4*)(cst + 8 * g + 4 * hf) + *(LAS f32x4*)(cst + 192 + 8 * g + 4 * hf);
#pragma unroll
                for (int j = 0; j < 4; ++j) aq[4 * g + j] -= mq * cc[j];
            }
#pragma unroll
            for (int p = 0; p < 2; ++p) {
                const u32x4 f = pack8(aq, p);
                const auto s0 = __builtin_amdgcn_permlane32_swap(f[0], f[2], false, false), s1 = __builtin_amdgcn_permlane32_swap(f[1], f[3], false, false);
                const bool sp = gi[p] == 3;
                st16_dev((u32x4){s0[0], s1[0], sp ? 0u : s0[1], sp ? 0u : s1[1]}, rws, go1[p], 0);
                st16_dev((u32x4){0u, 0u, s0[1], s1[1]}, rws, go2[p], 0);
            }
        }
    } else {
        const int wid = (xcd * 4 + loc - 28) * 8 + w;
        const unsigned vo = (unsigned)(row8 * 3584 + (lane & 7) * 16);
#pragma unroll 1
        for (int u = wid, i = 0; u < 784; u += 256, ++i) {
            const size_t so = (size_t)(u / 28) * 16 * 3584 + (u % 28) * 128;
            const unsigned d_ = lds0 + w * 16384 + i * 4096;
            glds(vo, wg + so, d_); glds(vo, wg + so + 8 * 3584, d_ + 1024); glds(vo, wu + so, d_ + 2048); glds(vo, wu + so + 8 * 3584, d_ + 3072);
        }
        waitv<0>();
#pragma unroll 1
        for (int u = wid, i = 0; u < 784; u += 256, ++i) {
            lds_ptr sg = lds + w * 16384 + i * 4096 + g1 * 2048 + (4 * hf + q4) * 128 + p4 * 8;
#pragma unroll
            for (int e = 0; e < 4; ++e) st16_dev(U4(cat(trd(sg + e * 32), trd(sg + e * 32 + 1024))), rws, l16, WS_WGU + (unsigned)(((u % 28) * 4 + e) * 28 + u / 28) * 1024);
        }
    }
    TS(24);
    waitv_bar<0>();
    const u64 tag = ((u64)(unsigned)__builtin_amdgcn_readfirstlane((unsigned)(epoch >> 32)) << 32 | (u64)(unsigned)__builtin_amdgcn_readfirstlane((unsigned)epoch)) + 1;
    if (tid == 0) set_flag(ws, ffn ? FL_KV + tb * 8 + m7 : FL_RT + xcd * 4 + loc - 28, tag);
    TS(4);

    const int drb = (xcd >> 1) * 4 + l7, dfg = (xcd & 1) * 7 + m7;
    bf16x8 wf[28];
    u32x2 xa, xb;
    {
        const int head = xcd * 2 + (loc & 1), rb = loc >> 1, qt = w & 3;
        lds_ptr ap = opq(lds + kh * 8192 + l16), apB = opq(lds + 65536 + kh * 8192 + l16), apC = opq(lds + 131072 + kh * 8192 + l16);
        const unsigned lv = kh * 8192 + (l16 ^ (kh * 256));
        lds_ptr vp = opq(lds + lv), vpB = opq(lds + 65536 + lv), vpC = opq(lds + 131072 + lv);
#define AP(off) ((off) < 65536 ? ap + (off) : (off) < 131072 ? apB + ((off) - 65536) : apC + ((off) - 131072))
#define VP(off) ((off) < 65536 ? vp + (off) : (off) < 131072 ? vpB + ((off) - 65536) : vpC + ((off) - 131072))
        const char* kvs = ws + WS_KV + (size_t)head * 262144 + w * 2048;
        const char* qs = ws + WS_QF + (size_t)(head * 64 + rb * 4 + qt) * 2048 + kh * 1024;
#define KV_ISSUE(t) glds2_dev(l16, kvs + (t) * 16384, lds0 + phys(t) + w * 2048)
        if (w == 0) poll_flag(ws, FL_KV + (lane >> 1) * 8 + (head * 28 + (lane & 1) * 27) / 64, tag);
        BAR();
        TS(5);
        glds_dev(l16, qs, lds0 + 147456 + qt * 2048 + kh * 1024);
        KV_ISSUE(0); KV_ISSUE(1); KV_ISSUE(2); KV_ISSUE(3);
        waitv_bar<4>();
        TS(6);
        const bf16x8 q0 = lds8(lds + 147456 + qt * 2048 + l16), q1 = lds8(lds + 147456 + qt * 2048 + 1024 + l16);
        f32x16 o = {}, sa = {}, sb;
        bf16x8 k0 = lds8(ap + phys(0)), k1 = lds8(ap + phys(0) + 1024), v0 = lds8(vp + phys(0) + 2048), v1 = {};
        u32x4 p0, p1 = {};
        sa = MFMA(k0, q0, sa); sa = MFMA(k1, q1, sa);
        k0 = lds8(ap + phys(0) + 4096); k1 = lds8(ap + phys(0) + 5120);
        PIN(sa); SB();
        const unsigned wgo = WS_WGU + (unsigned)((ffn ? dfg : 0) * 8 + w) * 28672;
        const char* wos = wo + m7 * 128 + (size_t)w * 56 * 896;
        const unsigned wov = (unsigned)(row8 * 896) + swz;
        const rsrc_t rx = __builtin_amdgcn_make_buffer_rsrc((void*)x, 0, 2048 * 448 * 2, 0x00020000);
        const unsigned xo_ = (unsigned)(((tb * 2 + tt) * 32 + r) * 896 + m7 * 128 + ct * 64 + kh * 32 + hf * 8);
        u64 rtf = 0;
#define EXP4(S, o4) _Pragma("unroll") for (int i_ = o4; i_ < o4 + 4; ++i_) S[i_] = __builtin_amdgcn_exp2f(S[i_]);
#define WLD(s) wf[s] = BF8(ld16_dev(rws, l16 + ((s) & 3) * 1024, wgo + ((s) >> 2) * 4096))
#define SIDE(t, g) do { \
            if ((t) == 0 && (g) == 0) KV_ISSUE(4); if ((t) == 0 && (g) == 2) KV_ISSUE(5); if ((t) == 1 && (g) == 0) KV_ISSUE(6); if ((t) == 1 && (g) == 2) KV_ISSUE(7); \
            if ((t) == 2 && (g) == 0) KV_ISSUE(8); if ((t) == 1 && (g) == 3) rtf = peek_flag(ws, FL_RT + r); \
            if ((t) == 3 && (g) < 3) KV_ISSUE(9 + (g)); \
            if ((t) == 7 && (g) == 0) KV_ISSUE(12); if ((t) == 7 && (g) == 2) KV_ISSUE(13); if ((t) == 8 && (g) == 0) KV_ISSUE(14); if ((t) == 8 && (g) == 2) KV_ISSUE(15); \
            if ((t) >= 4 && (t) <= 6) WLD(((t) - 4) * 4 + (g)); \
            if ((t) == 9 || (t) == 10) WLD(((t) - 6) * 4 + (g)); \
            if ((t) == 11 || ((t) == 12 && (g) < 3)) glds(wov, wos + (((t) - 11) * 4 + (g)) * 8 * 896, lds0 + (w * 7 + ((t) - 11) * 4 + (g)) * 1024); \
            if ((t) == 12 && (g) == 3) { xa = ld8(rx, xo_, 0); xb = ld8(rx, xo_, 16); } \
            if ((t) == 13 || (t) == 14) WLD(((t) - 8) * 4 + (g)); \
            if ((t) == 14 && (g) == 0) { if (tid < 32) *(LAS u32x4*)(lds + 57344 + tid * 16) = (u32x4){0, 0, 0, 0}; } \
        } while (0)
#define TOFF(i) (phys((i) >> 1) + ((i) & 1) * 4096)
#define TILE(i, SC, SN) do { \
            __builtin_amdgcn_s_setprio(((i) & 1) ? 1 : 3); \
            if ((i) < 31) { f32x16 z_ = {}; SN = MFMA(k0, q0, z_); PIN(SN); } if ((i) < 30) k0 = lds8(AP(TOFF((i) + 2))); SB(); \
            EXP4(SC, 0); PIN(SC); SB(); \
            o = MFMA(v1, BF8(p1), o); PIN(o); v1 = lds8(VP(TOFF(i) + 3072)); SIDE((i) >> 1, ((i) & 1) * 2); SB(); \
            EXP4(SC, 4); p0 = pack8(SC, 0); PIN(p0); SB(); \
            __builtin_amdgcn_s_setprio(((i) & 1) ? 0 : 2); \
            if ((i) < 31) { SN = MFMA(k1, q1, SN); PIN(SN); } if ((i) < 30) k1 = lds8(AP(TOFF((i) + 2) + 1024)); SB(); \
            EXP4(SC, 8); PIN(SC); SB(); \
            o = MFMA(v0, BF8(p0), o); PIN(o); if ((i) < 31) v0 = lds8(VP(TOFF((i) + 1) + 2048)); SIDE((i) >> 1, ((i) & 1) * 2 + 1); SB(); \
            EXP4(SC, 12); p1 = pack8(SC, 1); PIN(p1); SB(); } while (0)
#define STEP(t) do { TILE(2 * (t), sa, sb); TILE(2 * (t) + 1, sb, sa); } while (0)
        STEP(0);
        waitv_bar<4>();
        STEP(1); STEP(2);
        waitv_bar<3>();
        TS(21);
        if (__builtin_expect(!__all(rtf == tag), 0)) poll_flag(ws, FL_RT + r, tag);
        FENCE();
        STEP(3); STEP(4); STEP(5); STEP(6);
        waitv_bar<12>();
        TS(22);
        STEP(7); STEP(8); STEP(9); STEP(10);
        waitv_bar<8>();
        TS(23);
        lds_ptr xw = opq(lds + 147456 + w * 2048 + l16), xr = opq(lds + 147456 + (w ^ 4) * 2048 + l16);
        lds_ptr lw = opq(lds + 58368 + w * 256 + lane * 4), lr = opq(lds + 58368 + qt * 256 + 128 + r * 4);
        lds_ptr bw = opq(lds + 60416 + w * 4), br = opq(lds + 60416 + (lane & 7) * 4);
        unsigned oo = WS_OF + (unsigned)((rb * 4 + qt) * 32 + head * 2 + kh) * 1024 + l16;
        PIN(oo);
        STEP(11); STEP(12); STEP(13); STEP(14); STEP(15);
        o = MFMA(v1, BF8(p1), o);
        TS(7);
        u32x4 ov;
        {
            *(LAS f32x4*)xw = (f32x4){o[8], o[9], o[10], o[11]}; *(LAS f32x4*)(xw + 1024) = (f32x4){o[12], o[13], o[14], o[15]};
            const float l = kh ? o[4] : o[12];
            *(LAS float*)lw = l;
            float sa_ = 0.f;
#pragma unroll
            for (int i = 0; i < 16; ++i) sa_ += fabsf(o[i]);
            const bool ok = sa_ < 0x1p100f && (hf == 0 || l > 0x1p-80f);
            *(LAS unsigned*)bw = __all(ok) ? 0u : 1u;
            BAR();
            const f32x4 a = *(LAS f32x4*)xr, c = *(LAS f32x4*)(xr + 1024);
            const float inv = __builtin_amdgcn_rcpf(*(LAS float*)lr + *(LAS float*)(lr + 1024));
            const unsigned bad = *(LAS unsigned*)br;
            const float iz = (kh && hf) ? 0.f : inv;
            ov = (u32x4){cvtpk((o[0] + a[0]) * inv, (o[1] + a[1]) * inv), cvtpk((o[2] + a[2]) * inv, (o[3] + a[3]) * inv),
                         cvtpk((o[4] + c[0]) * iz, (o[5] + c[1]) * iz), cvtpk((o[6] + c[2]) * iz, (o[7] + c[3]) * iz)};
            if (__builtin_expect(__any(bad != 0), 0)) {
                f32x16 of = {};
                float m = -0x1p126f;
#pragma unroll 1
                for (int t = 0; t < 16; ++t) {
                    BAR();
                    glds2_dev(l16, kvs + t * 16384, lds0 + 65536 + w * 2048);
                    waitv_bar<0>();
#pragma unroll 1
                    for (int u = 0; u < 2; ++u) {
                        f32x16 s = {};
                        s = MFMA(lds8(apB + u * 4096), q0, s); s = MFMA(lds8(apB + u * 4096 + 1024), q1, s);
                        float mx = s[0];
#pragma unroll
                        for (int i = 1; i < 16; ++i) mx = fmaxf(mx, s[i]);
                        mx = fmaxf(swap_max(mx), m);
                        const float alpha = __builtin_amdgcn_exp2f(m - mx);
                        m = mx;
#pragma unroll
                        for (int i = 0; i < 16; ++i) { s[i] = __builtin_amdgcn_exp2f(s[i] - mx); of[i] *= alpha; }
                        of = MFMA(lds8(vpB + u * 4096 + 2048), BF8(pack8(s, 0)), of);
                        of = MFMA(lds8(vpB + u * 4096 + 3072), BF8(pack8(s, 1)), of);
                    }
                }
                *(LAS f32x4*)xw = (f32x4){of[8], of[9], of[10], of[11]}; *(LAS f32x4*)(xw + 1024) = (f32x4){of[12], of[13], of[14], of[15]};
                *(LAS float*)lw = hf ? (kh ? of[4] : of[12]) : m;
                BAR();
                const f32x4 a2 = *(LAS f32x4*)xr, c2 = *(LAS f32x4*)(xr + 1024);
                const float m0 = *(LAS float*)(lr - 128), m1 = *(LAS float*)(lr + 1024 - 128), mm = fmaxf(m0, m1);
                const float e0 = __builtin_amdgcn_exp2f(m0 - mm), e1 = __builtin_amdgcn_exp2f(m1 - mm);
                const float iv = __builtin_amdgcn_rcpf(*(LAS float*)lr * e0 + *(LAS float*)(lr + 1024) * e1);
                const float im = (kh ? e1 : e0) * iv, io = (kh ? e0 : e1) * iv;
                const bool z = kh && hf;
                ov = (u32x4){cvtpk(of[0] * im + a2[0] * io, of[1] * im + a2[1] * io), cvtpk(of[2] * im + a2[2] * io, of[3] * im + a2[3] * io),
                             z ? 0u : cvtpk(of[4] * im + c2[0] * io, of[5] * im + c2[1] * io), z ? 0u : cvtpk(of[6] * im + c2[2] * io, of[7] * im + c2[3] * io)};
            }
        }
        st16_dev(ov, rws, oo, 0);
        TS(25);
        waitv_bar<0>();
        if (tid == 0) set_flag(ws, FL_O + rb * 16 + head, tag);
        TS(8);
    }
    if (!ffn) {
        const int wid = (xcd * 4 + loc - 28) * 8 + w;
        const unsigned vo = (unsigned)(row8 * 896 + (lane & 7) * 16);
#pragma unroll 1
        for (int u = wid, i = 0; u < 784; u += 256, ++i) {
            const char* s_ = wd + (size_t)(u / 7) * 16 * 896 + (u % 7) * 128;
            glds(vo, s_, lds0 + w * 8192 + i * 2048); glds(vo, s_ + 8 * 896, lds0 + w * 8192 + i * 2048 + 1024);
        }
        waitv<0>();
#pragma unroll 1
        for (int u = wid, i = 0; u < 784; u += 256, ++i) {
            lds_ptr sd = lds + w * 8192 + i * 2048 + (4 * hf + q4) * 128 + g1 * 32 + p4 * 8;
#pragma unroll
            for (int e = 0; e < 2; ++e) st16_dev(U4(cat(trd(sd + e * 64), trd(sd + e * 64 + 1024))), rws, l16, WS_WD + (unsigned)(((u % 7) * 112 + u / 7) * 2 + e) * 1024);
        }
        waitv_bar<0>();
        if (tid == 0) set_flag(ws, FL_RD + xcd * 4 + loc - 28, tag);
        return;
    }
    PIN2(xa, xb);
#pragma unroll
    for (int s = 27; s >= 0; --s) PIN(wf[s]);

    f32x16 cs = {};
    const u32x4 one4 = {ones, ones, ones, ones};
    {
#pragma unroll
        for (int s = 0; s < 14; ++s) cs = MFMA(wf[s], BF8(one4), cs);
        PIN(cs);
        lds_ptr wb = lds + kh * (8 * 28 * 128) + (4 * hf + q4) * 128 + ((ct ^ (q4 >> 1)) * 64) + (g1 ^ kh) * 32 + p4 * 8;
        bf16x8 wr[16];
#pragma unroll
        for (int s = 0; s < 12; ++s) { const int k0 = ((s >> 1) * 28 + (s & 1) * 16) * 128; wr[s] = cat(trd(wb + k0), trd(wb + k0 + 1024)); }
        f32x16 acc = {};
        acc[0] = bflo(xa[0]); acc[1] = bfhi(xa[0]); acc[2] = bflo(xa[1]); acc[3] = bfhi(xa[1]); acc[4] = bflo(xb[0]); acc[5] = bfhi(xb[0]); acc[6] = bflo(xb[1]); acc[7] = bfhi(xb[1]);
        lds_ptr cw = opq(lds + 131072 + w * 2048 + l16), cr = opq(lds + 131072 + (w ^ 4) * 2048 + l16);
        const unsigned hi_ = (unsigned)((tb * 2 + tt) * 28 + m7 * 4 + ct * 2 + kh);
        unsigned ho = WS_HF + hi_ * 1024 + l16, so = hf ? OOB : WS_HS + hi_ * 256 + r * 8;
        PIN(acc); PIN2(ho, so);
        if (w == 0) poll_flag(ws, FL_O + (tb >> 1) * 16 + (lane & 15), tag);
        BAR();
        TS(9);
        const char* os = ws + WS_OF + (size_t)tb * 65536;
        const unsigned fo = (unsigned)(tt * 32768 + kh * 16384 + ct * 4096);
        glds4_dev(l16, os + fo, lds0 + 65536 + fo); glds4_dev(l16, os + fo + 8192, lds0 + 65536 + fo + 8192);
        if (bid == 0 && tid == 0) set_flag(ws, FL_EPOCH, tag);
        lds_ptr ob = opq(lds + 65536 + tt * 32768 + kh * 16384 + l16);
        bf16x8 of[8];
        waitv_bar<4>();
        TS(26);
#pragma unroll
        for (int s = 0; s < 8; ++s) of[s] = lds8(ob + s * 1024);
        SB();
#pragma unroll
        for (int s = 0; s < 8; ++s) acc = MFMA(wr[s], of[s], acc);
        PIN(acc); SB();
#pragma unroll
        for (int s = 12; s < 16; ++s) { const int k0 = ((s >> 1) * 28 + (s & 1) * 16) * 128; wr[s] = cat(trd(wb + k0), trd(wb + k0 + 1024)); }
        waitv_bar<0>();
#pragma unroll
        for (int s = 0; s < 8; ++s) of[s] = lds8(ob + (8 + s) * 1024);
        SB();
#pragma unroll
        for (int s = 0; s < 8; ++s) acc = MFMA(wr[8 + s], of[s], acc);
        TS(10);
        *(LAS f32x4*)cw = (f32x4){acc[8], acc[9], acc[10], acc[11]}; *(LAS f32x4*)(cw + 1024) = (f32x4){acc[12], acc[13], acc[14], acc[15]};
        BAR();
        const f32x4 a = *(LAS f32x4*)cr + (f32x4){acc[0], acc[1], acc[2], acc[3]}, b = *(LAS f32x4*)(cr + 1024) + (f32x4){acc[4], acc[5], acc[6], acc[7]};
        const u32x4 hv = {cvtpk(a[0], a[1]), cvtpk(a[2], a[3]), cvtpk(b[0], b[1]), cvtpk(b[2], b[3])};
        st16_dev(hv, rws, ho, 0);
        float s1 = 0.f, s2 = 0.f;
        acc_stats(hv, s1, s2);
        s1 = swap_add(s1); s2 = swap_add(s2);
        st8_dev((u32x2){__float_as_uint(s1), __float_as_uint(s2)}, rws, so, 0);
        waitv_bar<0>();
        if (tid == 0) set_flag(ws, FL_H + tb * 8 + m7, tag);
        TS(11);
    }

    const int erb = tb >> 1;
    const unsigned wdo = WS_WD + (unsigned)(m7 * 112 + w * 14) * 2048;
    bf16x8 wdf[28];
    u64 gfl = 0;
    const unsigned gfi = FL_G + erb * 32 + min(lane & 15, 13);
    {
#pragma unroll
        for (int s = 14; s < 28; ++s) cs = MFMA(wf[s], BF8(one4), cs);
        lds_ptr csp = opq(lds + 161792 + w * 128 + hf * 64);
        if (r == 0) {
#pragma unroll
            for (int g = 0; g < 4; ++g) *(LAS f32x4*)(csp + g * 16) = (f32x4){cs[4 * g], cs[4 * g + 1], cs[4 * g + 2], cs[4 * g + 3]};
        }
        const char* hs = ws + WS_HF + (size_t)drb * 114688 + w * 1024;
        const unsigned hd = lds0 + w * 1024;
        const char* wds = ws + wdo;
        lds_ptr hb = opq(lds + l16), hb2 = opq(lds + 57344 + l16);
        lds_ptr wl = opq(lds + 114688 + w * 5120 + l16), wl2 = opq(lds + w * 7168 + l16);
        LAS f32x2* stp = (LAS f32x2*)opq(lds + 159744 + r * 8);
        unsigned go = WS_GF + (unsigned)(drb * 4 * 112 + dfg * 8 + w) * 1024 + l16;
        PIN(go);
        if (w == 0) { const int i = lane & 15; poll_flag(ws, hf ? FL_RD + r : FL_H + drb * 16 + (i >> 3) * 8 + ((i & 7) == 7 ? 0 : (i & 7)), tag); }
        BAR();
        TS(12);
#define H_PIECE(j) glds_dev(l16, hs + (j) * 8192, hd + (j) * 8192)
#define WDLD(s) wdf[s] = BF8(ld16_dev(rws, l16 + ((s) & 3) * 1024, wdo + ((s) >> 2) * 4096))
#define HB(n) ((n) < 56 ? hb + (n) * 1024 : hb2 + ((n) - 56) * 1024)
        H_PIECE(0); H_PIECE(1); H_PIECE(2); H_PIECE(3);
        glds4_dev(l16, ws + WS_HS + (size_t)((drb * 4 + (w & 3)) * 28 + kh * 14) * 256, lds0 + 114688 + w * 5120);
        constexpr int PF = 5;
        bf16x8 bf[PF + 1];
        f32x16 acc[2];
        f32x2 sp[7], sv0, sv1;
        f32x4 c4[4];
        float mean = 0.f, r2 = 0.f;
        u32x4 hv;
        waitv_bar<7>();
        TS(27);
#pragma unroll
        for (int n = 0; n < PF; ++n) bf[n] = lds8(HB(n));
        SB();
        sfor<0, 122>([&](auto N_) __attribute__((always_inline)) {
            constexpr int n = decltype(N_)::v, p = n / 28, s = n % 28;
            if constexpr (n == 3) waitv_bar<5>();
            if constexpr (n == 27) { waitv_bar<3>(); TS(13); }
            if constexpr (n == 64) { waitv_bar<12>(); TS(14); }
            if constexpr (n == 84) { waitv_bar<7>(); if (tid == 0) set_flag(ws, FL_G + drb * 32 + dfg, tag); }
            if constexpr (n < 112) { if constexpr (s == 0) { f32x16 z = {}; acc[p & 1] = MFMA(wf[s], bf[n % (PF + 1)], z); } else acc[p & 1] = MFMA(wf[s], bf[n % (PF + 1)], acc[p & 1]); PIN(acc[p & 1]); }
            SB();
            if constexpr (n + PF < 112) bf[(n + PF) % (PF + 1)] = lds8(HB(n + PF));
            if constexpr (n % 3 == 2 && n < 30) H_PIECE(4 + n / 3);
            if constexpr (n == 16) { waitv<5>();
#pragma unroll
                for (int j = 0; j < 7; ++j) sp[j] = *(LAS f32x2*)(lds + 114688 + w * 5120 + (hf * 7 + j) * 256 + r * 8);
            }
            if constexpr (n == 20) { const f32x2 t = ((sp[0] + sp[1]) + (sp[2] + sp[3])) + ((sp[4] + sp[5]) + sp[6]); stp[w * 32] = (f32x2){swap_add(t[0]), swap_add(t[1])}; }
            if constexpr (n >= 33 && n < 48 && n % 3 == 0) glds_dev(l16, wds + (n - 33) / 3 * 1024, lds0 + 114688 + w * 5120 + (n - 33) / 3 * 1024);
            if constexpr (n >= 48 && n < 64 && n % 3 == 0) WDLD(12 + (n - 48) / 3);
            if constexpr (n >= 70 && n < 84 && n % 2 == 0) glds_dev(l16, wds + (5 + (n - 70) / 2) * 1024, lds0 + w * 7168 + (n - 70) / 2 * 1024);
            if constexpr (n >= 86 && n < 106 && n % 2 == 0) WDLD(18 + (n - 86) / 2);
            if constexpr (n == 106) gfl = peek_flag(ws, gfi);
            if constexpr (p >= 1) {
                const f32x16& a = acc[(p - 1) & 1];
                if constexpr (n == 100) { sv0 = stp[3 * 32]; sv1 = stp[7 * 32]; }
                if constexpr (s == 0 && p < 4) { sv0 = stp[(p - 1) * 32]; sv1 = stp[(p + 3) * 32];
#pragma unroll
                    for (int g = 0; g < 4; ++g) c4[g] = *(LAS f32x4*)(csp + g * 16); }
                if constexpr ((s == 3 && p < 4) || n == 104) { const f32x2 sv = sv0 + sv1; mean = sv[0] * (1.f / D); r2 = __builtin_amdgcn_rcpf(fmaxf(sv[1] * (1.f / D) - mean * mean, 0.f) + EPS); PIN2(mean, r2); }
                if constexpr (s >= 4 && s < 8) { constexpr int i = 2 * (s - 4);
                    const float h0 = fmaxf(a[i] - mean * c4[i >> 2][i & 3], 0.f) * r2 * (a[8 + i] - mean * c4[2 + (i >> 2)][i & 3]);
                    const float h1 = fmaxf(a[i + 1] - mean * c4[i >> 2][(i & 3) + 1], 0.f) * r2 * (a[9 + i] - mean * c4[2 + (i >> 2)][(i & 3) + 1]);
                    hv[s - 4] = cvtpk(h0, h1); PIN(hv); }
                if constexpr (s == 9) { st16_dev(hv, rws, go + (p - 1) * 114688, 0); FENCE(); }
            }
            SB();
        });
        TS(15);
        if (__builtin_expect(!__all(gfl == tag), 0)) poll_flag(ws, gfi, tag);
        FENCE();
#pragma unroll
        for (int s = 0; s < 12; ++s) wdf[s] = lds8(s < 5 ? wl + s * 1024 : wl2 + (s - 5) * 1024);
    }

    {
        const int etok = tid >> 4, eblk = tid & 15;
        const int ta = erb * 4 + (tb & 1);
        const unsigned hro = (unsigned)((((eblk >> 2) & 3) * 64 + (eblk & 1) * 32 + etok) * 16 + ((eblk >> 1) & 1) * 8);
        bf16x8 gf[14];
        u32x2 hres[2];
#define G_LOAD(hh) do { const unsigned go = WS_GF + (unsigned)((ta + 2 * (hh)) * 112 + w * 14) * 1024; \
            _Pragma("unroll") for (int s = 0; s < 14; ++s) { gf[s] = BF8(ld16_dev(rws, l16 + (s & 3) * 1024, go + (s >> 2) * 4096)); SB(); } \
            hres[hh] = ld8_dev(rws, hro, WS_HF + (unsigned)((ta + 2 * (hh)) * 28 + m7 * 4) * 1024); FENCE(); SB(); } while (0)
#define G_MMA(hh) do { f32x16 c0 = {}, c1 = {}; \
            _Pragma("unroll") for (int s = 0; s < 14; ++s) { c0 = MFMA(wdf[2 * s], gf[s], c0); c1 = MFMA(wdf[2 * s + 1], gf[s], c1); PIN2(c0, c1); SB(); } \
            lds_ptr rp = opq(lds + (hh) * 67584 + w * 8448 + hf * 528 + r * 16); \
            _Pragma("unroll") for (int g = 0; g < 4; ++g) { \
                *(LAS f32x4*)(rp + g * 1056) = (f32x4){c0[4 * g], c0[4 * g + 1], c0[4 * g + 2], c0[4 * g + 3]}; \
                *(LAS f32x4*)(rp + 4224 + g * 1056) = (f32x4){c1[4 * g], c1[4 * g + 1], c1[4 * g + 2], c1[4 * g + 3]}; } } while (0)
#define G_OUT(hh) do { lds_ptr rq = opq(lds + (hh) * 67584 + eblk * 528 + etok * 16); f32x4 pa[8]; \
            _Pragma("unroll") for (int ww = 0; ww < 8; ++ww) pa[ww] = *(LAS f32x4*)(rq + ww * 8448); SB(); \
            const f32x4 a = ((pa[0] + pa[1]) + (pa[2] + pa[3])) + ((pa[4] + pa[5]) + (pa[6] + pa[7])); \
            const u32x2 ov = {cvtpk(a[0] + bflo(hres[hh][0]), a[1] + bfhi(hres[hh][0])), cvtpk(a[2] + bflo(hres[hh][1]), a[3] + bfhi(hres[hh][1]))}; \
            *(u32x2*)(out + (size_t)((ta + 2 * (hh)) * 32 + etok) * 896 + m7 * 128 + eblk * 8) = ov; } while (0)
        G_LOAD(0);
        waitv_bar<15>();
        if (tid == 0) set_flag(ws, FL_G + drb * 32 + 16 + dfg, tag);
        TS(16);
        G_MMA(0);
        TS(17);
        if (w == 0) poll_flag(ws, gfi + 16, tag);
        BAR();
        TS(18);
        G_LOAD(1);
        G_OUT(0);
        FENCE(); SB();
        G_MMA(1);
        TS(19);
        BAR();
        G_OUT(1);
    }
    TS(20);
}

extern "C" void kernel_launch(void* const* d_in, const int* in_sizes, int n_in, void* d_out, int out_size, void* d_ws, size_t ws_size, hipStream_t stream) {
    (void)hipFuncSetAttribute((const void*)block_fwd, hipFuncAttributeMaxDynamicSharedMemorySize, LDS_BYTES);
    block_fwd<<<256, 512, LDS_BYTES, stream>>>((const char*)d_in[0], (const char*)d_in[1], (const char*)d_in[2], (const char*)d_in[4], (const char*)d_in[5], (const char*)d_in[6], (char*)d_out, (char*)d_ws);
}
```

```cpp
#include <hip/hip_runtime.h>
#include <hip/hip_bf16.h>
#include <stdint.h>

typedef __attribute__((ext_vector_type(8))) short bf16x8;
typedef __attribute__((ext_vector_type(4))) short s16x4;
typedef __attribute__((ext_vector_type(2))) __bf16 bf16x2;
typedef __attribute__((ext_vector_type(16))) float f32x16;
typedef __attribute__((ext_vector_type(4))) float f32x4;
typedef __attribute__((ext_vector_type(2))) float f32x2;
typedef __attribute__((ext_vector_type(4))) unsigned u32x4;
typedef __attribute__((ext_vector_type(2))) unsigned u32x2;
typedef short v4i16_t __attribute__((ext_vector_type(4)));
typedef unsigned long long u64;
#define LAS __attribute__((address_space(3)))
#define GAS __attribute__((address_space(1)))
typedef LAS char* lds_ptr;
#define DI __device__ __forceinline__
#define MFMA(a, b, c) __builtin_amdgcn_mfma_f32_32x32x16_bf16(a, b, c, 0, 0, 0)
#define MFMA16(a, b, c) __builtin_amdgcn_mfma_f32_16x16x32_bf16(a, b, c, 0, 0, 0)
#define BF8(x) __builtin_bit_cast(bf16x8, x)
#define U4(x) __builtin_bit_cast(u32x4, x)

constexpr int D = 448;
constexpr float EPS = 1e-5f;
constexpr unsigned WS_FLAG = 0;
constexpr unsigned FL_EPOCH = 0, FL_KV = 1, FL_RT = FL_KV + 256, FL_RD = FL_RT + 32, FL_O = FL_RD + 32, FL_H = FL_O + 256, FL_G = FL_H + 256;
constexpr unsigned WS_KV = 1u << 20;
constexpr unsigned WS_QF = WS_KV + 4194304;
constexpr unsigned WS_OF = WS_QF + 2097152;
constexpr unsigned WS_HF = WS_OF + 2097152;
constexpr unsigned WS_GF = WS_HF + 64 * 28 * 1024;
constexpr unsigned WS_WGU = WS_GF + 64 * 112 * 1024;
constexpr unsigned WS_WD = WS_WGU + 112 * 28 * 1024;
constexpr unsigned WS_HS = WS_WD + 7 * 112 * 2 * 1024;
constexpr unsigned WS_END = WS_HS + 64 * 28 * 256 + 1024;
constexpr unsigned OOB = 0x40000000u;
constexpr int LDS_BYTES = 163840;

DI unsigned cvtpk(float lo, float hi) { return __builtin_bit_cast(unsigned, __builtin_convertvector((f32x2){lo, hi}, bf16x2)); }
DI float bflo(unsigned u) { return __uint_as_float(u << 16); }
DI float bfhi(unsigned u) { return __uint_as_float(u & 0xffff0000u); }
DI void glds(unsigned voff, const void* sbase, unsigned dst) {
    asm volatile("s_mov_b32 m0, %2\n\ts_nop 0\n\tglobal_load_lds_dwordx4 %0, %1" :: "v"(voff), "s"(sbase), "s"(dst) : "memory");
}
DI void glds_dev(unsigned voff, const void* sbase, unsigned dst) {
    asm volatile("s_mov_b32 m0, %2\n\ts_nop 0\n\tglobal_load_lds_dwordx4 %0, %1 sc1" :: "v"(voff), "s"(sbase), "s"(dst) : "memory");
}
DI void glds2_dev(unsigned voff, const void* sbase, unsigned dst) {
    asm volatile("s_mov_b32 m0, %2\n\ts_nop 0\n\tglobal_load_lds_dwordx4 %0, %1 sc1\n\tglobal_load_lds_dwordx4 %0, %1 offset:1024 sc1" :: "v"(voff), "s"(sbase), "s"(dst) : "memory");
}
DI void glds4_dev(unsigned voff, const void* sbase, unsigned dst) {
    asm volatile("s_mov_b32 m0, %2\n\ts_nop 0\n\tglobal_load_lds_dwordx4 %0, %1 sc1\n\tglobal_load_lds_dwordx4 %0, %1 offset:1024 sc1\n\t"
                 "global_load_lds_dwordx4 %0, %1 offset:2048 sc1\n\tglobal_load_lds_dwordx4 %0, %1 offset:3072 sc1" :: "v"(voff), "s"(sbase), "s"(dst) : "memory");
}
DI s16x4 trd(lds_ptr p) { return __builtin_bit_cast(s16x4, __builtin_amdgcn_ds_read_tr16_b64_v4i16((LAS v4i16_t*)p)); }
DI bf16x8 cat(s16x4 a, s16x4 b) { return __builtin_shufflevector(a, b, 0, 1, 2, 3, 4, 5, 6, 7); }
DI bf16x8 lds8(lds_ptr p) { return *(LAS bf16x8*)p; }
DI float swap_add(float v) { auto r = __builtin_amdgcn_permlane32_swap(__float_as_uint(v), __float_as_uint(v), false, false); return __uint_as_float(r[0]) + __uint_as_float(r[1]); }
DI float swap_max(float v) { auto r = __builtin_amdgcn_permlane32_swap(__float_as_uint(v), __float_as_uint(v), false, false); return fmaxf(__uint_as_float(r[0]), __uint_as_float(r[1])); }
DI u32x4 pack8(const f32x16& a, int s) { return (u32x4){cvtpk(a[8 * s], a[8 * s + 1]), cvtpk(a[8 * s + 2], a[8 * s + 3]), cvtpk(a[8 * s + 4], a[8 * s + 5]), cvtpk(a[8 * s + 6], a[8 * s + 7])}; }
DI float dot2(unsigned a, unsigned b, float c) { return __builtin_amdgcn_fdot2_f32_bf16(__builtin_bit_cast(bf16x2, a), __builtin_bit_cast(bf16x2, b), c, false); }
DI void acc_stats(u32x4 u, float& s1, float& s2) {
#pragma unroll
    for (int i = 0; i < 4; ++i) { const float a = bflo(u[i]), b = bfhi(u[i]); s1 += a; s2 = fmaf(a, a, s2); s1 += b; s2 = fmaf(b, b, s2); }
}
template <int N> struct IC { static constexpr int v = N; };
template <int B, int E, class F> DI void sfor(F&& f) { if constexpr (B < E) { f(IC<B>{}); sfor<B + 1, E>(f); } }
template <int N> DI void waitv() { asm volatile("s_waitcnt vmcnt(%0)" :: "i"(N) : "memory"); }
template <int N> DI void waitv_bar() { asm volatile("s_waitcnt vmcnt(%0) lgkmcnt(0)\n\ts_barrier" :: "i"(N) : "memory"); }
#define BAR() asm volatile("s_waitcnt lgkmcnt(0)\n\ts_barrier" ::: "memory")
#define FENCE() asm volatile("" ::: "memory")
typedef __amdgpu_buffer_rsrc_t rsrc_t;
DI u32x4 ld16(rsrc_t rs, unsigned voff, unsigned soff) { return U4(__builtin_amdgcn_raw_buffer_load_b128(rs, voff, soff, 0)); }
DI u32x4 ld16_dev(rsrc_t rs, unsigned voff, unsigned soff) { return U4(__builtin_amdgcn_raw_buffer_load_b128(rs, voff, soff, 16)); }
DI u32x2 ld8(rsrc_t rs, unsigned voff, unsigned soff) { return __builtin_bit_cast(u32x2, __builtin_amdgcn_raw_buffer_load_b64(rs, voff, soff, 0)); }
DI u32x2 ld8_dev(rsrc_t rs, unsigned voff, unsigned soff) { return __builtin_bit_cast(u32x2, __builtin_amdgcn_raw_buffer_load_b64(rs, voff, soff, 16)); }
DI void st16_dev(u32x4 v, rsrc_t rs, unsigned voff, unsigned soff) { __builtin_amdgcn_raw_buffer_store_b128(v, rs, voff + soff, 0, 16); }
DI void st8_dev(u32x2 v, rsrc_t rs, unsigned voff, unsigned soff) { __builtin_amdgcn_raw_buffer_store_b64(v, rs, voff, soff, 16); }
DI void set_flag(char* ws, unsigned idx, u64 tag) { __hip_atomic_store((GAS u64*)(ws + WS_FLAG + (size_t)idx * 128), tag, __ATOMIC_RELAXED, __HIP_MEMORY_SCOPE_AGENT); }
DI void poll_flag(char* ws, unsigned idx, u64 tag) {
    GAS u64* p = (GAS u64*)(ws + WS_FLAG + (size_t)idx * 128);
#pragma unroll 1
    for (int spin = 0; spin < (1 << 16); ++spin) {
        const u64 v = __hip_atomic_load(p, __ATOMIC_RELAXED, __HIP_MEMORY_SCOPE_AGENT);
        if (__all(v == tag)) break;
        __builtin_amdgcn_s_sleep(1);
    }
}
DI u64 peek_flag(char* ws, unsigned idx) { return __hip_atomic_load((GAS u64*)(ws + WS_FLAG + (size_t)idx * 128), __ATOMIC_RELAXED, __HIP_MEMORY_SCOPE_AGENT); }
#define SB() __builtin_amdgcn_sched_barrier(0)
#define PIN(a) asm volatile("" : "+v"(a))
#define PIN2(a, b) asm volatile("" : "+v"(a), "+v"(b))
DI lds_ptr opq(lds_ptr p) { unsigned u = (unsigned)(uintptr_t)p; asm("" : "+v"(u)); return (lds_ptr)(uintptr_t)u; }
#define TS(i) do {} while (0)

constexpr int wd_slot(int s) { return (s * 17 + 27) / 28 != ((s + 1) * 17 + 27) / 28 ? (s * 17 + 27) / 28 : -1; }
constexpr int phys(int t) { return ((t % 9) + 2) % 9 * 16384; }

__global__ __launch_bounds__(512, 2) void block_fwd(const char* x, const char* wqkv, const char* wo, const char* wg, const char* wu, const char* wd, char* out, char* ws) {
    extern __shared__ __attribute__((aligned(16))) char smem[];
    lds_ptr lds = (lds_ptr)smem;
    const int tid = threadIdx.x, lane = tid & 63, w = __builtin_amdgcn_readfirstlane(tid >> 6);
    const int bid = blockIdx.x, xcd = bid & 7, loc = bid >> 3;
    const int r = lane & 31, hf = lane >> 5, q4 = (lane & 15) >> 2, g1 = (lane >> 4) & 1, p4 = lane & 3;
    const unsigned lds0 = (unsigned)(uintptr_t)smem;
    const unsigned l16 = lane * 16;
    TS(0);
    const rsrc_t rws = __builtin_amdgcn_make_buffer_rsrc((void*)ws, 0, WS_END, 0x00020000);
    const u64 epoch = peek_flag(ws, FL_EPOCH);
    {
        u64 pc; unsigned len;
        asm volatile("s_getpc_b64 %0\nblk_fwd_code_begin:\n\ts_mov_b32 %1, blk_fwd_code_end-blk_fwd_code_begin" : "=s"(pc), "=s"(len));
        const unsigned off = (unsigned)((loc % 5) * 64 + lane) * 128;
        if (w == 7) asm volatile("s_mov_b32 m0, %2\n\ts_nop 0\n\tglobal_load_lds_dword %0, %1" :: "v"(min(off, len - 4) & ~3u), "s"(pc), "s"(lds0 + 163584) : "memory");
    }
    const unsigned ones = 0x3f803f80u;
    const bool ffn = loc < 28;
    const int l7 = loc / 7, m7 = loc % 7;
    const int tb = xcd * 4 + l7;
    const int tt = w & 1, ct = (w >> 1) & 1, kh = w >> 2;
    const int row8 = lane >> 3;
    const unsigned swz = (unsigned)(((lane & 7) ^ (((row8 >> 1) & 1) << 2)) * 16);

    if (ffn) {
        constexpr int A_W = 57344, A_WS = 24576, A_STAT = 155648, A_CS = A_STAT + 1024;
        const int xr = (w & 3) * 8 + row8;
        const unsigned xo = (unsigned)(xr * 896 + (((lane & 7) ^ ((xr >> 1) & 7)) * 16));
        const char* xs = x + (size_t)(tb * 64 + kh * 32) * 896;
        const unsigned xd = lds0 + kh * 28672 + (w & 3) * 1024;
        const unsigned wvo = (unsigned)(row8 * 2688) + swz;
        const char* wsrc = wqkv + m7 * 128 + (size_t)w * 8 * 2688;
        const unsigned wdst = lds0 + A_W + w * 1024;
        const int rot = (2 * l7 + m7) % 7;
#define A_KC(c) (rot + (c) >= 7 ? rot + (c) - 7 : rot + (c))
#define A_PIECE(c, i) do { if ((i) == 0) { if ((c) < 6) glds(xo, xs + A_KC((c) + 1) * 128, xd + ((c) + 1) * 4096); } \
                           else glds(wvo, wsrc + A_KC(c) * (64 * 2688) + ((i) % 3) * 896, wdst + ((c) & 3) * A_WS + ((i) % 3) * 8192); } while (0)
        glds(xo, xs + rot * 128, xd);
#pragma unroll
        for (int c = 0; c < 3; ++c) { A_PIECE(c, 0); A_PIECE(c, 1); A_PIECE(c, 2); A_PIECE(c, 3); }
        lds_ptr xb[4], wb1[4], wb1h[4];
#pragma unroll
        for (int j = 0; j < 4; ++j) {
            const int ks = j ^ (w & 3);
            xb[j] = lds + tt * 28672 + (r >> 3) * 1024 + (r & 7) * 128 + (((2 * ks + hf) ^ ((r >> 1) & 7)) * 16);
            wb1[j] = opq(lds + A_W + (kh ? 0 : 8192) + ks * 2048 + (8 * hf + q4) * 128 + ((ct ^ (q4 >> 1)) * 64) + g1 * 32 + p4 * 8);
            wb1h[j] = opq(wb1[j] + 2 * A_WS);
        }
        LAS f32x2* sxp = (LAS f32x2*)(lds + A_STAT) + tt * 64;
        LAS float* cst = (LAS float*)(lds + A_CS) + ct * 32;
        LAS float* stat = (LAS float*)(lds + A_CS + 1536) + w * 64;
        const unsigned sel = ((lane & 15) == g1) ? ones : 0u;
        const bf16x8 self = BF8(((u32x4){sel, sel, sel, sel}));
        unsigned go1[2], go2[2]; int gi[2];
#pragma unroll
        for (int p = 0; p < 2; ++p) {
            const int G = m7 * 8 + ct * 4 + 2 * p + hf, hp = G / 7, i = G - hp * 7;
            const unsigned hs = kh ? 131072u : 262144u;
            const unsigned to = (kh ? WS_QF + (tb * 2 + tt) * 2048 : WS_KV + (tb * 2 + tt) * 4096) + r * 16;
            go1[p] = (unsigned)(2 * hp + (i > 3)) * hs + (unsigned)(i > 3 ? i - 3 : i) * 512 + to;
            go2[p] = i == 3 ? (unsigned)(2 * hp + 1) * hs + to : OOB;
            gi[p] = i;
        }
        const int vcol = m7 * 64 + ct * 32 + r, vh = vcol / 28, vd = vcol - vh * 28;
        unsigned vo = WS_KV + (tb * 2 + tt) * 4096 + (unsigned)vh * 262144 + 2048 + hf * 512 + vd * 16;
        if (w < 4) {
            const unsigned pv = vd == 0 ? ones : 0u, po = vd < 4 ? vo + 448 : OOB;
            st16_dev((u32x4){pv, pv, pv, pv}, rws, po, 0); st16_dev((u32x4){pv, pv, pv, pv}, rws, po + 1024, 0);
        }
        PIN2(go1[0], go1[1]); PIN2(go2[0], go2[1]); PIN(vo);
        bf16x8 fx[4], f1[4], f2[4];
#define A_WAIT(c) do { if ((c) < 4) waitv_bar<8>(); else if ((c) == 4) waitv_bar<7>(); else if ((c) == 5) waitv_bar<3>(); else waitv_bar<0>(); if ((c) == 0) TS(1); if ((c) == 6) TS(2); } while (0)
#define A_WP(c, ks) ((((c) & 2) ? wb1h[ks] : wb1[ks]) + ((c) & 1) * A_WS)
#define A_ROWSTAT() const f32x2 sv = sxp[r] + sxp[32 + r]; const float mean = sv[0] * (1.f / D); \
            rs = __builtin_amdgcn_rsqf(fmaxf(sv[1] * (1.f / D) - mean * mean, 0.f) + EPS); mr = mean * rs;
        float rs = 0.f, mr = 0.f;
        if (w < 4) {
            f32x16 ak = {}, av = {};
            f32x4 ck4 = {}, cv4 = {}, rv[4], mv[4];
#pragma unroll
            for (int n = 0; n < 30; ++n) {
                const int c = n >> 2, ks = n & 3, km = (n - 2) & 3;
                if (n < 28 && ks == 0) A_WAIT(c);
                if (n >= 2) { ak = MFMA(f1[km], fx[km], ak); PIN(ak); } SB();
                if (n < 28) { fx[ks] = lds8(xb[ks] + c * 4096); f1[ks] = cat(trd(A_WP(c, ks)), trd(A_WP(c, ks) + 512)); } SB();
                if (n >= 2) { av = MFMA(fx[km], f2[km], av); PIN(av); } SB();
                if (n < 28) f2[ks] = cat(trd(A_WP(c, ks) + 8192), trd(A_WP(c, ks) + 8192 + 512)); SB();
                if (n >= 2 && !(km & 1)) { ck4 = MFMA16(self, f1[km], ck4); PIN(ck4); } SB();
                if (c < 4) A_PIECE(c + 3, ks);
                if (n == 25) { A_ROWSTAT(); stat[lane] = hf ? mr : rs; PIN2(rs, mr); }
                if (n == 27) {
#pragma unroll
                    for (int g = 0; g < 4; ++g) { rv[g] = *(LAS f32x4*)(stat + 8 * g + 4 * hf); mv[g] = *(LAS f32x4*)(stat + 32 + 8 * g + 4 * hf); }
                }
                SB();
                if (n >= 2 && !(km & 1)) { cv4 = MFMA16(self, f2[km], cv4); PIN(cv4); } SB();
            }
            if (lane < 16) { cst[tt * 192 + 64 + lane] = ck4[0]; cst[tt * 192 + 80 + lane] = ck4[1]; cst[tt * 192 + 128 + lane] = cv4[0]; cst[tt * 192 + 144 + lane] = cv4[1]; }
#pragma unroll
            for (int i = 0; i < 16; ++i) { ak[i] *= rs; av[i] *= rv[i >> 2][i & 3]; }
            PIN2(ak, av);
            BAR();
            TS(3);
            f32x4 ca[4], cb[4];
#pragma unroll
            for (int g = 0; g < 4; ++g) { ca[g] = *(LAS f32x4*)(cst + 64 + 8 * g + 4 * hf); cb[g] = *(LAS f32x4*)(cst + 256 + 8 * g + 4 * hf); }
            const float cv = cst[128 + r] + cst[320 + r];
            SB();
#pragma unroll
            for (int i = 0; i < 16; ++i) { ak[i] -= mr * (ca[i >> 2][i & 3] + cb[i >> 2][i & 3]); av[i] -= mv[i >> 2][i & 3] * cv; }
#pragma unroll
            for (int p = 0; p < 2; ++p) {
                const u32x4 f = pack8(ak, p);
                const auto s0 = __builtin_amdgcn_permlane32_swap(f[0], f[2], false, false), s1 = __builtin_amdgcn_permlane32_swap(f[1], f[3], false, false);
                const u32x4 c = {s0[0], s1[0], s0[1], s1[1]};
                st16_dev(c, rws, go1[p], 0); st16_dev(c, rws, go2[p], 0);
            }
            st16_dev(pack8(av, 0), rws, vo, 0); st16_dev(pack8(av, 1), rws, vo + 1024, 0);
        } else {
            f32x16 aq = {};
            f32x4 cq4 = {};
            float sx = 0.f, sxx = 0.f;
            u32x4 su[2];
#pragma unroll
            for (int n = 0; n < 30; ++n) {
                const int c = n >> 2, ks = n & 3, km = (n - 2) & 3;
                if (n < 28 && ks == 0) A_WAIT(c);
                if (n >= 2) { aq = MFMA(f1[km], fx[km], aq); PIN(aq); } SB();
                if (n < 28) { fx[ks] = lds8(xb[ks] + c * 4096); f1[ks] = cat(trd(A_WP(c, ks)), trd(A_WP(c, ks) + 512)); }
                if (c < 6 && ks < 2) su[ks] = U4(lds8(xb[ks] + (c + 1) * 4096));
                SB();
                if (n >= 2 && !(km & 1)) { cq4 = MFMA16(self, f1[km], cq4); PIN(cq4); } SB();
                if (c < 4) A_PIECE(c + 3, ks);
                if (n == 2 || n == 3) { acc_stats(U4(fx[km]), sx, sxx); PIN2(sx, sxx); }
                if (c < 6 && ks >= 2) { acc_stats(su[ks - 2], sx, sxx); PIN2(sx, sxx); }
                if (n == 23) sxp[ct * 32 + r] = (f32x2){swap_add(sx), swap_add(sxx)};
                if (n == 25) { A_ROWSTAT(); PIN2(rs, mr); }
                SB();
            }
            if (lane < 16) { cst[tt * 192 + lane] = cq4[0]; cst[tt * 192 + 16 + lane] = cq4[1]; }
            const float QS = 0.18898223650461362f * 1.4426950408889634f;
            const float rq = rs * QS, mq = mr * QS;
#pragma unroll
            for (int i = 0; i < 16; ++i) aq[i] *= rq;
            PIN(aq);
            BAR();
#pragma unroll
            for (int g = 0; g < 4; ++g) {
                const f32x4 cc = *(LAS f32x4*)(cst + 8 * g + 4 * hf) + *(LAS f32x4*)(cst + 192 + 8 * g + 4 * hf);
#pragma unroll
                for (int j = 0; j < 4; ++j) aq[4 * g + j] -= mq * cc[j];
            }
#pragma unroll
            for (int p = 0; p < 2; ++p) {
                const u32x4 f = pack8(aq, p);
                const auto s0 = __builtin_amdgcn_permlane32_swap(f[0], f[2], false, false), s1 = __builtin_amdgcn_permlane32_swap(f[1], f[3], false, false);
                const bool sp = gi[p] == 3;
                st16_dev((u32x4){s0[0], s1[0], sp ? 0u : s0[1], sp ? 0u : s1[1]}, rws, go1[p], 0);
                st16_dev((u32x4){0u, 0u, s0[1], s1[1]}, rws, go2[p], 0);
            }
        }
    } else {
        const int wid = (xcd * 4 + loc - 28) * 8 + w;
        const unsigned vo = (unsigned)(row8 * 3584 + (lane & 7) * 16);
#pragma unroll 1
        for (int u = wid, i = 0; u < 784; u += 256, ++i) {
            const size_t so = (size_t)(u / 28) * 16 * 3584 + (u % 28) * 128;
            const unsigned d_ = lds0 + w * 16384 + i * 4096;
            glds(vo, wg + so, d_); glds(vo, wg + so + 8 * 3584, d_ + 1024); glds(vo, wu + so, d_ + 2048); glds(vo, wu + so + 8 * 3584, d_ + 3072);
        }
        waitv<0>();
#pragma unroll 1
        for (int u = wid, i = 0; u < 784; u += 256, ++i) {
            lds_ptr sg = lds + w * 16384 + i * 4096 + g1 * 2048 + (4 * hf + q4) * 128 + p4 * 8;
#pragma unroll
            for (int e = 0; e < 4; ++e) st16_dev(U4(cat(trd(sg + e * 32), trd(sg + e * 32 + 1024))), rws, l16, WS_WGU + (unsigned)(((u % 28) * 4 + e) * 28 + u / 28) * 1024);
        }
    }
    TS(24);
    waitv_bar<0>();
    const u64 tag = ((u64)(unsigned)__builtin_amdgcn_readfirstlane((unsigned)(epoch >> 32)) << 32 | (u64)(unsigned)__builtin_amdgcn_readfirstlane((unsigned)epoch)) + 1;
    if (tid == 0) set_flag(ws, ffn ? FL_KV + tb * 8 + m7 : FL_RT + xcd * 4 + loc - 28, tag);
    TS(4);

    const int drb = (xcd >> 1) * 4 + l7, dfg = (xcd & 1) * 7 + m7;
    bf16x8 wf[28];
    u32x2 xa, xb;
    {
        const int head = xcd * 2 + (loc & 1), rb = loc >> 1, qt = w & 3;
        lds_ptr ap = opq(lds + kh * 8192 + l16), apB = opq(lds + 65536 + kh * 8192 + l16), apC = opq(lds + 131072 + kh * 8192 + l16);
        const unsigned lv = kh * 8192 + (l16 ^ (kh * 256));
        lds_ptr vp = opq(lds + lv), vpB = opq(lds + 65536 + lv), vpC = opq(lds + 131072 + lv);
#define AP(off) ((off) < 65536 ? ap + (off) : (off) < 131072 ? apB + ((off) - 65536) : apC + ((off) - 131072))
#define VP(off) ((off) < 65536 ? vp + (off) : (off) < 131072 ? vpB + ((off) - 65536) : vpC + ((off) - 131072))
        const char* kvs = ws + WS_KV + (size_t)head * 262144 + w * 2048;
        const char* qs = ws + WS_QF + (size_t)(head * 64 + rb * 4 + qt) * 2048 + kh * 1024;
#define KV_ISSUE(t) glds2_dev(l16, kvs + (t) * 16384, lds0 + phys(t) + w * 2048)
        if (w == 0) poll_flag(ws, FL_KV + (lane >> 1) * 8 + (head * 28 + (lane & 1) * 27) / 64, tag);
        BAR();
        TS(5);
        glds_dev(l16, qs, lds0 + 147456 + qt * 2048 + kh * 1024);
        KV_ISSUE(0); KV_ISSUE(1); KV_ISSUE(2); KV_ISSUE(3);
        waitv_bar<4>();
        TS(6);
        const bf16x8 q0 = lds8(lds + 147456 + qt * 2048 + l16), q1 = lds8(lds + 147456 + qt * 2048 + 1024 + l16);
        f32x16 o = {}, sa = {}, sb;
        bf16x8 k0 = lds8(ap + phys(0)), k1 = lds8(ap + phys(0) + 1024), v0 = lds8(vp + phys(0) + 2048), v1 = {};
        u32x4 p0, p1 = {};
        sa = MFMA(k0, q0, sa); sa = MFMA(k1, q1, sa);
        k0 = lds8(ap + phys(0) + 4096); k1 = lds8(ap + phys(0) + 5120);
        PIN(sa); SB();
        const unsigned wgo = WS_WGU + (unsigned)((ffn ? dfg : 0) * 8 + w) * 28672;
        const char* wos = wo + m7 * 128 + (size_t)w * 56 * 896;
        const unsigned wov = (unsigned)(row8 * 896) + swz;
        const rsrc_t rx = __builtin_amdgcn_make_buffer_rsrc((void*)x, 0, 2048 * 448 * 2, 0x00020000);
        const unsigned xo_ = (unsigned)(((tb * 2 + tt) * 32 + r) * 896 + m7 * 128 + ct * 64 + kh * 32 + hf * 8);
        u64 rtf = 0;
#define EXP4(S, o4) _Pragma("unroll") for (int i_ = o4; i_ < o4 + 4; ++i_) S[i_] = __builtin_amdgcn_exp2f(S[i_]);
#define WLD(s) wf[s] = BF8(ld16_dev(rws, l16 + ((s) & 3) * 1024, wgo + ((s) >> 2) * 4096))
#define SIDE(t, g) do { \
            if ((t) == 0 && (g) == 0) KV_ISSUE(4); if ((t) == 0 && (g) == 2) KV_ISSUE(5); if ((t) == 1 && (g) == 0) KV_ISSUE(6); if ((t) == 1 && (g) == 2) KV_ISSUE(7); \
            if ((t) == 2 && (g) == 0) KV_ISSUE(8); if ((t) == 1 && (g) == 3) rtf = peek_flag(ws, FL_RT + r); \
            if ((t) == 3 && (g) < 3) KV_ISSUE(9 + (g)); \
            if ((t) == 7 && (g) == 0) KV_ISSUE(12); if ((t) == 7 && (g) == 2) KV_ISSUE(13); if ((t) == 8 && (g) == 0) KV_ISSUE(14); if ((t) == 8 && (g) == 2) KV_ISSUE(15); \
            if ((t) >= 4 && (t) <= 6) WLD(((t) - 4) * 4 + (g)); \
            if ((t) == 9 || (t) == 10) WLD(((t) - 6) * 4 + (g)); \
            if ((t) == 11 || ((t) == 12 && (g) < 3)) glds(wov, wos + (((t) - 11) * 4 + (g)) * 8 * 896, lds0 + (w * 7 + ((t) - 11) * 4 + (g)) * 1024); \
            if ((t) == 12 && (g) == 3) { xa = ld8(rx, xo_, 0); xb = ld8(rx, xo_, 16); } \
            if ((t) == 13 || (t) == 14) WLD(((t) - 8) * 4 + (g)); \
            if ((t) == 14 && (g) == 0) { if (tid < 32) *(LAS u32x4*)(lds + 57344 + tid * 16) = (u32x4){0, 0, 0, 0}; } \
        } while (0)
#define TOFF(i) (phys((i) >> 1) + ((i) & 1) * 4096)
#define TILE(i, SC, SN) do { \
            __builtin_amdgcn_s_setprio(((i) & 1) ? 1 : 3); \
            if ((i) < 31) { f32x16 z_ = {}; SN = MFMA(k0, q0, z_); PIN(SN); } if ((i) < 30) k0 = lds8(AP(TOFF((i) + 2))); SB(); \
            EXP4(SC, 0); PIN(SC); SB(); \
            o = MFMA(v1, BF8(p1), o); PIN(o); v1 = lds8(VP(TOFF(i) + 3072)); SIDE((i) >> 1, ((i) & 1) * 2); SB(); \
            EXP4(SC, 4); p0 = pack8(SC, 0); PIN(p0); SB(); \
            __builtin_amdgcn_s_setprio(((i) & 1) ? 0 : 2); \
            if ((i) < 31) { SN = MFMA(k1, q1, SN); PIN(SN); } if ((i) < 30) k1 = lds8(AP(TOFF((i) + 2) + 1024)); SB(); \
            EXP4(SC, 8); PIN(SC); SB(); \
            o = MFMA(v0, BF8(p0), o); PIN(o); if ((i) < 31) v0 = lds8(VP(TOFF((i) + 1) + 2048)); SIDE((i) >> 1, ((i) & 1) * 2 + 1); SB(); \
            EXP4(SC, 12); p1 = pack8(SC, 1); PIN(p1); SB(); } while (0)
#define STEP(t) do { TILE(2 * (t), sa, sb); TILE(2 * (t) + 1, sb, sa); } while (0)
        STEP(0);
        waitv_bar<4>();
        STEP(1); STEP(2);
        waitv_bar<3>();
        TS(21);
        if (__builtin_expect(!__all(rtf == tag), 0)) poll_flag(ws, FL_RT + r, tag);
        FENCE();
        STEP(3); STEP(4); STEP(5); STEP(6);
        waitv_bar<12>();
        TS(22);
        STEP(7); STEP(8); STEP(9); STEP(10);
        waitv_bar<8>();
        TS(23);
        lds_ptr xw = opq(lds + 147456 + w * 2048 + l16), xr = opq(lds + 147456 + (w ^ 4) * 2048 + l16);
        lds_ptr lw = opq(lds + 58368 + w * 256 + lane * 4), lr = opq(lds + 58368 + qt * 256 + 128 + r * 4);
        lds_ptr bw = opq(lds + 60416 + w * 4), br = opq(lds + 60416 + (lane & 7) * 4);
        unsigned oo = WS_OF + (unsigned)((rb * 4 + qt) * 32 + head * 2 + kh) * 1024 + l16;
        PIN(oo);
        STEP(11); STEP(12); STEP(13); STEP(14); STEP(15);
        o = MFMA(v1, BF8(p1), o);
        TS(7);
        u32x4 ov;
        {
            *(LAS f32x4*)xw = (f32x4){o[8], o[9], o[10], o[11]}; *(LAS f32x4*)(xw + 1024) = (f32x4){o[12], o[13], o[14], o[15]};
            const float l = kh ? o[4] : o[12];
            *(LAS float*)lw = l;
            float sa_ = 0.f;
#pragma unroll
            for (int i = 0; i < 16; ++i) sa_ += fabsf(o[i]);
            const bool ok = sa_ < 0x1p100f && (hf == 0 || l > 0x1p-80f);
            *(LAS unsigned*)bw = __all(ok) ? 0u : 1u;
            BAR();
            const f32x4 a = *(LAS f32x4*)xr, c = *(LAS f32x4*)(xr + 1024);
            const float inv = __builtin_amdgcn_rcpf(*(LAS float*)lr + *(LAS float*)(lr + 1024));
            const unsigned bad = *(LAS unsigned*)br;
            const float iz = (kh && hf) ? 0.f : inv;
            ov = (u32x4){cvtpk((o[0] + a[0]) * inv, (o[1] + a[1]) * inv), cvtpk((o[2] + a[2]) * inv, (o[3] + a[3]) * inv),
                         cvtpk((o[4] + c[0]) * iz, (o[5] + c[1]) * iz), cvtpk((o[6] + c[2]) * iz, (o[7] + c[3]) * iz)};
            if (__builtin_expect(__any(bad != 0), 0)) {
                f32x16 of = {};
                float m = -0x1p126f;
#pragma unroll 1
                for (int t = 0; t < 16; ++t) {
                    BAR();
                    glds2_dev(l16, kvs + t * 16384, lds0 + 65536 + w * 2048);
                    waitv_bar<0>();
#pragma unroll 1
                    for (int u = 0; u < 2; ++u) {
                        f32x16 s = {};
                        s = MFMA(lds8(apB + u * 4096), q0, s); s = MFMA(lds8(apB + u * 4096 + 1024), q1, s);
                        float mx = s[0];
#pragma unroll
                        for (int i = 1; i < 16; ++i) mx = fmaxf(mx, s[i]);
                        mx = fmaxf(swap_max(mx), m);
                        const float alpha = __builtin_amdgcn_exp2f(m - mx);
                        m = mx;
#pragma unroll
                        for (int i = 0; i < 16; ++i) { s[i] = __builtin_amdgcn_exp2f(s[i] - mx); of[i] *= alpha; }
                        of = MFMA(lds8(vpB + u * 4096 + 2048), BF8(pack8(s, 0)), of);
                        of = MFMA(lds8(vpB + u * 4096 + 3072), BF8(pack8(s, 1)), of);
                    }
                }
                *(LAS f32x4*)xw = (f32x4){of[8], of[9], of[10], of[11]}; *(LAS f32x4*)(xw + 1024) = (f32x4){of[12], of[13], of[14], of[15]};
                *(LAS float*)lw = hf ? (kh ? of[4] : of[12]) : m;
                BAR();
                const f32x4 a2 = *(LAS f32x4*)xr, c2 = *(LAS f32x4*)(xr + 1024);
                const float m0 = *(LAS float*)(lr - 128), m1 = *(LAS float*)(lr + 1024 - 128), mm = fmaxf(m0, m1);
                const float e0 = __builtin_amdgcn_exp2f(m0 - mm), e1 = __builtin_amdgcn_exp2f(m1 - mm);
                const float iv = __builtin_amdgcn_rcpf(*(LAS float*)lr * e0 + *(LAS float*)(lr + 1024) * e1);
                const float im = (kh ? e1 : e0) * iv, io = (kh ? e0 : e1) * iv;
                const bool z = kh && hf;
                ov = (u32x4){cvtpk(of[0] * im + a2[0] * io, of[1] * im + a2[1] * io), cvtpk(of[2] * im + a2[2] * io, of[3] * im + a2[3] * io),
                             z ? 0u : cvtpk(of[4] * im + c2[0] * io, of[5] * im + c2[1] * io), z ? 0u : cvtpk(of[6] * im + c2[2] * io, of[7] * im + c2[3] * io)};
            }
        }
        st16_dev(ov, rws, oo, 0);
        TS(25);
        waitv_bar<0>();
        if (tid == 0) set_flag(ws, FL_O + rb * 16 + head, tag);
        TS(8);
    }
    if (!ffn) {
        const int wid = (xcd * 4 + loc - 28) * 8 + w;
        const unsigned vo = (unsigned)(row8 * 896 + (lane & 7) * 16);
#pragma unroll 1
        for (int u = wid, i = 0; u < 784; u += 256, ++i) {
            const char* s_ = wd + (size_t)(u / 7) * 16 * 896 + (u % 7) * 128;
            glds(vo, s_, lds0 + w * 8192 + i * 2048); glds(vo, s_ + 8 * 896, lds0 + w * 8192 + i * 2048 + 1024);
        }
        waitv<0>();
#pragma unroll 1
        for (int u = wid, i = 0; u < 784; u += 256, ++i) {
            lds_ptr sd = lds + w * 8192 + i * 2048 + (4 * hf + q4) * 128 + g1 * 32 + p4 * 8;
#pragma unroll
            for (int e = 0; e < 2; ++e) st16_dev(U4(cat(trd(sd + e * 64), trd(sd + e * 64 + 1024))), rws, l16, WS_WD + (unsigned)(((u % 7) * 112 + u / 7) * 2 + e) * 1024);
        }
        waitv_bar<0>();
        if (tid == 0) set_flag(ws, FL_RD + xcd * 4 + loc - 28, tag);
        return;
    }
    PIN2(xa, xb);
#pragma unroll
    for (int s = 27; s >= 0; --s) PIN(wf[s]);

    f32x16 cs = {};
    const u32x4 one4 = {ones, ones, ones, ones};
    {
#pragma unroll
        for (int s = 0; s < 14; ++s) cs = MFMA(wf[s], BF8(one4), cs);
        PIN(cs);
        lds_ptr wb = lds + kh * (8 * 28 * 128) + (4 * hf + q4) * 128 + ((ct ^ (q4 >> 1)) * 64) + (g1 ^ kh) * 32 + p4 * 8;
        bf16x8 wr[16];
#pragma unroll
        for (int s = 0; s < 12; ++s) { const int k0 = ((s >> 1) * 28 + (s & 1) * 16) * 128; wr[s] = cat(trd(wb + k0), trd(wb + k0 + 1024)); }
        f32x16 acc = {};
        acc[0] = bflo(xa[0]); acc[1] = bfhi(xa[0]); acc[2] = bflo(xa[1]); acc[3] = bfhi(xa[1]); acc[4] = bflo(xb[0]); acc[5] = bfhi(xb[0]); acc[6] = bflo(xb[1]); acc[7] = bfhi(xb[1]);
        lds_ptr cw = opq(lds + 131072 + w * 2048 + l16), cr = opq(lds + 131072 + (w ^ 4) * 2048 + l16);
        const unsigned hi_ = (unsigned)((tb * 2 + tt) * 28 + m7 * 4 + ct * 2 + kh);
        unsigned ho = WS_HF + hi_ * 1024 + l16, so = hf ? OOB : WS_HS + hi_ * 256 + r * 8;
        PIN(acc); PIN2(ho, so);
        if (w == 0) poll_flag(ws, FL_O + (tb >> 1) * 16 + (lane & 15), tag);
        BAR();
        TS(9);
        const char* os = ws + WS_OF + (size_t)tb * 65536;
        const unsigned fo = (unsigned)(tt * 32768 + kh * 16384 + ct * 4096);
        glds4_dev(l16, os + fo, lds0 + 65536 + fo); glds4_dev(l16, os + fo + 8192, lds0 + 65536 + fo + 8192);
        if (bid == 0 && tid == 0) set_flag(ws, FL_EPOCH, tag);
        lds_ptr ob = opq(lds + 65536 + tt * 32768 + kh * 16384 + l16);
        bf16x8 of[8];
        waitv_bar<4>();
        TS(26);
#pragma unroll
        for (int s = 0; s < 8; ++s) of[s] = lds8(ob + s * 1024);
        SB();
#pragma unroll
        for (int s = 0; s < 8; ++s) acc = MFMA(wr[s], of[s], acc);
        PIN(acc); SB();
#pragma unroll
        for (int s = 12; s < 16; ++s) { const int k0 = ((s >> 1) * 28 + (s & 1) * 16) * 128; wr[s] = cat(trd(wb + k0), trd(wb + k0 + 1024)); }
        waitv_bar<0>();
#pragma unroll
        for (int s = 0; s < 8; ++s) of[s] = lds8(ob + (8 + s) * 1024);
        SB();
#pragma unroll
        for (int s = 0; s < 8; ++s) acc = MFMA(wr[8 + s], of[s], acc);
        TS(10);
        *(LAS f32x4*)cw = (f32x4){acc[8], acc[9], acc[10], acc[11]}; *(LAS f32x4*)(cw + 1024) = (f32x4){acc[12], acc[13], acc[14], acc[15]};
        BAR();
        const f32x4 a = *(LAS f32x4*)cr + (f32x4){acc[0], acc[1], acc[2], acc[3]}, b = *(LAS f32x4*)(cr + 1024) + (f32x4){acc[4], acc[5], acc[6], acc[7]};
        const u32x4 hv = {cvtpk(a[0], a[1]), cvtpk(a[2], a[3]), cvtpk(b[0], b[1]), cvtpk(b[2], b[3])};
        st16_dev(hv, rws, ho, 0);
        float s1 = 0.f, s2 = 0.f;
        acc_stats(hv, s1, s2);
        s1 = swap_add(s1); s2 = swap_add(s2);
        st8_dev((u32x2){__float_as_uint(s1), __float_as_uint(s2)}, rws, so, 0);
        waitv_bar<0>();
        if (tid == 0) set_flag(ws, FL_H + tb * 8 + m7, tag);
        TS(11);
    }

    const int erb = tb >> 1;
    const unsigned wdo = WS_WD + (unsigned)(m7 * 112 + w * 14) * 2048;
    bf16x8 wdf[28];
    u64 gfl = 0;
    const unsigned gfi = FL_G + erb * 32 + min(lane & 15, 13);
    {
#pragma unroll
        for (int s = 14; s < 28; ++s) cs = MFMA(wf[s], BF8(one4), cs);
        lds_ptr csp = opq(lds + 161792 + w * 128 + hf * 64);
        if (r == 0) {
#pragma unroll
            for (int g = 0; g < 4; ++g) *(LAS f32x4*)(csp + g * 16) = (f32x4){cs[4 * g], cs[4 * g + 1], cs[4 * g + 2], cs[4 * g + 3]};
        }
        const char* hs = ws + WS_HF + (size_t)drb * 114688 + w * 1024;
        const unsigned hd = lds0 + w * 1024;
        const char* wds = ws + wdo;
        lds_ptr hb = opq(lds + l16), hb2 = opq(lds + 57344 + l16);
        lds_ptr wl = opq(lds + 114688 + w * 5120 + l16), wl2 = opq(lds + w * 7168 + l16);
        LAS f32x2* stp = (LAS f32x2*)opq(lds + 159744 + r * 8);
        unsigned go = WS_GF + (unsigned)(drb * 4 * 112 + dfg * 8 + w) * 1024 + l16;
        PIN(go);
        if (w == 0) { const int i = lane & 15; poll_flag(ws, hf ? FL_RD + r : FL_H + drb * 16 + (i >> 3) * 8 + ((i & 7) == 7 ? 0 : (i & 7)), tag); }
        BAR();
        TS(12);
#define H_PIECE(j) glds_dev(l16, hs + (j) * 8192, hd + (j) * 8192)
#define WDLD(s) wdf[s] = BF8(ld16_dev(rws, l16 + ((s) & 3) * 1024, wdo + ((s) >> 2) * 4096))
#define HB(n) ((n) < 56 ? hb + (n) * 1024 : hb2 + ((n) - 56) * 1024)
        H_PIECE(0); H_PIECE(1); H_PIECE(2); H_PIECE(3);
        glds4_dev(l16, ws + WS_HS + (size_t)((drb * 4 + (w & 3)) * 28 + kh * 14) * 256, lds0 + 114688 + w * 5120);
        constexpr int PF = 5;
        bf16x8 bf[PF + 1];
        f32x16 acc[2];
        f32x2 sp[7], sv0, sv1;
        f32x4 c4[4];
        float mean = 0.f, r2 = 0.f;
        u32x4 hv;
        waitv_bar<7>();
        TS(27);
#pragma unroll
        for (int n = 0; n < PF; ++n) bf[n] = lds8(HB(n));
        SB();
        sfor<0, 122>([&](auto N_) __attribute__((always_inline)) {
            constexpr int n = decltype(N_)::v, p = n / 28, s = n % 28;
            if constexpr (n == 3) waitv_bar<5>();
            if constexpr (n == 27) { waitv_bar<3>(); TS(13); }
            if constexpr (n == 64) { waitv_bar<12>(); TS(14); }
            if constexpr (n == 84) { waitv_bar<7>(); if (tid == 0) set_flag(ws, FL_G + drb * 32 + dfg, tag); }
            if constexpr (n < 112) { if constexpr (s == 0) { f32x16 z = {}; acc[p & 1] = MFMA(wf[s], bf[n % (PF + 1)], z); } else acc[p & 1] = MFMA(wf[s], bf[n % (PF + 1)], acc[p & 1]); PIN(acc[p & 1]); }
            SB();
            if constexpr (n + PF < 112) bf[(n + PF) % (PF + 1)] = lds8(HB(n + PF));
            if constexpr (n % 3 == 2 && n < 30) H_PIECE(4 + n / 3);
            if constexpr (n == 16) { waitv<5>();
#pragma unroll
                for (int j = 0; j < 7; ++j) sp[j] = *(LAS f32x2*)(lds + 114688 + w * 5120 + (hf * 7 + j) * 256 + r * 8);
            }
            if constexpr (n == 20) { const f32x2 t = ((sp[0] + sp[1]) + (sp[2] + sp[3])) + ((sp[4] + sp[5]) + sp[6]); stp[w * 32] = (f32x2){swap_add(t[0]), swap_add(t[1])}; }
            if constexpr (n >= 33 && n < 48 && n % 3 == 0) glds_dev(l16, wds + (n - 33) / 3 * 1024, lds0 + 114688 + w * 5120 + (n - 33) / 3 * 1024);
            if constexpr (n >= 48 && n < 64 && n % 3 == 0) WDLD(12 + (n - 48) / 3);
            if constexpr (n >= 70 && n < 84 && n % 2 == 0) glds_dev(l16, wds + (5 + (n - 70) / 2) * 1024, lds0 + w * 7168 + (n - 70) / 2 * 1024);
            if constexpr (n >= 86 && n < 106 && n % 2 == 0) WDLD(18 + (n - 86) / 2);
            if constexpr (n == 106) gfl = peek_flag(ws, gfi);
            if constexpr (p >= 1) {
                const f32x16& a = acc[(p - 1) & 1];
                if constexpr (n == 100) { sv0 = stp[3 * 32]; sv1 = stp[7 * 32]; }
                if constexpr (s == 0 && p < 4) { sv0 = stp[(p - 1) * 32]; sv1 = stp[(p + 3) * 32];
#pragma unroll
                    for (int g = 0; g < 4; ++g) c4[g] = *(LAS f32x4*)(csp + g * 16); }
                if constexpr ((s == 3 && p < 4) || n == 104) { const f32x2 sv = sv0 + sv1; mean = sv[0] * (1.f / D); r2 = __builtin_amdgcn_rcpf(fmaxf(sv[1] * (1.f / D) - mean * mean, 0.f) + EPS); PIN2(mean, r2); }
                if constexpr (s >= 4 && s < 8) { constexpr int i = 2 * (s - 4);
                    const float h0 = fmaxf(a[i] - mean * c4[i >> 2][i & 3], 0.f) * r2 * (a[8 + i] - mean * c4[2 + (i >> 2)][i & 3]);
                    const float h1 = fmaxf(a[i + 1] - mean * c4[i >> 2][(i & 3) + 1], 0.f) * r2 * (a[9 + i] - mean * c4[2 + (i >> 2)][(i & 3) + 1]);
                    hv[s - 4] = cvtpk(h0, h1); PIN(hv); }
                if constexpr (s == 9) { st16_dev(hv, rws, go + (p - 1) * 114688, 0); FENCE(); }
            }
            SB();
        });
        TS(15);
        if (__builtin_expect(!__all(gfl == tag), 0)) poll_flag(ws, gfi, tag);
        FENCE();
#pragma unroll
        for (int s = 0; s < 12; ++s) wdf[s] = lds8(s < 5 ? wl + s * 1024 : wl2 + (s - 5) * 1024);
    }

    {
        const int etok = tid >> 4, eblk = tid & 15;
        const int ta = erb * 4 + (tb & 1);
        const unsigned hro = (unsigned)((((eblk >> 2) & 3) * 64 + (eblk & 1) * 32 + etok) * 16 + ((eblk >> 1) & 1) * 8);
        bf16x8 gf[14];
        u32x2 hres[2];
#define G_LOAD(hh) do { const unsigned go = WS_GF + (unsigned)((ta + 2 * (hh)) * 112 + w * 14) * 1024; \
            _Pragma("unroll") for (int s = 0; s < 14; ++s) { gf[s] = BF8(ld16_dev(rws, l16 + (s & 3) * 1024, go + (s >> 2) * 4096)); SB(); } \
            hres[hh] = ld8_dev(rws, hro, WS_HF + (unsigned)((ta + 2 * (hh)) * 28 + m7 * 4) * 1024); FENCE(); SB(); } while (0)
#define G_MMA(hh) do { f32x16 c0 = {}, c1 = {}; \
            _Pragma("unroll") for (int s = 0; s < 14; ++s) { c0 = MFMA(wdf[2 * s], gf[s], c0); c1 = MFMA(wdf[2 * s + 1], gf[s], c1); PIN2(c0, c1); SB(); } \
            lds_ptr rp = opq(lds + (hh) * 67584 + w * 8448 + hf * 528 + r * 16); \
            _Pragma("unroll") for (int g = 0; g < 4; ++g) { \
                *(LAS f32x4*)(rp + g * 1056) = (f32x4){c0[4 * g], c0[4 * g + 1], c0[4 * g + 2], c0[4 * g + 3]}; \
                *(LAS f32x4*)(rp + 4224 + g * 1056) = (f32x4){c1[4 * g], c1[4 * g + 1], c1[4 * g + 2], c1[4 * g + 3]}; } } while (0)
#define G_OUT(hh) do { lds_ptr rq = opq(lds + (hh) * 67584 + eblk * 528 + etok * 16); f32x4 pa[8]; \
            _Pragma("unroll") for (int ww = 0; ww < 8; ++ww) pa[ww] = *(LAS f32x4*)(rq + ww * 8448); SB(); \
            const f32x4 a = ((pa[0] + pa[1]) + (pa[2] + pa[3])) + ((pa[4] + pa[5]) + (pa[6] + pa[7])); \
            const u32x2 ov = {cvtpk(a[0] + bflo(hres[hh][0]), a[1] + bfhi(hres[hh][0])), cvtpk(a[2] + bflo(hres[hh][1]), a[3] + bfhi(hres[hh][1]))}; \
            __hip_atomic_store((GAS u64*)(out + (size_t)((ta + 2 * (hh)) * 32 + etok) * 896 + m7 * 128 + eblk * 8), __builtin_bit_cast(u64, ov), __ATOMIC_RELAXED, __HIP_MEMORY_SCOPE_AGENT); } while (0)
        G_LOAD(0);
        waitv_bar<15>();
        if (tid == 0) set_flag(ws, FL_G + drb * 32 + 16 + dfg, tag);
        TS(16);
        G_MMA(0);
        TS(17);
        if (w == 0) poll_flag(ws, gfi + 16, tag);
        BAR();
        TS(18);
        G_LOAD(1);
        G_OUT(0);
        FENCE(); SB();
        G_MMA(1);
        TS(19);
        BAR();
        G_OUT(1);
    }
    TS(20);
    asm volatile("blk_fwd_code_end:");
}

extern "C" void kernel_launch(void* const* d_in, const int* in_sizes, int n_in, void* d_out, int out_size, void* d_ws, size_t ws_size, hipStream_t stream) {
    (void)hipFuncSetAttribute((const void*)block_fwd, hipFuncAttributeMaxDynamicSharedMemorySize, LDS_BYTES);
    block_fwd<<<256, 512, LDS_BYTES, stream>>>((const char*)d_in[0], (const char*)d_in[1], (const char*)d_in[2], (const char*)d_in[4], (const char*)d_in[5], (const char*)d_in[6], (char*)d_out, (char*)d_ws);
}
```

```cpp
#include <hip/hip_runtime.h>
#include <hip/hip_bf16.h>
#include <stdint.h>

typedef __attribute__((ext_vector_type(8))) short bf16x8;
typedef __attribute__((ext_vector_type(4))) short s16x4;
typedef __attribute__((ext_vector_type(2))) __bf16 bf16x2;
typedef __attribute__((ext_vector_type(16))) float f32x16;
typedef __attribute__((ext_vector_type(4))) float f32x4;
typedef __attribute__((ext_vector_type(2))) float f32x2;
typedef __attribute__((ext_vector_type(4))) unsigned u32x4;
typedef __attribute__((ext_vector_type(2))) unsigned u32x2;
typedef short v4i16_t __attribute__((ext_vector_type(4)));
typedef unsigned long long u64;
#define LAS __attribute__((address_space(3)))
#define GAS __attribute__((address_space(1)))
typedef LAS char* lds_ptr;
#if defined(__HIP_DEVICE_COMPILE__)
#define NOPK __attribute__((target("no-packed-fp32-ops")))
#else
#define NOPK
#endif
#define DI __device__ __forceinline__
#define MFMA(a, b, c) __builtin_amdgcn_mfma_f32_32x32x16_bf16(a, b, c, 0, 0, 0)
#define MFMA16(a, b, c) __builtin_amdgcn_mfma_f32_16x16x32_bf16(a, b, c, 0, 0, 0)
#define BF8(x) __builtin_bit_cast(bf16x8, x)
#define U4(x) __builtin_bit_cast(u32x4, x)

constexpr int D = 448;
constexpr float EPS = 1e-5f;
constexpr unsigned WS_FLAG = 0;
constexpr unsigned FL_EPOCH = 0, FL_KV = 1, FL_RT = FL_KV + 256, FL_RD = FL_RT + 32, FL_O = FL_RD + 32, FL_H = FL_O + 256, FL_G = FL_H + 256;
constexpr unsigned WS_KV = 1u << 20;
constexpr unsigned WS_QF = WS_KV + 4194304;
constexpr unsigned WS_OF = WS_QF + 2097152;
constexpr unsigned WS_HF = WS_OF + 2097152;
constexpr unsigned WS_GF = WS_HF + 64 * 28 * 1024;
constexpr unsigned WS_WGU = WS_GF + 64 * 112 * 1024;
constexpr unsigned WS_WD = WS_WGU + 112 * 28 * 1024;
constexpr unsigned WS_HS = WS_WD + 7 * 112 * 2 * 1024;
constexpr unsigned WS_END = WS_HS + 64 * 28 * 256 + 1024;
constexpr unsigned OOB = 0x40000000u;
constexpr int LDS_BYTES = 163840;

DI float u2f(unsigned u) { return __builtin_bit_cast(float, u); }
DI unsigned f2u(float f) { return __builtin_bit_cast(unsigned, f); }
DI unsigned umin(unsigned a, unsigned b) { return a < b ? a : b; }
DI bool wave_any(bool p) { return __builtin_amdgcn_ballot_w64(p) != 0; }
DI bool wave_all(bool p) { return __builtin_amdgcn_ballot_w64(!p) == 0; }
DI unsigned cvtpk(float lo, float hi) { return __builtin_bit_cast(unsigned, __builtin_convertvector((f32x2){lo, hi}, bf16x2)); }
DI float bflo(unsigned u) { return u2f(u << 16); }
DI float bfhi(unsigned u) { return u2f(u & 0xffff0000u); }
DI void glds(unsigned voff, const void* sbase, unsigned dst) {
    asm volatile("s_mov_b32 m0, %2\n\ts_nop 0\n\tglobal_load_lds_dwordx4 %0, %1" :: "v"(voff), "s"(sbase), "s"(dst) : "memory");
}
DI void glds_dev(unsigned voff, const void* sbase, unsigned dst) {
    asm volatile("s_mov_b32 m0, %2\n\ts_nop 0\n\tglobal_load_lds_dwordx4 %0, %1 sc1" :: "v"(voff), "s"(sbase), "s"(dst) : "memory");
}
DI void glds2_dev(unsigned voff, const void* sbase, unsigned dst) {
    asm volatile("s_mov_b32 m0, %2\n\ts_nop 0\n\tglobal_load_lds_dwordx4 %0, %1 sc1\n\tglobal_load_lds_dwordx4 %0, %1 offset:1024 sc1" :: "v"(voff), "s"(sbase), "s"(dst) : "memory");
}
DI void glds4_dev(unsigned voff, const void* sbase, unsigned dst) {
    asm volatile("s_mov_b32 m0, %2\n\ts_nop 0\n\tglobal_load_lds_dwordx4 %0, %1 sc1\n\tglobal_load_lds_dwordx4 %0, %1 offset:1024 sc1\n\t"
                 "global_load_lds_dwordx4 %0, %1 offset:2048 sc1\n\tglobal_load_lds_dwordx4 %0, %1 offset:3072 sc1" :: "v"(voff), "s"(sbase), "s"(dst) : "memory");
}
DI s16x4 trd(lds_ptr p) { return __builtin_bit_cast(s16x4, __builtin_amdgcn_ds_read_tr16_b64_v4i16((LAS v4i16_t*)p)); }
DI bf16x8 cat(s16x4 a, s16x4 b) { return __builtin_shufflevector(a, b, 0, 1, 2, 3, 4, 5, 6, 7); }
DI bf16x8 lds8(lds_ptr p) { return *(LAS bf16x8*)p; }
DI float swap_add(float v) { auto r = __builtin_amdgcn_permlane32_swap(f2u(v), f2u(v), false, false); return u2f(r[0]) + u2f(r[1]); }
DI float swap_max(float v) { auto r = __builtin_amdgcn_permlane32_swap(f2u(v), f2u(v), false, false); return __builtin_fmaxf(u2f(r[0]), u2f(r[1])); }
DI u32x4 pack8(const f32x16& a, int s) { return (u32x4){cvtpk(a[8 * s], a[8 * s + 1]), cvtpk(a[8 * s + 2], a[8 * s + 3]), cvtpk(a[8 * s + 4], a[8 * s + 5]), cvtpk(a[8 * s + 6], a[8 * s + 7])}; }
DI float dot2(unsigned a, unsigned b, float c) { return __builtin_amdgcn_fdot2_f32_bf16(__builtin_bit_cast(bf16x2, a), __builtin_bit_cast(bf16x2, b), c, false); }
DI void acc_stats(u32x4 u, float& s1, float& s2) {
#pragma unroll
    for (int i = 0; i < 4; ++i) { const float a = bflo(u[i]), b = bfhi(u[i]); s1 += a; s2 = __builtin_fmaf(a, a, s2); s1 += b; s2 = __builtin_fmaf(b, b, s2); }
}
DI void acc_stats2(unsigned u0, unsigned u1, f32x4& s) {
    const float a = bflo(u0), b = bfhi(u0), c = bflo(u1), d = bfhi(u1);
    s[0] += a; s[1] += b; s[2] = __builtin_fmaf(a, a, s[2]); s[3] = __builtin_fmaf(b, b, s[3]); s[0] += c; s[1] += d; s[2] = __builtin_fmaf(c, c, s[2]); s[3] = __builtin_fmaf(d, d, s[3]);
}
DI float sum8(float v) {
    v += u2f(__builtin_amdgcn_update_dpp(0u, f2u(v), 0xB1, 0xf, 0xf, true));
    v += u2f(__builtin_amdgcn_update_dpp(0u, f2u(v), 0x4E, 0xf, 0xf, true));
    v += u2f(__builtin_amdgcn_update_dpp(0u, f2u(v), 0x141, 0xf, 0xf, true));
    return v;
}
DI u32x4 shift8(u32x4 x, float c) {
    u32x4 o; float t0, t1, t2, t3, t4, t5, t6, t7;
    asm("v_dot2_f32_bf16 %4, %12, %16, %18\n\tv_dot2_f32_bf16 %5, %12, %17, %18\n\tv_dot2_f32_bf16 %6, %13, %16, %18\n\tv_dot2_f32_bf16 %7, %13, %17, %18\n\t"
        "v_dot2_f32_bf16 %8, %14, %16, %18\n\tv_dot2_f32_bf16 %9, %14, %17, %18\n\tv_dot2_f32_bf16 %10, %15, %16, %18\n\tv_dot2_f32_bf16 %11, %15, %17, %18\n\t"
        "v_cvt_pk_bf16_f32 %0, %4, %5\n\tv_cvt_pk_bf16_f32 %1, %6, %7\n\tv_cvt_pk_bf16_f32 %2, %8, %9\n\tv_cvt_pk_bf16_f32 %3, %10, %11"
        : "=&v"(o[0]), "=&v"(o[1]), "=&v"(o[2]), "=&v"(o[3]), "=&v"(t0), "=&v"(t1), "=&v"(t2), "=&v"(t3), "=&v"(t4), "=&v"(t5), "=&v"(t6), "=&v"(t7)
        : "v"(x[0]), "v"(x[1]), "v"(x[2]), "v"(x[3]), "s"(0x00003f80u), "s"(0x3f800000u), "v"(c));
    return o;
}
template <int N> struct IC { static constexpr int v = N; };
template <int B, int E, class F> DI void sfor(F&& f) { if constexpr (B < E) { f(IC<B>{}); sfor<B + 1, E>(f); } }
template <int N> DI void waitv() { asm volatile("s_waitcnt vmcnt(%0)" :: "i"(N) : "memory"); }
template <int N> DI void waitv_bar() { asm volatile("s_waitcnt vmcnt(%0) lgkmcnt(0)\n\ts_barrier" :: "i"(N) : "memory"); }
template <int N> DI void waitv_barx() { asm volatile("s_waitcnt vmcnt(%0)\n\ts_barrier" :: "i"(N) : "memory"); }
DI void touch(unsigned voff, const void* sbase, unsigned junk) {
    asm volatile("s_mov_b32 m0, %2\n\ts_nop 0\n\tglobal_load_lds_dword %0, %1 sc1" :: "v"(voff), "s"(sbase), "s"(junk) : "memory");
}
#define BAR() asm volatile("s_waitcnt lgkmcnt(0)\n\ts_barrier" ::: "memory")
#define FENCE() asm volatile("" ::: "memory")
typedef __amdgpu_buffer_rsrc_t rsrc_t;
DI u32x4 ld16(rsrc_t rs, unsigned voff, unsigned soff) { return U4(__builtin_amdgcn_raw_buffer_load_b128(rs, voff, soff, 0)); }
DI u32x4 ld16_dev(rsrc_t rs, unsigned voff, unsigned soff) { return U4(__builtin_amdgcn_raw_buffer_load_b128(rs, voff, soff, 16)); }
DI u32x2 ld8(rsrc_t rs, unsigned voff, unsigned soff) { return __builtin_bit_cast(u32x2, __builtin_amdgcn_raw_buffer_load_b64(rs, voff, soff, 0)); }
DI u32x2 ld8_dev(rsrc_t rs, unsigned voff, unsigned soff) { return __builtin_bit_cast(u32x2, __builtin_amdgcn_raw_buffer_load_b64(rs, voff, soff, 16)); }
DI void st16_dev(u32x4 v, rsrc_t rs, unsigned voff, unsigned soff) { __builtin_amdgcn_raw_buffer_store_b128(v, rs, voff + soff, 0, 16); }
DI void st8_dev(u32x2 v, rsrc_t rs, unsigned voff, unsigned soff) { __builtin_amdgcn_raw_buffer_store_b64(v, rs, voff, soff, 16); }
DI void set_flag(char* ws, unsigned idx, u64 tag) { __hip_atomic_store((GAS u64*)(ws + WS_FLAG + (size_t)idx * 128), tag, __ATOMIC_RELAXED, __HIP_MEMORY_SCOPE_AGENT); }
DI void poll_flag(char* ws, unsigned idx, u64 tag) {
    GAS u64* p = (GAS u64*)(ws + WS_FLAG + (size_t)idx * 128);
#pragma unroll 1
    for (int spin = 0; spin < (1 << 16); ++spin) {
        const u64 v = __hip_atomic_load(p, __ATOMIC_RELAXED, __HIP_MEMORY_SCOPE_AGENT);
        if (wave_all(v == tag)) break;
        __builtin_amdgcn_s_sleep(1);
    }
}
DI u64 peek_flag(char* ws, unsigned idx) { return __hip_atomic_load((GAS u64*)(ws + WS_FLAG + (size_t)idx * 128), __ATOMIC_RELAXED, __HIP_MEMORY_SCOPE_AGENT); }
#define SB() __builtin_amdgcn_sched_barrier(0)
#define PIN(a) asm volatile("" : "+v"(a))
#define PIN2(a, b) asm volatile("" : "+v"(a), "+v"(b))
DI lds_ptr opq(lds_ptr p) { unsigned u = (unsigned)(uintptr_t)p; asm("" : "+v"(u)); return (lds_ptr)(uintptr_t)u; }
#define TS(i) do {} while (0)

DI constexpr int wd_slot(int s) { return (s * 17 + 27) / 28 != ((s + 1) * 17 + 27) / 28 ? (s * 17 + 27) / 28 : -1; }
DI constexpr int d_prio(int n) { return n < 0 ? -1 : n < 27 ? 3 - n / 7 : n < 64 ? 3 - (n - 27) / 10 : n < 84 ? 3 - (n - 64) / 5 : n < 112 ? 3 - (n - 84) / 7 : 0; }
DI constexpr int phys(int t) { return ((t % 9) + 2) % 9 * 16384; }

__global__ NOPK __launch_bounds__(512, 2) void block_fwd(const char* x, const char* wqkv, const char* wo, const char* wg, const char* wu, const char* wd, char* out, char* ws) {
    extern __shared__ __attribute__((aligned(16))) char smem[];
    lds_ptr lds = (lds_ptr)smem;
    const int tid = __builtin_amdgcn_workitem_id_x(), lane = tid & 63, w = __builtin_amdgcn_readfirstlane(tid >> 6);
    const int bid = __builtin_amdgcn_workgroup_id_x(), xcd = bid & 7, loc = bid >> 3;
    const int r = lane & 31, hf = lane >> 5, q4 = (lane & 15) >> 2, g1 = (lane >> 4) & 1, p4 = lane & 3;
    const unsigned lds0 = (unsigned)(uintptr_t)smem;
    const unsigned l16 = lane * 16;
    TS(0);
    const int sp3 = (loc + 8) >> 5;
    const int atb = (xcd >> 1) * 8 + (loc & 7) + sp3 * (xcd & 1) * 4, acg = (loc >> 3) + (1 - sp3) * (xcd & 1) * 4;
    const int row8 = lane >> 3;
    const rsrc_t rws = __builtin_amdgcn_make_buffer_rsrc((void*)ws, 0, WS_END, 0x00020000);
    u64 epoch;
    unsigned fl1;
    const unsigned ones = 0x3f803f80u;
    const bool ffn = loc < 28;
    const int l7 = loc / 7, m7 = loc % 7;
    const int tb = xcd * 4 + l7;
    const int tt = w & 1, ct = (w >> 1) & 1, kh = w >> 2;
    const unsigned swz = (unsigned)(((lane & 7) ^ (((row8 >> 1) & 1) << 2)) * 16);

    if (__builtin_expect(ffn, 1)) {
        constexpr int A_W = 57344, A_WS = 24576;
        const int rot8 = ((loc & 7) << sp3) + sp3 * (xcd & 1), rot = rot8 & (7 - ((rot8 + 1) >> 3) * 7);
#define A_KC(c) (rot + (c) >= 7 ? rot + (c) - 7 : rot + (c))
        {
            const int xr = (w & 3) * 8 + row8;
            const unsigned xo = (unsigned)(row8 * 896 + (((lane & 7) ^ ((xr >> 1) & 7)) * 16));
            const char* xs = x + (size_t)(atb * 64 + w * 8) * 896;
            unsigned xd = lds0 + kh * 28672 + (w & 3) * 1024, xk = rot * 128;
#pragma unroll
            for (int c = 0; c < 7; ++c) { glds(xo, xs + xk, xd + c * 4096); xk = xk == 768 ? 0 : xk + 128; }
            SB();
        }
        const unsigned wvo = (unsigned)(row8 * 2688) + swz;
        const char* wsrc = wqkv + acg * 128 + (size_t)w * 8 * 2688;
        const unsigned wdst = lds0 + A_W + w * 1024;
#define A_PIECE(c, i) glds(wvo, wsrc + A_KC(c) * (64 * 2688) + (i) * 896, wdst + ((c) & 3) * A_WS + (i) * 8192)
#define A_GROUP(c) do { A_PIECE(c, 1); A_PIECE(c, 2); A_PIECE(c, 0); } while (0)
        epoch = peek_flag(ws, FL_EPOCH); fl1 = FL_KV + atb * 8 + acg;
        asm volatile("s_barrier" ::: "memory");
        A_GROUP(0);
        lds_ptr xb[4], wb1, wb1h;
#pragma unroll
        for (int j = 0; j < 4; ++j) xb[j] = lds + tt * 28672 + (r >> 3) * 1024 + (r & 7) * 128 + (((2 * j + hf) ^ ((r >> 1) & 7)) * 16);
        wb1 = opq(lds + A_W + (kh ? 0 : 8192) + (8 * hf + q4) * 128 + ((ct ^ (q4 >> 1)) * 64) + g1 * 32 + p4 * 8);
        wb1h = opq(wb1 + 2 * A_WS);
        unsigned go1[2], go2[2]; int gi[2];
#pragma unroll
        for (int p = 0; p < 2; ++p) {
            const int G = acg * 8 + ct * 4 + 2 * p + hf, hp = G / 7, i = G - hp * 7;
            const unsigned hs = kh ? 131072u : 262144u;
            const unsigned to = (kh ? WS_QF + (atb * 2 + tt) * 2048 : WS_KV + (atb * 2 + tt) * 4096) + r * 16;
            go1[p] = (unsigned)(2 * hp + (i > 3)) * hs + (unsigned)(i > 3 ? i - 3 : i) * 512 + to;
            go2[p] = i == 3 ? (unsigned)(2 * hp + 1) * hs + to : OOB;
            gi[p] = i;
        }
        const int vcol = acg * 64 + ct * 32 + r, vh = vcol / 28, vd = vcol - vh * 28;
        unsigned vo = WS_KV + (atb * 2 + tt) * 4096 + (unsigned)vh * 262144 + 2048 + hf * 512 + vd * 16;
        PIN2(go1[0], go1[1]); PIN2(go2[0], go2[1]); PIN(vo);
        lds_ptr xw = opq(lds + kh * 28672 + (w & 3) * 1024 + l16);
        waitv<3>();
        TS(28);
        u32x4 xv[7];
#pragma unroll
        for (int c = 0; c < 7; ++c) xv[c] = *(LAS u32x4*)(xw + c * 4096);
        SB();
        f32x4 sm = {};
#pragma unroll
        for (int c = 0; c < 7; ++c)
#pragma unroll
            for (int j = 0; j < 4; ++j) sm[j] = dot2(xv[c][j], ones, sm[j]);
        float nm = sum8((sm[0] + sm[1]) + (sm[2] + sm[3])) * (-1.f / D);
        PIN(nm);
#pragma unroll
        for (int c = 0; c < 7; ++c) { xv[c] = shift8(xv[c], nm); *(LAS u32x4*)(xw + c * 4096) = xv[c]; }
        A_GROUP(1);
        SB();
        sm = (f32x4){};
#pragma unroll
        for (int c = 0; c < 7; ++c)
#pragma unroll
            for (int j = 0; j < 4; ++j) sm[j] = dot2(xv[c][j], xv[c][j], sm[j]);
        LAS float* rst = (LAS float*)(lds + 155648);
        rst[w * 8 + row8] = __builtin_amdgcn_rsqf(sum8((sm[0] + sm[1]) + (sm[2] + sm[3])) * (1.f / D) + EPS);
        A_GROUP(2);
        TS(31);
        bf16x8 fx[4], f1[4], f2[4];
#define A_WAIT(c) do { if ((c) == 0) waitv_bar<6>(); else if ((c) == 1) waitv_barx<6>(); else if ((c) < 6) waitv_barx<3>(); else waitv_barx<0>(); \
                       if ((c) == 0) TS(1); if ((c) == 6) TS(2); } while (0)
#define A_WP(c, ks) ((((c) & 2) ? wb1h : wb1) + ((c) & 1) * A_WS + (ks) * 2048)
        if (w < 4) {
            f32x16 ak = {}, av = {};
            f32x4 rv[4];
            float rs = 0.f;
#pragma unroll
            for (int n = 0; n < 30; ++n) {
                const int c = n >> 2, ks = n & 3, km = (n - 2) & 3;
                if (n < 28 && ks == 0) A_WAIT(c);
                if (n >= 2) { ak = MFMA(f1[km], fx[km], ak); PIN(ak); } SB();
                if (n < 28) { fx[ks] = lds8(xb[ks] + c * 4096); f1[ks] = cat(trd(A_WP(c, ks)), trd(A_WP(c, ks) + 512)); } SB();
                if (n >= 2) { av = MFMA(fx[km], f2[km], av); PIN(av); } SB();
                if (n < 28) f2[ks] = cat(trd(A_WP(c, ks) + 8192), trd(A_WP(c, ks) + 8192 + 512));
                if (c == 0 && ks < 3) A_PIECE(3, ks); if (c >= 2 && c < 5 && ks < 3) A_PIECE(c + 2, ks);
                if (n == 21) {
                    rs = rst[tt * 32 + r];
#pragma unroll
                    for (int g = 0; g < 4; ++g) rv[g] = *(LAS f32x4*)(rst + tt * 32 + 8 * g + 4 * hf);
                }
                if (n == 24) {
                    const unsigned pv = vd == 0 ? ones : 0u, po = vd < 4 ? vo + 448 : OOB;
                    st16_dev((u32x4){pv, pv, pv, pv}, rws, po, 0); st16_dev((u32x4){pv, pv, pv, pv}, rws, po + 1024, 0);
                }
                SB();
            }
            TS(3);
#pragma unroll
            for (int i = 0; i < 16; ++i) { ak[i] *= rs; av[i] *= rv[i >> 2][i & 3]; }
#pragma unroll
            for (int p = 0; p < 2; ++p) {
                const u32x4 f = pack8(ak, p);
                const auto s0 = __builtin_amdgcn_permlane32_swap(f[0], f[2], false, false), s1 = __builtin_amdgcn_permlane32_swap(f[1], f[3], false, false);
                const u32x4 c = {s0[0], s1[0], s0[1], s1[1]};
                st16_dev(c, rws, go1[p], 0); st16_dev(c, rws, go2[p], 0);
            }
            st16_dev(pack8(av, 0), rws, vo, 0); st16_dev(pack8(av, 1), rws, vo + 1024, 0);
        } else {
            f32x16 aq = {};
            __builtin_amdgcn_s_setprio(1);
#pragma unroll
            for (int n = 0; n < 30; ++n) {
                const int c = n >> 2, ks = n & 3, km = (n - 2) & 3;
                if (n < 28 && ks == 0) A_WAIT(c);
                if (n >= 2) { aq = MFMA(f1[km], fx[km], aq); PIN(aq); } SB();
                if (n < 28) { fx[ks] = lds8(xb[ks] + c * 4096); f1[ks] = cat(trd(A_WP(c, ks)), trd(A_WP(c, ks) + 512)); }
                if (c == 0 && ks < 3) A_PIECE(3, ks); if (c >= 2 && c < 5 && ks < 3) A_PIECE(c + 2, ks);
                SB();
            }
            __builtin_amdgcn_s_setprio(0);
            const float QS = rst[tt * 32 + r] * (0.18898223650461362f * 1.4426950408889634f);
#pragma unroll
            for (int i = 0; i < 16; ++i) aq[i] *= QS;
#pragma unroll
            for (int p = 0; p < 2; ++p) {
                const u32x4 f = pack8(aq, p);
                const auto s0 = __builtin_amdgcn_permlane32_swap(f[0], f[2], false, false), s1 = __builtin_amdgcn_permlane32_swap(f[1], f[3], false, false);
                const bool sp = gi[p] == 3;
                st16_dev((u32x4){s0[0], s1[0], sp ? 0u : s0[1], sp ? 0u : s1[1]}, rws, go1[p], 0);
                st16_dev((u32x4){0u, 0u, s0[1], s1[1]}, rws, go2[p], 0);
            }
        }
    } else {
        epoch = peek_flag(ws, FL_EPOCH); fl1 = FL_RT + xcd * 4 + loc - 28;
        const int ri = xcd * 4 + loc - 28, nu = (w == 0 && ri < 16) ? 4 : 3;
        const unsigned vo = (unsigned)(row8 * 3584 + (lane & 7) * 16);
#pragma unroll 1
        for (int i = 0; i < nu; ++i) {
            const int u = i < 3 ? i * 256 + ri * 8 + w : 768 + ri;
            const size_t so = (size_t)(u / 28) * 16 * 3584 + (u % 28) * 128;
            const unsigned d_ = lds0 + w * 16384 + i * 4096;
            glds(vo, wg + so, d_); glds(vo, wg + so + 8 * 3584, d_ + 1024); glds(vo, wu + so, d_ + 2048); glds(vo, wu + so + 8 * 3584, d_ + 3072);
        }
#pragma unroll 1
        for (int i = 0; i < nu; ++i) {
            const int u = i < 3 ? i * 256 + ri * 8 + w : 768 + ri;
            waitv<8>();
            lds_ptr sg = lds + w * 16384 + i * 4096 + g1 * 2048 + (4 * hf + q4) * 128 + p4 * 8;
#pragma unroll
            for (int e = 0; e < 4; ++e) st16_dev(U4(cat(trd(sg + e * 32), trd(sg + e * 32 + 1024))), rws, l16, WS_WGU + (unsigned)(((u % 28) * 4 + e) * 28 + u / 28) * 1024);
            FENCE();
        }
    }
    TS(24);
    waitv_bar<0>();
    const u64 tag = ((u64)(unsigned)__builtin_amdgcn_readfirstlane((unsigned)(epoch >> 32)) << 32 | (u64)(unsigned)__builtin_amdgcn_readfirstlane((unsigned)epoch)) + 1;
    if (tid == 0) set_flag(ws, fl1, tag);
    TS(4);

    const int drb = (xcd >> 1) * 4 + l7, dfg = (xcd & 1) * 7 + m7;
    bf16x8 wf[28];
    u32x2 xa, xb;
    {
        const int head = xcd * 2 + (loc & 1), rb = loc >> 1, qt = w & 3;
        lds_ptr ap = opq(lds + kh * 8192 + l16), apB = opq(lds + 65536 + kh * 8192 + l16), apC = opq(lds + 131072 + kh * 8192 + l16);
        const unsigned lv = kh * 8192 + (l16 ^ (kh * 256));
        lds_ptr vp = opq(lds + lv), vpB = opq(lds + 65536 + lv), vpC = opq(lds + 131072 + lv);
#define AP(off) ((off) < 65536 ? ap + (off) : (off) < 131072 ? apB + ((off) - 65536) : apC + ((off) - 131072))
#define VP(off) ((off) < 65536 ? vp + (off) : (off) < 131072 ? vpB + ((off) - 65536) : vpC + ((off) - 131072))
        const char* kvs = ws + WS_KV + (size_t)head * 262144 + w * 2048;
        const char* qs = ws + WS_QF + (size_t)(head * 64 + rb * 4 + qt) * 2048 + kh * 1024;
#define KV_ISSUE(t) glds2_dev(l16, kvs + (t) * 16384, lds0 + phys(t) + w * 2048)
        if (w == 0) poll_flag(ws, FL_KV + (lane >> 1) * 8 + (head * 28 + (lane & 1) * 27) / 64, tag);
        BAR();
        TS(5);
        glds_dev(l16, qs, lds0 + 147456 + qt * 2048 + kh * 1024);
        KV_ISSUE(0); KV_ISSUE(1);
        asm volatile("s_barrier" ::: "memory");
        KV_ISSUE(2); KV_ISSUE(3);
        waitv_bar<4>();
        TS(6);
        const bf16x8 q0 = lds8(lds + 147456 + qt * 2048 + l16), q1 = lds8(lds + 147456 + qt * 2048 + 1024 + l16);
        f32x16 o = {}, sa = {}, sb;
        bf16x8 k0 = lds8(ap + phys(0)), k1 = lds8(ap + phys(0) + 1024), v0 = lds8(vp + phys(0) + 2048), v1 = {};
        u32x4 p0, p1 = {};
        sa = MFMA(k0, q0, sa); sa = MFMA(k1, q1, sa);
        k0 = lds8(ap + phys(0) + 4096); k1 = lds8(ap + phys(0) + 5120);
        PIN(sa); SB();
        const unsigned wgo = WS_WGU + (unsigned)((ffn ? dfg : 0) * 8 + w) * 28672;
        const char* wos = wo + m7 * 128 + (size_t)w * 56 * 896;
        const unsigned wov = (unsigned)(row8 * 896) + swz;
        const rsrc_t rx = __builtin_amdgcn_make_buffer_rsrc((void*)x, 0, 2048 * 448 * 2, 0x00020000);
        const unsigned xo_ = (unsigned)(((tb * 2 + tt) * 32 + r) * 896 + m7 * 128 + ct * 64 + kh * 32 + hf * 8);
        u64 rtf = 0;
#define EXP4(S, o4) _Pragma("unroll") for (int i_ = o4; i_ < o4 + 4; ++i_) S[i_] = __builtin_amdgcn_exp2f(S[i_]);
#define WLD(s) wf[s] = BF8(ld16_dev(rws, l16 + ((s) & 3) * 1024, wgo + ((s) >> 2) * 4096))
#define SIDE(t, g) do { \
            if ((t) == 0 && (g) == 0) KV_ISSUE(4); if ((t) == 0 && (g) == 2) KV_ISSUE(5); if ((t) == 1 && (g) == 0) KV_ISSUE(6); if ((t) == 1 && (g) == 2) KV_ISSUE(7); \
            if ((t) == 2 && (g) == 0) KV_ISSUE(8); if ((t) == 1 && (g) == 3) rtf = peek_flag(ws, FL_RT + r); \
            if ((t) == 3 && (g) < 3) KV_ISSUE(9 + (g)); \
            if ((t) == 7 && (g) == 0) KV_ISSUE(12); if ((t) == 7 && (g) == 2) KV_ISSUE(13); if ((t) == 8 && (g) == 0) KV_ISSUE(14); if ((t) == 8 && (g) == 2) KV_ISSUE(15); \
            if ((t) >= 4 && (t) <= 6) WLD(((t) - 4) * 4 + (g)); \
            if (((t) == 7 || (t) == 8) && ((g) & 1)) WLD(12 + ((t) - 7) * 2 + ((g) >> 1)); \
            if ((t) >= 9 && (t) <= 11) WLD(((t) - 5) * 4 + (g)); \
            if ((t) == 12 || ((t) == 13 && (g) < 3)) glds(wov, wos + (((t) - 12) * 4 + (g)) * 8 * 896, lds0 + (w * 7 + ((t) - 12) * 4 + (g)) * 1024); \
            if ((t) == 13 && (g) == 3) { xa = ld8(rx, xo_, 0); xb = ld8(rx, xo_, 16); } \
            if ((t) == 14 && (g) == 0) { if (tid < 32) *(LAS u32x4*)(lds + 57344 + tid * 16) = (u32x4){0, 0, 0, 0}; } \
        } while (0)
#define TOFF(i) (phys((i) >> 1) + ((i) & 1) * 4096)
#define TILE(i, SC, SN) do { \
            __builtin_amdgcn_s_setprio(((i) & 1) ? 1 : 3); \
            if ((i) < 31) { f32x16 z_ = {}; SN = MFMA(k0, q0, z_); PIN(SN); } if ((i) < 30) k0 = lds8(AP(TOFF((i) + 2))); SB(); \
            EXP4(SC, 0); PIN(SC); SB(); \
            o = MFMA(v1, BF8(p1), o); PIN(o); v1 = lds8(VP(TOFF(i) + 3072)); SIDE((i) >> 1, ((i) & 1) * 2); SB(); \
            EXP4(SC, 4); p0 = pack8(SC, 0); PIN(p0); SB(); \
            __builtin_amdgcn_s_setprio(((i) & 1) ? 0 : 2); \
            if ((i) < 31) { SN = MFMA(k1, q1, SN); PIN(SN); } if ((i) < 30) k1 = lds8(AP(TOFF((i) + 2) + 1024)); SB(); \
            EXP4(SC, 8); PIN(SC); SB(); \
            o = MFMA(v0, BF8(p0), o); PIN(o); if ((i) < 31) v0 = lds8(VP(TOFF((i) + 1) + 2048)); SIDE((i) >> 1, ((i) & 1) * 2 + 1); SB(); \
            EXP4(SC, 12); p1 = pack8(SC, 1); PIN(p1); SB(); } while (0)
#define STEP(t) do { TILE(2 * (t), sa, sb); TILE(2 * (t) + 1, sb, sa); } while (0)
        STEP(0);
        waitv_bar<4>();
        STEP(1); STEP(2);
        waitv_bar<3>();
        TS(21);
        if (__builtin_expect(!wave_all(rtf == tag), 0)) poll_flag(ws, FL_RT + r, tag);
        FENCE();
        STEP(3); STEP(4); STEP(5); STEP(6);
        waitv_bar<12>();
        TS(22);
        STEP(7); STEP(8); STEP(9); STEP(10);
        waitv_bar<9>();
        TS(23);
        lds_ptr xw = opq(lds + 147456 + w * 2048 + l16), xr = opq(lds + 147456 + (w ^ 4) * 2048 + l16);
        lds_ptr lw = opq(lds + 58368 + w * 256 + lane * 4), lr = opq(lds + 58368 + qt * 256 + 128 + r * 4);
        lds_ptr bw = opq(lds + 60416 + w * 4), br = opq(lds + 60416 + (lane & 7) * 4);
        unsigned oo = WS_OF + (unsigned)((rb * 4 + qt) * 32 + head * 2 + kh) * 1024 + l16;
        PIN(oo);
        STEP(11); STEP(12); STEP(13); STEP(14); STEP(15);
        o = MFMA(v1, BF8(p1), o);
        TS(7);
        u32x4 ov;
        {
            *(LAS f32x4*)xw = (f32x4){o[8], o[9], o[10], o[11]}; *(LAS f32x4*)(xw + 1024) = (f32x4){o[12], o[13], o[14], o[15]};
            const float l = kh ? o[4] : o[12];
            *(LAS float*)lw = l;
            float sa_ = 0.f;
#pragma unroll
            for (int i = 0; i < 16; ++i) sa_ += __builtin_fabsf(o[i]);
            const bool ok = sa_ < 0x1p100f && (hf == 0 || l > 0x1p-80f);
            *(LAS unsigned*)bw = wave_all(ok) ? 0u : 1u;
            BAR();
            const f32x4 a = *(LAS f32x4*)xr, c = *(LAS f32x4*)(xr + 1024);
            const float inv = __builtin_amdgcn_rcpf(*(LAS float*)lr + *(LAS float*)(lr + 1024));
            const unsigned bad = *(LAS unsigned*)br;
            const float iz = (kh && hf) ? 0.f : inv;
            ov = (u32x4){cvtpk((o[0] + a[0]) * inv, (o[1] + a[1]) * inv), cvtpk((o[2] + a[2]) * inv, (o[3] + a[3]) * inv),
                         cvtpk((o[4] + c[0]) * iz, (o[5] + c[1]) * iz), cvtpk((o[6] + c[2]) * iz, (o[7] + c[3]) * iz)};
            if (__builtin_expect(wave_any(bad != 0), 0)) {
                f32x16 of = {};
                float m = -0x1p126f;
#pragma unroll 1
                for (int t = 0; t < 16; ++t) {
                    BAR();
                    glds2_dev(l16, kvs + t * 16384, lds0 + 65536 + w * 2048);
                    waitv_bar<0>();
#pragma unroll 1
                    for (int u = 0; u < 2; ++u) {
                        f32x16 s = {};
                        s = MFMA(lds8(apB + u * 4096), q0, s); s = MFMA(lds8(apB + u * 4096 + 1024), q1, s);
                        float mx = s[0];
#pragma unroll
                        for (int i = 1; i < 16; ++i) mx = __builtin_fmaxf(mx, s[i]);
                        mx = __builtin_fmaxf(swap_max(mx), m);
                        const float alpha = __builtin_amdgcn_exp2f(m - mx);
                        m = mx;
#pragma unroll
                        for (int i = 0; i < 16; ++i) { s[i] = __builtin_amdgcn_exp2f(s[i] - mx); of[i] *= alpha; }
                        of = MFMA(lds8(vpB + u * 4096 + 2048), BF8(pack8(s, 0)), of);
                        of = MFMA(lds8(vpB + u * 4096 + 3072), BF8(pack8(s, 1)), of);
                    }
                }
                *(LAS f32x4*)xw = (f32x4){of[8], of[9], of[10], of[11]}; *(LAS f32x4*)(xw + 1024) = (f32x4){of[12], of[13], of[14], of[15]};
                *(LAS float*)lw = hf ? (kh ? of[4] : of[12]) : m;
                BAR();
                const f32x4 a2 = *(LAS f32x4*)xr, c2 = *(LAS f32x4*)(xr + 1024);
                const float m0 = *(LAS float*)(lr - 128), m1 = *(LAS float*)(lr + 1024 - 128), mm = __builtin_fmaxf(m0, m1);
                const float e0 = __builtin_amdgcn_exp2f(m0 - mm), e1 = __builtin_amdgcn_exp2f(m1 - mm);
                const float iv = __builtin_amdgcn_rcpf(*(LAS float*)lr * e0 + *(LAS float*)(lr + 1024) * e1);
                const float im = (kh ? e1 : e0) * iv, io = (kh ? e0 : e1) * iv;
                const bool z = kh && hf;
                ov = (u32x4){cvtpk(of[0] * im + a2[0] * io, of[1] * im + a2[1] * io), cvtpk(of[2] * im + a2[2] * io, of[3] * im + a2[3] * io),
                             z ? 0u : cvtpk(of[4] * im + c2[0] * io, of[5] * im + c2[1] * io), z ? 0u : cvtpk(of[6] * im + c2[2] * io, of[7] * im + c2[3] * io)};
            }
        }
        st16_dev(ov, rws, oo, 0);
        TS(25);
        waitv_bar<0>();
        if (tid == 0) set_flag(ws, FL_O + rb * 16 + head, tag);
        TS(8);
    }
    if (__builtin_expect(!ffn, 0)) {
        const int ri = xcd * 4 + loc - 28, nu = (w == 0 && ri < 16) ? 4 : 3;
        const unsigned vo = (unsigned)(row8 * 896 + (lane & 7) * 16);
#pragma unroll 1
        for (int i = 0; i < nu; ++i) {
            const int u = i < 3 ? i * 256 + ri * 8 + w : 768 + ri;
            const char* s_ = wd + (size_t)(u / 7) * 16 * 896 + (u % 7) * 128;
            glds(vo, s_, lds0 + w * 8192 + i * 2048); glds(vo, s_ + 8 * 896, lds0 + w * 8192 + i * 2048 + 1024);
        }
        waitv<0>();
#pragma unroll 1
        for (int i = 0; i < nu; ++i) {
            const int u = i < 3 ? i * 256 + ri * 8 + w : 768 + ri;
            lds_ptr sd = lds + w * 8192 + i * 2048 + (4 * hf + q4) * 128 + g1 * 32 + p4 * 8;
#pragma unroll
            for (int e = 0; e < 2; ++e) st16_dev(U4(cat(trd(sd + e * 64), trd(sd + e * 64 + 1024))), rws, l16, WS_WD + (unsigned)(((u % 7) * 112 + u / 7) * 2 + e) * 1024);
        }
        waitv_bar<0>();
        if (tid == 0) set_flag(ws, FL_RD + xcd * 4 + loc - 28, tag);
        return;
    }
    PIN2(xa, xb);
#pragma unroll
    for (int s = 27; s >= 0; --s) PIN(wf[s]);

    f32x16 cs = {};
    const u32x4 one4 = {ones, ones, ones, ones};
    {
#pragma unroll
        for (int s = 0; s < 14; ++s) cs = MFMA(wf[s], BF8(one4), cs);
        PIN(cs);
        lds_ptr wb = lds + kh * (8 * 28 * 128) + (4 * hf + q4) * 128 + ((ct ^ (q4 >> 1)) * 64) + (g1 ^ kh) * 32 + p4 * 8;
        bf16x8 wr[16];
#pragma unroll
        for (int s = 0; s < 12; ++s) { const int k0 = ((s >> 1) * 28 + (s & 1) * 16) * 128; wr[s] = cat(trd(wb + k0), trd(wb + k0 + 1024)); }
        f32x16 acc = {};
        acc[0] = bflo(xa[0]); acc[1] = bfhi(xa[0]); acc[2] = bflo(xa[1]); acc[3] = bfhi(xa[1]); acc[4] = bflo(xb[0]); acc[5] = bfhi(xb[0]); acc[6] = bflo(xb[1]); acc[7] = bfhi(xb[1]);
        lds_ptr cw = opq(lds + 131072 + w * 2048 + l16), cr = opq(lds + 131072 + (w ^ 4) * 2048 + l16);
        const unsigned hi_ = (unsigned)((tb * 2 + tt) * 28 + m7 * 4 + ct * 2 + kh);
        unsigned ho = WS_HF + hi_ * 1024 + l16, so = hf ? OOB : WS_HS + hi_ * 256 + r * 8;
        PIN(acc); PIN2(ho, so);
        if (w == 0) poll_flag(ws, FL_O + (tb >> 1) * 16 + (lane & 15), tag);
        BAR();
        TS(9);
        const char* os = ws + WS_OF + (size_t)tb * 65536;
        const unsigned fo = (unsigned)(tt * 32768 + kh * 16384 + ct * 4096);
        glds4_dev(l16, os + fo, lds0 + 65536 + fo);
        asm volatile("s_barrier" ::: "memory");
        glds4_dev(l16, os + fo + 8192, lds0 + 65536 + fo + 8192);
        if (bid == 0 && tid == 0) set_flag(ws, FL_EPOCH, tag);
        lds_ptr ob = opq(lds + 65536 + tt * 32768 + kh * 16384 + l16);
        bf16x8 of[8];
        waitv_bar<4>();
        TS(26);
#pragma unroll
        for (int s = 0; s < 8; ++s) of[s] = lds8(ob + s * 1024);
        SB();
#pragma unroll
        for (int s = 0; s < 8; ++s) acc = MFMA(wr[s], of[s], acc);
        PIN(acc); SB();
#pragma unroll
        for (int s = 12; s < 16; ++s) { const int k0 = ((s >> 1) * 28 + (s & 1) * 16) * 128; wr[s] = cat(trd(wb + k0), trd(wb + k0 + 1024)); }
        waitv_bar<0>();
#pragma unroll
        for (int s = 0; s < 8; ++s) of[s] = lds8(ob + (8 + s) * 1024);
        SB();
#pragma unroll
        for (int s = 0; s < 8; ++s) acc = MFMA(wr[8 + s], of[s], acc);
        TS(10);
        *(LAS f32x4*)cw = (f32x4){acc[8], acc[9], acc[10], acc[11]}; *(LAS f32x4*)(cw + 1024) = (f32x4){acc[12], acc[13], acc[14], acc[15]};
        BAR();
        const f32x4 a = *(LAS f32x4*)cr + (f32x4){acc[0], acc[1], acc[2], acc[3]}, b = *(LAS f32x4*)(cr + 1024) + (f32x4){acc[4], acc[5], acc[6], acc[7]};
        const u32x4 hv = {cvtpk(a[0], a[1]), cvtpk(a[2], a[3]), cvtpk(b[0], b[1]), cvtpk(b[2], b[3])};
        st16_dev(hv, rws, ho, 0);
        float s1 = 0.f, s2 = 0.f;
        acc_stats(hv, s1, s2);
        s1 = swap_add(s1); s2 = swap_add(s2);
        st8_dev((u32x2){f2u(s1), f2u(s2)}, rws, so, 0);
        waitv_bar<0>();
        if (tid == 0) set_flag(ws, FL_H + tb * 8 + m7, tag);
        TS(11);
    }

    const int erb = tb >> 1;
    const unsigned wdo = WS_WD + (unsigned)(m7 * 112 + w * 14) * 2048;
    bf16x8 wdf[28];
    bf16x8 gf[8];
    const int ta = erb * 4 + (tb & 1);
    const unsigned gxo = WS_GF + (unsigned)(ta * 112 + w * 14) * 1024;
    u64 gfl = 0;
    const unsigned gfi = FL_G + erb * 32 + umin(lane & 15, 13);
    {
#pragma unroll
        for (int s = 14; s < 28; ++s) cs = MFMA(wf[s], BF8(one4), cs);
        lds_ptr csp = opq(lds + 161792 + w * 128 + hf * 64);
        if (r == 0) {
#pragma unroll
            for (int g = 0; g < 4; ++g) *(LAS f32x4*)(csp + g * 16) = (f32x4){cs[4 * g], cs[4 * g + 1], cs[4 * g + 2], cs[4 * g + 3]};
        }
        const char* hs = ws + WS_HF + (size_t)drb * 114688 + w * 1024;
        const unsigned hd = lds0 + w * 1024;
        const char* wds = ws + wdo;
        lds_ptr hb = opq(lds + l16), hb2 = opq(lds + 57344 + l16);
        lds_ptr wl = opq(lds + 114688 + w * 5120 + l16), wl2 = opq(lds + w * 7168 + l16);
        LAS f32x2* stp = (LAS f32x2*)opq(lds + 159744 + r * 8);
        unsigned go = WS_GF + (unsigned)(drb * 4 * 112 + dfg * 8 + w) * 1024 + l16;
        PIN(go);
        if (w == 0) { const int i = lane & 15; poll_flag(ws, hf ? FL_RD + r : FL_H + drb * 16 + (i >> 3) * 8 + ((i & 7) == 7 ? 0 : (i & 7)), tag); }
        BAR();
        TS(12);
#define H_PIECE(j) glds_dev(l16, hs + (j) * 8192, hd + (j) * 8192)
#define WDLD(s) wdf[s] = BF8(ld16_dev(rws, l16 + ((s) & 3) * 1024, wdo + ((s) >> 2) * 4096))
#define HB(n) ((n) < 56 ? hb + (n) * 1024 : hb2 + ((n) - 56) * 1024)
        H_PIECE(0);
        asm volatile("s_barrier" ::: "memory");
        H_PIECE(1); H_PIECE(2); H_PIECE(3);
        glds4_dev(l16, ws + WS_HS + (size_t)((drb * 4 + (w & 3)) * 28 + kh * 14) * 256, lds0 + 114688 + w * 5120);
        constexpr int PF = 5;
        bf16x8 bf[PF + 1];
        f32x16 acc[2];
        f32x2 sp[7], sv0, sv1;
        f32x4 c4[4];
        float mean = 0.f, r2 = 0.f, hp = 0.f;
        u32x4 hv;
        waitv_bar<7>();
        TS(27);
#pragma unroll
        for (int n = 0; n < PF; ++n) bf[n] = lds8(HB(n));
        SB();
        sfor<0, 125>([&](auto N_) __attribute__((always_inline)) {
            constexpr int n = decltype(N_)::v, p = n / 28, s = n % 28;
            if constexpr (n == 3) waitv_bar<5>();
            if constexpr (n == 27) { waitv_bar<3>(); TS(13); }
            if constexpr (n == 64) { waitv_bar<12>(); TS(14); }
            if constexpr (n == 84) { waitv_bar<7>(); if (tid == 0) set_flag(ws, FL_G + drb * 32 + dfg, tag); }
            if constexpr (d_prio(n) != d_prio(n - 1)) __builtin_amdgcn_s_setprio(d_prio(n));
            if constexpr (n < 112) { if constexpr (s == 0) { f32x16 z = {}; acc[p & 1] = MFMA(wf[s], bf[n % (PF + 1)], z); } else acc[p & 1] = MFMA(wf[s], bf[n % (PF + 1)], acc[p & 1]); PIN(acc[p & 1]); }
            SB();
            if constexpr (n + PF < 112) bf[(n + PF) % (PF + 1)] = lds8(HB(n + PF));
            if constexpr (n % 3 == 2 && n < 30) H_PIECE(4 + n / 3);
            if constexpr (n == 16) { waitv<5>();
#pragma unroll
                for (int j = 0; j < 7; ++j) sp[j] = *(LAS f32x2*)(lds + 114688 + w * 5120 + (hf * 7 + j) * 256 + r * 8);
            }
            if constexpr (n == 20) { const f32x2 t = ((sp[0] + sp[1]) + (sp[2] + sp[3])) + ((sp[4] + sp[5]) + sp[6]); stp[w * 32] = (f32x2){swap_add(t[0]), swap_add(t[1])}; }
            if constexpr (n >= 33 && n < 48 && n % 3 == 0) glds_dev(l16, wds + (n - 33) / 3 * 1024, lds0 + 114688 + w * 5120 + (n - 33) / 3 * 1024);
            if constexpr (n >= 48 && n < 64 && n % 3 == 0) WDLD(12 + (n - 48) / 3);
            if constexpr (n >= 70 && n < 84 && n % 2 == 0) glds_dev(l16, wds + (5 + (n - 70) / 2) * 1024, lds0 + w * 7168 + (n - 70) / 2 * 1024);
            if constexpr (n >= 85 && n < 105 && n % 2 == 1) WDLD(18 + (n - 85) / 2);
            if constexpr (n == 108) gfl = peek_flag(ws, gfi);
            if constexpr (p >= 1) {
                const f32x16& a = acc[(p - 1) & 1];
                if constexpr (n == 100) { sv0 = stp[3 * 32]; sv1 = stp[7 * 32]; }
                if constexpr (s == 0 && p < 4) { sv0 = stp[(p - 1) * 32]; sv1 = stp[(p + 3) * 32];
#pragma unroll
                    for (int g = 0; g < 4; ++g) c4[g] = *(LAS f32x4*)(csp + g * 16); }
                if constexpr ((s == 3 && p < 4) || n == 104) { const f32x2 sv = sv0 + sv1; mean = sv[0] * (1.f / D); r2 = __builtin_amdgcn_rcpf(__builtin_fmaxf(sv[1] * (1.f / D) - mean * mean, 0.f) + EPS); PIN2(mean, r2); }
                if constexpr (s >= 4 && s < 12) { constexpr int i = s - 4;
                    const float h0 = __builtin_fmaxf(a[i] - mean * c4[i >> 2][i & 3], 0.f) * r2 * (a[8 + i] - mean * c4[2 + (i >> 2)][i & 3]);
                    if constexpr (i & 1) { hv[i >> 1] = cvtpk(hp, h0); PIN(hv); } else { hp = h0; PIN(hp); } }
                if constexpr (s == 12) { st16_dev(hv, rws, go + (p - 1) * 114688, 0); FENCE(); }
            }
            SB();
        });
        TS(15);
        if (__builtin_expect(!wave_all(gfl == tag), 0)) poll_flag(ws, gfi, tag);
        FENCE();
#pragma unroll
        for (int s = 0; s < 8; ++s) { gf[s] = BF8(ld16_dev(rws, l16 + (s & 3) * 1024, gxo + (s >> 2) * 4096)); SB(); }
        FENCE();
#pragma unroll
        for (int s = 0; s < 12; ++s) wdf[s] = lds8(s < 5 ? wl + s * 1024 : wl2 + (s - 5) * 1024);
    }

    {
        const int etok = tid >> 4, eblk = tid & 15;
        const unsigned hro = (unsigned)((((eblk >> 2) & 3) * 64 + (eblk & 1) * 32 + etok) * 16 + ((eblk >> 1) & 1) * 8) + WS_HF + (unsigned)(ta * 28 + m7 * 4) * 1024;
        u32x2 hres[2];
        GAS u64* op = (GAS u64*)(out + (size_t)(ta * 32 + etok) * 896 + m7 * 128 + eblk * 8);
        lds_ptr rp = opq(lds + w * 8448 + hf * 528 + r * 16), rq = opq(lds + eblk * 528 + etok * 16);
        lds_ptr rp2 = opq(lds + 67584 + w * 8448 + hf * 528 + r * 16), rq2 = opq(lds + 67584 + eblk * 528 + etok * 16);
#define G_LD(hh, s) gf[(s) & 7] = BF8(ld16_dev(rws, l16 + ((s) & 3) * 1024, gxo + (hh) * 229376 + ((s) >> 2) * 4096))
#define G_MMA(hh, RP) do { f32x16 c0 = {}, c1 = {}; \
            _Pragma("unroll") for (int s = 0; s < 14; ++s) { c0 = MFMA(wdf[2 * s], gf[s & 7], c0); PIN(c0); c1 = MFMA(wdf[2 * s + 1], gf[s & 7], c1); PIN(c1); SB(); if (s < 6) G_LD(hh, s + 8); SB(); } \
            _Pragma("unroll") for (int g = 0; g < 4; ++g) *(LAS f32x4*)(RP + g * 1056) = (f32x4){c0[4 * g], c0[4 * g + 1], c0[4 * g + 2], c0[4 * g + 3]}; \
            _Pragma("unroll") for (int g = 0; g < 4; ++g) *(LAS f32x4*)(RP + 4224 + g * 1056) = (f32x4){c1[4 * g], c1[4 * g + 1], c1[4 * g + 2], c1[4 * g + 3]}; } while (0)
#define G_OUT(hh, RQ) do { f32x4 pa[8]; \
            _Pragma("unroll") for (int ww = 0; ww < 8; ++ww) pa[ww] = *(LAS f32x4*)(RQ + ww * 8448); SB(); \
            const f32x4 a = ((pa[0] + pa[1]) + (pa[2] + pa[3])) + ((pa[4] + pa[5]) + (pa[6] + pa[7])); \
            const u32x2 ov = {cvtpk(a[0] + bflo(hres[hh][0]), a[1] + bfhi(hres[hh][0])), cvtpk(a[2] + bflo(hres[hh][1]), a[3] + bfhi(hres[hh][1]))}; \
            __hip_atomic_store(op + (hh) * (64 * 896 / 8), __builtin_bit_cast(u64, ov), __ATOMIC_RELAXED, __HIP_MEMORY_SCOPE_AGENT); } while (0)
        waitv_bar<8>();
        if (tid == 0) set_flag(ws, FL_G + drb * 32 + 16 + dfg, tag);
        TS(16);
        hres[0] = ld8_dev(rws, hro, 0); hres[1] = ld8_dev(rws, hro, 57344);
        FENCE(); SB();
        G_MMA(0, rp);
        PIN2(hres[0], hres[1]);
        TS(17);
        if (w == 0) poll_flag(ws, gfi + 16, tag);
        BAR();
        TS(18);
#pragma unroll
        for (int s = 0; s < 8; ++s) { G_LD(1, s); SB(); }
        FENCE();
        G_OUT(0, rq);
        FENCE(); SB();
        G_MMA(1, rp2);
        TS(19);
        BAR();
        G_OUT(1, rq2);
    }
    TS(20);
}

extern "C" void kernel_launch(void* const* d_in, const int* in_sizes, int n_in, void* d_out, int out_size, void* d_ws, size_t ws_size, hipStream_t stream) {
    (void)hipFuncSetAttribute((const void*)block_fwd, hipFuncAttributeMaxDynamicSharedMemorySize, LDS_BYTES);
    block_fwd<<<256, 512, LDS_BYTES, stream>>>((const char*)d_in[0], (const char*)d_in[1], (const char*)d_in[2], (const char*)d_in[4], (const char*)d_in[5], (const char*)d_in[6], (char*)d_out, (char*)d_ws);
}
```

```cpp
#define WS_OFF (ws_size / 2)
#include <hip/hip_runtime.h>
#include <hip/hip_bf16.h>
#include <stdint.h>

typedef __attribute__((ext_vector_type(8))) short bf16x8;
typedef __attribute__((ext_vector_type(4))) short s16x4;
typedef __attribute__((ext_vector_type(2))) __bf16 bf16x2;
typedef __attribute__((ext_vector_type(16))) float f32x16;
typedef __attribute__((ext_vector_type(4))) float f32x4;
typedef __attribute__((ext_vector_type(2))) float f32x2;
typedef __attribute__((ext_vector_type(4))) unsigned u32x4;
typedef __attribute__((ext_vector_type(2))) unsigned u32x2;
typedef short v4i16_t __attribute__((ext_vector_type(4)));
typedef unsigned long long u64;
#define LAS __attribute__((address_space(3)))
#define GAS __attribute__((address_space(1)))
typedef LAS char* lds_ptr;
#if defined(__HIP_DEVICE_COMPILE__)
#define NOPK __attribute__((target("no-packed-fp32-ops")))
#else
#define NOPK
#endif
#define DI __device__ __forceinline__
#define MFMA(a, b, c) __builtin_amdgcn_mfma_f32_32x32x16_bf16(a, b, c, 0, 0, 0)
#define MFMA16(a, b, c) __builtin_amdgcn_mfma_f32_16x16x32_bf16(a, b, c, 0, 0, 0)
#define BF8(x) __builtin_bit_cast(bf16x8, x)
#define U4(x) __builtin_bit_cast(u32x4, x)

constexpr int D = 448;
constexpr float EPS = 1e-5f;
constexpr unsigned WS_FLAG = 0;
constexpr unsigned FL_EPOCH = 0, FL_KV = 1, FL_RT = FL_KV + 256, FL_RD = FL_RT + 32, FL_O = FL_RD + 32, FL_H = FL_O + 256, FL_G = FL_H + 256;
constexpr unsigned WS_KV = 1u << 20;
constexpr unsigned WS_QF = WS_KV + 4194304;
constexpr unsigned WS_OF = WS_QF + 2097152;
constexpr unsigned WS_HF = WS_OF + 2097152;
constexpr unsigned WS_GF = WS_HF + 64 * 28 * 1024;
constexpr unsigned WS_WGU = WS_GF + 64 * 112 * 1024;
constexpr unsigned WS_WD = WS_WGU + 112 * 28 * 1024;
constexpr unsigned WS_HS = WS_WD + 7 * 112 * 2 * 1024;
constexpr unsigned WS_END = WS_HS + 64 * 28 * 256 + 1024;
constexpr unsigned OOB = 0x40000000u;
constexpr int LDS_BYTES = 163840;

DI float u2f(unsigned u) { return __builtin_bit_cast(float, u); }
DI unsigned f2u(float f) { return __builtin_bit_cast(unsigned, f); }
DI unsigned umin(unsigned a, unsigned b) { return a < b ? a : b; }
DI bool wave_any(bool p) { return __builtin_amdgcn_ballot_w64(p) != 0; }
DI bool wave_all(bool p) { return __builtin_amdgcn_ballot_w64(!p) == 0; }
DI unsigned cvtpk(float lo, float hi) { return __builtin_bit_cast(unsigned, __builtin_convertvector((f32x2){lo, hi}, bf16x2)); }
DI float bflo(unsigned u) { return u2f(u << 16); }
DI float bfhi(unsigned u) { return u2f(u & 0xffff0000u); }
DI void glds(unsigned voff, const void* sbase, unsigned dst) {
    asm volatile("s_mov_b32 m0, %2\n\ts_nop 0\n\tglobal_load_lds_dwordx4 %0, %1" :: "v"(voff), "s"(sbase), "s"(dst) : "memory");
}
DI void glds_dev(unsigned voff, const void* sbase, unsigned dst) {
    asm volatile("s_mov_b32 m0, %2\n\ts_nop 0\n\tglobal_load_lds_dwordx4 %0, %1 sc1" :: "v"(voff), "s"(sbase), "s"(dst) : "memory");
}
DI void glds2_dev(unsigned voff, const void* sbase, unsigned dst) {
    asm volatile("s_mov_b32 m0, %2\n\ts_nop 0\n\tglobal_load_lds_dwordx4 %0, %1 sc1\n\tglobal_load_lds_dwordx4 %0, %1 offset:1024 sc1" :: "v"(voff), "s"(sbase), "s"(dst) : "memory");
}
DI void glds4_dev(unsigned voff, const void* sbase, unsigned dst) {
    asm volatile("s_mov_b32 m0, %2\n\ts_nop 0\n\tglobal_load_lds_dwordx4 %0, %1 sc1\n\tglobal_load_lds_dwordx4 %0, %1 offset:1024 sc1\n\t"
                 "global_load_lds_dwordx4 %0, %1 offset:2048 sc1\n\tglobal_load_lds_dwordx4 %0, %1 offset:3072 sc1" :: "v"(voff), "s"(sbase), "s"(dst) : "memory");
}
DI s16x4 trd(lds_ptr p) { return __builtin_bit_cast(s16x4, __builtin_amdgcn_ds_read_tr16_b64_v4i16((LAS v4i16_t*)p)); }
DI bf16x8 cat(s16x4 a, s16x4 b) { return __builtin_shufflevector(a, b, 0, 1, 2, 3, 4, 5, 6, 7); }
DI bf16x8 lds8(lds_ptr p) { return *(LAS bf16x8*)p; }
DI float swap_add(float v) { auto r = __builtin_amdgcn_permlane32_swap(f2u(v), f2u(v), false, false); return u2f(r[0]) + u2f(r[1]); }
DI float swap_max(float v) { auto r = __builtin_amdgcn_permlane32_swap(f2u(v), f2u(v), false, false); return __builtin_fmaxf(u2f(r[0]), u2f(r[1])); }
DI u32x4 pack8(const f32x16& a, int s) { return (u32x4){cvtpk(a[8 * s], a[8 * s + 1]), cvtpk(a[8 * s + 2], a[8 * s + 3]), cvtpk(a[8 * s + 4], a[8 * s + 5]), cvtpk(a[8 * s + 6], a[8 * s + 7])}; }
DI float dot2(unsigned a, unsigned b, float c) { return __builtin_amdgcn_fdot2_f32_bf16(__builtin_bit_cast(bf16x2, a), __builtin_bit_cast(bf16x2, b), c, false); }
DI void acc_stats(u32x4 u, float& s1, float& s2) {
#pragma unroll
    for (int i = 0; i < 4; ++i) { const float a = bflo(u[i]), b = bfhi(u[i]); s1 += a; s2 = __builtin_fmaf(a, a, s2); s1 += b; s2 = __builtin_fmaf(b, b, s2); }
}
DI void acc_stats2(unsigned u0, unsigned u1, f32x4& s) {
    const float a = bflo(u0), b = bfhi(u0), c = bflo(u1), d = bfhi(u1);
    s[0] += a; s[1] += b; s[2] = __builtin_fmaf(a, a, s[2]); s[3] = __builtin_fmaf(b, b, s[3]); s[0] += c; s[1] += d; s[2] = __builtin_fmaf(c, c, s[2]); s[3] = __builtin_fmaf(d, d, s[3]);
}
DI float sum8(float v) {
    v += u2f(__builtin_amdgcn_update_dpp(0u, f2u(v), 0xB1, 0xf, 0xf, true));
    v += u2f(__builtin_amdgcn_update_dpp(0u, f2u(v), 0x4E, 0xf, 0xf, true));
    v += u2f(__builtin_amdgcn_update_dpp(0u, f2u(v), 0x141, 0xf, 0xf, true));
    return v;
}
DI u32x4 shift8(u32x4 x, float c) {
    u32x4 o; float t0, t1, t2, t3, t4, t5, t6, t7;
    asm("v_dot2_f32_bf16 %4, %12, %16, %18\n\tv_dot2_f32_bf16 %5, %12, %17, %18\n\tv_dot2_f32_bf16 %6, %13, %16, %18\n\tv_dot2_f32_bf16 %7, %13, %17, %18\n\t"
        "v_dot2_f32_bf16 %8, %14, %16, %18\n\tv_dot2_f32_bf16 %9, %14, %17, %18\n\tv_dot2_f32_bf16 %10, %15, %16, %18\n\tv_dot2_f32_bf16 %11, %15, %17, %18\n\t"
        "v_cvt_pk_bf16_f32 %0, %4, %5\n\tv_cvt_pk_bf16_f32 %1, %6, %7\n\tv_cvt_pk_bf16_f32 %2, %8, %9\n\tv_cvt_pk_bf16_f32 %3, %10, %11"
        : "=&v"(o[0]), "=&v"(o[1]), "=&v"(o[2]), "=&v"(o[3]), "=&v"(t0), "=&v"(t1), "=&v"(t2), "=&v"(t3), "=&v"(t4), "=&v"(t5), "=&v"(t6), "=&v"(t7)
        : "v"(x[0]), "v"(x[1]), "v"(x[2]), "v"(x[3]), "s"(0x00003f80u), "s"(0x3f800000u), "v"(c));
    return o;
}
template <int N> struct IC { static constexpr int v = N; };
template <int B, int E, class F> DI void sfor(F&& f) { if constexpr (B < E) { f(IC<B>{}); sfor<B + 1, E>(f); } }
template <int N> DI void waitv() { asm volatile("s_waitcnt vmcnt(%0)" :: "i"(N) : "memory"); }
template <int N> DI void waitv_bar() { asm volatile("s_waitcnt vmcnt(%0) lgkmcnt(0)\n\ts_barrier" :: "i"(N) : "memory"); }
template <int N> DI void waitv_barx() { asm volatile("s_waitcnt vmcnt(%0)\n\ts_barrier" :: "i"(N) : "memory"); }
DI void touch(unsigned voff, const void* sbase, unsigned junk) {
    asm volatile("s_mov_b32 m0, %2\n\ts_nop 0\n\tglobal_load_lds_dword %0, %1 sc1" :: "v"(voff), "s"(sbase), "s"(junk) : "memory");
}
#define BAR() asm volatile("s_waitcnt lgkmcnt(0)\n\ts_barrier" ::: "memory")
#define FENCE() asm volatile("" ::: "memory")
typedef __amdgpu_buffer_rsrc_t rsrc_t;
DI u32x4 ld16(rsrc_t rs, unsigned voff, unsigned soff) { return U4(__builtin_amdgcn_raw_buffer_load_b128(rs, voff, soff, 0)); }
DI u32x4 ld16_dev(rsrc_t rs, unsigned voff, unsigned soff) { return U4(__builtin_amdgcn_raw_buffer_load_b128(rs, voff, soff, 16)); }
DI u32x2 ld8(rsrc_t rs, unsigned voff, unsigned soff) { return __builtin_bit_cast(u32x2, __builtin_amdgcn_raw_buffer_load_b64(rs, voff, soff, 0)); }
DI u32x2 ld8_dev(rsrc_t rs, unsigned voff, unsigned soff) { return __builtin_bit_cast(u32x2, __builtin_amdgcn_raw_buffer_load_b64(rs, voff, soff, 16)); }
DI void st16_dev(u32x4 v, rsrc_t rs, unsigned voff, unsigned soff) { __builtin_amdgcn_raw_buffer_store_b128(v, rs, voff + soff, 0, 16); }
DI void st8_dev(u32x2 v, rsrc_t rs, unsigned voff, unsigned soff) { __builtin_amdgcn_raw_buffer_store_b64(v, rs, voff, soff, 16); }
#define POLL_SLEEP 1
DI void set_flag(char* ws, unsigned fo, u64 tagv) {
    asm volatile("s_mov_b64 exec, 1\n\tglobal_store_dwordx2 %0, %1, %2 sc1\n\ts_mov_b64 exec, -1" :: "v"(fo), "v"(tagv), "s"(ws) : "memory");
}
DI u64 peek_flag(char* ws, unsigned fo) { return __hip_atomic_load((GAS u64*)(ws + fo), __ATOMIC_RELAXED, __HIP_MEMORY_SCOPE_AGENT); }
DI void poll_flag(char* ws, unsigned fo, u64 tag) {
#pragma unroll 1
    for (int spin = 0; spin < (1 << 16); ++spin) {
        if (wave_all(peek_flag(ws, fo) == tag)) break;
        __builtin_amdgcn_s_sleep(POLL_SLEEP);
    }
    FENCE();
}
#define SB() __builtin_amdgcn_sched_barrier(0)
#define PINS(a) asm volatile("" : "+s"(a))
#define PIN(a) asm volatile("" : "+v"(a))
#define PIN2(a, b) asm volatile("" : "+v"(a), "+v"(b))
DI lds_ptr opq(lds_ptr p) { unsigned u = (unsigned)(uintptr_t)p; asm volatile("" : "+v"(u)); return (lds_ptr)(uintptr_t)u; }
#define TS(i) do {} while (0)

DI constexpr int wd_slot(int s) { return (s * 17 + 27) / 28 != ((s + 1) * 17 + 27) / 28 ? (s * 17 + 27) / 28 : -1; }
#define HOP3_POLLER 1
DI constexpr int d_prio(int n) { return n < 0 ? -1 : n < 27 ? 3 - n / 7 : n < 64 ? 3 - (n - 27) / 10 : n < 84 ? 3 - (n - 64) / 5 : n < 112 ? 3 - (n - 84) / 7 : 0; }
DI constexpr int phys(int t) { return ((t % 9) + 2) % 9 * 16384; }

__global__ NOPK __launch_bounds__(512, 2) void block_fwd(const char* x, const char* wqkv, const char* wo, const char* wg, const char* wu, const char* wd, char* out, char* ws) {
    extern __shared__ __attribute__((aligned(16))) char smem[];
    lds_ptr lds = (lds_ptr)smem;
    const int tid = __builtin_amdgcn_workitem_id_x(), lane = tid & 63, w = __builtin_amdgcn_readfirstlane(tid >> 6);
    const int bid = __builtin_amdgcn_workgroup_id_x(), xcd = bid & 7, loc = bid >> 3;
    const int r = lane & 31, hf = lane >> 5, q4 = (lane & 15) >> 2, g1 = (lane >> 4) & 1, p4 = lane & 3;
    const unsigned lds0 = (unsigned)(uintptr_t)smem;
    const unsigned l16 = lane * 16;
    TS(0);
    const int sp3 = (loc + 8) >> 5;
    const int atb = (xcd >> 1) * 8 + (loc & 7) + sp3 * (xcd & 1) * 4, acg = (loc >> 3) + (1 - sp3) * (xcd & 1) * 4;
    const int row8 = lane >> 3;
    const rsrc_t rws = __builtin_amdgcn_make_buffer_rsrc((void*)ws, 0, WS_END, 0x00020000);
    u64 epoch, tagv;
    unsigned fl1;
    const unsigned ones = 0x3f803f80u;
    const bool ffn = loc < 28;
    const int l7 = loc / 7, m7 = loc % 7;
    const int tb = xcd * 4 + l7;
    const int tt = w & 1, ct = (w >> 1) & 1, kh = w >> 2;
    const unsigned swz = (unsigned)(((lane & 7) ^ (((row8 >> 1) & 1) << 2)) * 16);

    if (__builtin_expect(ffn, 1)) {
        constexpr int A_W = 57344, A_WS = 24576;
        const int rot8 = ((loc & 7) << sp3) + sp3 * (xcd & 1), rot = rot8 & (7 - ((rot8 + 1) >> 3) * 7);
#define A_KC(c) (rot + (c) >= 7 ? rot + (c) - 7 : rot + (c))
        {
            const int xr = (w & 3) * 8 + row8;
            const unsigned xo = (unsigned)(row8 * 896 + (((lane & 7) ^ ((xr >> 1) & 7)) * 16));
            const char* xs = x + (size_t)(atb * 64 + w * 8) * 896;
            unsigned xd = lds0 + kh * 28672 + (w & 3) * 1024, xk = rot * 128;
#pragma unroll
            for (int c = 0; c < 7; ++c) { glds(xo, xs + xk, xd + c * 4096); xk = xk == 768 ? 0 : xk + 128; }
            SB();
        }
        const unsigned wvo = (unsigned)(row8 * 2688) + swz;
        const char* wsrc = wqkv + acg * 128 + (size_t)w * 8 * 2688;
        const unsigned wdst = lds0 + A_W + w * 1024;
#define A_PIECE(c, i) glds(wvo, wsrc + A_KC(c) * (64 * 2688) + (i) * 896, wdst + ((c) & 3) * A_WS + (i) * 8192)
#define A_GROUP(c) do { A_PIECE(c, 1); A_PIECE(c, 2); A_PIECE(c, 0); } while (0)
        epoch = peek_flag(ws, FL_EPOCH * 128); fl1 = (FL_KV + atb * 8 + acg) * 128;
        asm volatile("s_barrier" ::: "memory");
        A_GROUP(0);
        lds_ptr xb[4], wb1, wb1h;
#pragma unroll
        for (int j = 0; j < 4; ++j) xb[j] = lds + tt * 28672 + (r >> 3) * 1024 + (r & 7) * 128 + (((2 * j + hf) ^ ((r >> 1) & 7)) * 16);
        wb1 = opq(lds + A_W + (kh ? 0 : 8192) + (8 * hf + q4) * 128 + ((ct ^ (q4 >> 1)) * 64) + g1 * 32 + p4 * 8);
        wb1h = opq(wb1 + 2 * A_WS);
        unsigned go1[2], go2[2]; int gi[2];
#pragma unroll
        for (int p = 0; p < 2; ++p) {
            const int G = acg * 8 + ct * 4 + 2 * p + hf, hp = G / 7, i = G - hp * 7;
            const unsigned hs = kh ? 131072u : 262144u;
            const unsigned to = (kh ? WS_QF + (atb * 2 + tt) * 2048 : WS_KV + (atb * 2 + tt) * 4096) + r * 16;
            go1[p] = (unsigned)(2 * hp + (i > 3)) * hs + (unsigned)(i > 3 ? i - 3 : i) * 512 + to;
            go2[p] = i == 3 ? (unsigned)(2 * hp + 1) * hs + to : OOB;
            gi[p] = i;
        }
        const int vcol = acg * 64 + ct * 32 + r, vh = vcol / 28, vd = vcol - vh * 28;
        unsigned vo = WS_KV + (atb * 2 + tt) * 4096 + (unsigned)vh * 262144 + 2048 + hf * 512 + vd * 16;
        PIN2(go1[0], go1[1]); PIN2(go2[0], go2[1]); PIN(vo);
        lds_ptr xw = opq(lds + kh * 28672 + (w & 3) * 1024 + l16);
        waitv<3>();
        TS(28);
        u32x4 xv[7];
#pragma unroll
        for (int c = 0; c < 7; ++c) xv[c] = *(LAS u32x4*)(xw + c * 4096);
        SB();
        f32x4 sm = {};
#pragma unroll
        for (int c = 0; c < 7; ++c)
#pragma unroll
            for (int j = 0; j < 4; ++j) sm[j] = dot2(xv[c][j], ones, sm[j]);
        float nm = sum8((sm[0] + sm[1]) + (sm[2] + sm[3])) * (-1.f / D);
        PIN(nm);
#pragma unroll
        for (int c = 0; c < 7; ++c) { xv[c] = shift8(xv[c], nm); *(LAS u32x4*)(xw + c * 4096) = xv[c]; }
        A_GROUP(1);
        SB();
        sm = (f32x4){};
#pragma unroll
        for (int c = 0; c < 7; ++c)
#pragma unroll
            for (int j = 0; j < 4; ++j) sm[j] = dot2(xv[c][j], xv[c][j], sm[j]);
        LAS float* rst = (LAS float*)(lds + 155648);
        rst[w * 8 + row8] = __builtin_amdgcn_rsqf(sum8((sm[0] + sm[1]) + (sm[2] + sm[3])) * (1.f / D) + EPS);
        A_GROUP(2);
        TS(31);
        bf16x8 fx[4], f1[4], f2[4];
#define A_WAIT(c) do { if ((c) == 0) waitv_bar<6>(); else if ((c) == 1) waitv_barx<6>(); else if ((c) < 6) waitv_barx<3>(); else waitv_barx<0>(); \
                       if ((c) == 0) TS(1); if ((c) == 6) TS(2); } while (0)
#define A_WP(c, ks) ((((c) & 2) ? wb1h : wb1) + ((c) & 1) * A_WS + (ks) * 2048)
        if (w < 4) {
            f32x16 ak = {}, av = {};
            f32x4 rv[4];
            float rs = 0.f;
#pragma unroll
            for (int n = 0; n < 30; ++n) {
                const int c = n >> 2, ks = n & 3, km = (n - 2) & 3;
                if (n < 28 && ks == 0) A_WAIT(c);
                if (n >= 2) { ak = MFMA(f1[km], fx[km], ak); PIN(ak); } SB();
                if (n < 28) { fx[ks] = lds8(xb[ks] + c * 4096); f1[ks] = cat(trd(A_WP(c, ks)), trd(A_WP(c, ks) + 512)); } SB();
                if (n >= 2) { av = MFMA(fx[km], f2[km], av); PIN(av); } SB();
                if (n < 28) f2[ks] = cat(trd(A_WP(c, ks) + 8192), trd(A_WP(c, ks) + 8192 + 512));
                if (c == 0 && ks < 3) A_PIECE(3, ks); if (c >= 2 && c < 5 && ks < 3) A_PIECE(c + 2, ks);
                if (n == 21) {
                    rs = rst[tt * 32 + r];
#pragma unroll
                    for (int g = 0; g < 4; ++g) rv[g] = *(LAS f32x4*)(rst + tt * 32 + 8 * g + 4 * hf);
                }
                if (n == 24) { tagv = epoch + 1; PIN(tagv); }
                if (n == 24) {
                    const unsigned pv = vd == 0 ? ones : 0u, po = vd < 4 ? vo + 448 : OOB;
                    st16_dev((u32x4){pv, pv, pv, pv}, rws, po, 0); st16_dev((u32x4){pv, pv, pv, pv}, rws, po + 1024, 0);
                }
                SB();
            }
            TS(3);
#pragma unroll
            for (int i = 0; i < 16; ++i) { ak[i] *= rs; av[i] *= rv[i >> 2][i & 3]; }
#pragma unroll
            for (int p = 0; p < 2; ++p) {
                const u32x4 f = pack8(ak, p);
                const auto s0 = __builtin_amdgcn_permlane32_swap(f[0], f[2], false, false), s1 = __builtin_amdgcn_permlane32_swap(f[1], f[3], false, false);
                const u32x4 c = {s0[0], s1[0], s0[1], s1[1]};
                st16_dev(c, rws, go1[p], 0); st16_dev(c, rws, go2[p], 0);
            }
            st16_dev(pack8(av, 0), rws, vo, 0); st16_dev(pack8(av, 1), rws, vo + 1024, 0);
        } else {
            f32x16 aq = {};
            __builtin_amdgcn_s_setprio(1);
#pragma unroll
            for (int n = 0; n < 30; ++n) {
                const int c = n >> 2, ks = n & 3, km = (n - 2) & 3;
                if (n < 28 && ks == 0) A_WAIT(c);
                if (n == 24) { tagv = epoch + 1; PIN(tagv); }
                if (n >= 2) { aq = MFMA(f1[km], fx[km], aq); PIN(aq); } SB();
                if (n < 28) { fx[ks] = lds8(xb[ks] + c * 4096); f1[ks] = cat(trd(A_WP(c, ks)), trd(A_WP(c, ks) + 512)); }
                if (c == 0 && ks < 3) A_PIECE(3, ks); if (c >= 2 && c < 5 && ks < 3) A_PIECE(c + 2, ks);
                SB();
            }
            __builtin_amdgcn_s_setprio(0);
            const float QS = rst[tt * 32 + r] * (0.18898223650461362f * 1.4426950408889634f);
#pragma unroll
            for (int i = 0; i < 16; ++i) aq[i] *= QS;
#pragma unroll
            for (int p = 0; p < 2; ++p) {
                const u32x4 f = pack8(aq, p);
                const auto s0 = __builtin_amdgcn_permlane32_swap(f[0], f[2], false, false), s1 = __builtin_amdgcn_permlane32_swap(f[1], f[3], false, false);
                const bool sp = gi[p] == 3;
                st16_dev((u32x4){s0[0], s1[0], sp ? 0u : s0[1], sp ? 0u : s1[1]}, rws, go1[p], 0);
                st16_dev((u32x4){0u, 0u, s0[1], s1[1]}, rws, go2[p], 0);
            }
        }
    } else {
        epoch = peek_flag(ws, FL_EPOCH * 128); fl1 = (FL_RT + xcd * 4 + loc - 28) * 128;
        const int ri = xcd * 4 + loc - 28, nu = (w == 0 && ri < 16) ? 4 : 3;
        const unsigned vo = (unsigned)(row8 * 3584 + (lane & 7) * 16);
#pragma unroll 1
        for (int i = 0; i < nu; ++i) {
            const int u = i < 3 ? i * 256 + ri * 8 + w : 768 + ri;
            const size_t so = (size_t)(u / 28) * 16 * 3584 + (u % 28) * 128;
            const unsigned d_ = lds0 + w * 16384 + i * 4096;
            glds(vo, wg + so, d_); glds(vo, wg + so + 8 * 3584, d_ + 1024); glds(vo, wu + so, d_ + 2048); glds(vo, wu + so + 8 * 3584, d_ + 3072);
        }
#pragma unroll 1
        for (int i = 0; i < nu; ++i) {
            const int u = i < 3 ? i * 256 + ri * 8 + w : 768 + ri;
            waitv<8>();
            lds_ptr sg = lds + w * 16384 + i * 4096 + g1 * 2048 + (4 * hf + q4) * 128 + p4 * 8;
#pragma unroll
            for (int e = 0; e < 4; ++e) st16_dev(U4(cat(trd(sg + e * 32), trd(sg + e * 32 + 1024))), rws, l16, WS_WGU + (unsigned)(((u % 28) * 4 + e) * 28 + u / 28) * 1024);
            FENCE();
        }
        tagv = epoch + 1;
    }
    TS(24);
    const int head = xcd * 2 + (loc & 1), rb = loc >> 1, qt = w & 3;
    const char* kvs = ws + WS_KV + (size_t)head * 262144 + w * 2048;
    const char* qs = ws + WS_QF + (size_t)(head * 64 + rb * 4 + qt) * 2048 + kh * 1024;
    unsigned kvd = lds0 + w * 2048, qd = lds0 + 147456 + qt * 2048 + kh * 1024;
    unsigned pf1 = (FL_KV + (lane >> 1) * 8 + (head * 28 + (lane & 1) * 27) / 64) * 128;
    PINS(kvs); PINS(qs); PINS(kvd); PINS(qd); PIN2(pf1, fl1);
    waitv_bar<0>();
    if (w == 0) set_flag(ws, fl1, tagv);
    const u64 tag = ((u64)(unsigned)__builtin_amdgcn_readfirstlane((unsigned)(tagv >> 32)) << 32 | (u64)(unsigned)__builtin_amdgcn_readfirstlane((unsigned)tagv));
    TS(4);

    const int drb = (xcd >> 1) * 4 + l7, dfg = (xcd & 1) * 7 + m7;
    bf16x8 wf[28];
    u32x2 xa, xb;
    f32x16 cs = {}, acc = {};
    const u32x4 one4 = {ones, ones, ones, ones};
    const unsigned hi_ = (unsigned)((tb * 2 + tt) * 28 + m7 * 4 + ct * 2 + kh), ofo = (unsigned)(tt * 32768 + kh * 16384 + ct * 4096);
    unsigned ho = WS_HF + hi_ * 1024 + l16, so = hf ? OOB : WS_HS + hi_ * 256 + r * 8;
    const char* os = ws + WS_OF + (size_t)tb * 65536 + ofo;
    unsigned od = lds0 + 65536 + ofo;
    unsigned pf2 = (FL_O + (tb >> 1) * 16 + kh * 8 + ct * 2 + ((lane >> 1) & 1) + (lane & 4)) * 128 + ((lane & 1) * 4 + (tb & 1) * 2 + tt) * 8;
    unsigned fo3 = (FL_H + tb * 8 + m7) * 128;
    {
        lds_ptr ap = opq(lds + kh * 8192 + l16), apB = opq(lds + 65536 + kh * 8192 + l16), apC = opq(lds + 131072 + kh * 8192 + l16);
        const unsigned lv = kh * 8192 + (l16 ^ (kh * 256));
        lds_ptr vp = opq(lds + lv), vpB = opq(lds + 65536 + lv), vpC = opq(lds + 131072 + lv);
#define AP(off) ((off) < 65536 ? ap + (off) : (off) < 131072 ? apB + ((off) - 65536) : apC + ((off) - 131072))
#define VP(off) ((off) < 65536 ? vp + (off) : (off) < 131072 ? vpB + ((off) - 65536) : vpC + ((off) - 131072))
#define KV_ISSUE(t) glds2_dev(l16, kvs + (t) * 16384, kvd + phys(t))
        unsigned wgo = WS_WGU + (unsigned)((ffn ? dfg : 0) * 8 + w) * 28672;
        const int ri = umin(xcd * 4 + loc - 28, 27);
        const char* wos = (ffn ? wo + m7 * 128 : wd + (ri % 7) * 128 + (size_t)(ri / 7) * 448 * 896) + (size_t)w * 56 * 896;
        unsigned wov = (unsigned)(row8 * 896) + swz;
        const rsrc_t rx = __builtin_amdgcn_make_buffer_rsrc((void*)x, 0, 2048 * 448 * 2, 0x00020000);
        unsigned xo_ = (unsigned)(((tb * 2 + tt) * 32 + r) * 896 + m7 * 128 + ct * 64 + kh * 32 + hf * 8);
        unsigned rto = (FL_RT + r) * 128;
        lds_ptr qp = opq(lds + 147456 + qt * 2048 + l16);
        PINS(wgo); PINS(wos); PIN2(wov, xo_); PIN(rto); PIN2(ho, so); PIN2(pf2, fo3);
        lds_ptr xw = opq(lds + 147456 + w * 2048 + l16), xr = opq(lds + 147456 + (w ^ 4) * 2048 + l16);
        lds_ptr lw = opq(lds + 58368 + w * 256 + lane * 4), lr = opq(lds + 58368 + qt * 256 + 128 + r * 4);
        lds_ptr bw = opq(lds + 60416 + w * 4), br = opq(lds + 60416 + (lane & 7) * 4);
        unsigned oo = WS_OF + (unsigned)((rb * 4 + qt) * 32 + head * 2 + kh) * 1024 + l16;
        unsigned fo2 = (FL_O + rb * 16 + head) * 128 + w * 8;
        PIN2(oo, fo2);
        if (w == HOP3_POLLER) poll_flag(ws, pf1, tag);
        BAR();
        TS(5);
        glds_dev(l16, qs, qd);
        KV_ISSUE(0); KV_ISSUE(1);
        asm volatile("s_barrier" ::: "memory");
        KV_ISSUE(2); KV_ISSUE(3);
        waitv_bar<4>();
        TS(6);
        const bf16x8 q0 = lds8(qp), q1 = lds8(qp + 1024);
        f32x16 o = {}, sa = {}, sb;
        bf16x8 k0 = lds8(ap + phys(0)), k1 = lds8(ap + phys(0) + 1024), v0 = lds8(vp + phys(0) + 2048), v1 = {};
        u32x4 p0, p1 = {};
        sa = MFMA(k0, q0, sa); sa = MFMA(k1, q1, sa);
        k0 = lds8(ap + phys(0) + 4096); k1 = lds8(ap + phys(0) + 5120);
        PIN(sa); SB();
        u64 rtf = 0;
#define EXP4(S, o4) _Pragma("unroll") for (int i_ = o4; i_ < o4 + 4; ++i_) S[i_] = __builtin_amdgcn_exp2f(S[i_]);
#define WLD(s) wf[s] = BF8(ld16_dev(rws, l16 + ((s) & 3) * 1024, wgo + ((s) >> 2) * 4096))
#define SIDE(t, g) do { \
            if ((t) == 0 && (g) == 0) KV_ISSUE(4); if ((t) == 0 && (g) == 2) KV_ISSUE(5); if ((t) == 1 && (g) == 0) KV_ISSUE(6); if ((t) == 1 && (g) == 2) KV_ISSUE(7); \
            if ((t) == 2 && (g) == 0) KV_ISSUE(8); if ((t) == 1 && (g) == 3) rtf = peek_flag(ws, rto); \
            if ((t) == 3 && (g) < 3) KV_ISSUE(9 + (g)); \
            if ((t) == 7 && (g) == 0) KV_ISSUE(12); if ((t) == 7 && (g) == 2) KV_ISSUE(13); if ((t) == 8 && (g) == 0) KV_ISSUE(14); if ((t) == 8 && (g) == 2) KV_ISSUE(15); \
            if ((t) >= 4 && (t) <= 6) WLD(((t) - 4) * 4 + (g)); \
            if (((t) == 7 || (t) == 8) && ((g) & 1)) WLD(12 + ((t) - 7) * 2 + ((g) >> 1)); \
            if ((t) >= 9 && (t) <= 11) WLD(((t) - 5) * 4 + (g)); \
            if ((t) == 12 || ((t) == 13 && (g) < 3)) glds(wov, wos + (((t) - 12) * 4 + (g)) * 8 * 896, lds0 + (w * 7 + ((t) - 12) * 4 + (g)) * 1024); \
            if ((t) == 13 && (g) == 3) { xa = ld8(rx, xo_, 0); xb = ld8(rx, xo_, 16); } \
            if ((t) == 14 && (g) == 1) { if (w >= 6) touch(lane * (w == 6 ? (WS_END / 64 & ~127u) : 28672), w == 6 ? ws : out, lds0 + 61440); } \
            if ((t) == 14 && (g) == 0) { if (tid < 32) *(LAS u32x4*)(lds + 57344 + tid * 16) = (u32x4){0, 0, 0, 0}; } \
        } while (0)
#define TOFF(i) (phys((i) >> 1) + ((i) & 1) * 4096)
#define TILE(i, SC, SN) do { \
            __builtin_amdgcn_s_setprio(((i) & 1) ? 1 : 3); \
            if ((i) < 31) { f32x16 z_ = {}; SN = MFMA(k0, q0, z_); PIN(SN); } if ((i) < 30) k0 = lds8(AP(TOFF((i) + 2))); SB(); \
            EXP4(SC, 0); PIN(SC); SB(); \
            o = MFMA(v1, BF8(p1), o); PIN(o); v1 = lds8(VP(TOFF(i) + 3072)); SIDE((i) >> 1, ((i) & 1) * 2); SB(); \
            EXP4(SC, 4); p0 = pack8(SC, 0); PIN(p0); SB(); \
            __builtin_amdgcn_s_setprio(((i) & 1) ? 0 : 2); \
            if ((i) < 31) { SN = MFMA(k1, q1, SN); PIN(SN); } if ((i) < 30) k1 = lds8(AP(TOFF((i) + 2) + 1024)); SB(); \
            EXP4(SC, 8); PIN(SC); SB(); \
            o = MFMA(v0, BF8(p0), o); PIN(o); if ((i) < 31) v0 = lds8(VP(TOFF((i) + 1) + 2048)); SIDE((i) >> 1, ((i) & 1) * 2 + 1); SB(); \
            EXP4(SC, 12); p1 = pack8(SC, 1); PIN(p1); SB(); } while (0)
#define STEP(t) do { TILE(2 * (t), sa, sb); TILE(2 * (t) + 1, sb, sa); } while (0)
        STEP(0);
        waitv_bar<4>();
        STEP(1); STEP(2);
        waitv_bar<3>();
        TS(21);
        if (__builtin_expect(!wave_all(rtf == tag), 0)) poll_flag(ws, rto, tag);
        FENCE();
        STEP(3); STEP(4); STEP(5); STEP(6);
        waitv_bar<12>();
        TS(22);
        STEP(7); STEP(8); STEP(9); STEP(10);
        waitv_bar<9>();
        TS(23);
        STEP(11); STEP(12); STEP(13); STEP(14); STEP(15);
        o = MFMA(v1, BF8(p1), o);
        TS(7);
        u32x4 ov;
        {
            *(LAS f32x4*)xw = (f32x4){o[8], o[9], o[10], o[11]}; *(LAS f32x4*)(xw + 1024) = (f32x4){o[12], o[13], o[14], o[15]};
            const float l = kh ? o[4] : o[12];
            *(LAS float*)lw = l;
            float sa_ = 0.f;
#pragma unroll
            for (int i = 0; i < 16; ++i) sa_ += __builtin_fabsf(o[i]);
            const bool ok = sa_ < 0x1p100f && (hf == 0 || l > 0x1p-80f);
            *(LAS unsigned*)bw = wave_all(ok) ? 0u : 1u;
            BAR();
            const f32x4 a = *(LAS f32x4*)xr, c = *(LAS f32x4*)(xr + 1024);
            const float inv = __builtin_amdgcn_rcpf(*(LAS float*)lr + *(LAS float*)(lr + 1024));
            const unsigned bad = *(LAS unsigned*)br;
            const float iz = (kh && hf) ? 0.f : inv;
            ov = (u32x4){cvtpk((o[0] + a[0]) * inv, (o[1] + a[1]) * inv), cvtpk((o[2] + a[2]) * inv, (o[3] + a[3]) * inv),
                         cvtpk((o[4] + c[0]) * iz, (o[5] + c[1]) * iz), cvtpk((o[6] + c[2]) * iz, (o[7] + c[3]) * iz)};
            if (__builtin_expect(wave_any(bad != 0), 0)) {
                f32x16 of = {};
                float m = -0x1p126f;
#pragma unroll 1
                for (int t = 0; t < 16; ++t) {
                    BAR();
                    glds2_dev(l16, kvs + t * 16384, lds0 + 65536 + w * 2048);
                    waitv_bar<0>();
#pragma unroll 1
                    for (int u = 0; u < 2; ++u) {
                        f32x16 s = {};
                        s = MFMA(lds8(apB + u * 4096), q0, s); s = MFMA(lds8(apB + u * 4096 + 1024), q1, s);
                        float mx = s[0];
#pragma unroll
                        for (int i = 1; i < 16; ++i) mx = __builtin_fmaxf(mx, s[i]);
                        mx = __builtin_fmaxf(swap_max(mx), m);
                        const float alpha = __builtin_amdgcn_exp2f(m - mx);
                        m = mx;
#pragma unroll
                        for (int i = 0; i < 16; ++i) { s[i] = __builtin_amdgcn_exp2f(s[i] - mx); of[i] *= alpha; }
                        of = MFMA(lds8(vpB + u * 4096 + 2048), BF8(pack8(s, 0)), of);
                        of = MFMA(lds8(vpB + u * 4096 + 3072), BF8(pack8(s, 1)), of);
                    }
                }
                *(LAS f32x4*)xw = (f32x4){of[8], of[9], of[10], of[11]}; *(LAS f32x4*)(xw + 1024) = (f32x4){of[12], of[13], of[14], of[15]};
                *(LAS float*)lw = hf ? (kh ? of[4] : of[12]) : m;
                BAR();
                const f32x4 a2 = *(LAS f32x4*)xr, c2 = *(LAS f32x4*)(xr + 1024);
                const float m0 = *(LAS float*)(lr - 128), m1 = *(LAS float*)(lr + 1024 - 128), mm = __builtin_fmaxf(m0, m1);
                const float e0 = __builtin_amdgcn_exp2f(m0 - mm), e1 = __builtin_amdgcn_exp2f(m1 - mm);
                const float iv = __builtin_amdgcn_rcpf(*(LAS float*)lr * e0 + *(LAS float*)(lr + 1024) * e1);
                const float im = (kh ? e1 : e0) * iv, io = (kh ? e0 : e1) * iv;
                const bool z = kh && hf;
                ov = (u32x4){cvtpk(of[0] * im + a2[0] * io, of[1] * im + a2[1] * io), cvtpk(of[2] * im + a2[2] * io, of[3] * im + a2[3] * io),
                             z ? 0u : cvtpk(of[4] * im + c2[0] * io, of[5] * im + c2[1] * io), z ? 0u : cvtpk(of[6] * im + c2[2] * io, of[7] * im + c2[3] * io)};
            }
        }
        st16_dev(ov, rws, oo, 0);
        TS(25);
        FENCE();
        PIN2(xa, xb);
#pragma unroll
        for (int s = 27; s >= 0; --s) PIN(wf[s]);
#pragma unroll
        for (int s = 0; s < 8; ++s) cs = MFMA(wf[s], BF8(one4), cs);
        acc[0] = bflo(xa[0]); acc[1] = bfhi(xa[0]); acc[2] = bflo(xa[1]); acc[3] = bfhi(xa[1]); acc[4] = bflo(xb[0]); acc[5] = bfhi(xb[0]); acc[6] = bflo(xb[1]); acc[7] = bfhi(xb[1]);
        PIN(acc); PIN2(ho, so); PIN2(pf2, fo3); PINS(os); PINS(od); PIN(cs);
        waitv<0>();
        set_flag(ws, fo2, tagv);
        BAR();
        TS(8);
    }
    if (__builtin_expect(!ffn, 0)) {
        const int ri = xcd * 4 + loc - 28;
        if (ri < 28) {
#pragma unroll 1
            for (int u = w; u < 28; u += 8) {
                lds_ptr sd = lds + u * 2048 + (4 * hf + q4) * 128 + g1 * 32 + p4 * 8;
#pragma unroll
                for (int e = 0; e < 2; ++e) { lds_ptr p_ = sd + ((e ^ (q4 >> 1)) * 64);
                    st16_dev(U4(cat(trd(p_), trd(p_ + 1024))), rws, l16, WS_WD + (unsigned)(((ri % 7) * 112 + (ri / 7) * 28 + u) * 2 + e) * 1024); }
            }
        }
        waitv_bar<0>();
        if (w == 0) set_flag(ws, (FL_RD + xcd * 4 + loc - 28) * 128, tagv);
        return;
    }

    const int erb = tb >> 1;
    const char* hs = ws + WS_HF + (size_t)drb * 114688 + w * 1024;
    const char* hss = ws + WS_HS + (size_t)((drb * 4 + (w & 3)) * 28 + kh * 14) * 256;
    unsigned go = WS_GF + (unsigned)(drb * 4 * 112 + dfg * 8 + w) * 1024 + l16;
    unsigned pf3, fo4 = (FL_G + drb * 64 + dfg) * 128 + w * 8;
    {
        lds_ptr wb = lds + kh * (8 * 28 * 128) + (4 * hf + q4) * 128 + ((ct ^ (q4 >> 1)) * 64) + (g1 ^ kh) * 32 + p4 * 8;
        bf16x8 wr[16];
#pragma unroll
        for (int s = 0; s < 12; ++s) { const int k0 = ((s >> 1) * 28 + (s & 1) * 16) * 128; wr[s] = cat(trd(wb + k0), trd(wb + k0 + 1024)); }
        lds_ptr cw = opq(lds + 131072 + w * 2048 + l16), cr = opq(lds + 131072 + (w ^ 4) * 2048 + l16);
        poll_flag(ws, pf2, tag);
        TS(9);
        glds4_dev(l16, os, od);
        asm volatile("s_barrier" ::: "memory");
        glds4_dev(l16, os + 8192, od + 8192);
        lds_ptr ob = opq(lds + 65536 + tt * 32768 + kh * 16384 + l16);
        bf16x8 of[8];
        waitv_bar<4>();
        TS(26);
#pragma unroll
        for (int s = 0; s < 8; ++s) of[s] = lds8(ob + s * 1024);
        SB();
#pragma unroll
        for (int s = 0; s < 8; ++s) acc = MFMA(wr[s], of[s], acc);
        PIN(acc); SB();
#pragma unroll
        for (int s = 12; s < 16; ++s) { const int k0 = ((s >> 1) * 28 + (s & 1) * 16) * 128; wr[s] = cat(trd(wb + k0), trd(wb + k0 + 1024)); }
        waitv_bar<0>();
#pragma unroll
        for (int s = 0; s < 8; ++s) of[s] = lds8(ob + (8 + s) * 1024);
        SB();
#pragma unroll
        for (int s = 0; s < 8; ++s) acc = MFMA(wr[8 + s], of[s], acc);
        TS(10);
        *(LAS f32x4*)cw = (f32x4){acc[8], acc[9], acc[10], acc[11]}; *(LAS f32x4*)(cw + 1024) = (f32x4){acc[12], acc[13], acc[14], acc[15]};
        BAR();
        const f32x4 a = *(LAS f32x4*)cr + (f32x4){acc[0], acc[1], acc[2], acc[3]}, b = *(LAS f32x4*)(cr + 1024) + (f32x4){acc[4], acc[5], acc[6], acc[7]};
        const u32x4 hv = {cvtpk(a[0], a[1]), cvtpk(a[2], a[3]), cvtpk(b[0], b[1]), cvtpk(b[2], b[3])};
        st16_dev(hv, rws, ho, 0);
        float s1 = 0.f, s2 = 0.f;
        acc_stats(hv, s1, s2);
        s1 = swap_add(s1); s2 = swap_add(s2);
        st8_dev((u32x2){f2u(s1), f2u(s2)}, rws, so, 0);
        FENCE();
#pragma unroll
        for (int s = 8; s < 14; ++s) cs = MFMA(wf[s], BF8(one4), cs);
        pf3 = (FL_H + drb * 16 + (lane & 8) + umin(lane & 7, 6)) * 128;
        PIN(go); PIN2(pf3, fo4); PINS(hs); PINS(hss); PIN(cs);
        waitv_bar<0>();
        if (w == 0) set_flag(ws, fo3, tagv);
        if (bid == 0 && w == 0) set_flag(ws, FL_EPOCH * 128, tagv);
        TS(11);
    }

    const unsigned wdo = WS_WD + (unsigned)(m7 * 112 + w * 14) * 2048;
    bf16x8 wdf[28];
    bf16x8 gf[8];
    const int ta = erb * 4 + (tb & 1);
    const unsigned gxo = WS_GF + (unsigned)(ta * 112 + w * 14) * 1024;
    unsigned fo4e;
    u64 gfl = 0;
    const unsigned gks = w * 14 + umin(lane & 15, 13), gfi = (FL_G + (erb * 4 + (tb & 1)) * 16 + (gks >> 3)) * 128 + (gks & 7) * 8;
    {
#pragma unroll
        for (int s = 14; s < 28; ++s) cs = MFMA(wf[s], BF8(one4), cs);
        lds_ptr csp = opq(lds + 161792 + w * 128 + hf * 64);
        if (r == 0) {
#pragma unroll
            for (int g = 0; g < 4; ++g) *(LAS f32x4*)(csp + g * 16) = (f32x4){cs[4 * g], cs[4 * g + 1], cs[4 * g + 2], cs[4 * g + 3]};
        }
        const unsigned hd = lds0 + w * 1024;
        const char* wds = ws + wdo;
        lds_ptr hb = opq(lds + l16), hb2 = opq(lds + 57344 + l16);
        lds_ptr wl = opq(lds + 114688 + w * 5120 + l16), wl2 = opq(lds + w * 7168 + l16);
        LAS f32x2* stp = (LAS f32x2*)opq(lds + 159744 + r * 8);
        if (w == HOP3_POLLER) poll_flag(ws, pf3, tag);
        BAR();
        TS(12);
#define H_PIECE(j) glds_dev(l16, hs + (j) * 8192, hd + (j) * 8192)
#define WDLD(s) wdf[s] = BF8(ld16_dev(rws, l16 + ((s) & 3) * 1024, wdo + ((s) >> 2) * 4096))
#define HB(n) ((n) < 56 ? hb + (n) * 1024 : hb2 + ((n) - 56) * 1024)
        H_PIECE(0);
        asm volatile("s_barrier" ::: "memory");
        H_PIECE(1); H_PIECE(2); H_PIECE(3);
        glds4_dev(l16, hss, lds0 + 114688 + w * 5120);
        constexpr int PF = 5;
        bf16x8 bf[PF + 1];
        f32x16 acc[2];
        f32x2 sp[7], sv0, sv1;
        f32x4 c4[4];
        float mean = 0.f, r2 = 0.f, hp = 0.f;
        u32x4 hv;
        u64 rdf = 0;
        waitv_bar<7>();
        TS(27);
#pragma unroll
        for (int n = 0; n < PF; ++n) bf[n] = lds8(HB(n));
        SB();
        sfor<0, 125>([&](auto N_) __attribute__((always_inline)) {
            constexpr int n = decltype(N_)::v, p = n / 28, s = n % 28;
            if constexpr (n == 3) waitv_bar<5>();
            if constexpr (n == 27) { waitv_bar<3>(); TS(13); }
            if constexpr (n == 64) { waitv_bar<10>(); set_flag(ws, fo4, tagv); TS(14); }
            if constexpr (n == 84) { waitv<7>(); set_flag(ws, fo4 + 2048, tagv); }
            if constexpr (n == 106) { waitv<4>(); set_flag(ws, fo4 + 4096, tagv); }
            if constexpr (n == 39) { if (__builtin_expect(!wave_all(rdf == tag), 0)) poll_flag(ws, (FL_RD + r) * 128, tag); FENCE(); }
            if constexpr (d_prio(n) != d_prio(n - 1)) __builtin_amdgcn_s_setprio(d_prio(n));
            if constexpr (n < 112) { if constexpr (s == 0) { f32x16 z = {}; acc[p & 1] = MFMA(wf[s], bf[n % (PF + 1)], z); } else acc[p & 1] = MFMA(wf[s], bf[n % (PF + 1)], acc[p & 1]); PIN(acc[p & 1]); }
            SB();
            if constexpr (n + PF < 112) bf[(n + PF) % (PF + 1)] = lds8(HB(n + PF));
            if constexpr (n % 3 == 2 && n < 30) H_PIECE(4 + n / 3);
            if constexpr (n == 16) { waitv<5>();
#pragma unroll
                for (int j = 0; j < 7; ++j) sp[j] = *(LAS f32x2*)(lds + 114688 + w * 5120 + (hf * 7 + j) * 256 + r * 8);
            }
            if constexpr (n == 20) { const f32x2 t = ((sp[0] + sp[1]) + (sp[2] + sp[3])) + ((sp[4] + sp[5]) + sp[6]); stp[w * 32] = (f32x2){swap_add(t[0]), swap_add(t[1])}; }
            if constexpr (n >= 39 && n < 48 && n % 2 == 1) glds_dev(l16, wds + (n - 39) / 2 * 1024, lds0 + 114688 + w * 5120 + (n - 39) / 2 * 1024);
            if constexpr (n >= 48 && n < 64 && n % 3 == 0) WDLD(12 + (n - 48) / 3);
            if constexpr (n >= 70 && n < 84 && n % 2 == 0) glds_dev(l16, wds + (5 + (n - 70) / 2) * 1024, lds0 + w * 7168 + (n - 70) / 2 * 1024);
            if constexpr (n >= 85 && n < 105 && n % 2 == 1) WDLD(18 + (n - 85) / 2);
            if constexpr (n == 107) gfl = peek_flag(ws, gfi);
            if constexpr (n == 30) rdf = peek_flag(ws, (FL_RD + r) * 128);
            if constexpr (p >= 1) {
                const f32x16& a = acc[(p - 1) & 1];
                if constexpr (n == 100) { sv0 = stp[3 * 32]; sv1 = stp[7 * 32]; }
                if constexpr (s == 0 && p < 4) { sv0 = stp[(p - 1) * 32]; sv1 = stp[(p + 3) * 32];
#pragma unroll
                    for (int g = 0; g < 4; ++g) c4[g] = *(LAS f32x4*)(csp + g * 16); }
                if constexpr ((s == 3 && p < 4) || n == 104) { const f32x2 sv = sv0 + sv1; mean = sv[0] * (1.f / D); r2 = __builtin_amdgcn_rcpf(__builtin_fmaxf(sv[1] * (1.f / D) - mean * mean, 0.f) + EPS); PIN2(mean, r2); }
                if constexpr (s >= 4 && s < 12) { constexpr int i = s - 4;
                    const float h0 = __builtin_fmaxf(a[i] - mean * c4[i >> 2][i & 3], 0.f) * r2 * (a[8 + i] - mean * c4[2 + (i >> 2)][i & 3]);
                    if constexpr (i & 1) { hv[i >> 1] = cvtpk(hp, h0); PIN(hv); } else { hp = h0; PIN(hp); } }
                if constexpr (s == 12) { st16_dev(hv, rws, go + (p - 1) * 114688, 0); FENCE(); }
            }
            SB();
        });
        TS(15);
        fo4e = fo4 + 6144;
        if (__builtin_expect(!wave_all(gfl == tag), 0)) poll_flag(ws, gfi, tag);
        FENCE();
#pragma unroll
        for (int s = 0; s < 8; ++s) { gf[s] = BF8(ld16_dev(rws, l16 + (s & 3) * 1024, gxo + (s >> 2) * 4096)); SB(); }
        FENCE();
#pragma unroll
        for (int s = 0; s < 12; ++s) wdf[s] = lds8(s < 5 ? wl + s * 1024 : wl2 + (s - 5) * 1024);
    }

    {
        const int etok = tid >> 4, eblk = tid & 15;
        const unsigned hro = (unsigned)((((eblk >> 2) & 3) * 64 + (eblk & 1) * 32 + etok) * 16 + ((eblk >> 1) & 1) * 8) + WS_HF + (unsigned)(ta * 28 + m7 * 4) * 1024;
        u32x2 hres[2];
        GAS u64* op = (GAS u64*)(out + (size_t)(ta * 32 + etok) * 896 + m7 * 128 + eblk * 8);
        lds_ptr rp = opq(lds + w * 8448 + hf * 528 + r * 16), rq = opq(lds + eblk * 528 + etok * 16);
        lds_ptr rp2 = opq(lds + 67584 + w * 8448 + hf * 528 + r * 16), rq2 = opq(lds + 67584 + eblk * 528 + etok * 16);
#define G_LD(hh, s) gf[(s) & 7] = BF8(ld16_dev(rws, l16 + ((s) & 3) * 1024, gxo + (hh) * 229376 + ((s) >> 2) * 4096))
#define G_MMA(hh, RP) do { f32x16 c0 = {}, c1 = {}; \
            _Pragma("unroll") for (int s = 0; s < 14; ++s) { c0 = MFMA(wdf[2 * s], gf[s & 7], c0); PIN(c0); c1 = MFMA(wdf[2 * s + 1], gf[s & 7], c1); PIN(c1); SB(); if (s < 6) G_LD(hh, s + 8); SB(); } \
            _Pragma("unroll") for (int g = 0; g < 4; ++g) *(LAS f32x4*)(RP + g * 1056) = (f32x4){c0[4 * g], c0[4 * g + 1], c0[4 * g + 2], c0[4 * g + 3]}; \
            _Pragma("unroll") for (int g = 0; g < 4; ++g) *(LAS f32x4*)(RP + 4224 + g * 1056) = (f32x4){c1[4 * g], c1[4 * g + 1], c1[4 * g + 2], c1[4 * g + 3]}; } while (0)
#define G_OUT(hh, RQ) do { f32x4 pa[8]; \
            _Pragma("unroll") for (int ww = 0; ww < 8; ++ww) pa[ww] = *(LAS f32x4*)(RQ + ww * 8448); SB(); \
            const f32x4 a = ((pa[0] + pa[1]) + (pa[2] + pa[3])) + ((pa[4] + pa[5]) + (pa[6] + pa[7])); \
            const u32x2 ov = {cvtpk(a[0] + bflo(hres[hh][0]), a[1] + bfhi(hres[hh][0])), cvtpk(a[2] + bflo(hres[hh][1]), a[3] + bfhi(hres[hh][1]))}; \
            __hip_atomic_store(op + (hh) * (64 * 896 / 8), __builtin_bit_cast(u64, ov), __ATOMIC_RELAXED, __HIP_MEMORY_SCOPE_AGENT); } while (0)
        waitv<8>();
        set_flag(ws, fo4e, tagv);
        BAR();
        TS(16);
        hres[0] = ld8_dev(rws, hro, 0); hres[1] = ld8_dev(rws, hro, 57344);
        FENCE(); SB();
        G_MMA(0, rp);
        PIN2(hres[0], hres[1]);
        TS(17);
        poll_flag(ws, gfi + 4096, tag);
        TS(18);
#pragma unroll
        for (int s = 0; s < 8; ++s) { G_LD(1, s); SB(); }
        BAR();
        G_OUT(0, rq);
        FENCE(); SB();
        G_MMA(1, rp2);
        TS(19);
        BAR();
        G_OUT(1, rq2);
    }
    TS(20);
}

extern "C" void kernel_launch(void* const* d_in, const int* in_sizes, int n_in, void* d_out, int out_size, void* d_ws, size_t ws_size, hipStream_t stream) {
    (void)hipFuncSetAttribute((const void*)block_fwd, hipFuncAttributeMaxDynamicSharedMemorySize, LDS_BYTES);
    block_fwd<<<256, 512, LDS_BYTES, stream>>>((const char*)d_in[0], (const char*)d_in[1], (const char*)d_in[2], (const char*)d_in[4], (const char*)d_in[5], (const char*)d_in[6], (char*)d_out, (char*)d_ws + WS_OFF);
}
```

```cpp
#define PROBE 0
#include <hip/hip_runtime.h>
#include <hip/hip_bf16.h>
#include <stdint.h>

typedef __attribute__((ext_vector_type(8))) short bf16x8;
typedef __attribute__((ext_vector_type(4))) short s16x4;
typedef __attribute__((ext_vector_type(2))) __bf16 bf16x2;
typedef __attribute__((ext_vector_type(16))) float f32x16;
typedef __attribute__((ext_vector_type(4))) float f32x4;
typedef __attribute__((ext_vector_type(2))) float f32x2;
typedef __attribute__((ext_vector_type(4))) unsigned u32x4;
typedef __attribute__((ext_vector_type(2))) unsigned u32x2;
typedef short v4i16_t __attribute__((ext_vector_type(4)));
typedef unsigned long long u64;
#define LAS __attribute__((address_space(3)))
#define GAS __attribute__((address_space(1)))
typedef LAS char* lds_ptr;
#if defined(__HIP_DEVICE_COMPILE__)
#define NOPK __attribute__((target("no-packed-fp32-ops")))
#else
#define NOPK
#endif
#define DI __device__ __forceinline__
#define MFMA(a, b, c) __builtin_amdgcn_mfma_f32_32x32x16_bf16(a, b, c, 0, 0, 0)
#define MFMA16(a, b, c) __builtin_amdgcn_mfma_f32_16x16x32_bf16(a, b, c, 0, 0, 0)
#define BF8(x) __builtin_bit_cast(bf16x8, x)
#define U4(x) __builtin_bit_cast(u32x4, x)

constexpr int D = 448;
constexpr float EPS = 1e-5f;
constexpr unsigned WS_FLAG = 0;
constexpr unsigned FL_EPOCH = 0, FL_KV = 1, FL_RT = FL_KV + 256, FL_RD = FL_RT + 32, FL_O = FL_RD + 32, FL_H = FL_O + 256, FL_G = FL_H + 256;
constexpr unsigned WS_KV = 1u << 20;
constexpr unsigned WS_QF = WS_KV + 4194304;
constexpr unsigned WS_OF = WS_QF + 2097152;
constexpr unsigned WS_HF = WS_OF + 2097152;
constexpr unsigned WS_GF = WS_HF + 64 * 28 * 1024;
constexpr unsigned WS_WGU = WS_GF + 64 * 112 * 1024;
constexpr unsigned WS_WD = WS_WGU + 112 * 28 * 1024;
constexpr unsigned WS_HS = WS_WD + 7 * 112 * 2 * 1024;
constexpr unsigned WS_END = WS_HS + 64 * 28 * 256 + 1024;
constexpr unsigned OOB = 0x40000000u;
constexpr int LDS_BYTES = 163840;

DI float u2f(unsigned u) { return __builtin_bit_cast(float, u); }
DI unsigned f2u(float f) { return __builtin_bit_cast(unsigned, f); }
DI unsigned umin(unsigned a, unsigned b) { return a < b ? a : b; }
DI bool wave_any(bool p) { return __builtin_amdgcn_ballot_w64(p) != 0; }
DI bool wave_all(bool p) { return __builtin_amdgcn_ballot_w64(!p) == 0; }
DI unsigned cvtpk(float lo, float hi) { return __builtin_bit_cast(unsigned, __builtin_convertvector((f32x2){lo, hi}, bf16x2)); }
DI float bflo(unsigned u) { return u2f(u << 16); }
DI float bfhi(unsigned u) { return u2f(u & 0xffff0000u); }
DI void glds(unsigned voff, const void* sbase, unsigned dst) {
    asm volatile("s_mov_b32 m0, %2\n\ts_nop 0\n\tglobal_load_lds_dwordx4 %0, %1" :: "v"(voff), "s"(sbase), "s"(dst) : "memory");
}
DI void glds_dev(unsigned voff, const void* sbase, unsigned dst) {
    asm volatile("s_mov_b32 m0, %2\n\ts_nop 0\n\tglobal_load_lds_dwordx4 %0, %1 sc1" :: "v"(voff), "s"(sbase), "s"(dst) : "memory");
}
DI void glds2_dev(unsigned voff, const void* sbase, unsigned dst) {
    asm volatile("s_mov_b32 m0, %2\n\ts_nop 0\n\tglobal_load_lds_dwordx4 %0, %1 sc1\n\tglobal_load_lds_dwordx4 %0, %1 offset:1024 sc1" :: "v"(voff), "s"(sbase), "s"(dst) : "memory");
}
DI void glds4_dev(unsigned voff, const void* sbase, unsigned dst) {
    asm volatile("s_mov_b32 m0, %2\n\ts_nop 0\n\tglobal_load_lds_dwordx4 %0, %1 sc1\n\tglobal_load_lds_dwordx4 %0, %1 offset:1024 sc1\n\t"
                 "global_load_lds_dwordx4 %0, %1 offset:2048 sc1\n\tglobal_load_lds_dwordx4 %0, %1 offset:3072 sc1" :: "v"(voff), "s"(sbase), "s"(dst) : "memory");
}
DI s16x4 trd(lds_ptr p) { return __builtin_bit_cast(s16x4, __builtin_amdgcn_ds_read_tr16_b64_v4i16((LAS v4i16_t*)p)); }
DI bf16x8 cat(s16x4 a, s16x4 b) { return __builtin_shufflevector(a, b, 0, 1, 2, 3, 4, 5, 6, 7); }
DI bf16x8 lds8(lds_ptr p) { return *(LAS bf16x8*)p; }
DI float swap_add(float v) { auto r = __builtin_amdgcn_permlane32_swap(f2u(v), f2u(v), false, false); return u2f(r[0]) + u2f(r[1]); }
DI float swap_max(float v) { auto r = __builtin_amdgcn_permlane32_swap(f2u(v), f2u(v), false, false); return __builtin_fmaxf(u2f(r[0]), u2f(r[1])); }
DI u32x4 pack8(const f32x16& a, int s) { return (u32x4){cvtpk(a[8 * s], a[8 * s + 1]), cvtpk(a[8 * s + 2], a[8 * s + 3]), cvtpk(a[8 * s + 4], a[8 * s + 5]), cvtpk(a[8 * s + 6], a[8 * s + 7])}; }
DI float dot2(unsigned a, unsigned b, float c) { return __builtin_amdgcn_fdot2_f32_bf16(__builtin_bit_cast(bf16x2, a), __builtin_bit_cast(bf16x2, b), c, false); }
DI void acc_stats(u32x4 u, float& s1, float& s2) {
#pragma unroll
    for (int i = 0; i < 4; ++i) { const float a = bflo(u[i]), b = bfhi(u[i]); s1 += a; s2 = __builtin_fmaf(a, a, s2); s1 += b; s2 = __builtin_fmaf(b, b, s2); }
}
DI void acc_stats2(unsigned u0, unsigned u1, f32x4& s) {
    const float a = bflo(u0), b = bfhi(u0), c = bflo(u1), d = bfhi(u1);
    s[0] += a; s[1] += b; s[2] = __builtin_fmaf(a, a, s[2]); s[3] = __builtin_fmaf(b, b, s[3]); s[0] += c; s[1] += d; s[2] = __builtin_fmaf(c, c, s[2]); s[3] = __builtin_fmaf(d, d, s[3]);
}
DI float sum8(float v) {
    v += u2f(__builtin_amdgcn_update_dpp(0u, f2u(v), 0xB1, 0xf, 0xf, true));
    v += u2f(__builtin_amdgcn_update_dpp(0u, f2u(v), 0x4E, 0xf, 0xf, true));
    v += u2f(__builtin_amdgcn_update_dpp(0u, f2u(v), 0x141, 0xf, 0xf, true));
    return v;
}
DI u32x4 shift8(u32x4 x, float c) {
    u32x4 o; float t0, t1, t2, t3, t4, t5, t6, t7;
    asm("v_dot2_f32_bf16 %4, %12, %16, %18\n\tv_dot2_f32_bf16 %5, %12, %17, %18\n\tv_dot2_f32_bf16 %6, %13, %16, %18\n\tv_dot2_f32_bf16 %7, %13, %17, %18\n\t"
        "v_dot2_f32_bf16 %8, %14, %16, %18\n\tv_dot2_f32_bf16 %9, %14, %17, %18\n\tv_dot2_f32_bf16 %10, %15, %16, %18\n\tv_dot2_f32_bf16 %11, %15, %17, %18\n\t"
        "v_cvt_pk_bf16_f32 %0, %4, %5\n\tv_cvt_pk_bf16_f32 %1, %6, %7\n\tv_cvt_pk_bf16_f32 %2, %8, %9\n\tv_cvt_pk_bf16_f32 %3, %10, %11"
        : "=&v"(o[0]), "=&v"(o[1]), "=&v"(o[2]), "=&v"(o[3]), "=&v"(t0), "=&v"(t1), "=&v"(t2), "=&v"(t3), "=&v"(t4), "=&v"(t5), "=&v"(t6), "=&v"(t7)
        : "v"(x[0]), "v"(x[1]), "v"(x[2]), "v"(x[3]), "s"(0x00003f80u), "s"(0x3f800000u), "v"(c));
    return o;
}
template <int N> struct IC { static constexpr int v = N; };
template <int B, int E, class F> DI void sfor(F&& f) { if constexpr (B < E) { f(IC<B>{}); sfor<B + 1, E>(f); } }
template <int N> DI void waitv() { asm volatile("s_waitcnt vmcnt(%0)" :: "i"(N) : "memory"); }
template <int N> DI void waitv_bar() { asm volatile("s_waitcnt vmcnt(%0) lgkmcnt(0)\n\ts_barrier" :: "i"(N) : "memory"); }
template <int N> DI void waitv_barx() { asm volatile("s_waitcnt vmcnt(%0)\n\ts_barrier" :: "i"(N) : "memory"); }
DI void touch(unsigned voff, const void* sbase, unsigned junk) {
    asm volatile("s_mov_b32 m0, %2\n\ts_nop 0\n\tglobal_load_lds_dword %0, %1 sc1" :: "v"(voff), "s"(sbase), "s"(junk) : "memory");
}
#define BAR() asm volatile("s_waitcnt lgkmcnt(0)\n\ts_barrier" ::: "memory")
#define FENCE() asm volatile("" ::: "memory")
typedef __amdgpu_buffer_rsrc_t rsrc_t;
DI u32x4 ld16(rsrc_t rs, unsigned voff, unsigned soff) { return U4(__builtin_amdgcn_raw_buffer_load_b128(rs, voff, soff, 0)); }
DI u32x4 ld16_dev(rsrc_t rs, unsigned voff, unsigned soff) { return U4(__builtin_amdgcn_raw_buffer_load_b128(rs, voff, soff, 16)); }
DI u32x2 ld8(rsrc_t rs, unsigned voff, unsigned soff) { return __builtin_bit_cast(u32x2, __builtin_amdgcn_raw_buffer_load_b64(rs, voff, soff, 0)); }
DI u32x2 ld8_dev(rsrc_t rs, unsigned voff, unsigned soff) { return __builtin_bit_cast(u32x2, __builtin_amdgcn_raw_buffer_load_b64(rs, voff, soff, 16)); }
DI void st16_dev(u32x4 v, rsrc_t rs, unsigned voff, unsigned soff) { __builtin_amdgcn_raw_buffer_store_b128(v, rs, voff + soff, 0, 16); }
DI void st8_dev(u32x2 v, rsrc_t rs, unsigned voff, unsigned soff) { __builtin_amdgcn_raw_buffer_store_b64(v, rs, voff, soff, 16); }
#define POLL_SLEEP 1
DI void set_flag(char* ws, unsigned fo, u64 tagv) {
    asm volatile("s_mov_b64 exec, 1\n\tglobal_store_dwordx2 %0, %1, %2 sc1\n\ts_mov_b64 exec, -1" :: "v"(fo), "v"(tagv), "s"(ws) : "memory");
}
DI u64 peek_flag(char* ws, unsigned fo) { return __hip_atomic_load((GAS u64*)(ws + fo), __ATOMIC_RELAXED, __HIP_MEMORY_SCOPE_AGENT); }
DI void poll_flag(char* ws, unsigned fo, u64 tag) {
#pragma unroll 1
    for (int spin = 0; spin < (1 << 16); ++spin) {
        if (wave_all(peek_flag(ws, fo) == tag)) break;
        __builtin_amdgcn_s_sleep(POLL_SLEEP);
    }
    FENCE();
}
#define SB() __builtin_amdgcn_sched_barrier(0)
#define PINS(a) asm volatile("" : "+s"(a))
#define PIN(a) asm volatile("" : "+v"(a))
#define PIN2(a, b) asm volatile("" : "+v"(a), "+v"(b))
DI lds_ptr opq(lds_ptr p) { unsigned u = (unsigned)(uintptr_t)p; asm volatile("" : "+v"(u)); return (lds_ptr)(uintptr_t)u; }
#define TS(i) do { if ((i) == 0) pt0 = __builtin_amdgcn_s_memrealtime(); if ((i) == PROBE) pt1 = __builtin_amdgcn_s_memrealtime(); \
    if ((i) == 99) { const u64 e_ = __builtin_amdgcn_s_memrealtime() + (pt1 - pt0); while (__builtin_amdgcn_s_memrealtime() < e_) __builtin_amdgcn_s_sleep(1); } } while (0)

DI constexpr int wd_slot(int s) { return (s * 17 + 27) / 28 != ((s + 1) * 17 + 27) / 28 ? (s * 17 + 27) / 28 : -1; }
#define HOP3_POLLER 1
DI constexpr int d_prio(int n) { return n < 0 ? -1 : n < 27 ? 3 - n / 7 : n < 64 ? 3 - (n - 27) / 10 : n < 84 ? 3 - (n - 64) / 5 : n < 112 ? 3 - (n - 84) / 7 : 0; }
DI constexpr int phys(int t) { return ((t % 9) + 2) % 9 * 16384; }

__global__ NOPK __launch_bounds__(512, 2) void block_fwd(const char* x, const char* wqkv, const char* wo, const char* wg, const char* wu, const char* wd, char* out, char* ws) {
    extern __shared__ __attribute__((aligned(16))) char smem[];
    lds_ptr lds = (lds_ptr)smem;
    const int tid = __builtin_amdgcn_workitem_id_x(), lane = tid & 63, w = __builtin_amdgcn_readfirstlane(tid >> 6);
    const int bid = __builtin_amdgcn_workgroup_id_x(), xcd = bid & 7, loc = bid >> 3;
    const int r = lane & 31, hf = lane >> 5, q4 = (lane & 15) >> 2, g1 = (lane >> 4) & 1, p4 = lane & 3;
    const unsigned lds0 = (unsigned)(uintptr_t)smem;
    const unsigned l16 = lane * 16;
    u64 pt0 = 0, pt1 = 0;
    TS(0);
    const int sp3 = (loc + 8) >> 5;
    const int atb = (xcd >> 1) * 8 + (loc & 7) + sp3 * (xcd & 1) * 4, acg = (loc >> 3) + (1 - sp3) * (xcd & 1) * 4;
    const int row8 = lane >> 3;
    const rsrc_t rws = __builtin_amdgcn_make_buffer_rsrc((void*)ws, 0, WS_END, 0x00020000);
    u64 epoch, tagv;
    unsigned fl1;
    const unsigned ones = 0x3f803f80u;
    const bool ffn = loc < 28;
    const int l7 = loc / 7, m7 = loc % 7;
    const int tb = xcd * 4 + l7;
    const int tt = w & 1, ct = (w >> 1) & 1, kh = w >> 2;
    const unsigned swz = (unsigned)(((lane & 7) ^ (((row8 >> 1) & 1) << 2)) * 16);

    if (__builtin_expect(ffn, 1)) {
        constexpr int A_W = 57344, A_WS = 24576;
        const int rot8 = ((loc & 7) << sp3) + sp3 * (xcd & 1), rot = rot8 & (7 - ((rot8 + 1) >> 3) * 7);
#define A_KC(c) (rot + (c) >= 7 ? rot + (c) - 7 : rot + (c))
        {
            const int xr = (w & 3) * 8 + row8;
            const unsigned xo = (unsigned)(row8 * 896 + (((lane & 7) ^ ((xr >> 1) & 7)) * 16));
            const char* xs = x + (size_t)(atb * 64 + w * 8) * 896;
            unsigned xd = lds0 + kh * 28672 + (w & 3) * 1024, xk = rot * 128;
#pragma unroll
            for (int c = 0; c < 7; ++c) { glds(xo, xs + xk, xd + c * 4096); xk = xk == 768 ? 0 : xk + 128; }
            SB();
        }
        const unsigned wvo = (unsigned)(row8 * 2688) + swz;
        const char* wsrc = wqkv + acg * 128 + (size_t)w * 8 * 2688;
        const unsigned wdst = lds0 + A_W + w * 1024;
#define A_PIECE(c, i) glds(wvo, wsrc + A_KC(c) * (64 * 2688) + (i) * 896, wdst + ((c) & 3) * A_WS + (i) * 8192)
#define A_GROUP(c) do { A_PIECE(c, 1); A_PIECE(c, 2); A_PIECE(c, 0); } while (0)
        epoch = peek_flag(ws, FL_EPOCH * 128); fl1 = (FL_KV + atb * 8 + acg) * 128;
        asm volatile("s_barrier" ::: "memory");
        A_GROUP(0);
        lds_ptr xb[4], wb1, wb1h;
#pragma unroll
        for (int j = 0; j < 4; ++j) xb[j] = lds + tt * 28672 + (r >> 3) * 1024 + (r & 7) * 128 + (((2 * j + hf) ^ ((r >> 1) & 7)) * 16);
        wb1 = opq(lds + A_W + (kh ? 0 : 8192) + (8 * hf + q4) * 128 + ((ct ^ (q4 >> 1)) * 64) + g1 * 32 + p4 * 8);
        wb1h = opq(wb1 + 2 * A_WS);
        unsigned go1[2], go2[2]; int gi[2];
#pragma unroll
        for (int p = 0; p < 2; ++p) {
            const int G = acg * 8 + ct * 4 + 2 * p + hf, hp = G / 7, i = G - hp * 7;
            const unsigned hs = kh ? 131072u : 262144u;
            const unsigned to = (kh ? WS_QF + (atb * 2 + tt) * 2048 : WS_KV + (atb * 2 + tt) * 4096) + r * 16;
            go1[p] = (unsigned)(2 * hp + (i > 3)) * hs + (unsigned)(i > 3 ? i - 3 : i) * 512 + to;
            go2[p] = i == 3 ? (unsigned)(2 * hp + 1) * hs + to : OOB;
            gi[p] = i;
        }
        const int vcol = acg * 64 + ct * 32 + r, vh = vcol / 28, vd = vcol - vh * 28;
        unsigned vo = WS_KV + (atb * 2 + tt) * 4096 + (unsigned)vh * 262144 + 2048 + hf * 512 + vd * 16;
        PIN2(go1[0], go1[1]); PIN2(go2[0], go2[1]); PIN(vo);
        lds_ptr xw = opq(lds + kh * 28672 + (w & 3) * 1024 + l16);
        waitv<3>();
        TS(28);
        u32x4 xv[7];
#pragma unroll
        for (int c = 0; c < 7; ++c) xv[c] = *(LAS u32x4*)(xw + c * 4096);
        SB();
        f32x4 sm = {};
#pragma unroll
        for (int c = 0; c < 7; ++c)
#pragma unroll
            for (int j = 0; j < 4; ++j) sm[j] = dot2(xv[c][j], ones, sm[j]);
        float nm = sum8((sm[0] + sm[1]) + (sm[2] + sm[3])) * (-1.f / D);
        PIN(nm);
#pragma unroll
        for (int c = 0; c < 7; ++c) { xv[c] = shift8(xv[c], nm); *(LAS u32x4*)(xw + c * 4096) = xv[c]; }
        A_GROUP(1);
        SB();
        sm = (f32x4){};
#pragma unroll
        for (int c = 0; c < 7; ++c)
#pragma unroll
            for (int j = 0; j < 4; ++j) sm[j] = dot2(xv[c][j], xv[c][j], sm[j]);
        LAS float* rst = (LAS float*)(lds + 155648);
        rst[w * 8 + row8] = __builtin_amdgcn_rsqf(sum8((sm[0] + sm[1]) + (sm[2] + sm[3])) * (1.f / D) + EPS);
        A_GROUP(2);
        TS(31);
        bf16x8 fx[4], f1[4], f2[4];
#define A_WAIT(c) do { if ((c) == 0) waitv_bar<6>(); else if ((c) == 1) waitv_barx<6>(); else if ((c) < 6) waitv_barx<3>(); else waitv_barx<0>(); \
                       if ((c) == 0) TS(1); if ((c) == 6) TS(2); } while (0)
#define A_WP(c, ks) ((((c) & 2) ? wb1h : wb1) + ((c) & 1) * A_WS + (ks) * 2048)
        if (w < 4) {
            f32x16 ak = {}, av = {};
            f32x4 rv[4];
            float rs = 0.f;
#pragma unroll
            for (int n = 0; n < 30; ++n) {
                const int c = n >> 2, ks = n & 3, km = (n - 2) & 3;
                if (n < 28 && ks == 0) A_WAIT(c);
                if (n >= 2) { ak = MFMA(f1[km], fx[km], ak); PIN(ak); } SB();
                if (n < 28) { fx[ks] = lds8(xb[ks] + c * 4096); f1[ks] = cat(trd(A_WP(c, ks)), trd(A_WP(c, ks) + 512)); } SB();
                if (n >= 2) { av = MFMA(fx[km], f2[km], av); PIN(av); } SB();
                if (n < 28) f2[ks] = cat(trd(A_WP(c, ks) + 8192), trd(A_WP(c, ks) + 8192 + 512));
                if (c == 0 && ks < 3) A_PIECE(3, ks); if (c >= 2 && c < 5 && ks < 3) A_PIECE(c + 2, ks);
                if (n == 21) {
                    rs = rst[tt * 32 + r];
#pragma unroll
                    for (int g = 0; g < 4; ++g) rv[g] = *(LAS f32x4*)(rst + tt * 32 + 8 * g + 4 * hf);
                }
                if (n == 24) { tagv = epoch + 1; PIN(tagv); }
                if (n == 24) {
                    const unsigned pv = vd == 0 ? ones : 0u, po = vd < 4 ? vo + 448 : OOB;
                    st16_dev((u32x4){pv, pv, pv, pv}, rws, po, 0); st16_dev((u32x4){pv, pv, pv, pv}, rws, po + 1024, 0);
                }
                SB();
            }
            TS(3);
#pragma unroll
            for (int i = 0; i < 16; ++i) { ak[i] *= rs; av[i] *= rv[i >> 2][i & 3]; }
#pragma unroll
            for (int p = 0; p < 2; ++p) {
                const u32x4 f = pack8(ak, p);
                const auto s0 = __builtin_amdgcn_permlane32_swap(f[0], f[2], false, false), s1 = __builtin_amdgcn_permlane32_swap(f[1], f[3], false, false);
                const u32x4 c = {s0[0], s1[0], s0[1], s1[1]};
                st16_dev(c, rws, go1[p], 0); st16_dev(c, rws, go2[p], 0);
            }
            st16_dev(pack8(av, 0), rws, vo, 0); st16_dev(pack8(av, 1), rws, vo + 1024, 0);
        } else {
            f32x16 aq = {};
            __builtin_amdgcn_s_setprio(1);
#pragma unroll
            for (int n = 0; n < 30; ++n) {
                const int c = n >> 2, ks = n & 3, km = (n - 2) & 3;
                if (n < 28 && ks == 0) A_WAIT(c);
                if (n == 24) { tagv = epoch + 1; PIN(tagv); }
                if (n >= 2) { aq = MFMA(f1[km], fx[km], aq); PIN(aq); } SB();
                if (n < 28) { fx[ks] = lds8(xb[ks] + c * 4096); f1[ks] = cat(trd(A_WP(c, ks)), trd(A_WP(c, ks) + 512)); }
                if (c == 0 && ks < 3) A_PIECE(3, ks); if (c >= 2 && c < 5 && ks < 3) A_PIECE(c + 2, ks);
                SB();
            }
            __builtin_amdgcn_s_setprio(0);
            const float QS = rst[tt * 32 + r] * (0.18898223650461362f * 1.4426950408889634f);
#pragma unroll
            for (int i = 0; i < 16; ++i) aq[i] *= QS;
#pragma unroll
            for (int p = 0; p < 2; ++p) {
                const u32x4 f = pack8(aq, p);
                const auto s0 = __builtin_amdgcn_permlane32_swap(f[0], f[2], false, false), s1 = __builtin_amdgcn_permlane32_swap(f[1], f[3], false, false);
                const bool sp = gi[p] == 3;
                st16_dev((u32x4){s0[0], s1[0], sp ? 0u : s0[1], sp ? 0u : s1[1]}, rws, go1[p], 0);
                st16_dev((u32x4){0u, 0u, s0[1], s1[1]}, rws, go2[p], 0);
            }
        }
    } else {
        epoch = peek_flag(ws, FL_EPOCH * 128); fl1 = (FL_RT + xcd * 4 + loc - 28) * 128;
        const int ri = xcd * 4 + loc - 28, nu = (w == 0 && ri < 16) ? 4 : 3;
        const unsigned vo = (unsigned)(row8 * 3584 + (lane & 7) * 16);
#pragma unroll 1
        for (int i = 0; i < nu; ++i) {
            const int u = i < 3 ? i * 256 + ri * 8 + w : 768 + ri;
            const size_t so = (size_t)(u / 28) * 16 * 3584 + (u % 28) * 128;
            const unsigned d_ = lds0 + w * 16384 + i * 4096;
            glds(vo, wg + so, d_); glds(vo, wg + so + 8 * 3584, d_ + 1024); glds(vo, wu + so, d_ + 2048); glds(vo, wu + so + 8 * 3584, d_ + 3072);
        }
#pragma unroll 1
        for (int i = 0; i < nu; ++i) {
            const int u = i < 3 ? i * 256 + ri * 8 + w : 768 + ri;
            waitv<8>();
            lds_ptr sg = lds + w * 16384 + i * 4096 + g1 * 2048 + (4 * hf + q4) * 128 + p4 * 8;
#pragma unroll
            for (int e = 0; e < 4; ++e) st16_dev(U4(cat(trd(sg + e * 32), trd(sg + e * 32 + 1024))), rws, l16, WS_WGU + (unsigned)(((u % 28) * 4 + e) * 28 + u / 28) * 1024);
            FENCE();
        }
        tagv = epoch + 1;
    }
    TS(24);
    const int head = xcd * 2 + (loc & 1), rb = loc >> 1, qt = w & 3;
    const char* kvs = ws + WS_KV + (size_t)head * 262144 + w * 2048;
    const char* qs = ws + WS_QF + (size_t)(head * 64 + rb * 4 + qt) * 2048 + kh * 1024;
    unsigned kvd = lds0 + w * 2048, qd = lds0 + 147456 + qt * 2048 + kh * 1024;
    unsigned pf1 = (FL_KV + (lane >> 1) * 8 + (head * 28 + (lane & 1) * 27) / 64) * 128;
    PINS(kvs); PINS(qs); PINS(kvd); PINS(qd); PIN2(pf1, fl1);
    waitv_bar<0>();
    if (w == 0) set_flag(ws, fl1, tagv);
    const u64 tag = ((u64)(unsigned)__builtin_amdgcn_readfirstlane((unsigned)(tagv >> 32)) << 32 | (u64)(unsigned)__builtin_amdgcn_readfirstlane((unsigned)tagv));
    TS(4);

    const int drb = (xcd >> 1) * 4 + l7, dfg = (xcd & 1) * 7 + m7;
    bf16x8 wf[28];
    u32x2 xa, xb;
    f32x16 cs = {}, acc = {};
    const u32x4 one4 = {ones, ones, ones, ones};
    const unsigned hi_ = (unsigned)((tb * 2 + tt) * 28 + m7 * 4 + ct * 2 + kh), ofo = (unsigned)(tt * 32768 + kh * 16384 + ct * 4096);
    unsigned ho = WS_HF + hi_ * 1024 + l16, so = hf ? OOB : WS_HS + hi_ * 256 + r * 8;
    const char* os = ws + WS_OF + (size_t)tb * 65536 + ofo;
    unsigned od = lds0 + 65536 + ofo;
    unsigned pf2 = (FL_O + (tb >> 1) * 16 + kh * 8 + ct * 2 + ((lane >> 1) & 1) + (lane & 4)) * 128 + ((lane & 1) * 4 + (tb & 1) * 2 + tt) * 8;
    unsigned fo3 = (FL_H + tb * 8 + m7) * 128;
    {
        lds_ptr ap = opq(lds + kh * 8192 + l16), apB = opq(lds + 65536 + kh * 8192 + l16), apC = opq(lds + 131072 + kh * 8192 + l16);
        const unsigned lv = kh * 8192 + (l16 ^ (kh * 256));
        lds_ptr vp = opq(lds + lv), vpB = opq(lds + 65536 + lv), vpC = opq(lds + 131072 + lv);
#define AP(off) ((off) < 65536 ? ap + (off) : (off) < 131072 ? apB + ((off) - 65536) : apC + ((off) - 131072))
#define VP(off) ((off) < 65536 ? vp + (off) : (off) < 131072 ? vpB + ((off) - 65536) : vpC + ((off) - 131072))
#define KV_ISSUE(t) glds2_dev(l16, kvs + (t) * 16384, kvd + phys(t))
        unsigned wgo = WS_WGU + (unsigned)((ffn ? dfg : 0) * 8 + w) * 28672;
        const int ri = umin(xcd * 4 + loc - 28, 27);
        const char* wos = (ffn ? wo + m7 * 128 : wd + (ri % 7) * 128 + (size_t)(ri / 7) * 448 * 896) + (size_t)w * 56 * 896;
        unsigned wov = (unsigned)(row8 * 896) + swz;
        const rsrc_t rx = __builtin_amdgcn_make_buffer_rsrc((void*)x, 0, 2048 * 448 * 2, 0x00020000);
        unsigned xo_ = (unsigned)(((tb * 2 + tt) * 32 + r) * 896 + m7 * 128 + ct * 64 + kh * 32 + hf * 8);
        unsigned rto = (FL_RT + r) * 128;
        lds_ptr qp = opq(lds + 147456 + qt * 2048 + l16);
        PINS(wgo); PINS(wos); PIN2(wov, xo_); PIN(rto); PIN2(ho, so); PIN2(pf2, fo3);
        lds_ptr xw = opq(lds + 147456 + w * 2048 + l16), xr = opq(lds + 147456 + (w ^ 4) * 2048 + l16);
        lds_ptr lw = opq(lds + 58368 + w * 256 + lane * 4), lr = opq(lds + 58368 + qt * 256 + 128 + r * 4);
        lds_ptr bw = opq(lds + 60416 + w * 4), br = opq(lds + 60416 + (lane & 7) * 4);
        unsigned oo = WS_OF + (unsigned)((rb * 4 + qt) * 32 + head * 2 + kh) * 1024 + l16;
        unsigned fo2 = (FL_O + rb * 16 + head) * 128 + w * 8;
        PIN2(oo, fo2);
        if (w == HOP3_POLLER) poll_flag(ws, pf1, tag);
        BAR();
        TS(5);
        glds_dev(l16, qs, qd);
        KV_ISSUE(0); KV_ISSUE(1);
        asm volatile("s_barrier" ::: "memory");
        KV_ISSUE(2); KV_ISSUE(3);
        waitv_bar<4>();
        TS(6);
        const bf16x8 q0 = lds8(qp), q1 = lds8(qp + 1024);
        f32x16 o = {}, sa = {}, sb;
        bf16x8 k0 = lds8(ap + phys(0)), k1 = lds8(ap + phys(0) + 1024), v0 = lds8(vp + phys(0) + 2048), v1 = {};
        u32x4 p0, p1 = {};
        sa = MFMA(k0, q0, sa); sa = MFMA(k1, q1, sa);
        k0 = lds8(ap + phys(0) + 4096); k1 = lds8(ap + phys(0) + 5120);
        PIN(sa); SB();
        u64 rtf = 0;
#define EXP4(S, o4) _Pragma("unroll") for (int i_ = o4; i_ < o4 + 4; ++i_) S[i_] = __builtin_amdgcn_exp2f(S[i_]);
#define WLD(s) wf[s] = BF8(ld16_dev(rws, l16 + ((s) & 3) * 1024, wgo + ((s) >> 2) * 4096))
#define SIDE(t, g) do { \
            if ((t) == 0 && (g) == 0) KV_ISSUE(4); if ((t) == 0 && (g) == 2) KV_ISSUE(5); if ((t) == 1 && (g) == 0) KV_ISSUE(6); if ((t) == 1 && (g) == 2) KV_ISSUE(7); \
            if ((t) == 2 && (g) == 0) KV_ISSUE(8); if ((t) == 1 && (g) == 3) rtf = peek_flag(ws, rto); \
            if ((t) == 3 && (g) < 3) KV_ISSUE(9 + (g)); \
            if ((t) == 7 && (g) == 0) KV_ISSUE(12); if ((t) == 7 && (g) == 2) KV_ISSUE(13); if ((t) == 8 && (g) == 0) KV_ISSUE(14); if ((t) == 8 && (g) == 2) KV_ISSUE(15); \
            if ((t) >= 4 && (t) <= 6) WLD(((t) - 4) * 4 + (g)); \
            if (((t) == 7 || (t) == 8) && ((g) & 1)) WLD(12 + ((t) - 7) * 2 + ((g) >> 1)); \
            if ((t) >= 9 && (t) <= 11) WLD(((t) - 5) * 4 + (g)); \
            if ((t) == 12 || ((t) == 13 && (g) < 3)) glds(wov, wos + (((t) - 12) * 4 + (g)) * 8 * 896, lds0 + (w * 7 + ((t) - 12) * 4 + (g)) * 1024); \
            if ((t) == 13 && (g) == 3) { xa = ld8(rx, xo_, 0); xb = ld8(rx, xo_, 16); } \
            if ((t) == 14 && (g) == 1) { if (w >= 6) touch(lane * (w == 6 ? (WS_END / 64 & ~127u) : 28672), w == 6 ? ws : out, lds0 + 61440); } \
            if ((t) == 14 && (g) == 0) { if (tid < 32) *(LAS u32x4*)(lds + 57344 + tid * 16) = (u32x4){0, 0, 0, 0}; } \
        } while (0)
#define TOFF(i) (phys((i) >> 1) + ((i) & 1) * 4096)
#define TILE(i, SC, SN) do { \
            __builtin_amdgcn_s_setprio(((i) & 1) ? 1 : 3); \
            if ((i) < 31) { f32x16 z_ = {}; SN = MFMA(k0, q0, z_); PIN(SN); } if ((i) < 30) k0 = lds8(AP(TOFF((i) + 2))); SB(); \
            EXP4(SC, 0); PIN(SC); SB(); \
            o = MFMA(v1, BF8(p1), o); PIN(o); v1 = lds8(VP(TOFF(i) + 3072)); SIDE((i) >> 1, ((i) & 1) * 2); SB(); \
            EXP4(SC, 4); p0 = pack8(SC, 0); PIN(p0); SB(); \
            __builtin_amdgcn_s_setprio(((i) & 1) ? 0 : 2); \
            if ((i) < 31) { SN = MFMA(k1, q1, SN); PIN(SN); } if ((i) < 30) k1 = lds8(AP(TOFF((i) + 2) + 1024)); SB(); \
            EXP4(SC, 8); PIN(SC); SB(); \
            o = MFMA(v0, BF8(p0), o); PIN(o); if ((i) < 31) v0 = lds8(VP(TOFF((i) + 1) + 2048)); SIDE((i) >> 1, ((i) & 1) * 2 + 1); SB(); \
            EXP4(SC, 12); p1 = pack8(SC, 1); PIN(p1); SB(); } while (0)
#define STEP(t) do { TILE(2 * (t), sa, sb); TILE(2 * (t) + 1, sb, sa); } while (0)
        STEP(0);
        waitv_bar<4>();
        STEP(1); STEP(2);
        waitv_bar<3>();
        TS(21);
        if (__builtin_expect(!wave_all(rtf == tag), 0)) poll_flag(ws, rto, tag);
        FENCE();
        STEP(3); STEP(4); STEP(5); STEP(6);
        waitv_bar<12>();
        TS(22);
        STEP(7); STEP(8); STEP(9); STEP(10);
        waitv_bar<9>();
        TS(23);
        STEP(11); STEP(12); STEP(13); STEP(14); STEP(15);
        o = MFMA(v1, BF8(p1), o);
        TS(7);
        u32x4 ov;
        {
            *(LAS f32x4*)xw = (f32x4){o[8], o[9], o[10], o[11]}; *(LAS f32x4*)(xw + 1024) = (f32x4){o[12], o[13], o[14], o[15]};
            const float l = kh ? o[4] : o[12];
            *(LAS float*)lw = l;
            float sa_ = 0.f;
#pragma unroll
            for (int i = 0; i < 16; ++i) sa_ += __builtin_fabsf(o[i]);
            const bool ok = sa_ < 0x1p100f && (hf == 0 || l > 0x1p-80f);
            *(LAS unsigned*)bw = wave_all(ok) ? 0u : 1u;
            BAR();
            const f32x4 a = *(LAS f32x4*)xr, c = *(LAS f32x4*)(xr + 1024);
            const float inv = __builtin_amdgcn_rcpf(*(LAS float*)lr + *(LAS float*)(lr + 1024));
            const unsigned bad = *(LAS unsigned*)br;
            const float iz = (kh && hf) ? 0.f : inv;
            ov = (u32x4){cvtpk((o[0] + a[0]) * inv, (o[1] + a[1]) * inv), cvtpk((o[2] + a[2]) * inv, (o[3] + a[3]) * inv),
                         cvtpk((o[4] + c[0]) * iz, (o[5] + c[1]) * iz), cvtpk((o[6] + c[2]) * iz, (o[7] + c[3]) * iz)};
            if (__builtin_expect(wave_any(bad != 0), 0)) {
                f32x16 of = {};
                float m = -0x1p126f;
#pragma unroll 1
                for (int t = 0; t < 16; ++t) {
                    BAR();
                    glds2_dev(l16, kvs + t * 16384, lds0 + 65536 + w * 2048);
                    waitv_bar<0>();
#pragma unroll 1
                    for (int u = 0; u < 2; ++u) {
                        f32x16 s = {};
                        s = MFMA(lds8(apB + u * 4096), q0, s); s = MFMA(lds8(apB + u * 4096 + 1024), q1, s);
                        float mx = s[0];
#pragma unroll
                        for (int i = 1; i < 16; ++i) mx = __builtin_fmaxf(mx, s[i]);
                        mx = __builtin_fmaxf(swap_max(mx), m);
                        const float alpha = __builtin_amdgcn_exp2f(m - mx);
                        m = mx;
#pragma unroll
                        for (int i = 0; i < 16; ++i) { s[i] = __builtin_amdgcn_exp2f(s[i] - mx); of[i] *= alpha; }
                        of = MFMA(lds8(vpB + u * 4096 + 2048), BF8(pack8(s, 0)), of);
                        of = MFMA(lds8(vpB + u * 4096 + 3072), BF8(pack8(s, 1)), of);
                    }
                }
                *(LAS f32x4*)xw = (f32x4){of[8], of[9], of[10], of[11]}; *(LAS f32x4*)(xw + 1024) = (f32x4){of[12], of[13], of[14], of[15]};
                *(LAS float*)lw = hf ? (kh ? of[4] : of[12]) : m;
                BAR();
                const f32x4 a2 = *(LAS f32x4*)xr, c2 = *(LAS f32x4*)(xr + 1024);
                const float m0 = *(LAS float*)(lr - 128), m1 = *(LAS float*)(lr + 1024 - 128), mm = __builtin_fmaxf(m0, m1);
                const float e0 = __builtin_amdgcn_exp2f(m0 - mm), e1 = __builtin_amdgcn_exp2f(m1 - mm);
                const float iv = __builtin_amdgcn_rcpf(*(LAS float*)lr * e0 + *(LAS float*)(lr + 1024) * e1);
                const float im = (kh ? e1 : e0) * iv, io = (kh ? e0 : e1) * iv;
                const bool z = kh && hf;
                ov = (u32x4){cvtpk(of[0] * im + a2[0] * io, of[1] * im + a2[1] * io), cvtpk(of[2] * im + a2[2] * io, of[3] * im + a2[3] * io),
                             z ? 0u : cvtpk(of[4] * im + c2[0] * io, of[5] * im + c2[1] * io), z ? 0u : cvtpk(of[6] * im + c2[2] * io, of[7] * im + c2[3] * io)};
            }
        }
        st16_dev(ov, rws, oo, 0);
        TS(25);
        FENCE();
        PIN2(xa, xb);
#pragma unroll
        for (int s = 27; s >= 0; --s) PIN(wf[s]);
#pragma unroll
        for (int s = 0; s < 8; ++s) cs = MFMA(wf[s], BF8(one4), cs);
        acc[0] = bflo(xa[0]); acc[1] = bfhi(xa[0]); acc[2] = bflo(xa[1]); acc[3] = bfhi(xa[1]); acc[4] = bflo(xb[0]); acc[5] = bfhi(xb[0]); acc[6] = bflo(xb[1]); acc[7] = bfhi(xb[1]);
        PIN(acc); PIN2(ho, so); PIN2(pf2, fo3); PINS(os); PINS(od); PIN(cs);
        waitv<0>();
        set_flag(ws, fo2, tagv);
        BAR();
        TS(8);
    }
    if (__builtin_expect(!ffn, 0)) {
        const int ri = xcd * 4 + loc - 28;
        if (ri < 28) {
#pragma unroll 1
            for (int u = w; u < 28; u += 8) {
                lds_ptr sd = lds + u * 2048 + (4 * hf + q4) * 128 + g1 * 32 + p4 * 8;
#pragma unroll
                for (int e = 0; e < 2; ++e) { lds_ptr p_ = sd + ((e ^ (q4 >> 1)) * 64);
                    st16_dev(U4(cat(trd(p_), trd(p_ + 1024))), rws, l16, WS_WD + (unsigned)(((ri % 7) * 112 + (ri / 7) * 28 + u) * 2 + e) * 1024); }
            }
        }
        waitv_bar<0>();
        if (w == 0) set_flag(ws, (FL_RD + xcd * 4 + loc - 28) * 128, tagv);
        { const u64 e_ = pt1 + 4000; while (__builtin_amdgcn_s_memrealtime() < e_) __builtin_amdgcn_s_sleep(1); }
        return;
    }

    const int erb = tb >> 1;
    const char* hs = ws + WS_HF + (size_t)drb * 114688 + w * 1024;
    const char* hss = ws + WS_HS + (size_t)((drb * 4 + (w & 3)) * 28 + kh * 14) * 256;
    unsigned go = WS_GF + (unsigned)(drb * 4 * 112 + dfg * 8 + w) * 1024 + l16;
    unsigned pf3, fo4 = (FL_G + drb * 64 + dfg) * 128 + w * 8;
    {
        lds_ptr wb = lds + kh * (8 * 28 * 128) + (4 * hf + q4) * 128 + ((ct ^ (q4 >> 1)) * 64) + (g1 ^ kh) * 32 + p4 * 8;
        bf16x8 wr[16];
#pragma unroll
        for (int s = 0; s < 12; ++s) { const int k0 = ((s >> 1) * 28 + (s & 1) * 16) * 128; wr[s] = cat(trd(wb + k0), trd(wb + k0 + 1024)); }
        lds_ptr cw = opq(lds + 131072 + w * 2048 + l16), cr = opq(lds + 131072 + (w ^ 4) * 2048 + l16);
        poll_flag(ws, pf2, tag);
        TS(9);
        glds4_dev(l16, os, od);
        asm volatile("s_barrier" ::: "memory");
        glds4_dev(l16, os + 8192, od + 8192);
        lds_ptr ob = opq(lds + 65536 + tt * 32768 + kh * 16384 + l16);
        bf16x8 of[8];
        waitv_bar<4>();
        TS(26);
#pragma unroll
        for (int s = 0; s < 8; ++s) of[s] = lds8(ob + s * 1024);
        SB();
#pragma unroll
        for (int s = 0; s < 8; ++s) acc = MFMA(wr[s], of[s], acc);
        PIN(acc); SB();
#pragma unroll
        for (int s = 12; s < 16; ++s) { const int k0 = ((s >> 1) * 28 + (s & 1) * 16) * 128; wr[s] = cat(trd(wb + k0), trd(wb + k0 + 1024)); }
        waitv_bar<0>();
#pragma unroll
        for (int s = 0; s < 8; ++s) of[s] = lds8(ob + (8 + s) * 1024);
        SB();
#pragma unroll
        for (int s = 0; s < 8; ++s) acc = MFMA(wr[8 + s], of[s], acc);
        TS(10);
        *(LAS f32x4*)cw = (f32x4){acc[8], acc[9], acc[10], acc[11]}; *(LAS f32x4*)(cw + 1024) = (f32x4){acc[12], acc[13], acc[14], acc[15]};
        BAR();
        const f32x4 a = *(LAS f32x4*)cr + (f32x4){acc[0], acc[1], acc[2], acc[3]}, b = *(LAS f32x4*)(cr + 1024) + (f32x4){acc[4], acc[5], acc[6], acc[7]};
        const u32x4 hv = {cvtpk(a[0], a[1]), cvtpk(a[2], a[3]), cvtpk(b[0], b[1]), cvtpk(b[2], b[3])};
        st16_dev(hv, rws, ho, 0);
        float s1 = 0.f, s2 = 0.f;
        acc_stats(hv, s1, s2);
        s1 = swap_add(s1); s2 = swap_add(s2);
        st8_dev((u32x2){f2u(s1), f2u(s2)}, rws, so, 0);
        FENCE();
#pragma unroll
        for (int s = 8; s < 14; ++s) cs = MFMA(wf[s], BF8(one4), cs);
        pf3 = (FL_H + drb * 16 + (lane & 8) + umin(lane & 7, 6)) * 128;
        PIN(go); PIN2(pf3, fo4); PINS(hs); PINS(hss); PIN(cs);
        waitv_bar<0>();
        if (w == 0) set_flag(ws, fo3, tagv);
        if (bid == 0 && w == 0) set_flag(ws, FL_EPOCH * 128, tagv);
        TS(11);
    }

    const unsigned wdo = WS_WD + (unsigned)(m7 * 112 + w * 14) * 2048;
    bf16x8 wdf[28];
    bf16x8 gf[8];
    const int ta = erb * 4 + (tb & 1);
    const unsigned gxo = WS_GF + (unsigned)(ta * 112 + w * 14) * 1024;
    unsigned fo4e;
    u64 gfl = 0;
    const unsigned gks = w * 14 + umin(lane & 15, 13), gfi = (FL_G + (erb * 4 + (tb & 1)) * 16 + (gks >> 3)) * 128 + (gks & 7) * 8;
    {
#pragma unroll
        for (int s = 14; s < 28; ++s) cs = MFMA(wf[s], BF8(one4), cs);
        lds_ptr csp = opq(lds + 161792 + w * 128 + hf * 64);
        if (r == 0) {
#pragma unroll
            for (int g = 0; g < 4; ++g) *(LAS f32x4*)(csp + g * 16) = (f32x4){cs[4 * g], cs[4 * g + 1], cs[4 * g + 2], cs[4 * g + 3]};
        }
        const unsigned hd = lds0 + w * 1024;
        const char* wds = ws + wdo;
        lds_ptr hb = opq(lds + l16), hb2 = opq(lds + 57344 + l16);
        lds_ptr wl = opq(lds + 114688 + w * 5120 + l16), wl2 = opq(lds + w * 7168 + l16);
        LAS f32x2* stp = (LAS f32x2*)opq(lds + 159744 + r * 8);
        if (w == HOP3_POLLER) poll_flag(ws, pf3, tag);
        BAR();
        TS(12);
#define H_PIECE(j) glds_dev(l16, hs + (j) * 8192, hd + (j) * 8192)
#define WDLD(s) wdf[s] = BF8(ld16_dev(rws, l16 + ((s) & 3) * 1024, wdo + ((s) >> 2) * 4096))
#define HB(n) ((n) < 56 ? hb + (n) * 1024 : hb2 + ((n) - 56) * 1024)
        H_PIECE(0);
        asm volatile("s_barrier" ::: "memory");
        H_PIECE(1); H_PIECE(2); H_PIECE(3);
        glds4_dev(l16, hss, lds0 + 114688 + w * 5120);
        constexpr int PF = 5;
        bf16x8 bf[PF + 1];
        f32x16 acc[2];
        f32x2 sp[7], sv0, sv1;
        f32x4 c4[4];
        float mean = 0.f, r2 = 0.f, hp = 0.f;
        u32x4 hv;
        u64 rdf = 0;
        waitv_bar<7>();
        TS(27);
#pragma unroll
        for (int n = 0; n < PF; ++n) bf[n] = lds8(HB(n));
        SB();
        sfor<0, 125>([&](auto N_) __attribute__((always_inline)) {
            constexpr int n = decltype(N_)::v, p = n / 28, s = n % 28;
            if constexpr (n == 3) waitv_bar<5>();
            if constexpr (n == 27) { waitv_bar<3>(); TS(13); }
            if constexpr (n == 64) { waitv_bar<10>(); set_flag(ws, fo4, tagv); TS(14); }
            if constexpr (n == 84) { waitv<7>(); set_flag(ws, fo4 + 2048, tagv); }
            if constexpr (n == 106) { waitv<4>(); set_flag(ws, fo4 + 4096, tagv); }
            if constexpr (n == 39) { if (__builtin_expect(!wave_all(rdf == tag), 0)) poll_flag(ws, (FL_RD + r) * 128, tag); FENCE(); }
            if constexpr (d_prio(n) != d_prio(n - 1)) __builtin_amdgcn_s_setprio(d_prio(n));
            if constexpr (n < 112) { if constexpr (s == 0) { f32x16 z = {}; acc[p & 1] = MFMA(wf[s], bf[n % (PF + 1)], z); } else acc[p & 1] = MFMA(wf[s], bf[n % (PF + 1)], acc[p & 1]); PIN(acc[p & 1]); }
            SB();
            if constexpr (n + PF < 112) bf[(n + PF) % (PF + 1)] = lds8(HB(n + PF));
            if constexpr (n % 3 == 2 && n < 30) H_PIECE(4 + n / 3);
            if constexpr (n == 16) { waitv<5>();
#pragma unroll
                for (int j = 0; j < 7; ++j) sp[j] = *(LAS f32x2*)(lds + 114688 + w * 5120 + (hf * 7 + j) * 256 + r * 8);
            }
            if constexpr (n == 20) { const f32x2 t = ((sp[0] + sp[1]) + (sp[2] + sp[3])) + ((sp[4] + sp[5]) + sp[6]); stp[w * 32] = (f32x2){swap_add(t[0]), swap_add(t[1])}; }
            if constexpr (n >= 39 && n < 48 && n % 2 == 1) glds_dev(l16, wds + (n - 39) / 2 * 1024, lds0 + 114688 + w * 5120 + (n - 39) / 2 * 1024);
            if constexpr (n >= 48 && n < 64 && n % 3 == 0) WDLD(12 + (n - 48) / 3);
            if constexpr (n >= 70 && n < 84 && n % 2 == 0) glds_dev(l16, wds + (5 + (n - 70) / 2) * 1024, lds0 + w * 7168 + (n - 70) / 2 * 1024);
            if constexpr (n >= 85 && n < 105 && n % 2 == 1) WDLD(18 + (n - 85) / 2);
            if constexpr (n == 107) gfl = peek_flag(ws, gfi);
            if constexpr (n == 30) rdf = peek_flag(ws, (FL_RD + r) * 128);
            if constexpr (p >= 1) {
                const f32x16& a = acc[(p - 1) & 1];
                if constexpr (n == 100) { sv0 = stp[3 * 32]; sv1 = stp[7 * 32]; }
                if constexpr (s == 0 && p < 4) { sv0 = stp[(p - 1) * 32]; sv1 = stp[(p + 3) * 32];
#pragma unroll
                    for (int g = 0; g < 4; ++g) c4[g] = *(LAS f32x4*)(csp + g * 16); }
                if constexpr ((s == 3 && p < 4) || n == 104) { const f32x2 sv = sv0 + sv1; mean = sv[0] * (1.f / D); r2 = __builtin_amdgcn_rcpf(__builtin_fmaxf(sv[1] * (1.f / D) - mean * mean, 0.f) + EPS); PIN2(mean, r2); }
                if constexpr (s >= 4 && s < 12) { constexpr int i = s - 4;
                    const float h0 = __builtin_fmaxf(a[i] - mean * c4[i >> 2][i & 3], 0.f) * r2 * (a[8 + i] - mean * c4[2 + (i >> 2)][i & 3]);
                    if constexpr (i & 1) { hv[i >> 1] = cvtpk(hp, h0); PIN(hv); } else { hp = h0; PIN(hp); } }
                if constexpr (s == 12) { st16_dev(hv, rws, go + (p - 1) * 114688, 0); FENCE(); }
            }
            SB();
        });
        TS(15);
        fo4e = fo4 + 6144;
        if (__builtin_expect(!wave_all(gfl == tag), 0)) poll_flag(ws, gfi, tag);
        FENCE();
#pragma unroll
        for (int s = 0; s < 8; ++s) { gf[s] = BF8(ld16_dev(rws, l16 + (s & 3) * 1024, gxo + (s >> 2) * 4096)); SB(); }
        FENCE();
#pragma unroll
        for (int s = 0; s < 12; ++s) wdf[s] = lds8(s < 5 ? wl + s * 1024 : wl2 + (s - 5) * 1024);
    }

    {
        const int etok = tid >> 4, eblk = tid & 15;
        const unsigned hro = (unsigned)((((eblk >> 2) & 3) * 64 + (eblk & 1) * 32 + etok) * 16 + ((eblk >> 1) & 1) * 8) + WS_HF + (unsigned)(ta * 28 + m7 * 4) * 1024;
        u32x2 hres[2];
        GAS u64* op = (GAS u64*)(out + (size_t)(ta * 32 + etok) * 896 + m7 * 128 + eblk * 8);
        lds_ptr rp = opq(lds + w * 8448 + hf * 528 + r * 16), rq = opq(lds + eblk * 528 + etok * 16);
        lds_ptr rp2 = opq(lds + 67584 + w * 8448 + hf * 528 + r * 16), rq2 = opq(lds + 67584 + eblk * 528 + etok * 16);
#define G_LD(hh, s) gf[(s) & 7] = BF8(ld16_dev(rws, l16 + ((s) & 3) * 1024, gxo + (hh) * 229376 + ((s) >> 2) * 4096))
#define G_MMA(hh, RP) do { f32x16 c0 = {}, c1 = {}; \
            _Pragma("unroll") for (int s = 0; s < 14; ++s) { c0 = MFMA(wdf[2 * s], gf[s & 7], c0); PIN(c0); c1 = MFMA(wdf[2 * s + 1], gf[s & 7], c1); PIN(c1); SB(); if (s < 6) G_LD(hh, s + 8); SB(); } \
            _Pragma("unroll") for (int g = 0; g < 4; ++g) *(LAS f32x4*)(RP + g * 1056) = (f32x4){c0[4 * g], c0[4 * g + 1], c0[4 * g + 2], c0[4 * g + 3]}; \
            _Pragma("unroll") for (int g = 0; g < 4; ++g) *(LAS f32x4*)(RP + 4224 + g * 1056) = (f32x4){c1[4 * g], c1[4 * g + 1], c1[4 * g + 2], c1[4 * g + 3]}; } while (0)
#define G_OUT(hh, RQ) do { f32x4 pa[8]; \
            _Pragma("unroll") for (int ww = 0; ww < 8; ++ww) pa[ww] = *(LAS f32x4*)(RQ + ww * 8448); SB(); \
            const f32x4 a = ((pa[0] + pa[1]) + (pa[2] + pa[3])) + ((pa[4] + pa[5]) + (pa[6] + pa[7])); \
            const u32x2 ov = {cvtpk(a[0] + bflo(hres[hh][0]), a[1] + bfhi(hres[hh][0])), cvtpk(a[2] + bflo(hres[hh][1]), a[3] + bfhi(hres[hh][1]))}; \
            __hip_atomic_store(op + (hh) * (64 * 896 / 8), __builtin_bit_cast(u64, ov), __ATOMIC_RELAXED, __HIP_MEMORY_SCOPE_AGENT); } while (0)
        waitv<8>();
        set_flag(ws, fo4e, tagv);
        BAR();
        TS(16);
        hres[0] = ld8_dev(rws, hro, 0); hres[1] = ld8_dev(rws, hro, 57344);
        FENCE(); SB();
        G_MMA(0, rp);
        PIN2(hres[0], hres[1]);
        TS(17);
        poll_flag(ws, gfi + 4096, tag);
        TS(18);
#pragma unroll
        for (int s = 0; s < 8; ++s) { G_LD(1, s); SB(); }
        BAR();
        G_OUT(0, rq);
        FENCE(); SB();
        G_MMA(1, rp2);
        TS(19);
        BAR();
        G_OUT(1, rq2);
    }
    TS(20);
}

#define WS_OFF 0
extern "C" void kernel_launch(void* const* d_in, const int* in_sizes, int n_in, void* d_out, int out_size, void* d_ws, size_t ws_size, hipStream_t stream) {
    (void)hipFuncSetAttribute((const void*)block_fwd, hipFuncAttributeMaxDynamicSharedMemorySize, LDS_BYTES);
    block_fwd<<<256, 512, LDS_BYTES, stream>>>((const char*)d_in[0], (const char*)d_in[1], (const char*)d_in[2], (const char*)d_in[4], (const char*)d_in[5], (const char*)d_in[6], (char*)d_out, (char*)d_ws + WS_OFF);
}
```

```cpp
#include <hip/hip_runtime.h>
#include <hip/hip_bf16.h>
#include <stdint.h>

typedef __attribute__((ext_vector_type(8))) short bf16x8;
typedef __attribute__((ext_vector_type(4))) short s16x4;
typedef __attribute__((ext_vector_type(2))) __bf16 bf16x2;
typedef __attribute__((ext_vector_type(16))) float f32x16;
typedef __attribute__((ext_vector_type(4))) float f32x4;
typedef __attribute__((ext_vector_type(2))) float f32x2;
typedef __attribute__((ext_vector_type(4))) unsigned u32x4;
typedef __attribute__((ext_vector_type(2))) unsigned u32x2;
typedef short v4i16_t __attribute__((ext_vector_type(4)));
typedef unsigned long long u64;
#define LAS __attribute__((address_space(3)))
#define GAS __attribute__((address_space(1)))
typedef LAS char* lds_ptr;
#if defined(__HIP_DEVICE_COMPILE__)
#define NOPK __attribute__((target("no-packed-fp32-ops")))
#else
#define NOPK
#endif
#define DI __device__ __forceinline__
#define MFMA(a, b, c) __builtin_amdgcn_mfma_f32_32x32x16_bf16(a, b, c, 0, 0, 0)
#define MFMA16(a, b, c) __builtin_amdgcn_mfma_f32_16x16x32_bf16(a, b, c, 0, 0, 0)
#define BF8(x) __builtin_bit_cast(bf16x8, x)
#define U4(x) __builtin_bit_cast(u32x4, x)

constexpr int D = 448;
constexpr float EPS = 1e-5f;
constexpr unsigned WS_FLAG = 0;
constexpr unsigned FL_EPOCH = 0, FL_KV = 1, FL_RT = FL_KV + 256, FL_RD = FL_RT + 32, FL_CL = FL_RD + 32, FL_O = FL_CL + 32, FL_H = FL_O + 256, FL_G = FL_H + 256;
constexpr unsigned WS_KV = 1u << 20;
constexpr unsigned WS_QF = WS_KV + 4194304;
constexpr unsigned WS_OF = WS_QF + 2097152;
constexpr unsigned WS_HF = WS_OF + 2097152;
constexpr unsigned WS_GF = WS_HF + 64 * 28 * 1024;
constexpr unsigned WS_WGU = WS_GF + 64 * 112 * 1024;
constexpr unsigned WS_WD = WS_WGU + 112 * 28 * 1024;
constexpr unsigned WS_HS = WS_WD + 7 * 112 * 2 * 1024;
constexpr unsigned WS_END = WS_HS + 64 * 28 * 256 + 1024;
constexpr unsigned OOB = 0x40000000u;
constexpr int LDS_BYTES = 163840;

DI float u2f(unsigned u) { return __builtin_bit_cast(float, u); }
DI unsigned f2u(float f) { return __builtin_bit_cast(unsigned, f); }
DI unsigned umin(unsigned a, unsigned b) { return a < b ? a : b; }
DI unsigned umax3(unsigned a, unsigned b, unsigned c) { const unsigned m = a > b ? a : b; return m > c ? m : c; }
DI bool wave_any(bool p) { return __builtin_amdgcn_ballot_w64(p) != 0; }
DI bool wave_all(bool p) { return __builtin_amdgcn_ballot_w64(!p) == 0; }
DI unsigned cvtpk(float lo, float hi) { return __builtin_bit_cast(unsigned, __builtin_convertvector((f32x2){lo, hi}, bf16x2)); }
DI float bflo(unsigned u) { return u2f(u << 16); }
DI float bfhi(unsigned u) { return u2f(u & 0xffff0000u); }
DI void glds(unsigned voff, const void* sbase, unsigned dst) {
    asm volatile("s_mov_b32 m0, %2\n\ts_nop 0\n\tglobal_load_lds_dwordx4 %0, %1" :: "v"(voff), "s"(sbase), "s"(dst) : "memory");
}
DI void glds_dev(unsigned voff, const void* sbase, unsigned dst) {
    asm volatile("s_mov_b32 m0, %2\n\ts_nop 0\n\tglobal_load_lds_dwordx4 %0, %1 sc1" :: "v"(voff), "s"(sbase), "s"(dst) : "memory");
}
DI void glds2_dev(unsigned voff, const void* sbase, unsigned dst) {
    asm volatile("s_mov_b32 m0, %2\n\ts_nop 0\n\tglobal_load_lds_dwordx4 %0, %1 sc1\n\tglobal_load_lds_dwordx4 %0, %1 offset:1024 sc1" :: "v"(voff), "s"(sbase), "s"(dst) : "memory");
}
DI void glds4_dev(unsigned voff, const void* sbase, unsigned dst) {
    asm volatile("s_mov_b32 m0, %2\n\ts_nop 0\n\tglobal_load_lds_dwordx4 %0, %1 sc1\n\tglobal_load_lds_dwordx4 %0, %1 offset:1024 sc1\n\t"
                 "global_load_lds_dwordx4 %0, %1 offset:2048 sc1\n\tglobal_load_lds_dwordx4 %0, %1 offset:3072 sc1" :: "v"(voff), "s"(sbase), "s"(dst) : "memory");
}
DI s16x4 trd(lds_ptr p) { return __builtin_bit_cast(s16x4, __builtin_amdgcn_ds_read_tr16_b64_v4i16((LAS v4i16_t*)p)); }
DI bf16x8 cat(s16x4 a, s16x4 b) { return __builtin_shufflevector(a, b, 0, 1, 2, 3, 4, 5, 6, 7); }
DI bf16x8 lds8(lds_ptr p) { return *(LAS bf16x8*)p; }
DI float swap_add(float v) { auto r = __builtin_amdgcn_permlane32_swap(f2u(v), f2u(v), false, false); return u2f(r[0]) + u2f(r[1]); }
DI float swap_max(float v) { auto r = __builtin_amdgcn_permlane32_swap(f2u(v), f2u(v), false, false); return __builtin_fmaxf(u2f(r[0]), u2f(r[1])); }
DI u32x4 pack8(const f32x16& a, int s) { return (u32x4){cvtpk(a[8 * s], a[8 * s + 1]), cvtpk(a[8 * s + 2], a[8 * s + 3]), cvtpk(a[8 * s + 4], a[8 * s + 5]), cvtpk(a[8 * s + 6], a[8 * s + 7])}; }
DI float dot2(unsigned a, unsigned b, float c) { return __builtin_amdgcn_fdot2_f32_bf16(__builtin_bit_cast(bf16x2, a), __builtin_bit_cast(bf16x2, b), c, false); }
DI void acc_stats(u32x4 u, float& s1, float& s2) {
#pragma unroll
    for (int i = 0; i < 4; ++i) { const float a = bflo(u[i]), b = bfhi(u[i]); s1 += a; s2 = __builtin_fmaf(a, a, s2); s1 += b; s2 = __builtin_fmaf(b, b, s2); }
}
DI void acc_stats2(unsigned u0, unsigned u1, f32x4& s) {
    const float a = bflo(u0), b = bfhi(u0), c = bflo(u1), d = bfhi(u1);
    s[0] += a; s[1] += b; s[2] = __builtin_fmaf(a, a, s[2]); s[3] = __builtin_fmaf(b, b, s[3]); s[0] += c; s[1] += d; s[2] = __builtin_fmaf(c, c, s[2]); s[3] = __builtin_fmaf(d, d, s[3]);
}
DI float sum8(float v) {
    v += u2f(__builtin_amdgcn_update_dpp(0u, f2u(v), 0xB1, 0xf, 0xf, true));
    v += u2f(__builtin_amdgcn_update_dpp(0u, f2u(v), 0x4E, 0xf, 0xf, true));
    v += u2f(__builtin_amdgcn_update_dpp(0u, f2u(v), 0x141, 0xf, 0xf, true));
    return v;
}
DI u32x4 shift8(u32x4 x, float c) {
    u32x4 o; float t0, t1, t2, t3, t4, t5, t6, t7;
    asm("v_dot2_f32_bf16 %4, %12, %16, %18\n\tv_dot2_f32_bf16 %5, %12, %17, %18\n\tv_dot2_f32_bf16 %6, %13, %16, %18\n\tv_dot2_f32_bf16 %7, %13, %17, %18\n\t"
        "v_dot2_f32_bf16 %8, %14, %16, %18\n\tv_dot2_f32_bf16 %9, %14, %17, %18\n\tv_dot2_f32_bf16 %10, %15, %16, %18\n\tv_dot2_f32_bf16 %11, %15, %17, %18\n\t"
        "v_cvt_pk_bf16_f32 %0, %4, %5\n\tv_cvt_pk_bf16_f32 %1, %6, %7\n\tv_cvt_pk_bf16_f32 %2, %8, %9\n\tv_cvt_pk_bf16_f32 %3, %10, %11"
        : "=&v"(o[0]), "=&v"(o[1]), "=&v"(o[2]), "=&v"(o[3]), "=&v"(t0), "=&v"(t1), "=&v"(t2), "=&v"(t3), "=&v"(t4), "=&v"(t5), "=&v"(t6), "=&v"(t7)
        : "v"(x[0]), "v"(x[1]), "v"(x[2]), "v"(x[3]), "s"(0x00003f80u), "s"(0x3f800000u), "v"(c));
    return o;
}
template <int N> struct IC { static constexpr int v = N; };
template <int B, int E, class F> DI void sfor(F&& f) { if constexpr (B < E) { f(IC<B>{}); sfor<B + 1, E>(f); } }
template <int N> DI void waitv() { asm volatile("s_waitcnt vmcnt(%0)" :: "i"(N) : "memory"); }
template <int N> DI void waitv_bar() { asm volatile("s_waitcnt vmcnt(%0) lgkmcnt(0)\n\ts_barrier" :: "i"(N) : "memory"); }
template <int N> DI void waitv_barx() { asm volatile("s_waitcnt vmcnt(%0)\n\ts_barrier" :: "i"(N) : "memory"); }
template <int N> DI void waitv_bar2() { asm volatile("s_waitcnt vmcnt(%0) lgkmcnt(2)\n\ts_barrier" :: "i"(N) : "memory"); }
DI void touch(unsigned voff, const void* sbase, unsigned junk) {
    asm volatile("s_mov_b32 m0, %2\n\ts_nop 0\n\tglobal_load_lds_dword %0, %1 sc1" :: "v"(voff), "s"(sbase), "s"(junk) : "memory");
}
#define BAR() asm volatile("s_waitcnt lgkmcnt(0)\n\ts_barrier" ::: "memory")
#define FENCE() asm volatile("" ::: "memory")
typedef __amdgpu_buffer_rsrc_t rsrc_t;
DI u32x4 ld16(rsrc_t rs, unsigned voff, unsigned soff) { return U4(__builtin_amdgcn_raw_buffer_load_b128(rs, voff, soff, 0)); }
DI u32x4 ld16_dev(rsrc_t rs, unsigned voff, unsigned soff) { return U4(__builtin_amdgcn_raw_buffer_load_b128(rs, voff, soff, 16)); }
DI u32x2 ld8(rsrc_t rs, unsigned voff, unsigned soff) { return __builtin_bit_cast(u32x2, __builtin_amdgcn_raw_buffer_load_b64(rs, voff, soff, 0)); }
DI u32x2 ld8_dev(rsrc_t rs, unsigned voff, unsigned soff) { return __builtin_bit_cast(u32x2, __builtin_amdgcn_raw_buffer_load_b64(rs, voff, soff, 16)); }
DI void st16_dev(u32x4 v, rsrc_t rs, unsigned voff, unsigned soff) { __builtin_amdgcn_raw_buffer_store_b128(v, rs, voff + soff, 0, 16); }
DI void st8_dev(u32x2 v, rsrc_t rs, unsigned voff, unsigned soff) { __builtin_amdgcn_raw_buffer_store_b64(v, rs, voff, soff, 16); }
#define POLL_SLEEP 1
DI void set_flag(char* ws, unsigned fo, u64 tagv) {
    asm volatile("s_mov_b64 exec, 1\n\tglobal_store_dwordx2 %0, %1, %2 sc1\n\ts_mov_b64 exec, -1" :: "v"(fo), "v"(tagv), "s"(ws) : "memory");
}
DI u64 peek_flag(char* ws, unsigned fo) { return __hip_atomic_load((GAS u64*)(ws + fo), __ATOMIC_RELAXED, __HIP_MEMORY_SCOPE_AGENT); }
DI void poll_flag(char* ws, unsigned fo, u64 tag) {
#pragma unroll 1
    for (int spin = 0; spin < (1 << 16); ++spin) {
        if (wave_all(peek_flag(ws, fo) == tag)) break;
        __builtin_amdgcn_s_sleep(POLL_SLEEP);
    }
    FENCE();
}
#define SB() __builtin_amdgcn_sched_barrier(0)
#define PINS(a) asm volatile("" : "+s"(a))
#define PIN(a) asm volatile("" : "+v"(a))
#define PIN2(a, b) asm volatile("" : "+v"(a), "+v"(b))
DI lds_ptr opq(lds_ptr p) { unsigned u = (unsigned)(uintptr_t)p; asm volatile("" : "+v"(u)); return (lds_ptr)(uintptr_t)u; }
#define TS(i) do {} while (0)

DI constexpr int wd_slot(int s) { return (s * 17 + 27) / 28 != ((s + 1) * 17 + 27) / 28 ? (s * 17 + 27) / 28 : -1; }
#define EROT 0
#define CLR_SLEEP 90
#define ENL 4
#define HOP3_POLLER 1
DI constexpr int d_prio(int n) { return n < 0 ? -1 : n < 27 ? 3 - n / 7 : n < 64 ? 3 - (n - 27) / 10 : n < 84 ? 3 - (n - 64) / 5 : n < 112 ? 3 - (n - 84) / 7 : 0; }
DI constexpr int phys(int t) { return ((t % 9) + 2) % 9 * 16384; }

__global__ NOPK __launch_bounds__(512, 2) void block_fwd(const char* x, const char* wqkv, const char* wo, const char* wg, const char* wu, const char* wd, char* out, char* ws) {
    extern __shared__ __attribute__((aligned(16))) char smem[];
    lds_ptr lds = (lds_ptr)smem;
    const int tid = __builtin_amdgcn_workitem_id_x(), lane = tid & 63, w = __builtin_amdgcn_readfirstlane(tid >> 6);
    const int bid = __builtin_amdgcn_workgroup_id_x(), xcd = bid & 7, loc = bid >> 3;
    const int r = lane & 31, hf = lane >> 5, q4 = (lane & 15) >> 2, g1 = (lane >> 4) & 1, p4 = lane & 3;
    const unsigned lds0 = (unsigned)(uintptr_t)smem;
    const unsigned l16 = lane * 16;
    TS(0);
    const int sp3 = (loc + 8) >> 5;
    const int atb = (xcd >> 1) * 8 + (loc & 7) + sp3 * (xcd & 1) * 4, acg = (loc >> 3) + (1 - sp3) * (xcd & 1) * 4;
    const int row8 = lane >> 3;
    const rsrc_t rws = __builtin_amdgcn_make_buffer_rsrc((void*)ws, 0, WS_END, 0x00020000);
    u64 epoch, tagv;
    unsigned fl1;
    const unsigned ones = 0x3f803f80u;
    const bool ffn = loc < 28;
    const int l7 = loc / 7, m7 = loc % 7;
    const int tb = xcd * 4 + l7;
    const int tt = w & 1, ct = (w >> 1) & 1, kh = w >> 2;
    const unsigned swz = (unsigned)(((lane & 7) ^ (((row8 >> 1) & 1) << 2)) * 16);

    if (__builtin_expect(ffn, 1)) {
        constexpr int A_W = 57344, A_WS = 24576;
        const int rot8 = ((loc & 7) << sp3) + sp3 * (xcd & 1), rot = rot8 & (7 - ((rot8 + 1) >> 3) * 7);
#define A_KC(c) (rot + (c) >= 7 ? rot + (c) - 7 : rot + (c))
        {
            const int xr = (w & 3) * 8 + row8;
            const unsigned xo = (unsigned)(row8 * 896 + (((lane & 7) ^ ((xr >> 1) & 7)) * 16));
            const char* xs = x + (size_t)(atb * 64 + w * 8) * 896;
            unsigned xd = lds0 + kh * 28672 + (w & 3) * 1024, xk = rot * 128;
#pragma unroll
            for (int c = 0; c < 7; ++c) { glds(xo, xs + xk, xd + c * 4096); xk = xk == 768 ? 0 : xk + 128; }
            SB();
        }
        const unsigned wvo = (unsigned)(row8 * 2688) + swz;
        const char* wsrc = wqkv + acg * 128 + (size_t)w * 8 * 2688;
        const unsigned wdst = lds0 + A_W + w * 1024;
#define A_PIECE(c, i) glds(wvo, wsrc + A_KC(c) * (64 * 2688) + (i) * 896, wdst + ((c) & 3) * A_WS + (i) * 8192)
#define A_GROUP(c) do { A_PIECE(c, 1); A_PIECE(c, 2); A_PIECE(c, 0); } while (0)
        epoch = peek_flag(ws, FL_EPOCH * 128); fl1 = (FL_KV + atb * 8 + acg) * 128;
        asm volatile("s_barrier" ::: "memory");
        A_GROUP(0);
        lds_ptr xb[4], wb1, wb1h;
#pragma unroll
        for (int j = 0; j < 4; ++j) xb[j] = lds + tt * 28672 + (r >> 3) * 1024 + (r & 7) * 128 + (((2 * j + hf) ^ ((r >> 1) & 7)) * 16);
        wb1 = opq(lds + A_W + (kh ? 0 : 8192) + (8 * hf + q4) * 128 + ((ct ^ (q4 >> 1)) * 64) + g1 * 32 + p4 * 8);
        wb1h = opq(wb1 + 2 * A_WS);
        unsigned go1[2], go2[2]; int gi[2];
#pragma unroll
        for (int p = 0; p < 2; ++p) {
            const int G = acg * 8 + ct * 4 + 2 * p + hf, hp = G / 7, i = G - hp * 7;
            const unsigned hs = kh ? 131072u : 262144u;
            const unsigned to = (kh ? WS_QF + (atb * 2 + tt) * 2048 : WS_KV + (atb * 2 + tt) * 4096) + r * 16;
            go1[p] = (unsigned)(2 * hp + (i > 3)) * hs + (unsigned)(i > 3 ? i - 3 : i) * 512 + to;
            go2[p] = i == 3 ? (unsigned)(2 * hp + 1) * hs + to : OOB;
            gi[p] = i;
        }
        const int vcol = acg * 64 + ct * 32 + r, vh = vcol / 28, vd = vcol - vh * 28;
        unsigned vo = WS_KV + (atb * 2 + tt) * 4096 + (unsigned)vh * 262144 + 2048 + hf * 512 + vd * 16;
        PIN2(go1[0], go1[1]); PIN2(go2[0], go2[1]); PIN(vo);
        lds_ptr xw = opq(lds + kh * 28672 + (w & 3) * 1024 + l16);
        waitv<3>();
        TS(28);
        u32x4 xv[7];
#pragma unroll
        for (int c = 0; c < 7; ++c) xv[c] = *(LAS u32x4*)(xw + c * 4096);
        SB();
        f32x4 sm = {};
#pragma unroll
        for (int c = 0; c < 7; ++c)
#pragma unroll
            for (int j = 0; j < 4; ++j) sm[j] = dot2(xv[c][j], ones, sm[j]);
        float nm = sum8((sm[0] + sm[1]) + (sm[2] + sm[3])) * (-1.f / D);
        PIN(nm);
#pragma unroll
        for (int c = 0; c < 7; ++c) { xv[c] = shift8(xv[c], nm); *(LAS u32x4*)(xw + c * 4096) = xv[c]; }
        A_GROUP(1);
        SB();
        sm = (f32x4){};
#pragma unroll
        for (int c = 0; c < 7; ++c)
#pragma unroll
            for (int j = 0; j < 4; ++j) sm[j] = dot2(xv[c][j], xv[c][j], sm[j]);
        LAS float* rst = (LAS float*)(lds + 155648);
        rst[w * 8 + row8] = __builtin_amdgcn_rsqf(sum8((sm[0] + sm[1]) + (sm[2] + sm[3])) * (1.f / D) + EPS);
        A_GROUP(2);
        TS(31);
        bf16x8 fx[4], f1[4], f2[4];
#define A_WAIT(c) do { if ((c) == 0) waitv_bar<6>(); else if ((c) == 1) waitv_barx<6>(); else if ((c) < 6) waitv_barx<3>(); else waitv_barx<0>(); \
                       if ((c) == 0) TS(1); if ((c) == 6) TS(2); } while (0)
#define A_WP(c, ks) ((((c) & 2) ? wb1h : wb1) + ((c) & 1) * A_WS + (ks) * 2048)
        if (w < 4) {
            f32x16 ak = {}, av = {};
            f32x4 rv[4];
            float rs = 0.f;
#pragma unroll
            for (int n = 0; n < 30; ++n) {
                const int c = n >> 2, ks = n & 3, km = (n - 2) & 3;
                if (n < 28 && ks == 0) A_WAIT(c);
                if (n >= 2) { ak = MFMA(f1[km], fx[km], ak); PIN(ak); } SB();
                if (n < 28) { fx[ks] = lds8(xb[ks] + c * 4096); f1[ks] = cat(trd(A_WP(c, ks)), trd(A_WP(c, ks) + 512)); } SB();
                if (n >= 2) { av = MFMA(fx[km], f2[km], av); PIN(av); } SB();
                if (n < 28) f2[ks] = cat(trd(A_WP(c, ks) + 8192), trd(A_WP(c, ks) + 8192 + 512));
                if (c == 0 && ks < 3) A_PIECE(3, ks); if (c >= 2 && c < 5 && ks < 3) A_PIECE(c + 2, ks);
                if (n == 21) {
                    rs = rst[tt * 32 + r];
#pragma unroll
                    for (int g = 0; g < 4; ++g) rv[g] = *(LAS f32x4*)(rst + tt * 32 + 8 * g + 4 * hf);
                }
                if (n == 24) { tagv = epoch + 1; PIN(tagv); }
                if (n == 24) {
                    const unsigned pv = vd == 0 ? ones : 0u, po = vd < 4 ? vo + 448 : OOB;
                    st16_dev((u32x4){pv, pv, pv, pv}, rws, po, 0); st16_dev((u32x4){pv, pv, pv, pv}, rws, po + 1024, 0);
                }
                SB();
            }
            TS(3);
#pragma unroll
            for (int i = 0; i < 16; ++i) { ak[i] *= rs; av[i] *= rv[i >> 2][i & 3]; }
#pragma unroll
            for (int p = 0; p < 2; ++p) {
                const u32x4 f = pack8(ak, p);
                const auto s0 = __builtin_amdgcn_permlane32_swap(f[0], f[2], false, false), s1 = __builtin_amdgcn_permlane32_swap(f[1], f[3], false, false);
                const u32x4 c = {s0[0], s1[0], s0[1], s1[1]};
                st16_dev(c, rws, go1[p], 0); st16_dev(c, rws, go2[p], 0);
            }
            st16_dev(pack8(av, 0), rws, vo, 0); st16_dev(pack8(av, 1), rws, vo + 1024, 0);
        } else {
            f32x16 aq = {};
            __builtin_amdgcn_s_setprio(1);
#pragma unroll
            for (int n = 0; n < 30; ++n) {
                const int c = n >> 2, ks = n & 3, km = (n - 2) & 3;
                if (n < 28 && ks == 0) A_WAIT(c);
                if (n == 24) { tagv = epoch + 1; PIN(tagv); }
                if (n >= 2) { aq = MFMA(f1[km], fx[km], aq); PIN(aq); } SB();
                if (n < 28) { fx[ks] = lds8(xb[ks] + c * 4096); f1[ks] = cat(trd(A_WP(c, ks)), trd(A_WP(c, ks) + 512)); }
                if (c == 0 && ks < 3) A_PIECE(3, ks); if (c >= 2 && c < 5 && ks < 3) A_PIECE(c + 2, ks);
                SB();
            }
            __builtin_amdgcn_s_setprio(0);
            const float QS = rst[tt * 32 + r] * (0.18898223650461362f * 1.4426950408889634f);
#pragma unroll
            for (int i = 0; i < 16; ++i) aq[i] *= QS;
#pragma unroll
            for (int p = 0; p < 2; ++p) {
                const u32x4 f = pack8(aq, p);
                const auto s0 = __builtin_amdgcn_permlane32_swap(f[0], f[2], false, false), s1 = __builtin_amdgcn_permlane32_swap(f[1], f[3], false, false);
                const bool sp = gi[p] == 3;
                st16_dev((u32x4){s0[0], s1[0], sp ? 0u : s0[1], sp ? 0u : s1[1]}, rws, go1[p], 0);
                st16_dev((u32x4){0u, 0u, s0[1], s1[1]}, rws, go2[p], 0);
            }
        }
    } else {
        epoch = peek_flag(ws, FL_EPOCH * 128); fl1 = (FL_RT + xcd * 4 + loc - 28) * 128;
        const int ri = xcd * 4 + loc - 28, nu = (w == 0 && ri < 16) ? 4 : 3;
        const unsigned vo = (unsigned)(row8 * 3584 + (lane & 7) * 16);
#pragma unroll 1
        for (int i = 0; i < nu; ++i) {
            const int u = i < 3 ? i * 256 + ri * 8 + w : 768 + ri;
            const size_t so = (size_t)(u / 28) * 16 * 3584 + (u % 28) * 128;
            const unsigned d_ = lds0 + w * 16384 + i * 4096;
            glds(vo, wg + so, d_); glds(vo, wg + so + 8 * 3584, d_ + 1024); glds(vo, wu + so, d_ + 2048); glds(vo, wu + so + 8 * 3584, d_ + 3072);
        }
#pragma unroll 1
        for (int i = 0; i < nu; ++i) {
            const int u = i < 3 ? i * 256 + ri * 8 + w : 768 + ri;
            waitv<8>();
            lds_ptr sg = lds + w * 16384 + i * 4096 + g1 * 2048 + (4 * hf + q4) * 128 + p4 * 8;
#pragma unroll
            for (int e = 0; e < 4; ++e) st16_dev(U4(cat(trd(sg + e * 32), trd(sg + e * 32 + 1024))), rws, l16, WS_WGU + (unsigned)(((u % 28) * 4 + e) * 28 + u / 28) * 1024);
            FENCE();
        }
        tagv = epoch + 1;
    }
    TS(24);
    const int head = xcd * 2 + (loc & 1), rb = loc >> 1, qt = w & 3;
    const char* kvs = ws + WS_KV + (size_t)head * 262144 + w * 2048;
    const char* qs = ws + WS_QF + (size_t)(head * 64 + rb * 4 + qt) * 2048 + kh * 1024;
    unsigned kvd = lds0 + w * 2048, qd = lds0 + 147456 + qt * 2048 + kh * 1024;
    unsigned pf1 = (FL_KV + (lane >> 1) * 8 + (head * 28 + (lane & 1) * 27) / 64) * 128;
    PINS(kvs); PINS(qs); PINS(kvd); PINS(qd); PIN2(pf1, fl1);
    waitv_bar<0>();
    if (w == 0) set_flag(ws, fl1, tagv);
    const u64 tag = ((u64)(unsigned)__builtin_amdgcn_readfirstlane((unsigned)(tagv >> 32)) << 32 | (u64)(unsigned)__builtin_amdgcn_readfirstlane((unsigned)tagv));
    TS(4);

    const int drb = (xcd >> 1) * 4 + l7, dfg = (xcd & 1) * 7 + m7;
    bf16x8 wf[28];
    u32x2 xa, xb;
    f32x16 cs = {}, acc = {};
    const u32x4 one4 = {ones, ones, ones, ones};
    const unsigned hi_ = (unsigned)((tb * 2 + tt) * 28 + m7 * 4 + ct * 2 + kh), ofo = (unsigned)(tt * 32768 + kh * 16384 + ct * 4096);
    unsigned ho = WS_HF + hi_ * 1024 + l16, so = hf ? OOB : WS_HS + hi_ * 256 + r * 8;
    const char* os = ws + WS_OF + (size_t)tb * 65536 + ofo;
    unsigned od = lds0 + 65536 + ofo;
    unsigned pf2 = (FL_O + (tb >> 1) * 16 + kh * 8 + ct * 2 + ((lane >> 1) & 1) + (lane & 4)) * 128 + ((lane & 1) * 4 + (tb & 1) * 2 + tt) * 8;
    unsigned go = ffn ? WS_GF + (unsigned)(drb * 4 * 112 + dfg * 8 + w) * 1024 + l16 : OOB;
    unsigned fo3 = (FL_H + tb * 8 + m7) * 128;
    {
        lds_ptr ap = opq(lds + kh * 8192 + l16), apB = opq(lds + 65536 + kh * 8192 + l16), apC = opq(lds + 131072 + kh * 8192 + l16);
        const unsigned lv = kh * 8192 + (l16 ^ (kh * 256));
        lds_ptr vp = opq(lds + lv), vpB = opq(lds + 65536 + lv), vpC = opq(lds + 131072 + lv);
#define AP(off) ((off) < 65536 ? ap + (off) : (off) < 131072 ? apB + ((off) - 65536) : apC + ((off) - 131072))
#define VP(off) ((off) < 65536 ? vp + (off) : (off) < 131072 ? vpB + ((off) - 65536) : vpC + ((off) - 131072))
#define KV_ISSUE(t) glds2_dev(l16, kvs + (t) * 16384, kvd + phys(t))
        unsigned wgo = WS_WGU + (unsigned)((ffn ? dfg : 0) * 8 + w) * 28672;
        const int ri = umin(xcd * 4 + loc - 28, 27);
        const char* wos = (ffn ? wo + m7 * 128 : wd + (ri % 7) * 128 + (size_t)(ri / 7) * 448 * 896) + (size_t)w * 56 * 896;
        unsigned wov = (unsigned)(row8 * 896) + swz;
        const rsrc_t rx = __builtin_amdgcn_make_buffer_rsrc((void*)x, 0, 2048 * 448 * 2, 0x00020000);
        unsigned xo_ = (unsigned)(((tb * 2 + tt) * 32 + r) * 896 + m7 * 128 + ct * 64 + kh * 32 + hf * 8);
        unsigned rto = (FL_RT + r) * 128;
        lds_ptr qp = opq(lds + 147456 + qt * 2048 + l16);
        PINS(wgo); PINS(wos); PIN2(wov, xo_); PIN(rto); PIN2(ho, so); PIN2(pf2, fo3); PIN(go);
        lds_ptr xw = opq(lds + 147456 + w * 2048 + l16), xr = opq(lds + 147456 + (w ^ 4) * 2048 + l16);
        lds_ptr lw = opq(lds + 58368 + w * 256 + lane * 4), lr = opq(lds + 58368 + qt * 256 + 128 + r * 4);
        lds_ptr bw = opq(lds + 60416 + w * 4), br = opq(lds + 60416 + (lane & 7) * 4);
        unsigned oo = WS_OF + (unsigned)((rb * 4 + qt) * 32 + head * 2 + kh) * 1024 + l16;
        unsigned fo2 = (FL_O + rb * 16 + head) * 128 + w * 8;
        PIN2(oo, fo2);
        if (w == HOP3_POLLER) poll_flag(ws, pf1, tag);
        BAR();
        TS(5);
        glds_dev(l16, qs, qd);
        KV_ISSUE(0); KV_ISSUE(1);
        asm volatile("s_barrier" ::: "memory");
        KV_ISSUE(2); KV_ISSUE(3);
        waitv_bar<4>();
        TS(6);
        const bf16x8 q0 = lds8(qp), q1 = lds8(qp + 1024);
        f32x16 o = {}, sa = {}, sb;
        bf16x8 k0 = lds8(ap + phys(0)), k1 = lds8(ap + phys(0) + 1024), v0 = lds8(vp + phys(0) + 2048), v1 = {};
        u32x4 p0, p1 = {};
        sa = MFMA(k0, q0, sa); sa = MFMA(k1, q1, sa);
        k0 = lds8(ap + phys(0) + 4096); k1 = lds8(ap + phys(0) + 5120);
        PIN(sa); SB();
        u64 rtf = 0;
#define EXP4(S, o4) _Pragma("unroll") for (int i_ = o4; i_ < o4 + 4; ++i_) S[i_] = __builtin_amdgcn_exp2f(S[i_]);
#define WLD(s) wf[s] = BF8(ld16_dev(rws, l16 + ((s) & 3) * 1024, wgo + ((s) >> 2) * 4096))
#define SIDE(t, g) do { \
            if ((t) == 0 && (g) == 0) KV_ISSUE(4); if ((t) == 0 && (g) == 2) KV_ISSUE(5); if ((t) == 1 && (g) == 0) KV_ISSUE(6); if ((t) == 1 && (g) == 2) KV_ISSUE(7); \
            if ((t) == 2 && (g) == 0) KV_ISSUE(8); if ((t) == 1 && (g) == 3) rtf = peek_flag(ws, rto); \
            if ((t) == 3 && (g) < 3) KV_ISSUE(9 + (g)); \
            if ((t) == 7 && (g) == 0) KV_ISSUE(12); if ((t) == 7 && (g) == 2) KV_ISSUE(13); if ((t) == 8 && (g) == 0) KV_ISSUE(14); if ((t) == 8 && (g) == 2) KV_ISSUE(15); \
            if ((t) >= 4 && (t) <= 6) WLD(((t) - 4) * 4 + (g)); \
            if (((t) == 7 || (t) == 8) && ((g) & 1)) WLD(12 + ((t) - 7) * 2 + ((g) >> 1)); \
            if ((t) >= 9 && (t) <= 11) WLD(((t) - 5) * 4 + (g)); \
            if ((t) == 12 || ((t) == 13 && (g) < 3)) glds(wov, wos + (((t) - 12) * 4 + (g)) * 8 * 896, lds0 + (w * 7 + ((t) - 12) * 4 + (g)) * 1024); \
            if ((t) == 13 && (g) == 3) { xa = ld8(rx, xo_, 0); xb = ld8(rx, xo_, 16); } \
            if ((t) == 14 && (g) == 1) { if (w >= 6) touch(lane * (w == 6 ? (WS_END / 64 & ~127u) : 28672), w == 6 ? ws : out, lds0 + 61440); } \
            if ((t) == 14 && (g) == 0) { if (tid < 32) *(LAS u32x4*)(lds + 57344 + tid * 16) = (u32x4){0, 0, 0, 0}; } \
        } while (0)
#define TOFF(i) (phys((i) >> 1) + ((i) & 1) * 4096)
#define TILE(i, SC, SN) do { \
            __builtin_amdgcn_s_setprio(((i) & 1) ? 1 : 3); \
            if ((i) < 31) { f32x16 z_ = {}; SN = MFMA(k0, q0, z_); PIN(SN); } if ((i) < 30) k0 = lds8(AP(TOFF((i) + 2))); SB(); \
            EXP4(SC, 0); PIN(SC); SB(); \
            o = MFMA(v1, BF8(p1), o); PIN(o); v1 = lds8(VP(TOFF(i) + 3072)); SIDE((i) >> 1, ((i) & 1) * 2); SB(); \
            EXP4(SC, 4); p0 = pack8(SC, 0); PIN(p0); SB(); \
            __builtin_amdgcn_s_setprio(((i) & 1) ? 0 : 2); \
            if ((i) < 31) { SN = MFMA(k1, q1, SN); PIN(SN); } if ((i) < 30) k1 = lds8(AP(TOFF((i) + 2) + 1024)); SB(); \
            EXP4(SC, 8); PIN(SC); SB(); \
            o = MFMA(v0, BF8(p0), o); PIN(o); if ((i) < 31) v0 = lds8(VP(TOFF((i) + 1) + 2048)); SIDE((i) >> 1, ((i) & 1) * 2 + 1); SB(); \
            EXP4(SC, 12); p1 = pack8(SC, 1); PIN(p1); SB(); } while (0)
#define STEP(t) do { TILE(2 * (t), sa, sb); TILE(2 * (t) + 1, sb, sa); } while (0)
        STEP(0);
        waitv_barx<4>();
        STEP(1); STEP(2);
        waitv_bar2<3>();
        TS(21);
        if (__builtin_expect(!wave_all(rtf == tag), 0)) poll_flag(ws, rto, tag);
        FENCE();
        STEP(3); STEP(4); STEP(5); STEP(6);
        waitv_bar2<12>();
        TS(22);
        STEP(7); STEP(8); STEP(9); STEP(10);
        waitv_bar2<9>();
        TS(23);
        STEP(11); STEP(12); STEP(13); STEP(14); STEP(15);
        o = MFMA(v1, BF8(p1), o);
        TS(7);
        u32x4 ov;
        {
            *(LAS f32x4*)xw = (f32x4){o[8], o[9], o[10], o[11]}; *(LAS f32x4*)(xw + 1024) = (f32x4){o[12], o[13], o[14], o[15]};
            const float l = kh ? o[4] : o[12];
            *(LAS float*)lw = l;
            float sa_ = 0.f;
#pragma unroll
            for (int i = 0; i < 16; ++i) sa_ += __builtin_fabsf(o[i]);
            const bool ok = sa_ < 0x1p100f && (hf == 0 || l > 0x1p-80f);
            *(LAS unsigned*)bw = wave_all(ok) ? 0u : 1u;
            BAR();
            const f32x4 a = *(LAS f32x4*)xr, c = *(LAS f32x4*)(xr + 1024);
            const float inv = __builtin_amdgcn_rcpf(*(LAS float*)lr + *(LAS float*)(lr + 1024));
            const unsigned bad = *(LAS unsigned*)br;
            const float iz = (kh && hf) ? 0.f : inv;
            ov = (u32x4){cvtpk((o[0] + a[0]) * inv, (o[1] + a[1]) * inv), cvtpk((o[2] + a[2]) * inv, (o[3] + a[3]) * inv),
                         cvtpk((o[4] + c[0]) * iz, (o[5] + c[1]) * iz), cvtpk((o[6] + c[2]) * iz, (o[7] + c[3]) * iz)};
            if (__builtin_expect(wave_any(bad != 0), 0)) {
                f32x16 of = {};
                float m = -0x1p126f;
#pragma unroll 1
                for (int t = 0; t < 16; ++t) {
                    BAR();
                    glds2_dev(l16, kvs + t * 16384, lds0 + 65536 + w * 2048);
                    waitv_bar<0>();
#pragma unroll 1
                    for (int u = 0; u < 2; ++u) {
                        f32x16 s = {};
                        s = MFMA(lds8(apB + u * 4096), q0, s); s = MFMA(lds8(apB + u * 4096 + 1024), q1, s);
                        float mx = s[0];
#pragma unroll
                        for (int i = 1; i < 16; ++i) mx = __builtin_fmaxf(mx, s[i]);
                        mx = __builtin_fmaxf(swap_max(mx), m);
                        const float alpha = __builtin_amdgcn_exp2f(m - mx);
                        m = mx;
#pragma unroll
                        for (int i = 0; i < 16; ++i) { s[i] = __builtin_amdgcn_exp2f(s[i] - mx); of[i] *= alpha; }
                        of = MFMA(lds8(vpB + u * 4096 + 2048), BF8(pack8(s, 0)), of);
                        of = MFMA(lds8(vpB + u * 4096 + 3072), BF8(pack8(s, 1)), of);
                    }
                }
                *(LAS f32x4*)xw = (f32x4){of[8], of[9], of[10], of[11]}; *(LAS f32x4*)(xw + 1024) = (f32x4){of[12], of[13], of[14], of[15]};
                *(LAS float*)lw = hf ? (kh ? of[4] : of[12]) : m;
                BAR();
                const f32x4 a2 = *(LAS f32x4*)xr, c2 = *(LAS f32x4*)(xr + 1024);
                const float m0 = *(LAS float*)(lr - 128), m1 = *(LAS float*)(lr + 1024 - 128), mm = __builtin_fmaxf(m0, m1);
                const float e0 = __builtin_amdgcn_exp2f(m0 - mm), e1 = __builtin_amdgcn_exp2f(m1 - mm);
                const float iv = __builtin_amdgcn_rcpf(*(LAS float*)lr * e0 + *(LAS float*)(lr + 1024) * e1);
                const float im = (kh ? e1 : e0) * iv, io = (kh ? e0 : e1) * iv;
                const bool z = kh && hf;
                ov = (u32x4){cvtpk(of[0] * im + a2[0] * io, of[1] * im + a2[1] * io), cvtpk(of[2] * im + a2[2] * io, of[3] * im + a2[3] * io),
                             z ? 0u : cvtpk(of[4] * im + c2[0] * io, of[5] * im + c2[1] * io), z ? 0u : cvtpk(of[6] * im + c2[2] * io, of[7] * im + c2[3] * io)};
            }
        }
        st16_dev(ov, rws, oo, 0);
        TS(25);
        FENCE();
        PIN2(xa, xb);
#pragma unroll
        for (int s = 27; s >= 0; --s) PIN(wf[s]);
#pragma unroll
        for (int s = 0; s < 8; ++s) cs = MFMA(wf[s], BF8(one4), cs);
        acc[0] = bflo(xa[0]); acc[1] = bfhi(xa[0]); acc[2] = bflo(xa[1]); acc[3] = bfhi(xa[1]); acc[4] = bflo(xb[0]); acc[5] = bfhi(xb[0]); acc[6] = bflo(xb[1]); acc[7] = bfhi(xb[1]);
        PIN(acc); PIN2(ho, so); PIN2(pf2, fo3); PINS(os); PINS(od); PIN(cs);
        waitv<0>();
        set_flag(ws, fo2, tagv);
        BAR();
        TS(8);
    }
    if (__builtin_expect(!ffn, 0)) {
        const int ri = xcd * 4 + loc - 28;
        if (ri < 28) {
#pragma unroll 1
            for (int u = w; u < 28; u += 8) {
                lds_ptr sd = lds + u * 2048 + (4 * hf + q4) * 128 + g1 * 32 + p4 * 8;
#pragma unroll
                for (int e = 0; e < 2; ++e) { lds_ptr p_ = sd + ((e ^ (q4 >> 1)) * 64);
                    st16_dev(U4(cat(trd(p_), trd(p_ + 1024))), rws, l16, WS_WD + (unsigned)(((ri % 7) * 112 + (ri / 7) * 28 + u) * 2 + e) * 1024); }
            }
        }
        waitv_bar<0>();
        if (w == 0) set_flag(ws, (FL_RD + ri) * 128, tagv);
        __builtin_amdgcn_s_sleep(CLR_SLEEP);
#pragma unroll 1
        for (int i = 0; i < 14; ++i) st16_dev((u32x4){~0u, ~0u, ~0u, ~0u}, rws, l16, WS_GF + (unsigned)(((ri >> 1) * 4 + 2 + (ri & 1)) * 112 + w * 14 + i) * 1024);
        waitv_bar<0>();
        if (w == 0) set_flag(ws, (FL_CL + ri) * 128, tagv);
        return;
    }

    const int erb = tb >> 1;
    const char* hs = ws + WS_HF + (size_t)drb * 114688 + w * 1024;
    const char* hss = ws + WS_HS + (size_t)((drb * 4 + (w & 3)) * 28 + kh * 14) * 256;
    unsigned pf3, fo4 = (FL_G + drb * 64 + dfg) * 128 + w * 8;
    {
        lds_ptr wb = lds + kh * (8 * 28 * 128) + (4 * hf + q4) * 128 + ((ct ^ (q4 >> 1)) * 64) + (g1 ^ kh) * 32 + p4 * 8;
        bf16x8 wr[16];
#pragma unroll
        for (int s = 0; s < 12; ++s) { const int k0 = ((s >> 1) * 28 + (s & 1) * 16) * 128; wr[s] = cat(trd(wb + k0), trd(wb + k0 + 1024)); }
        lds_ptr cw = opq(lds + 131072 + w * 2048 + l16), cr = opq(lds + 131072 + (w ^ 4) * 2048 + l16);
        poll_flag(ws, pf2, tag);
        TS(9);
        glds4_dev(l16, os, od);
        asm volatile("s_barrier" ::: "memory");
        glds4_dev(l16, os + 8192, od + 8192);
        lds_ptr ob = opq(lds + 65536 + tt * 32768 + kh * 16384 + l16);
        bf16x8 of[8];
        waitv_bar<4>();
        TS(26);
#pragma unroll
        for (int s = 0; s < 8; ++s) of[s] = lds8(ob + s * 1024);
        SB();
#pragma unroll
        for (int s = 0; s < 8; ++s) acc = MFMA(wr[s], of[s], acc);
        PIN(acc); SB();
#pragma unroll
        for (int s = 12; s < 16; ++s) { const int k0 = ((s >> 1) * 28 + (s & 1) * 16) * 128; wr[s] = cat(trd(wb + k0), trd(wb + k0 + 1024)); }
        waitv_bar<0>();
#pragma unroll
        for (int s = 0; s < 8; ++s) of[s] = lds8(ob + (8 + s) * 1024);
        SB();
#pragma unroll
        for (int s = 0; s < 8; ++s) acc = MFMA(wr[8 + s], of[s], acc);
        TS(10);
        *(LAS f32x4*)cw = (f32x4){acc[8], acc[9], acc[10], acc[11]}; *(LAS f32x4*)(cw + 1024) = (f32x4){acc[12], acc[13], acc[14], acc[15]};
        BAR();
        const f32x4 a = *(LAS f32x4*)cr + (f32x4){acc[0], acc[1], acc[2], acc[3]}, b = *(LAS f32x4*)(cr + 1024) + (f32x4){acc[4], acc[5], acc[6], acc[7]};
        const u32x4 hv = {cvtpk(a[0], a[1]), cvtpk(a[2], a[3]), cvtpk(b[0], b[1]), cvtpk(b[2], b[3])};
        st16_dev(hv, rws, ho, 0);
        float s1 = 0.f, s2 = 0.f;
        acc_stats(hv, s1, s2);
        s1 = swap_add(s1); s2 = swap_add(s2);
        st8_dev((u32x2){f2u(s1), f2u(s2)}, rws, so, 0);
        FENCE();
#pragma unroll
        for (int s = 8; s < 14; ++s) cs = MFMA(wf[s], BF8(one4), cs);
        pf3 = (FL_H + drb * 16 + (lane & 8) + umin(lane & 7, 6)) * 128;
        PIN(go); PIN2(pf3, fo4); PINS(hs); PINS(hss); PIN(cs);
        waitv_bar<0>();
        if (w == 0) set_flag(ws, fo3, tagv);
        if (bid == 0 && w == 0) set_flag(ws, FL_EPOCH * 128, tagv);
        TS(11);
    }

    const unsigned wdo = WS_WD + (unsigned)(m7 * 112 + w * 14) * 2048;
    bf16x8 wdf[28];
    bf16x8 gf[8];
    const unsigned erot = EROT * m7;
#define E_KS(s) (erot + (s) >= 14 ? erot + (s) - 14 : erot + (s))
    const int ta = erb * 4 + (tb & 1);
    const unsigned gxo = WS_GF + (unsigned)(ta * 112 + w * 14) * 1024;
    u64 gfl = 0;
    const unsigned gks = w * 14 + umin(lane & 15, 13), gfi = (FL_G + (erb * 4 + (tb & 1)) * 16 + (gks >> 3)) * 128 + (gks & 7) * 8;
    {
#pragma unroll
        for (int s = 14; s < 28; ++s) cs = MFMA(wf[s], BF8(one4), cs);
        lds_ptr csp = opq(lds + 161792 + w * 128 + hf * 64);
        if (r == 0) {
#pragma unroll
            for (int g = 0; g < 4; ++g) *(LAS f32x4*)(csp + g * 16) = (f32x4){cs[4 * g], cs[4 * g + 1], cs[4 * g + 2], cs[4 * g + 3]};
        }
        const unsigned hd = lds0 + w * 1024;
        const char* wds = ws + wdo;
        lds_ptr hb = opq(lds + l16), hb2 = opq(lds + 57344 + l16);
        lds_ptr wl = opq(lds + 114688 + w * 5120 + l16), wl2 = opq(lds + w * 7168 + l16);
        LAS f32x2* stp = (LAS f32x2*)opq(lds + 159744 + r * 8);
        if (w == HOP3_POLLER) poll_flag(ws, pf3, tag);
        BAR();
        TS(12);
#define H_PIECE(j) glds_dev(l16, hs + (j) * 8192, hd + (j) * 8192)
#define WDO(s) (E_KS((s) >> 1) * 2048 + ((s) & 1) * 1024)
#define WDLD(s) wdf[s] = BF8(ld16_dev(rws, l16, wdo + WDO(s)))
#define HB(n) ((n) < 56 ? hb + (n) * 1024 : hb2 + ((n) - 56) * 1024)
        H_PIECE(0);
        asm volatile("s_barrier" ::: "memory");
        H_PIECE(1); H_PIECE(2); H_PIECE(3);
        constexpr int PF = 5;
        bf16x8 bf[PF + 1];
        f32x16 acc[2];
        f32x2 sp[7], sv0, sv1;
        f32x4 c4[4];
        float mean = 0.f, r2 = 0.f, hp = 0.f;
        u32x4 hv;
        u64 rdf = 0;
        const unsigned rdo = (FL_RD + (w >> 1) * 7 + m7) * 128, clo = (FL_CL + (hf ? tb : drb * 2 + (lane & 1))) * 128;
        waitv_barx<3>();
        TS(27);
#pragma unroll
        for (int n = 0; n < PF; ++n) bf[n] = lds8(HB(n));
        SB();
        sfor<0, 125>([&](auto N_) __attribute__((always_inline)) {
            constexpr int n = decltype(N_)::v, p = n / 28, s = n % 28;
            if constexpr (n == 3) waitv_barx<3>();
            if constexpr (n == 11) waitv_barx<7>();
            if constexpr (n == 27) { waitv_barx<3>(); TS(13); }
            if constexpr (n == 64) { waitv_barx<10>(); set_flag(ws, fo4, tagv); TS(14); }
            if constexpr (n == 81) { waitv<7>(); set_flag(ws, fo4 + 2048, tagv); }
            if constexpr (n == 39) { if (__builtin_expect(!wave_all(rdf == tag), 0)) poll_flag(ws, rdo, tag); FENCE(); }
            if constexpr (d_prio(n) != d_prio(n - 1)) __builtin_amdgcn_s_setprio(d_prio(n));
            if constexpr (n < 112) { if constexpr (s == 0) { f32x16 z = {}; acc[p & 1] = MFMA(wf[s], bf[n % (PF + 1)], z); } else acc[p & 1] = MFMA(wf[s], bf[n % (PF + 1)], acc[p & 1]); PIN(acc[p & 1]); }
            SB();
            if constexpr (n + PF < 112) bf[(n + PF) % (PF + 1)] = lds8(HB(n + PF));
            if constexpr (n % 3 == 2 && n < 30) H_PIECE(4 + n / 3);
            if constexpr (n == 4) glds4_dev(l16, hss, lds0 + 114688 + w * 5120);
            if constexpr (n == 16) { waitv<4>();
#pragma unroll
                for (int j = 0; j < 7; ++j) sp[j] = *(LAS f32x2*)(lds + 114688 + w * 5120 + (hf * 7 + j) * 256 + r * 8);
            }
            if constexpr (n == 20) { const f32x2 t = ((sp[0] + sp[1]) + (sp[2] + sp[3])) + ((sp[4] + sp[5]) + sp[6]); stp[w * 32] = (f32x2){swap_add(t[0]), swap_add(t[1])}; }
            if constexpr (n >= 39 && n < 48 && n % 2 == 1) glds_dev(l16, wds + WDO((n - 39) / 2), lds0 + 114688 + w * 5120 + (n - 39) / 2 * 1024);
            if constexpr (n >= 48 && n < 64 && n % 3 == 0) WDLD(12 + (n - 48) / 3);
            if constexpr (n >= 70 && n < 84 && n % 2 == 0) glds_dev(l16, wds + WDO(5 + (n - 70) / 2), lds0 + w * 7168 + (n - 70) / 2 * 1024);
            if constexpr (n >= 85 && n < 105 && n % 2 == 1) WDLD(18 + (n - 85) / 2);
            if constexpr (n == 106) { waitv<4>(); set_flag(ws, fo4 + 4096, tagv); }
            if constexpr (n == 107) gfl = peek_flag(ws, gfi);
            if constexpr (n == 30) rdf = peek_flag(ws, rdo);
            if constexpr (n == 79) rdf = peek_flag(ws, clo);
            if constexpr (n == 95) { if (__builtin_expect(!wave_all(rdf == tag), 0)) poll_flag(ws, clo, tag); FENCE(); }
            if constexpr (p >= 1) {
                const f32x16& a = acc[(p - 1) & 1];
                if constexpr (n == 100) { sv0 = stp[3 * 32]; sv1 = stp[7 * 32]; }
                if constexpr (s == 0 && p < 4) { sv0 = stp[(p - 1) * 32]; sv1 = stp[(p + 3) * 32];
#pragma unroll
                    for (int g = 0; g < 4; ++g) c4[g] = *(LAS f32x4*)(csp + g * 16); }
                if constexpr ((s == 3 && p < 4) || n == 104) { const f32x2 sv = sv0 + sv1; mean = sv[0] * (1.f / D); r2 = __builtin_amdgcn_rcpf(__builtin_fmaxf(sv[1] * (1.f / D) - mean * mean, 0.f) + EPS); PIN2(mean, r2); }
                if constexpr (s >= 4 && s < 12) { constexpr int i = s - 4;
                    const float h0 = __builtin_fmaxf(a[i] - mean * c4[i >> 2][i & 3], 0.f) * r2 * (a[8 + i] - mean * c4[2 + (i >> 2)][i & 3]);
                    if constexpr (i & 1) { hv[i >> 1] = p >= 3 ? umin(cvtpk(hp, h0), ~1u) : cvtpk(hp, h0); PIN(hv); } else { hp = h0; PIN(hp); } }
                if constexpr (s == 12) { st16_dev(hv, rws, go + (p - 1) * 114688, 0); FENCE(); }
            }
            SB();
        });
        TS(15);
        if (__builtin_expect(!wave_all(gfl == tag), 0)) poll_flag(ws, gfi, tag);
        FENCE();
#pragma unroll
        for (int s = 0; s < 8; ++s) { gf[s] = BF8(ld16_dev(rws, l16, gxo + E_KS(s) * 1024)); SB(); }
        FENCE();
#pragma unroll
        for (int s = 0; s < 12; ++s) wdf[s] = lds8(s < 5 ? wl + s * 1024 : wl2 + (s - 5) * 1024);
    }

    {
        const int etok = tid >> 4, eblk = tid & 15;
        const unsigned hro = (unsigned)((((eblk >> 2) & 3) * 64 + (eblk & 1) * 32 + etok) * 16 + ((eblk >> 1) & 1) * 8) + WS_HF + (unsigned)(ta * 28 + m7 * 4) * 1024;
        u32x2 hres[2];
        GAS u64* op = (GAS u64*)(out + (size_t)(ta * 32 + etok) * 896 + m7 * 128 + eblk * 8);
        lds_ptr rp = opq(lds + w * 8448 + hf * 528 + r * 16), rq = opq(lds + eblk * 528 + etok * 16);
        lds_ptr rp2 = opq(lds + 67584 + w * 8448 + hf * 528 + r * 16), rq2 = opq(lds + 67584 + eblk * 528 + etok * 16);
#define G_LD(s) gf[(s) & 7] = BF8(ld16_dev(rws, l16, gxo + ((s) / 14) * 229376 + E_KS((s) % 14) * 1024))
#define G_PUT(RP) do { \
            _Pragma("unroll") for (int g = 0; g < 4; ++g) *(LAS f32x4*)(RP + g * 1056) = (f32x4){c0[4 * g], c0[4 * g + 1], c0[4 * g + 2], c0[4 * g + 3]}; \
            _Pragma("unroll") for (int g = 0; g < 4; ++g) *(LAS f32x4*)(RP + 4224 + g * 1056) = (f32x4){c1[4 * g], c1[4 * g + 1], c1[4 * g + 2], c1[4 * g + 3]}; } while (0)
#define G_OUT(hh, RQ) do { f32x4 pa[8]; \
            _Pragma("unroll") for (int ww = 0; ww < 8; ++ww) pa[ww] = *(LAS f32x4*)(RQ + ww * 8448); SB(); \
            const f32x4 a = ((pa[0] + pa[1]) + (pa[2] + pa[3])) + ((pa[4] + pa[5]) + (pa[6] + pa[7])); \
            const u32x2 ov = {cvtpk(a[0] + bflo(hres[hh][0]), a[1] + bfhi(hres[hh][0])), cvtpk(a[2] + bflo(hres[hh][1]), a[3] + bfhi(hres[hh][1]))}; \
            __hip_atomic_store(op + (hh) * (64 * 896 / 8), __builtin_bit_cast(u64, ov), __ATOMIC_RELAXED, __HIP_MEMORY_SCOPE_AGENT); } while (0)
        f32x16 c0 = {}, c1 = {};
        TS(16);
        unsigned seen = 0;
#pragma unroll
        for (int s = 0; s < 28; ++s) {
            if (s == 1) { hres[0] = ld8_dev(rws, hro, 0); hres[1] = ld8_dev(rws, hro, 57344); FENCE(); }
            if (s == 10) BAR();
            if (s == 14) { G_PUT(rp); c0 = (f32x16){}; c1 = (f32x16){}; TS(17); }
            if (s == 20) { waitv<8>(); set_flag(ws, fo4 + 6144, tagv); PIN2(hres[0], hres[1]); BAR(); G_OUT(0, rq); FENCE(); TS(18); }
            SB();
            c0 = MFMA(wdf[2 * (s % 14)], gf[s & 7], c0); PIN(c0); c1 = MFMA(wdf[2 * (s % 14) + 1], gf[s & 7], c1); PIN(c1);
            if (s >= 14) { const u32x4 u = U4(gf[s & 7]); seen = umax3(umax3(seen, u[0], u[1]), u[2], u[3]); PIN(seen); }
            SB();
            if (s < 20) G_LD(s + 8);
        }
        if (__builtin_expect(wave_any(seen == ~0u), 0)) {
            poll_flag(ws, gfi + 4096, tag);
            c0 = (f32x16){}; c1 = (f32x16){};
#pragma unroll
            for (int s = 14; s < 22; ++s) G_LD(s);
#pragma unroll
            for (int s = 14; s < 28; ++s) { c0 = MFMA(wdf[2 * (s % 14)], gf[s & 7], c0); c1 = MFMA(wdf[2 * (s % 14) + 1], gf[s & 7], c1); PIN2(c0, c1); SB(); if (s < 20) G_LD(s + 8); SB(); }
        }
        G_PUT(rp2);
        TS(19);
        BAR();
        G_OUT(1, rq2);
    }
    TS(20);
}

#define WS_OFF 0
extern "C" void kernel_launch(void* const* d_in, const int* in_sizes, int n_in, void* d_out, int out_size, void* d_ws, size_t ws_size, hipStream_t stream) {
    (void)hipFuncSetAttribute((const void*)block_fwd, hipFuncAttributeMaxDynamicSharedMemorySize, LDS_BYTES);
    block_fwd<<<256, 512, LDS_BYTES, stream>>>((const char*)d_in[0], (const char*)d_in[1], (const char*)d_in[2], (const char*)d_in[4], (const char*)d_in[5], (const char*)d_in[6], (char*)d_out, (char*)d_ws + WS_OFF);
}
```

```cpp
#define PROBE 6
#include <hip/hip_runtime.h>
#include <hip/hip_bf16.h>
#include <stdint.h>

typedef __attribute__((ext_vector_type(8))) short bf16x8;
typedef __attribute__((ext_vector_type(4))) short s16x4;
typedef __attribute__((ext_vector_type(2))) __bf16 bf16x2;
typedef __attribute__((ext_vector_type(16))) float f32x16;
typedef __attribute__((ext_vector_type(4))) float f32x4;
typedef __attribute__((ext_vector_type(2))) float f32x2;
typedef __attribute__((ext_vector_type(4))) unsigned u32x4;
typedef __attribute__((ext_vector_type(2))) unsigned u32x2;
typedef short v4i16_t __attribute__((ext_vector_type(4)));
typedef unsigned long long u64;
#define LAS __attribute__((address_space(3)))
#define GAS __attribute__((address_space(1)))
typedef LAS char* lds_ptr;
#if defined(__HIP_DEVICE_COMPILE__)
#define NOPK __attribute__((target("no-packed-fp32-ops")))
#else
#define NOPK
#endif
#define DI __device__ __forceinline__
#define MFMA(a, b, c) __builtin_amdgcn_mfma_f32_32x32x16_bf16(a, b, c, 0, 0, 0)
#define MFMA16(a, b, c) __builtin_amdgcn_mfma_f32_16x16x32_bf16(a, b, c, 0, 0, 0)
#define BF8(x) __builtin_bit_cast(bf16x8, x)
#define U4(x) __builtin_bit_cast(u32x4, x)

constexpr int D = 448;
constexpr float EPS = 1e-5f;
constexpr unsigned WS_FLAG = 0;
constexpr unsigned FL_EPOCH = 0, FL_KV = 1, FL_RT = FL_KV + 256, FL_RD = FL_RT + 32, FL_O = FL_RD + 32, FL_H = FL_O + 256, FL_G = FL_H + 256;
constexpr unsigned WS_KV = 1u << 20;
constexpr unsigned WS_QF = WS_KV + 4194304;
constexpr unsigned WS_OF = WS_QF + 2097152;
constexpr unsigned WS_HF = WS_OF + 2097152;
constexpr unsigned WS_GF = WS_HF + 64 * 28 * 1024;
constexpr unsigned WS_WGU = WS_GF + 64 * 112 * 1024;
constexpr unsigned WS_WD = WS_WGU + 112 * 28 * 1024;
constexpr unsigned WS_HS = WS_WD + 7 * 112 * 2 * 1024;
constexpr unsigned WS_END = WS_HS + 64 * 28 * 256 + 1024;
constexpr unsigned OOB = 0x40000000u;
constexpr int LDS_BYTES = 163840;

DI float u2f(unsigned u) { return __builtin_bit_cast(float, u); }
DI unsigned f2u(float f) { return __builtin_bit_cast(unsigned, f); }
DI unsigned umin(unsigned a, unsigned b) { return a < b ? a : b; }
DI bool wave_any(bool p) { return __builtin_amdgcn_ballot_w64(p) != 0; }
DI bool wave_all(bool p) { return __builtin_amdgcn_ballot_w64(!p) == 0; }
DI unsigned cvtpk(float lo, float hi) { return __builtin_bit_cast(unsigned, __builtin_convertvector((f32x2){lo, hi}, bf16x2)); }
DI float bflo(unsigned u) { return u2f(u << 16); }
DI float bfhi(unsigned u) { return u2f(u & 0xffff0000u); }
DI void glds(unsigned voff, const void* sbase, unsigned dst) {
    asm volatile("s_mov_b32 m0, %2\n\ts_nop 0\n\tglobal_load_lds_dwordx4 %0, %1" :: "v"(voff), "s"(sbase), "s"(dst) : "memory");
}
DI void glds_dev(unsigned voff, const void* sbase, unsigned dst) {
    asm volatile("s_mov_b32 m0, %2\n\ts_nop 0\n\tglobal_load_lds_dwordx4 %0, %1 sc1" :: "v"(voff), "s"(sbase), "s"(dst) : "memory");
}
DI void glds2_dev(unsigned voff, const void* sbase, unsigned dst) {
    asm volatile("s_mov_b32 m0, %2\n\ts_nop 0\n\tglobal_load_lds_dwordx4 %0, %1 sc1\n\tglobal_load_lds_dwordx4 %0, %1 offset:1024 sc1" :: "v"(voff), "s"(sbase), "s"(dst) : "memory");
}
DI void glds4_dev(unsigned voff, const void* sbase, unsigned dst) {
    asm volatile("s_mov_b32 m0, %2\n\ts_nop 0\n\tglobal_load_lds_dwordx4 %0, %1 sc1\n\tglobal_load_lds_dwordx4 %0, %1 offset:1024 sc1\n\t"
                 "global_load_lds_dwordx4 %0, %1 offset:2048 sc1\n\tglobal_load_lds_dwordx4 %0, %1 offset:3072 sc1" :: "v"(voff), "s"(sbase), "s"(dst) : "memory");
}
DI s16x4 trd(lds_ptr p) { return __builtin_bit_cast(s16x4, __builtin_amdgcn_ds_read_tr16_b64_v4i16((LAS v4i16_t*)p)); }
DI bf16x8 cat(s16x4 a, s16x4 b) { return __builtin_shufflevector(a, b, 0, 1, 2, 3, 4, 5, 6, 7); }
DI bf16x8 lds8(lds_ptr p) { return *(LAS bf16x8*)p; }
DI float swap_add(float v) { auto r = __builtin_amdgcn_permlane32_swap(f2u(v), f2u(v), false, false); return u2f(r[0]) + u2f(r[1]); }
DI float swap_max(float v) { auto r = __builtin_amdgcn_permlane32_swap(f2u(v), f2u(v), false, false); return __builtin_fmaxf(u2f(r[0]), u2f(r[1])); }
DI u32x4 pack8(const f32x16& a, int s) { return (u32x4){cvtpk(a[8 * s], a[8 * s + 1]), cvtpk(a[8 * s + 2], a[8 * s + 3]), cvtpk(a[8 * s + 4], a[8 * s + 5]), cvtpk(a[8 * s + 6], a[8 * s + 7])}; }
DI float dot2(unsigned a, unsigned b, float c) { return __builtin_amdgcn_fdot2_f32_bf16(__builtin_bit_cast(bf16x2, a), __builtin_bit_cast(bf16x2, b), c, false); }
DI void acc_stats(u32x4 u, float& s1, float& s2) {
#pragma unroll
    for (int i = 0; i < 4; ++i) { const float a = bflo(u[i]), b = bfhi(u[i]); s1 += a; s2 = __builtin_fmaf(a, a, s2); s1 += b; s2 = __builtin_fmaf(b, b, s2); }
}
DI void acc_stats2(unsigned u0, unsigned u1, f32x4& s) {
    const float a = bflo(u0), b = bfhi(u0), c = bflo(u1), d = bfhi(u1);
    s[0] += a; s[1] += b; s[2] = __builtin_fmaf(a, a, s[2]); s[3] = __builtin_fmaf(b, b, s[3]); s[0] += c; s[1] += d; s[2] = __builtin_fmaf(c, c, s[2]); s[3] = __builtin_fmaf(d, d, s[3]);
}
DI float sum8(float v) {
    v += u2f(__builtin_amdgcn_update_dpp(0u, f2u(v), 0xB1, 0xf, 0xf, true));
    v += u2f(__builtin_amdgcn_update_dpp(0u, f2u(v), 0x4E, 0xf, 0xf, true));
    v += u2f(__builtin_amdgcn_update_dpp(0u, f2u(v), 0x141, 0xf, 0xf, true));
    return v;
}
DI u32x4 shift8(u32x4 x, float c) {
    u32x4 o; float t0, t1, t2, t3, t4, t5, t6, t7;
    asm("v_dot2_f32_bf16 %4, %12, %16, %18\n\tv_dot2_f32_bf16 %5, %12, %17, %18\n\tv_dot2_f32_bf16 %6, %13, %16, %18\n\tv_dot2_f32_bf16 %7, %13, %17, %18\n\t"
        "v_dot2_f32_bf16 %8, %14, %16, %18\n\tv_dot2_f32_bf16 %9, %14, %17, %18\n\tv_dot2_f32_bf16 %10, %15, %16, %18\n\tv_dot2_f32_bf16 %11, %15, %17, %18\n\t"
        "v_cvt_pk_bf16_f32 %0, %4, %5\n\tv_cvt_pk_bf16_f32 %1, %6, %7\n\tv_cvt_pk_bf16_f32 %2, %8, %9\n\tv_cvt_pk_bf16_f32 %3, %10, %11"
        : "=&v"(o[0]), "=&v"(o[1]), "=&v"(o[2]), "=&v"(o[3]), "=&v"(t0), "=&v"(t1), "=&v"(t2), "=&v"(t3), "=&v"(t4), "=&v"(t5), "=&v"(t6), "=&v"(t7)
        : "v"(x[0]), "v"(x[1]), "v"(x[2]), "v"(x[3]), "s"(0x00003f80u), "s"(0x3f800000u), "v"(c));
    return o;
}
template <int N> struct IC { static constexpr int v = N; };
template <int B, int E, class F> DI void sfor(F&& f) { if constexpr (B < E) { f(IC<B>{}); sfor<B + 1, E>(f); } }
template <int N> DI void waitv() { asm volatile("s_waitcnt vmcnt(%0)" :: "i"(N) : "memory"); }
template <int N> DI void waitv_bar() { asm volatile("s_waitcnt vmcnt(%0) lgkmcnt(0)\n\ts_barrier" :: "i"(N) : "memory"); }
template <int N> DI void waitv_barx() { asm volatile("s_waitcnt vmcnt(%0)\n\ts_barrier" :: "i"(N) : "memory"); }
DI void touch(unsigned voff, const void* sbase, unsigned junk) {
    asm volatile("s_mov_b32 m0, %2\n\ts_nop 0\n\tglobal_load_lds_dword %0, %1 sc1" :: "v"(voff), "s"(sbase), "s"(junk) : "memory");
}
#define BAR() asm volatile("s_waitcnt lgkmcnt(0)\n\ts_barrier" ::: "memory")
#define FENCE() asm volatile("" ::: "memory")
typedef __amdgpu_buffer_rsrc_t rsrc_t;
DI u32x4 ld16(rsrc_t rs, unsigned voff, unsigned soff) { return U4(__builtin_amdgcn_raw_buffer_load_b128(rs, voff, soff, 0)); }
DI u32x4 ld16_dev(rsrc_t rs, unsigned voff, unsigned soff) { return U4(__builtin_amdgcn_raw_buffer_load_b128(rs, voff, soff, 16)); }
DI u32x2 ld8(rsrc_t rs, unsigned voff, unsigned soff) { return __builtin_bit_cast(u32x2, __builtin_amdgcn_raw_buffer_load_b64(rs, voff, soff, 0)); }
DI u32x2 ld8_dev(rsrc_t rs, unsigned voff, unsigned soff) { return __builtin_bit_cast(u32x2, __builtin_amdgcn_raw_buffer_load_b64(rs, voff, soff, 16)); }
DI void st16_dev(u32x4 v, rsrc_t rs, unsigned voff, unsigned soff) { __builtin_amdgcn_raw_buffer_store_b128(v, rs, voff + soff, 0, 16); }
DI void st8_dev(u32x2 v, rsrc_t rs, unsigned voff, unsigned soff) { __builtin_amdgcn_raw_buffer_store_b64(v, rs, voff, soff, 16); }
#define POLL_SLEEP 1
DI void set_flag(char* ws, unsigned fo, u64 tagv) {
    asm volatile("s_mov_b64 exec, 1\n\tglobal_store_dwordx2 %0, %1, %2 sc1\n\ts_mov_b64 exec, -1" :: "v"(fo), "v"(tagv), "s"(ws) : "memory");
}
DI u64 peek_flag(char* ws, unsigned fo) { return __hip_atomic_load((GAS u64*)(ws + fo), __ATOMIC_RELAXED, __HIP_MEMORY_SCOPE_AGENT); }
DI void poll_flag(char* ws, unsigned fo, u64 tag) {
#pragma unroll 1
    for (int spin = 0; spin < (1 << 16); ++spin) {
        if (wave_all(peek_flag(ws, fo) == tag)) break;
        __builtin_amdgcn_s_sleep(POLL_SLEEP);
    }
    FENCE();
}
#define SB() __builtin_amdgcn_sched_barrier(0)
#define PINS(a) asm volatile("" : "+s"(a))
#define PIN(a) asm volatile("" : "+v"(a))
#define PIN2(a, b) asm volatile("" : "+v"(a), "+v"(b))
DI lds_ptr opq(lds_ptr p) { unsigned u = (unsigned)(uintptr_t)p; asm volatile("" : "+v"(u)); return (lds_ptr)(uintptr_t)u; }
#define TS(i) do { if ((i) == 0) pt0 = __builtin_amdgcn_s_memrealtime(); if ((i) == PROBE) pt1 = __builtin_amdgcn_s_memrealtime(); \
    if ((i) == 20) { const u64 e_ = pt1 + 4000; while (__builtin_amdgcn_s_memrealtime() < e_) __builtin_amdgcn_s_sleep(1); } } while (0)

DI constexpr int wd_slot(int s) { return (s * 17 + 27) / 28 != ((s + 1) * 17 + 27) / 28 ? (s * 17 + 27) / 28 : -1; }
#define HOP3_POLLER 1
DI constexpr int d_prio(int n) { return n < 0 ? -1 : n < 27 ? 3 - n / 7 : n < 64 ? 3 - (n - 27) / 10 : n < 84 ? 3 - (n - 64) / 5 : n < 112 ? 3 - (n - 84) / 7 : 0; }
DI constexpr int phys(int t) { return ((t % 9) + 2) % 9 * 16384; }

__global__ NOPK __launch_bounds__(512, 2) void block_fwd(const char* x, const char* wqkv, const char* wo, const char* wg, const char* wu, const char* wd, char* out, char* ws) {
    extern __shared__ __attribute__((aligned(16))) char smem[];
    lds_ptr lds = (lds_ptr)smem;
    const int tid = __builtin_amdgcn_workitem_id_x(), lane = tid & 63, w = __builtin_amdgcn_readfirstlane(tid >> 6);
    const int bid = __builtin_amdgcn_workgroup_id_x(), xcd = bid & 7, loc = bid >> 3;
    const int r = lane & 31, hf = lane >> 5, q4 = (lane & 15) >> 2, g1 = (lane >> 4) & 1, p4 = lane & 3;
    const unsigned lds0 = (unsigned)(uintptr_t)smem;
    const unsigned l16 = lane * 16;
    u64 pt0 = 0, pt1 = 0;
    TS(0);
    const int sp3 = (loc + 8) >> 5;
    const int atb = (xcd >> 1) * 8 + (loc & 7) + sp3 * (xcd & 1) * 4, acg = (loc >> 3) + (1 - sp3) * (xcd & 1) * 4;
    const int row8 = lane >> 3;
    const rsrc_t rws = __builtin_amdgcn_make_buffer_rsrc((void*)ws, 0, WS_END, 0x00020000);
    u64 epoch, tagv;
    unsigned fl1;
    const unsigned ones = 0x3f803f80u;
    const bool ffn = loc < 28;
    const int l7 = loc / 7, m7 = loc % 7;
    const int tb = xcd * 4 + l7;
    const int tt = w & 1, ct = (w >> 1) & 1, kh = w >> 2;
    const unsigned swz = (unsigned)(((lane & 7) ^ (((row8 >> 1) & 1) << 2)) * 16);

    if (__builtin_expect(ffn, 1)) {
        constexpr int A_W = 57344, A_WS = 24576;
        const int rot8 = ((loc & 7) << sp3) + sp3 * (xcd & 1), rot = rot8 & (7 - ((rot8 + 1) >> 3) * 7);
#define A_KC(c) (rot + (c) >= 7 ? rot + (c) - 7 : rot + (c))
        {
            const int xr = (w & 3) * 8 + row8;
            const unsigned xo = (unsigned)(row8 * 896 + (((lane & 7) ^ ((xr >> 1) & 7)) * 16));
            const char* xs = x + (size_t)(atb * 64 + w * 8) * 896;
            unsigned xd = lds0 + kh * 28672 + (w & 3) * 1024, xk = rot * 128;
#pragma unroll
            for (int c = 0; c < 7; ++c) { glds(xo, xs + xk, xd + c * 4096); xk = xk == 768 ? 0 : xk + 128; }
            SB();
        }
        const unsigned wvo = (unsigned)(row8 * 2688) + swz;
        const char* wsrc = wqkv + acg * 128 + (size_t)w * 8 * 2688;
        const unsigned wdst = lds0 + A_W + w * 1024;
#define A_PIECE(c, i) glds(wvo, wsrc + A_KC(c) * (64 * 2688) + (i) * 896, wdst + ((c) & 3) * A_WS + (i) * 8192)
#define A_GROUP(c) do { A_PIECE(c, 1); A_PIECE(c, 2); A_PIECE(c, 0); } while (0)
        epoch = peek_flag(ws, FL_EPOCH * 128); fl1 = (FL_KV + atb * 8 + acg) * 128;
        asm volatile("s_barrier" ::: "memory");
        A_GROUP(0);
        lds_ptr xb[4], wb1, wb1h;
#pragma unroll
        for (int j = 0; j < 4; ++j) xb[j] = lds + tt * 28672 + (r >> 3) * 1024 + (r & 7) * 128 + (((2 * j + hf) ^ ((r >> 1) & 7)) * 16);
        wb1 = opq(lds + A_W + (kh ? 0 : 8192) + (8 * hf + q4) * 128 + ((ct ^ (q4 >> 1)) * 64) + g1 * 32 + p4 * 8);
        wb1h = opq(wb1 + 2 * A_WS);
        unsigned go1[2], go2[2]; int gi[2];
#pragma unroll
        for (int p = 0; p < 2; ++p) {
            const int G = acg * 8 + ct * 4 + 2 * p + hf, hp = G / 7, i = G - hp * 7;
            const unsigned hs = kh ? 131072u : 262144u;
            const unsigned to = (kh ? WS_QF + (atb * 2 + tt) * 2048 : WS_KV + (atb * 2 + tt) * 4096) + r * 16;
            go1[p] = (unsigned)(2 * hp + (i > 3)) * hs + (unsigned)(i > 3 ? i - 3 : i) * 512 + to;
            go2[p] = i == 3 ? (unsigned)(2 * hp + 1) * hs + to : OOB;
            gi[p] = i;
        }
        const int vcol = acg * 64 + ct * 32 + r, vh = vcol / 28, vd = vcol - vh * 28;
        unsigned vo = WS_KV + (atb * 2 + tt) * 4096 + (unsigned)vh * 262144 + 2048 + hf * 512 + vd * 16;
        PIN2(go1[0], go1[1]); PIN2(go2[0], go2[1]); PIN(vo);
        lds_ptr xw = opq(lds + kh * 28672 + (w & 3) * 1024 + l16);
        waitv<3>();
        TS(28);
        u32x4 xv[7];
#pragma unroll
        for (int c = 0; c < 7; ++c) xv[c] = *(LAS u32x4*)(xw + c * 4096);
        SB();
        f32x4 sm = {};
#pragma unroll
        for (int c = 0; c < 7; ++c)
#pragma unroll
            for (int j = 0; j < 4; ++j) sm[j] = dot2(xv[c][j], ones, sm[j]);
        float nm = sum8((sm[0] + sm[1]) + (sm[2] + sm[3])) * (-1.f / D);
        PIN(nm);
#pragma unroll
        for (int c = 0; c < 7; ++c) { xv[c] = shift8(xv[c], nm); *(LAS u32x4*)(xw + c * 4096) = xv[c]; }
        A_GROUP(1);
        SB();
        sm = (f32x4){};
#pragma unroll
        for (int c = 0; c < 7; ++c)
#pragma unroll
            for (int j = 0; j < 4; ++j) sm[j] = dot2(xv[c][j], xv[c][j], sm[j]);
        LAS float* rst = (LAS float*)(lds + 155648);
        rst[w * 8 + row8] = __builtin_amdgcn_rsqf(sum8((sm[0] + sm[1]) + (sm[2] + sm[3])) * (1.f / D) + EPS);
        A_GROUP(2);
        TS(31);
        bf16x8 fx[4], f1[4], f2[4];
#define A_WAIT(c) do { if ((c) == 0) waitv_bar<6>(); else if ((c) == 1) waitv_barx<6>(); else if ((c) < 6) waitv_barx<3>(); else waitv_barx<0>(); \
                       if ((c) == 0) TS(1); if ((c) == 6) TS(2); } while (0)
#define A_WP(c, ks) ((((c) & 2) ? wb1h : wb1) + ((c) & 1) * A_WS + (ks) * 2048)
        if (w < 4) {
            f32x16 ak = {}, av = {};
            f32x4 rv[4];
            float rs = 0.f;
#pragma unroll
            for (int n = 0; n < 30; ++n) {
                const int c = n >> 2, ks = n & 3, km = (n - 2) & 3;
                if (n < 28 && ks == 0) A_WAIT(c);
                if (n >= 2) { ak = MFMA(f1[km], fx[km], ak); PIN(ak); } SB();
                if (n < 28) { fx[ks] = lds8(xb[ks] + c * 4096); f1[ks] = cat(trd(A_WP(c, ks)), trd(A_WP(c, ks) + 512)); } SB();
                if (n >= 2) { av = MFMA(fx[km], f2[km], av); PIN(av); } SB();
                if (n < 28) f2[ks] = cat(trd(A_WP(c, ks) + 8192), trd(A_WP(c, ks) + 8192 + 512));
                if (c == 0 && ks < 3) A_PIECE(3, ks); if (c >= 2 && c < 5 && ks < 3) A_PIECE(c + 2, ks);
                if (n == 21) {
                    rs = rst[tt * 32 + r];
#pragma unroll
                    for (int g = 0; g < 4; ++g) rv[g] = *(LAS f32x4*)(rst + tt * 32 + 8 * g + 4 * hf);
                }
                if (n == 24) { tagv = epoch + 1; PIN(tagv); }
                if (n == 24) {
                    const unsigned pv = vd == 0 ? ones : 0u, po = vd < 4 ? vo + 448 : OOB;
                    st16_dev((u32x4){pv, pv, pv, pv}, rws, po, 0); st16_dev((u32x4){pv, pv, pv, pv}, rws, po + 1024, 0);
                }
                SB();
            }
            TS(3);
#pragma unroll
            for (int i = 0; i < 16; ++i) { ak[i] *= rs; av[i] *= rv[i >> 2][i & 3]; }
#pragma unroll
            for (int p = 0; p < 2; ++p) {
                const u32x4 f = pack8(ak, p);
                const auto s0 = __builtin_amdgcn_permlane32_swap(f[0], f[2], false, false), s1 = __builtin_amdgcn_permlane32_swap(f[1], f[3], false, false);
                const u32x4 c = {s0[0], s1[0], s0[1], s1[1]};
                st16_dev(c, rws, go1[p], 0); st16_dev(c, rws, go2[p], 0);
            }
            st16_dev(pack8(av, 0), rws, vo, 0); st16_dev(pack8(av, 1), rws, vo + 1024, 0);
        } else {
            f32x16 aq = {};
            __builtin_amdgcn_s_setprio(1);
#pragma unroll
            for (int n = 0; n < 30; ++n) {
                const int c = n >> 2, ks = n & 3, km = (n - 2) & 3;
                if (n < 28 && ks == 0) A_WAIT(c);
                if (n == 24) { tagv = epoch + 1; PIN(tagv); }
                if (n >= 2) { aq = MFMA(f1[km], fx[km], aq); PIN(aq); } SB();
                if (n < 28) { fx[ks] = lds8(xb[ks] + c * 4096); f1[ks] = cat(trd(A_WP(c, ks)), trd(A_WP(c, ks) + 512)); }
                if (c == 0 && ks < 3) A_PIECE(3, ks); if (c >= 2 && c < 5 && ks < 3) A_PIECE(c + 2, ks);
                SB();
            }
            __builtin_amdgcn_s_setprio(0);
            const float QS = rst[tt * 32 + r] * (0.18898223650461362f * 1.4426950408889634f);
#pragma unroll
            for (int i = 0; i < 16; ++i) aq[i] *= QS;
#pragma unroll
            for (int p = 0; p < 2; ++p) {
                const u32x4 f = pack8(aq, p);
                const auto s0 = __builtin_amdgcn_permlane32_swap(f[0], f[2], false, false), s1 = __builtin_amdgcn_permlane32_swap(f[1], f[3], false, false);
                const bool sp = gi[p] == 3;
                st16_dev((u32x4){s0[0], s1[0], sp ? 0u : s0[1], sp ? 0u : s1[1]}, rws, go1[p], 0);
                st16_dev((u32x4){0u, 0u, s0[1], s1[1]}, rws, go2[p], 0);
            }
        }
    } else {
        epoch = peek_flag(ws, FL_EPOCH * 128); fl1 = (FL_RT + xcd * 4 + loc - 28) * 128;
        const int ri = xcd * 4 + loc - 28, nu = (w == 0 && ri < 16) ? 4 : 3;
        const unsigned vo = (unsigned)(row8 * 3584 + (lane & 7) * 16);
#pragma unroll 1
        for (int i = 0; i < nu; ++i) {
            const int u = i < 3 ? i * 256 + ri * 8 + w : 768 + ri;
            const size_t so = (size_t)(u / 28) * 16 * 3584 + (u % 28) * 128;
            const unsigned d_ = lds0 + w * 16384 + i * 4096;
            glds(vo, wg + so, d_); glds(vo, wg + so + 8 * 3584, d_ + 1024); glds(vo, wu + so, d_ + 2048); glds(vo, wu + so + 8 * 3584, d_ + 3072);
        }
#pragma unroll 1
        for (int i = 0; i < nu; ++i) {
            const int u = i < 3 ? i * 256 + ri * 8 + w : 768 + ri;
            waitv<8>();
            lds_ptr sg = lds + w * 16384 + i * 4096 + g1 * 2048 + (4 * hf + q4) * 128 + p4 * 8;
#pragma unroll
            for (int e = 0; e < 4; ++e) st16_dev(U4(cat(trd(sg + e * 32), trd(sg + e * 32 + 1024))), rws, l16, WS_WGU + (unsigned)(((u % 28) * 4 + e) * 28 + u / 28) * 1024);
            FENCE();
        }
        tagv = epoch + 1;
    }
    TS(24);
    const int head = xcd * 2 + (loc & 1), rb = loc >> 1, qt = w & 3;
    const char* kvs = ws + WS_KV + (size_t)head * 262144 + w * 2048;
    const char* qs = ws + WS_QF + (size_t)(head * 64 + rb * 4 + qt) * 2048 + kh * 1024;
    unsigned kvd = lds0 + w * 2048, qd = lds0 + 147456 + qt * 2048 + kh * 1024;
    unsigned pf1 = (FL_KV + (lane >> 1) * 8 + (head * 28 + (lane & 1) * 27) / 64) * 128;
    PINS(kvs); PINS(qs); PINS(kvd); PINS(qd); PIN2(pf1, fl1);
    waitv_bar<0>();
    if (w == 0) set_flag(ws, fl1, tagv);
    const u64 tag = ((u64)(unsigned)__builtin_amdgcn_readfirstlane((unsigned)(tagv >> 32)) << 32 | (u64)(unsigned)__builtin_amdgcn_readfirstlane((unsigned)tagv));
    TS(4);

    const int drb = (xcd >> 1) * 4 + l7, dfg = (xcd & 1) * 7 + m7;
    bf16x8 wf[28];
    u32x2 xa, xb;
    f32x16 cs = {}, acc = {};
    const u32x4 one4 = {ones, ones, ones, ones};
    const unsigned hi_ = (unsigned)((tb * 2 + tt) * 28 + m7 * 4 + ct * 2 + kh), ofo = (unsigned)(tt * 32768 + kh * 16384 + ct * 4096);
    unsigned ho = WS_HF + hi_ * 1024 + l16, so = hf ? OOB : WS_HS + hi_ * 256 + r * 8;
    const char* os = ws + WS_OF + (size_t)tb * 65536 + ofo;
    unsigned od = lds0 + 65536 + ofo;
    unsigned pf2 = (FL_O + (tb >> 1) * 16 + kh * 8 + ct * 2 + ((lane >> 1) & 1) + (lane & 4)) * 128 + ((lane & 1) * 4 + (tb & 1) * 2 + tt) * 8;
    unsigned fo3 = (FL_H + tb * 8 + m7) * 128;
    {
        lds_ptr ap = opq(lds + kh * 8192 + l16), apB = opq(lds + 65536 + kh * 8192 + l16), apC = opq(lds + 131072 + kh * 8192 + l16);
        const unsigned lv = kh * 8192 + (l16 ^ (kh * 256));
        lds_ptr vp = opq(lds + lv), vpB = opq(lds + 65536 + lv), vpC = opq(lds + 131072 + lv);
#define AP(off) ((off) < 65536 ? ap + (off) : (off) < 131072 ? apB + ((off) - 65536) : apC + ((off) - 131072))
#define VP(off) ((off) < 65536 ? vp + (off) : (off) < 131072 ? vpB + ((off) - 65536) : vpC + ((off) - 131072))
#define KV_ISSUE(t) glds2_dev(l16, kvs + (t) * 16384, kvd + phys(t))
        unsigned wgo = WS_WGU + (unsigned)((ffn ? dfg : 0) * 8 + w) * 28672;
        const int ri = umin(xcd * 4 + loc - 28, 27);
        const char* wos = (ffn ? wo + m7 * 128 : wd + (ri % 7) * 128 + (size_t)(ri / 7) * 448 * 896) + (size_t)w * 56 * 896;
        unsigned wov = (unsigned)(row8 * 896) + swz;
        const rsrc_t rx = __builtin_amdgcn_make_buffer_rsrc((void*)x, 0, 2048 * 448 * 2, 0x00020000);
        unsigned xo_ = (unsigned)(((tb * 2 + tt) * 32 + r) * 896 + m7 * 128 + ct * 64 + kh * 32 + hf * 8);
        unsigned rto = (FL_RT + r) * 128;
        lds_ptr qp = opq(lds + 147456 + qt * 2048 + l16);
        PINS(wgo); PINS(wos); PIN2(wov, xo_); PIN(rto); PIN2(ho, so); PIN2(pf2, fo3);
        lds_ptr xw = opq(lds + 147456 + w * 2048 + l16), xr = opq(lds + 147456 + (w ^ 4) * 2048 + l16);
        lds_ptr lw = opq(lds + 58368 + w * 256 + lane * 4), lr = opq(lds + 58368 + qt * 256 + 128 + r * 4);
        lds_ptr bw = opq(lds + 60416 + w * 4), br = opq(lds + 60416 + (lane & 7) * 4);
        unsigned oo = WS_OF + (unsigned)((rb * 4 + qt) * 32 + head * 2 + kh) * 1024 + l16;
        unsigned fo2 = (FL_O + rb * 16 + head) * 128 + w * 8;
        PIN2(oo, fo2);
        if (w == HOP3_POLLER) poll_flag(ws, pf1, tag);
        BAR();
        TS(5);
        glds_dev(l16, qs, qd);
        KV_ISSUE(0); KV_ISSUE(1);
        asm volatile("s_barrier" ::: "memory");
        KV_ISSUE(2); KV_ISSUE(3);
        waitv_bar<4>();
        TS(6);
        const bf16x8 q0 = lds8(qp), q1 = lds8(qp + 1024);
        f32x16 o = {}, sa = {}, sb;
        bf16x8 k0 = lds8(ap + phys(0)), k1 = lds8(ap + phys(0) + 1024), v0 = lds8(vp + phys(0) + 2048), v1 = {};
        u32x4 p0, p1 = {};
        sa = MFMA(k0, q0, sa); sa = MFMA(k1, q1, sa);
        k0 = lds8(ap + phys(0) + 4096); k1 = lds8(ap + phys(0) + 5120);
        PIN(sa); SB();
        u64 rtf = 0;
#define EXP4(S, o4) _Pragma("unroll") for (int i_ = o4; i_ < o4 + 4; ++i_) S[i_] = __builtin_amdgcn_exp2f(S[i_]);
#define WLD(s) wf[s] = BF8(ld16_dev(rws, l16 + ((s) & 3) * 1024, wgo + ((s) >> 2) * 4096))
#define SIDE(t, g) do { \
            if ((t) == 0 && (g) == 0) KV_ISSUE(4); if ((t) == 0 && (g) == 2) KV_ISSUE(5); if ((t) == 1 && (g) == 0) KV_ISSUE(6); if ((t) == 1 && (g) == 2) KV_ISSUE(7); \
            if ((t) == 2 && (g) == 0) KV_ISSUE(8); if ((t) == 1 && (g) == 3) rtf = peek_flag(ws, rto); \
            if ((t) == 3 && (g) < 3) KV_ISSUE(9 + (g)); \
            if ((t) == 7 && (g) == 0) KV_ISSUE(12); if ((t) == 7 && (g) == 2) KV_ISSUE(13); if ((t) == 8 && (g) == 0) KV_ISSUE(14); if ((t) == 8 && (g) == 2) KV_ISSUE(15); \
            if ((t) >= 4 && (t) <= 6) WLD(((t) - 4) * 4 + (g)); \
            if (((t) == 7 || (t) == 8) && ((g) & 1)) WLD(12 + ((t) - 7) * 2 + ((g) >> 1)); \
            if ((t) >= 9 && (t) <= 11) WLD(((t) - 5) * 4 + (g)); \
            if ((t) == 12 || ((t) == 13 && (g) < 3)) glds(wov, wos + (((t) - 12) * 4 + (g)) * 8 * 896, lds0 + (w * 7 + ((t) - 12) * 4 + (g)) * 1024); \
            if ((t) == 13 && (g) == 3) { xa = ld8(rx, xo_, 0); xb = ld8(rx, xo_, 16); } \
            if ((t) == 14 && (g) == 1) { if (w >= 6) touch(lane * (w == 6 ? (WS_END / 64 & ~127u) : 28672), w == 6 ? ws : out, lds0 + 61440); } \
            if ((t) == 14 && (g) == 0) { if (tid < 32) *(LAS u32x4*)(lds + 57344 + tid * 16) = (u32x4){0, 0, 0, 0}; } \
        } while (0)
#define TOFF(i) (phys((i) >> 1) + ((i) & 1) * 4096)
#define TILE(i, SC, SN) do { \
            __builtin_amdgcn_s_setprio(((i) & 1) ? 1 : 3); \
            if ((i) < 31) { f32x16 z_ = {}; SN = MFMA(k0, q0, z_); PIN(SN); } if ((i) < 30) k0 = lds8(AP(TOFF((i) + 2))); SB(); \
            EXP4(SC, 0); PIN(SC); SB(); \
            o = MFMA(v1, BF8(p1), o); PIN(o); v1 = lds8(VP(TOFF(i) + 3072)); SIDE((i) >> 1, ((i) & 1) * 2); SB(); \
            EXP4(SC, 4); p0 = pack8(SC, 0); PIN(p0); SB(); \
            __builtin_amdgcn_s_setprio(((i) & 1) ? 0 : 2); \
            if ((i) < 31) { SN = MFMA(k1, q1, SN); PIN(SN); } if ((i) < 30) k1 = lds8(AP(TOFF((i) + 2) + 1024)); SB(); \
            EXP4(SC, 8); PIN(SC); SB(); \
            o = MFMA(v0, BF8(p0), o); PIN(o); if ((i) < 31) v0 = lds8(VP(TOFF((i) + 1) + 2048)); SIDE((i) >> 1, ((i) & 1) * 2 + 1); SB(); \
            EXP4(SC, 12); p1 = pack8(SC, 1); PIN(p1); SB(); } while (0)
#define STEP(t) do { TILE(2 * (t), sa, sb); TILE(2 * (t) + 1, sb, sa); } while (0)
        STEP(0);
        waitv_bar<4>();
        STEP(1); STEP(2);
        waitv_bar<3>();
        TS(21);
        if (__builtin_expect(!wave_all(rtf == tag), 0)) poll_flag(ws, rto, tag);
        FENCE();
        STEP(3); STEP(4); STEP(5); STEP(6);
        waitv_bar<12>();
        TS(22);
        STEP(7); STEP(8); STEP(9); STEP(10);
        waitv_bar<9>();
        TS(23);
        STEP(11); STEP(12); STEP(13); STEP(14); STEP(15);
        o = MFMA(v1, BF8(p1), o);
        TS(7);
        u32x4 ov;
        {
            *(LAS f32x4*)xw = (f32x4){o[8], o[9], o[10], o[11]}; *(LAS f32x4*)(xw + 1024) = (f32x4){o[12], o[13], o[14], o[15]};
            const float l = kh ? o[4] : o[12];
            *(LAS float*)lw = l;
            float sa_ = 0.f;
#pragma unroll
            for (int i = 0; i < 16; ++i) sa_ += __builtin_fabsf(o[i]);
            const bool ok = sa_ < 0x1p100f && (hf == 0 || l > 0x1p-80f);
            *(LAS unsigned*)bw = wave_all(ok) ? 0u : 1u;
            BAR();
            const f32x4 a = *(LAS f32x4*)xr, c = *(LAS f32x4*)(xr + 1024);
            const float inv = __builtin_amdgcn_rcpf(*(LAS float*)lr + *(LAS float*)(lr + 1024));
            const unsigned bad = *(LAS unsigned*)br;
            const float iz = (kh && hf) ? 0.f : inv;
            ov = (u32x4){cvtpk((o[0] + a[0]) * inv, (o[1] + a[1]) * inv), cvtpk((o[2] + a[2]) * inv, (o[3] + a[3]) * inv),
                         cvtpk((o[4] + c[0]) * iz, (o[5] + c[1]) * iz), cvtpk((o[6] + c[2]) * iz, (o[7] + c[3]) * iz)};
            if (__builtin_expect(wave_any(bad != 0), 0)) {
                f32x16 of = {};
                float m = -0x1p126f;
#pragma unroll 1
                for (int t = 0; t < 16; ++t) {
                    BAR();
                    glds2_dev(l16, kvs + t * 16384, lds0 + 65536 + w * 2048);
                    waitv_bar<0>();
#pragma unroll 1
                    for (int u = 0; u < 2; ++u) {
                        f32x16 s = {};
                        s = MFMA(lds8(apB + u * 4096), q0, s); s = MFMA(lds8(apB + u * 4096 + 1024), q1, s);
                        float mx = s[0];
#pragma unroll
                        for (int i = 1; i < 16; ++i) mx = __builtin_fmaxf(mx, s[i]);
                        mx = __builtin_fmaxf(swap_max(mx), m);
                        const float alpha = __builtin_amdgcn_exp2f(m - mx);
                        m = mx;
#pragma unroll
                        for (int i = 0; i < 16; ++i) { s[i] = __builtin_amdgcn_exp2f(s[i] - mx); of[i] *= alpha; }
                        of = MFMA(lds8(vpB + u * 4096 + 2048), BF8(pack8(s, 0)), of);
                        of = MFMA(lds8(vpB + u * 4096 + 3072), BF8(pack8(s, 1)), of);
                    }
                }
                *(LAS f32x4*)xw = (f32x4){of[8], of[9], of[10], of[11]}; *(LAS f32x4*)(xw + 1024) = (f32x4){of[12], of[13], of[14], of[15]};
                *(LAS float*)lw = hf ? (kh ? of[4] : of[12]) : m;
                BAR();
                const f32x4 a2 = *(LAS f32x4*)xr, c2 = *(LAS f32x4*)(xr + 1024);
                const float m0 = *(LAS float*)(lr - 128), m1 = *(LAS float*)(lr + 1024 - 128), mm = __builtin_fmaxf(m0, m1);
                const float e0 = __builtin_amdgcn_exp2f(m0 - mm), e1 = __builtin_amdgcn_exp2f(m1 - mm);
                const float iv = __builtin_amdgcn_rcpf(*(LAS float*)lr * e0 + *(LAS float*)(lr + 1024) * e1);
                const float im = (kh ? e1 : e0) * iv, io = (kh ? e0 : e1) * iv;
                const bool z = kh && hf;
                ov = (u32x4){cvtpk(of[0] * im + a2[0] * io, of[1] * im + a2[1] * io), cvtpk(of[2] * im + a2[2] * io, of[3] * im + a2[3] * io),
                             z ? 0u : cvtpk(of[4] * im + c2[0] * io, of[5] * im + c2[1] * io), z ? 0u : cvtpk(of[6] * im + c2[2] * io, of[7] * im + c2[3] * io)};
            }
        }
        st16_dev(ov, rws, oo, 0);
        TS(25);
        FENCE();
        PIN2(xa, xb);
#pragma unroll
        for (int s = 27; s >= 0; --s) PIN(wf[s]);
#pragma unroll
        for (int s = 0; s < 8; ++s) cs = MFMA(wf[s], BF8(one4), cs);
        acc[0] = bflo(xa[0]); acc[1] = bfhi(xa[0]); acc[2] = bflo(xa[1]); acc[3] = bfhi(xa[1]); acc[4] = bflo(xb[0]); acc[5] = bfhi(xb[0]); acc[6] = bflo(xb[1]); acc[7] = bfhi(xb[1]);
        PIN(acc); PIN2(ho, so); PIN2(pf2, fo3); PINS(os); PINS(od); PIN(cs);
        waitv<0>();
        set_flag(ws, fo2, tagv);
        BAR();
        TS(8);
    }
    if (__builtin_expect(!ffn, 0)) {
        const int ri = xcd * 4 + loc - 28;
        if (ri < 28) {
#pragma unroll 1
            for (int u = w; u < 28; u += 8) {
                lds_ptr sd = lds + u * 2048 + (4 * hf + q4) * 128 + g1 * 32 + p4 * 8;
#pragma unroll
                for (int e = 0; e < 2; ++e) { lds_ptr p_ = sd + ((e ^ (q4 >> 1)) * 64);
                    st16_dev(U4(cat(trd(p_), trd(p_ + 1024))), rws, l16, WS_WD + (unsigned)(((ri % 7) * 112 + (ri / 7) * 28 + u) * 2 + e) * 1024); }
            }
        }
        waitv_bar<0>();
        if (w == 0) set_flag(ws, (FL_RD + xcd * 4 + loc - 28) * 128, tagv);
        return;
    }

    const int erb = tb >> 1;
    const char* hs = ws + WS_HF + (size_t)drb * 114688 + w * 1024;
    const char* hss = ws + WS_HS + (size_t)((drb * 4 + (w & 3)) * 28 + kh * 14) * 256;
    unsigned go = WS_GF + (unsigned)(drb * 4 * 112 + dfg * 8 + w) * 1024 + l16;
    unsigned pf3, fo4 = (FL_G + drb * 64 + dfg) * 128 + w * 8;
    {
        lds_ptr wb = lds + kh * (8 * 28 * 128) + (4 * hf + q4) * 128 + ((ct ^ (q4 >> 1)) * 64) + (g1 ^ kh) * 32 + p4 * 8;
        bf16x8 wr[16];
#pragma unroll
        for (int s = 0; s < 12; ++s) { const int k0 = ((s >> 1) * 28 + (s & 1) * 16) * 128; wr[s] = cat(trd(wb + k0), trd(wb + k0 + 1024)); }
        lds_ptr cw = opq(lds + 131072 + w * 2048 + l16), cr = opq(lds + 131072 + (w ^ 4) * 2048 + l16);
        poll_flag(ws, pf2, tag);
        TS(9);
        glds4_dev(l16, os, od);
        asm volatile("s_barrier" ::: "memory");
        glds4_dev(l16, os + 8192, od + 8192);
        lds_ptr ob = opq(lds + 65536 + tt * 32768 + kh * 16384 + l16);
        bf16x8 of[8];
        waitv_bar<4>();
        TS(26);
#pragma unroll
        for (int s = 0; s < 8; ++s) of[s] = lds8(ob + s * 1024);
        SB();
#pragma unroll
        for (int s = 0; s < 8; ++s) acc = MFMA(wr[s], of[s], acc);
        PIN(acc); SB();
#pragma unroll
        for (int s = 12; s < 16; ++s) { const int k0 = ((s >> 1) * 28 + (s & 1) * 16) * 128; wr[s] = cat(trd(wb + k0), trd(wb + k0 + 1024)); }
        waitv_bar<0>();
#pragma unroll
        for (int s = 0; s < 8; ++s) of[s] = lds8(ob + (8 + s) * 1024);
        SB();
#pragma unroll
        for (int s = 0; s < 8; ++s) acc = MFMA(wr[8 + s], of[s], acc);
        TS(10);
        *(LAS f32x4*)cw = (f32x4){acc[8], acc[9], acc[10], acc[11]}; *(LAS f32x4*)(cw + 1024) = (f32x4){acc[12], acc[13], acc[14], acc[15]};
        BAR();
        const f32x4 a = *(LAS f32x4*)cr + (f32x4){acc[0], acc[1], acc[2], acc[3]}, b = *(LAS f32x4*)(cr + 1024) + (f32x4){acc[4], acc[5], acc[6], acc[7]};
        const u32x4 hv = {cvtpk(a[0], a[1]), cvtpk(a[2], a[3]), cvtpk(b[0], b[1]), cvtpk(b[2], b[3])};
        st16_dev(hv, rws, ho, 0);
        float s1 = 0.f, s2 = 0.f;
        acc_stats(hv, s1, s2);
        s1 = swap_add(s1); s2 = swap_add(s2);
        st8_dev((u32x2){f2u(s1), f2u(s2)}, rws, so, 0);
        FENCE();
#pragma unroll
        for (int s = 8; s < 14; ++s) cs = MFMA(wf[s], BF8(one4), cs);
        pf3 = (FL_H + drb * 16 + (lane & 8) + umin(lane & 7, 6)) * 128;
        PIN(go); PIN2(pf3, fo4); PINS(hs); PINS(hss); PIN(cs);
        waitv_bar<0>();
        if (w == 0) set_flag(ws, fo3, tagv);
        if (bid == 0 && w == 0) set_flag(ws, FL_EPOCH * 128, tagv);
        TS(11);
    }

    const unsigned wdo = WS_WD + (unsigned)(m7 * 112 + w * 14) * 2048;
    bf16x8 wdf[28];
    bf16x8 gf[8];
    const int ta = erb * 4 + (tb & 1);
    const unsigned gxo = WS_GF + (unsigned)(ta * 112 + w * 14) * 1024;
    unsigned fo4e;
    u64 gfl = 0;
    const unsigned gks = w * 14 + umin(lane & 15, 13), gfi = (FL_G + (erb * 4 + (tb & 1)) * 16 + (gks >> 3)) * 128 + (gks & 7) * 8;
    {
#pragma unroll
        for (int s = 14; s < 28; ++s) cs = MFMA(wf[s], BF8(one4), cs);
        lds_ptr csp = opq(lds + 161792 + w * 128 + hf * 64);
        if (r == 0) {
#pragma unroll
            for (int g = 0; g < 4; ++g) *(LAS f32x4*)(csp + g * 16) = (f32x4){cs[4 * g], cs[4 * g + 1], cs[4 * g + 2], cs[4 * g + 3]};
        }
        const unsigned hd = lds0 + w * 1024;
        const char* wds = ws + wdo;
        lds_ptr hb = opq(lds + l16), hb2 = opq(lds + 57344 + l16);
        lds_ptr wl = opq(lds + 114688 + w * 5120 + l16), wl2 = opq(lds + w * 7168 + l16);
        LAS f32x2* stp = (LAS f32x2*)opq(lds + 159744 + r * 8);
        if (w == HOP3_POLLER) poll_flag(ws, pf3, tag);
        BAR();
        TS(12);
#define H_PIECE(j) glds_dev(l16, hs + (j) * 8192, hd + (j) * 8192)
#define WDLD(s) wdf[s] = BF8(ld16_dev(rws, l16 + ((s) & 3) * 1024, wdo + ((s) >> 2) * 4096))
#define HB(n) ((n) < 56 ? hb + (n) * 1024 : hb2 + ((n) - 56) * 1024)
        H_PIECE(0);
        asm volatile("s_barrier" ::: "memory");
        H_PIECE(1); H_PIECE(2); H_PIECE(3);
        glds4_dev(l16, hss, lds0 + 114688 + w * 5120);
        constexpr int PF = 5;
        bf16x8 bf[PF + 1];
        f32x16 acc[2];
        f32x2 sp[7], sv0, sv1;
        f32x4 c4[4];
        float mean = 0.f, r2 = 0.f, hp = 0.f;
        u32x4 hv;
        u64 rdf = 0;
        waitv_bar<7>();
        TS(27);
#pragma unroll
        for (int n = 0; n < PF; ++n) bf[n] = lds8(HB(n));
        SB();
        sfor<0, 125>([&](auto N_) __attribute__((always_inline)) {
            constexpr int n = decltype(N_)::v, p = n / 28, s = n % 28;
            if constexpr (n == 3) waitv_bar<5>();
            if constexpr (n == 27) { waitv_bar<3>(); TS(13); }
            if constexpr (n == 64) { waitv_bar<10>(); set_flag(ws, fo4, tagv); TS(14); }
            if constexpr (n == 84) { waitv<7>(); set_flag(ws, fo4 + 2048, tagv); }
            if constexpr (n == 106) { waitv<4>(); set_flag(ws, fo4 + 4096, tagv); }
            if constexpr (n == 39) { if (__builtin_expect(!wave_all(rdf == tag), 0)) poll_flag(ws, (FL_RD + r) * 128, tag); FENCE(); }
            if constexpr (d_prio(n) != d_prio(n - 1)) __builtin_amdgcn_s_setprio(d_prio(n));
            if constexpr (n < 112) { if constexpr (s == 0) { f32x16 z = {}; acc[p & 1] = MFMA(wf[s], bf[n % (PF + 1)], z); } else acc[p & 1] = MFMA(wf[s], bf[n % (PF + 1)], acc[p & 1]); PIN(acc[p & 1]); }
            SB();
            if constexpr (n + PF < 112) bf[(n + PF) % (PF + 1)] = lds8(HB(n + PF));
            if constexpr (n % 3 == 2 && n < 30) H_PIECE(4 + n / 3);
            if constexpr (n == 16) { waitv<5>();
#pragma unroll
                for (int j = 0; j < 7; ++j) sp[j] = *(LAS f32x2*)(lds + 114688 + w * 5120 + (hf * 7 + j) * 256 + r * 8);
            }
            if constexpr (n == 20) { const f32x2 t = ((sp[0] + sp[1]) + (sp[2] + sp[3])) + ((sp[4] + sp[5]) + sp[6]); stp[w * 32] = (f32x2){swap_add(t[0]), swap_add(t[1])}; }
            if constexpr (n >= 39 && n < 48 && n % 2 == 1) glds_dev(l16, wds + (n - 39) / 2 * 1024, lds0 + 114688 + w * 5120 + (n - 39) / 2 * 1024);
            if constexpr (n >= 48 && n < 64 && n % 3 == 0) WDLD(12 + (n - 48) / 3);
            if constexpr (n >= 70 && n < 84 && n % 2 == 0) glds_dev(l16, wds + (5 + (n - 70) / 2) * 1024, lds0 + w * 7168 + (n - 70) / 2 * 1024);
            if constexpr (n >= 85 && n < 105 && n % 2 == 1) WDLD(18 + (n - 85) / 2);
            if constexpr (n == 107) gfl = peek_flag(ws, gfi);
            if constexpr (n == 30) rdf = peek_flag(ws, (FL_RD + r) * 128);
            if constexpr (p >= 1) {
                const f32x16& a = acc[(p - 1) & 1];
                if constexpr (n == 100) { sv0 = stp[3 * 32]; sv1 = stp[7 * 32]; }
                if constexpr (s == 0 && p < 4) { sv0 = stp[(p - 1) * 32]; sv1 = stp[(p + 3) * 32];
#pragma unroll
                    for (int g = 0; g < 4; ++g) c4[g] = *(LAS f32x4*)(csp + g * 16); }
                if constexpr ((s == 3 && p < 4) || n == 104) { const f32x2 sv = sv0 + sv1; mean = sv[0] * (1.f / D); r2 = __builtin_amdgcn_rcpf(__builtin_fmaxf(sv[1] * (1.f / D) - mean * mean, 0.f) + EPS); PIN2(mean, r2); }
                if constexpr (s >= 4 && s < 12) { constexpr int i = s - 4;
                    const float h0 = __builtin_fmaxf(a[i] - mean * c4[i >> 2][i & 3], 0.f) * r2 * (a[8 + i] - mean * c4[2 + (i >> 2)][i & 3]);
                    if constexpr (i & 1) { hv[i >> 1] = cvtpk(hp, h0); PIN(hv); } else { hp = h0; PIN(hp); } }
                if constexpr (s == 12) { st16_dev(hv, rws, go + (p - 1) * 114688, 0); FENCE(); }
            }
            SB();
        });
        TS(15);
        fo4e = fo4 + 6144;
        if (__builtin_expect(!wave_all(gfl == tag), 0)) poll_flag(ws, gfi, tag);
        FENCE();
#pragma unroll
        for (int s = 0; s < 8; ++s) { gf[s] = BF8(ld16_dev(rws, l16 + (s & 3) * 1024, gxo + (s >> 2) * 4096)); SB(); }
        FENCE();
#pragma unroll
        for (int s = 0; s < 12; ++s) wdf[s] = lds8(s < 5 ? wl + s * 1024 : wl2 + (s - 5) * 1024);
    }

    {
        const int etok = tid >> 4, eblk = tid & 15;
        const unsigned hro = (unsigned)((((eblk >> 2) & 3) * 64 + (eblk & 1) * 32 + etok) * 16 + ((eblk >> 1) & 1) * 8) + WS_HF + (unsigned)(ta * 28 + m7 * 4) * 1024;
        u32x2 hres[2];
        GAS u64* op = (GAS u64*)(out + (size_t)(ta * 32 + etok) * 896 + m7 * 128 + eblk * 8);
        lds_ptr rp = opq(lds + w * 8448 + hf * 528 + r * 16), rq = opq(lds + eblk * 528 + etok * 16);
        lds_ptr rp2 = opq(lds + 67584 + w * 8448 + hf * 528 + r * 16), rq2 = opq(lds + 67584 + eblk * 528 + etok * 16);
#define G_LD(hh, s) gf[(s) & 7] = BF8(ld16_dev(rws, l16 + ((s) & 3) * 1024, gxo + (hh) * 229376 + ((s) >> 2) * 4096))
#define G_MMA(hh, RP) do { f32x16 c0 = {}, c1 = {}; \
            _Pragma("unroll") for (int s = 0; s < 14; ++s) { c0 = MFMA(wdf[2 * s], gf[s & 7], c0); PIN(c0); c1 = MFMA(wdf[2 * s + 1], gf[s & 7], c1); PIN(c1); SB(); if (s < 6) G_LD(hh, s + 8); SB(); } \
            _Pragma("unroll") for (int g = 0; g < 4; ++g) *(LAS f32x4*)(RP + g * 1056) = (f32x4){c0[4 * g], c0[4 * g + 1], c0[4 * g + 2], c0[4 * g + 3]}; \
            _Pragma("unroll") for (int g = 0; g < 4; ++g) *(LAS f32x4*)(RP + 4224 + g * 1056) = (f32x4){c1[4 * g], c1[4 * g + 1], c1[4 * g + 2], c1[4 * g + 3]}; } while (0)
#define G_OUT(hh, RQ) do { f32x4 pa[8]; \
            _Pragma("unroll") for (int ww = 0; ww < 8; ++ww) pa[ww] = *(LAS f32x4*)(RQ + ww * 8448); SB(); \
            const f32x4 a = ((pa[0] + pa[1]) + (pa[2] + pa[3])) + ((pa[4] + pa[5]) + (pa[6] + pa[7])); \
            const u32x2 ov = {cvtpk(a[0] + bflo(hres[hh][0]), a[1] + bfhi(hres[hh][0])), cvtpk(a[2] + bflo(hres[hh][1]), a[3] + bfhi(hres[hh][1]))}; \
            __hip_atomic_store(op + (hh) * (64 * 896 / 8), __builtin_bit_cast(u64, ov), __ATOMIC_RELAXED, __HIP_MEMORY_SCOPE_AGENT); } while (0)
        waitv<8>();
        set_flag(ws, fo4e, tagv);
        BAR();
        TS(16);
        hres[0] = ld8_dev(rws, hro, 0); hres[1] = ld8_dev(rws, hro, 57344);
        FENCE(); SB();
        G_MMA(0, rp);
        PIN2(hres[0], hres[1]);
        TS(17);
        poll_flag(ws, gfi + 4096, tag);
        TS(18);
#pragma unroll
        for (int s = 0; s < 8; ++s) { G_LD(1, s); SB(); }
        BAR();
        G_OUT(0, rq);
        FENCE(); SB();
        G_MMA(1, rp2);
        TS(19);
        BAR();
        G_OUT(1, rq2);
    }
    TS(20);
}

#define WS_OFF 0
extern "C" void kernel_launch(void* const* d_in, const int* in_sizes, int n_in, void* d_out, int out_size, void* d_ws, size_t ws_size, hipStream_t stream) {
    (void)hipFuncSetAttribute((const void*)block_fwd, hipFuncAttributeMaxDynamicSharedMemorySize, LDS_BYTES);
    block_fwd<<<256, 512, LDS_BYTES, stream>>>((const char*)d_in[0], (const char*)d_in[1], (const char*)d_in[2], (const char*)d_in[4], (const char*)d_in[5], (const char*)d_in[6], (char*)d_out, (char*)d_ws + WS_OFF);
}
```

```cpp
#include <hip/hip_runtime.h>
#include <hip/hip_bf16.h>
#include <stdint.h>

typedef __attribute__((ext_vector_type(8))) short bf16x8;
typedef __attribute__((ext_vector_type(4))) short s16x4;
typedef __attribute__((ext_vector_type(2))) __bf16 bf16x2;
typedef __attribute__((ext_vector_type(16))) float f32x16;
typedef __attribute__((ext_vector_type(4))) float f32x4;
typedef __attribute__((ext_vector_type(2))) float f32x2;
typedef __attribute__((ext_vector_type(4))) unsigned u32x4;
typedef __attribute__((ext_vector_type(2))) unsigned u32x2;
typedef short v4i16_t __attribute__((ext_vector_type(4)));
typedef unsigned long long u64;
#define LAS __attribute__((address_space(3)))
#define GAS __attribute__((address_space(1)))
typedef LAS char* lds_ptr;
#if defined(__HIP_DEVICE_COMPILE__)
#define NOPK __attribute__((target("no-packed-fp32-ops")))
#else
#define NOPK
#endif
#define DI __device__ __forceinline__
#define MFMA(a, b, c) __builtin_amdgcn_mfma_f32_32x32x16_bf16(a, b, c, 0, 0, 0)
#define MFMA16(a, b, c) __builtin_amdgcn_mfma_f32_16x16x32_bf16(a, b, c, 0, 0, 0)
#define BF8(x) __builtin_bit_cast(bf16x8, x)
#define U4(x) __builtin_bit_cast(u32x4, x)

constexpr int D = 448;
constexpr float EPS = 1e-5f;
constexpr unsigned WS_FLAG = 0;
constexpr unsigned FL_EPOCH = 0, FL_KV = 1, FL_RT = FL_KV + 256, FL_RD = FL_RT + 32, FL_CL = FL_RD + 32, FL_O = FL_CL + 32, FL_H = FL_O + 256, FL_G = FL_H + 256;
constexpr unsigned WS_KV = 1u << 20;
constexpr unsigned WS_QF = WS_KV + 4194304;
constexpr unsigned WS_OF = WS_QF + 2097152;
constexpr unsigned WS_HF = WS_OF + 2097152;
constexpr unsigned WS_GF = WS_HF + 64 * 28 * 1024;
constexpr unsigned WS_WGU = WS_GF + 64 * 112 * 1024;
constexpr unsigned WS_WD = WS_WGU + 112 * 28 * 1024;
constexpr unsigned WS_HS = WS_WD + 7 * 112 * 2 * 1024;
constexpr unsigned WS_END = WS_HS + 64 * 28 * 256 + 1024;
constexpr unsigned OOB = 0x40000000u;
constexpr int LDS_BYTES = 163840;

DI float u2f(unsigned u) { return __builtin_bit_cast(float, u); }
DI unsigned f2u(float f) { return __builtin_bit_cast(unsigned, f); }
DI unsigned umin(unsigned a, unsigned b) { return a < b ? a : b; }
DI unsigned umax3(unsigned a, unsigned b, unsigned c) { const unsigned m = a > b ? a : b; return m > c ? m : c; }
DI bool wave_any(bool p) { return __builtin_amdgcn_ballot_w64(p) != 0; }
DI bool wave_all(bool p) { return __builtin_amdgcn_ballot_w64(!p) == 0; }
DI unsigned cvtpk(float lo, float hi) { return __builtin_bit_cast(unsigned, __builtin_convertvector((f32x2){lo, hi}, bf16x2)); }
DI float bflo(unsigned u) { return u2f(u << 16); }
DI float bfhi(unsigned u) { return u2f(u & 0xffff0000u); }
DI void glds(unsigned voff, const void* sbase, unsigned dst) {
    asm volatile("s_mov_b32 m0, %2\n\ts_nop 0\n\tglobal_load_lds_dwordx4 %0, %1" :: "v"(voff), "s"(sbase), "s"(dst) : "memory");
}
DI void glds_dev(unsigned voff, const void* sbase, unsigned dst) {
    asm volatile("s_mov_b32 m0, %2\n\ts_nop 0\n\tglobal_load_lds_dwordx4 %0, %1 sc1" :: "v"(voff), "s"(sbase), "s"(dst) : "memory");
}
DI void glds2_dev(unsigned voff, const void* sbase, unsigned dst) {
    asm volatile("s_mov_b32 m0, %2\n\ts_nop 0\n\tglobal_load_lds_dwordx4 %0, %1 sc1\n\tglobal_load_lds_dwordx4 %0, %1 offset:1024 sc1" :: "v"(voff), "s"(sbase), "s"(dst) : "memory");
}
DI void glds4_dev(unsigned voff, const void* sbase, unsigned dst) {
    asm volatile("s_mov_b32 m0, %2\n\ts_nop 0\n\tglobal_load_lds_dwordx4 %0, %1 sc1\n\tglobal_load_lds_dwordx4 %0, %1 offset:1024 sc1\n\t"
                 "global_load_lds_dwordx4 %0, %1 offset:2048 sc1\n\tglobal_load_lds_dwordx4 %0, %1 offset:3072 sc1" :: "v"(voff), "s"(sbase), "s"(dst) : "memory");
}
DI s16x4 trd(lds_ptr p) { return __builtin_bit_cast(s16x4, __builtin_amdgcn_ds_read_tr16_b64_v4i16((LAS v4i16_t*)p)); }
DI bf16x8 cat(s16x4 a, s16x4 b) { return __builtin_shufflevector(a, b, 0, 1, 2, 3, 4, 5, 6, 7); }
DI bf16x8 lds8(lds_ptr p) { return *(LAS bf16x8*)p; }
DI float swap_add(float v) { auto r = __builtin_amdgcn_permlane32_swap(f2u(v), f2u(v), false, false); return u2f(r[0]) + u2f(r[1]); }
DI float swap_max(float v) { auto r = __builtin_amdgcn_permlane32_swap(f2u(v), f2u(v), false, false); return __builtin_fmaxf(u2f(r[0]), u2f(r[1])); }
DI u32x4 pack8(const f32x16& a, int s) { return (u32x4){cvtpk(a[8 * s], a[8 * s + 1]), cvtpk(a[8 * s + 2], a[8 * s + 3]), cvtpk(a[8 * s + 4], a[8 * s + 5]), cvtpk(a[8 * s + 6], a[8 * s + 7])}; }
DI float dot2(unsigned a, unsigned b, float c) { return __builtin_amdgcn_fdot2_f32_bf16(__builtin_bit_cast(bf16x2, a), __builtin_bit_cast(bf16x2, b), c, false); }
DI void acc_stats(u32x4 u, float& s1, float& s2) {
#pragma unroll
    for (int i = 0; i < 4; ++i) { const float a = bflo(u[i]), b = bfhi(u[i]); s1 += a; s2 = __builtin_fmaf(a, a, s2); s1 += b; s2 = __builtin_fmaf(b, b, s2); }
}
DI void acc_stats2(unsigned u0, unsigned u1, f32x4& s) {
    const float a = bflo(u0), b = bfhi(u0), c = bflo(u1), d = bfhi(u1);
    s[0] += a; s[1] += b; s[2] = __builtin_fmaf(a, a, s[2]); s[3] = __builtin_fmaf(b, b, s[3]); s[0] += c; s[1] += d; s[2] = __builtin_fmaf(c, c, s[2]); s[3] = __builtin_fmaf(d, d, s[3]);
}
DI float sum8(float v) {
    v += u2f(__builtin_amdgcn_update_dpp(0u, f2u(v), 0xB1, 0xf, 0xf, true));
    v += u2f(__builtin_amdgcn_update_dpp(0u, f2u(v), 0x4E, 0xf, 0xf, true));
    v += u2f(__builtin_amdgcn_update_dpp(0u, f2u(v), 0x141, 0xf, 0xf, true));
    return v;
}
DI u32x4 shift8(u32x4 x, float c) {
    u32x4 o; float t0, t1, t2, t3, t4, t5, t6, t7;
    asm("v_dot2_f32_bf16 %4, %12, %16, %18\n\tv_dot2_f32_bf16 %5, %12, %17, %18\n\tv_dot2_f32_bf16 %6, %13, %16, %18\n\tv_dot2_f32_bf16 %7, %13, %17, %18\n\t"
        "v_dot2_f32_bf16 %8, %14, %16, %18\n\tv_dot2_f32_bf16 %9, %14, %17, %18\n\tv_dot2_f32_bf16 %10, %15, %16, %18\n\tv_dot2_f32_bf16 %11, %15, %17, %18\n\t"
        "v_cvt_pk_bf16_f32 %0, %4, %5\n\tv_cvt_pk_bf16_f32 %1, %6, %7\n\tv_cvt_pk_bf16_f32 %2, %8, %9\n\tv_cvt_pk_bf16_f32 %3, %10, %11"
        : "=&v"(o[0]), "=&v"(o[1]), "=&v"(o[2]), "=&v"(o[3]), "=&v"(t0), "=&v"(t1), "=&v"(t2), "=&v"(t3), "=&v"(t4), "=&v"(t5), "=&v"(t6), "=&v"(t7)
        : "v"(x[0]), "v"(x[1]), "v"(x[2]), "v"(x[3]), "s"(0x00003f80u), "s"(0x3f800000u), "v"(c));
    return o;
}
template <int N> struct IC { static constexpr int v = N; };
template <int B, int E, class F> DI void sfor(F&& f) { if constexpr (B < E) { f(IC<B>{}); sfor<B + 1, E>(f); } }
template <int N> DI void waitv() { asm volatile("s_waitcnt vmcnt(%0)" :: "i"(N) : "memory"); }
template <int N> DI void waitv_bar() { asm volatile("s_waitcnt vmcnt(%0) lgkmcnt(0)\n\ts_barrier" :: "i"(N) : "memory"); }
template <int N> DI void waitv_barx() { asm volatile("s_waitcnt vmcnt(%0)\n\ts_barrier" :: "i"(N) : "memory"); }
template <int N> DI void waitv_bar2() { asm volatile("s_waitcnt vmcnt(%0) lgkmcnt(2)\n\ts_barrier" :: "i"(N) : "memory"); }
DI void touch(unsigned voff, const void* sbase, unsigned junk) {
    asm volatile("s_mov_b32 m0, %2\n\ts_nop 0\n\tglobal_load_lds_dword %0, %1 sc1" :: "v"(voff), "s"(sbase), "s"(junk) : "memory");
}
#define BAR() asm volatile("s_waitcnt lgkmcnt(0)\n\ts_barrier" ::: "memory")
#define FENCE() asm volatile("" ::: "memory")
typedef __amdgpu_buffer_rsrc_t rsrc_t;
DI u32x4 ld16(rsrc_t rs, unsigned voff, unsigned soff) { return U4(__builtin_amdgcn_raw_buffer_load_b128(rs, voff, soff, 0)); }
DI u32x4 ld16_dev(rsrc_t rs, unsigned voff, unsigned soff) { return U4(__builtin_amdgcn_raw_buffer_load_b128(rs, voff, soff, 16)); }
DI u32x2 ld8(rsrc_t rs, unsigned voff, unsigned soff) { return __builtin_bit_cast(u32x2, __builtin_amdgcn_raw_buffer_load_b64(rs, voff, soff, 0)); }
DI u32x2 ld8_dev(rsrc_t rs, unsigned voff, unsigned soff) { return __builtin_bit_cast(u32x2, __builtin_amdgcn_raw_buffer_load_b64(rs, voff, soff, 16)); }
DI void st16_dev(u32x4 v, rsrc_t rs, unsigned voff, unsigned soff) { __builtin_amdgcn_raw_buffer_store_b128(v, rs, voff + soff, 0, 16); }
DI void st8_dev(u32x2 v, rsrc_t rs, unsigned voff, unsigned soff) { __builtin_amdgcn_raw_buffer_store_b64(v, rs, voff, soff, 16); }
#define POLL_SLEEP 1
DI void set_flag(char* ws, unsigned fo, u64 tagv) {
    asm volatile("s_mov_b64 exec, 1\n\tglobal_store_dwordx2 %0, %1, %2 sc1\n\ts_mov_b64 exec, -1" :: "v"(fo), "v"(tagv), "s"(ws) : "memory");
}
DI u64 peek_flag(char* ws, unsigned fo) { return __hip_atomic_load((GAS u64*)(ws + fo), __ATOMIC_RELAXED, __HIP_MEMORY_SCOPE_AGENT); }
DI void poll_flag(char* ws, unsigned fo, u64 tag) {
#pragma unroll 1
    for (int spin = 0; spin < (1 << 16); ++spin) {
        if (wave_all(peek_flag(ws, fo) == tag)) break;
        __builtin_amdgcn_s_sleep(POLL_SLEEP);
    }
    FENCE();
}
#define SB() __builtin_amdgcn_sched_barrier(0)
#define PINS(a) asm volatile("" : "+s"(a))
#define PIN(a) asm volatile("" : "+v"(a))
#define PIN2(a, b) asm volatile("" : "+v"(a), "+v"(b))
DI lds_ptr opq(lds_ptr p) { unsigned u = (unsigned)(uintptr_t)p; asm volatile("" : "+v"(u)); return (lds_ptr)(uintptr_t)u; }
#define TS(i) do {} while (0)

DI constexpr int wd_slot(int s) { return (s * 17 + 27) / 28 != ((s + 1) * 17 + 27) / 28 ? (s * 17 + 27) / 28 : -1; }
#define EROT 0
#define CLR_SLEEP 90
#define ENL 4
#define HOP3_POLLER 1
DI constexpr int d_prio(int n) { return n < 0 ? -1 : n < 27 ? 3 - n / 7 : n < 64 ? 3 - (n - 27) / 10 : n < 84 ? 3 - (n - 64) / 5 : n < 112 ? 3 - (n - 84) / 7 : 0; }
DI constexpr int phys(int t) { return ((t % 9) + 2) % 9 * 16384; }

__global__ NOPK __launch_bounds__(512, 2) void block_fwd(const char* x, const char* wqkv, const char* wo, const char* wg, const char* wu, const char* wd, char* out, char* ws) {
    extern __shared__ __attribute__((aligned(16))) char smem[];
    lds_ptr lds = (lds_ptr)smem;
    const int tid = __builtin_amdgcn_workitem_id_x(), lane = tid & 63, w = __builtin_amdgcn_readfirstlane(tid >> 6);
    const int bid = __builtin_amdgcn_workgroup_id_x(), xcd = bid & 7, loc = bid >> 3;
    const int r = lane & 31, hf = lane >> 5, q4 = (lane & 15) >> 2, g1 = (lane >> 4) & 1, p4 = lane & 3;
    const unsigned lds0 = (unsigned)(uintptr_t)smem;
    const unsigned l16 = lane * 16;
    TS(0);
    const int sp3 = (loc + 8) >> 5;
    const int atb = (xcd >> 1) * 8 + (loc & 7) + sp3 * (xcd & 1) * 4, acg = (loc >> 3) + (1 - sp3) * (xcd & 1) * 4;
    const int row8 = lane >> 3;
    const int rot8 = ((loc & 7) << sp3) + sp3 * (xcd & 1), rot = rot8 == 7 ? 0 : rot8;
    if (loc < 28) {
        const unsigned xo = (unsigned)((atb * 64 + (tid >> 3)) * 896 + (((tid ^ (tid >> 4)) & 7) * 16));
        const unsigned xd0 = lds0 + 1024 + (w >> 2) * 28672 + (w & 3) * 1024;
        unsigned xd = xd0 + (7 - rot) * 4096;
        const unsigned w0 = (unsigned)((rot * 64 + (tid >> 3)) * 2688 + acg * 128 + (((tid & 7) ^ ((tid >> 2) & 4)) * 16));
        asm volatile("s_cmp_eq_u32 %0, %3\n\ts_cselect_b32 %0, %2, %0\n\ts_mov_b32 m0, %0\n\ts_add_u32 %0, %0, 0x1000\n\tglobal_load_lds_dwordx4 %1, %4\n\t"
#define XP(o) "s_cmp_eq_u32 %0, %3\n\ts_cselect_b32 %0, %2, %0\n\ts_sub_u32 m0, %0, " #o "\n\ts_add_u32 %0, %0, 0x1000\n\tglobal_load_lds_dwordx4 %1, %4 offset:" #o "\n\t"
                     XP(128) XP(256) XP(384) XP(512) XP(640) XP(768)
                     "s_add_u32 m0, %6, 8192-896\n\ts_barrier\n\tglobal_load_lds_dwordx4 %5, %7 offset:896\n\t"
                     "s_add_u32 m0, %6, 16384-1792\n\ts_nop 0\n\tglobal_load_lds_dwordx4 %5, %7 offset:1792\n\t"
                     "s_mov_b32 m0, %6\n\ts_nop 0\n\tglobal_load_lds_dwordx4 %5, %7"
                     : "+s"(xd) : "v"(xo), "s"(xd0), "s"(xd0 + 7 * 4096), "s"(x), "v"(w0), "s"(lds0 + 58368 + w * 1024), "s"(wqkv) : "memory", "scc");
    }
    SB();
    const rsrc_t rws = __builtin_amdgcn_make_buffer_rsrc((void*)ws, 0, WS_END, 0x00020000);
    u64 epoch, tagv;
    unsigned fl1;
    const unsigned ones = 0x3f803f80u;
    const bool ffn = loc < 28;
    const int l7 = loc / 7, m7 = loc % 7;
    const int tb = xcd * 4 + l7;
    const int tt = w & 1, ct = (w >> 1) & 1, kh = w >> 2;
    const unsigned swz = (unsigned)(((lane & 7) ^ (((row8 >> 1) & 1) << 2)) * 16);

    if (__builtin_expect(ffn, 1)) {
        constexpr int A_W = 58368, A_WS = 24576;
#define A_KC(c) (rot + (c) >= 7 ? rot + (c) - 7 : rot + (c))
        const unsigned wvo = (unsigned)(row8 * 2688) + swz;
        const char* wsrc = wqkv + acg * 128 + (size_t)w * 8 * 2688;
        const unsigned wdst = lds0 + A_W + w * 1024;
#define A_PIECE(c, i) glds(wvo, wsrc + A_KC(c) * (64 * 2688) + (i) * 896, wdst + ((c) & 3) * A_WS + (i) * 8192)
#define A_GROUP(c) do { A_PIECE(c, 1); A_PIECE(c, 2); A_PIECE(c, 0); } while (0)
        epoch = peek_flag(ws, FL_EPOCH * 128); FENCE(); fl1 = (FL_KV + atb * 8 + acg) * 128;
        lds_ptr xb[4], wb1, wb1h;
#pragma unroll
        for (int j = 0; j < 4; ++j) xb[j] = lds + 1024 + tt * 28672 + (r >> 3) * 1024 + (r & 7) * 128 + (((2 * j + hf) ^ ((r >> 1) & 7)) * 16);
        wb1 = opq(lds + A_W + (kh ? 0 : 8192) + (8 * hf + q4) * 128 + ((ct ^ (q4 >> 1)) * 64) + g1 * 32 + p4 * 8);
        wb1h = opq(wb1 + 2 * A_WS);
        unsigned go1[2], go2[2]; int gi[2];
#pragma unroll
        for (int p = 0; p < 2; ++p) {
            const int G = acg * 8 + ct * 4 + 2 * p + hf, hp = G / 7, i = G - hp * 7;
            const unsigned hs = kh ? 131072u : 262144u;
            const unsigned to = (kh ? WS_QF + (atb * 2 + tt) * 2048 : WS_KV + (atb * 2 + tt) * 4096) + r * 16;
            go1[p] = (unsigned)(2 * hp + (i > 3)) * hs + (unsigned)(i > 3 ? i - 3 : i) * 512 + to;
            go2[p] = i == 3 ? (unsigned)(2 * hp + 1) * hs + to : OOB;
            gi[p] = i;
        }
        const int vcol = acg * 64 + ct * 32 + r, vh = vcol / 28, vd = vcol - vh * 28;
        unsigned vo = WS_KV + (atb * 2 + tt) * 4096 + (unsigned)vh * 262144 + 2048 + hf * 512 + vd * 16;
        PIN2(go1[0], go1[1]); PIN2(go2[0], go2[1]); PIN(vo);
        lds_ptr xw = opq(lds + 1024 + kh * 28672 + (w & 3) * 1024 + l16);
        waitv<4>();
        TS(28);
        u32x4 xv[7];
#pragma unroll
        for (int c = 0; c < 7; ++c) xv[c] = *(LAS u32x4*)(xw + c * 4096);
        SB();
        f32x4 sm = {};
#pragma unroll
        for (int c = 0; c < 7; ++c)
#pragma unroll
            for (int j = 0; j < 4; ++j) sm[j] = dot2(xv[c][j], ones, sm[j]);
        float nm = sum8((sm[0] + sm[1]) + (sm[2] + sm[3])) * (-1.f / D);
        PIN(nm);
#pragma unroll
        for (int c = 0; c < 7; ++c) { xv[c] = shift8(xv[c], nm); *(LAS u32x4*)(xw + c * 4096) = xv[c]; }
        A_GROUP(1);
        SB();
        sm = (f32x4){};
#pragma unroll
        for (int c = 0; c < 7; ++c)
#pragma unroll
            for (int j = 0; j < 4; ++j) sm[j] = dot2(xv[c][j], xv[c][j], sm[j]);
        LAS float* rst = (LAS float*)lds;
        rst[w * 8 + row8] = __builtin_amdgcn_rsqf(sum8((sm[0] + sm[1]) + (sm[2] + sm[3])) * (1.f / D) + EPS);
        A_GROUP(2);
        TS(31);
        bf16x8 fx[4], f1[4], f2[4];
#define A_WAIT(c) do { if ((c) == 0) waitv_bar<6>(); else if ((c) == 1) waitv_barx<6>(); else if ((c) < 6) waitv_barx<3>(); else waitv_barx<0>(); \
                       if ((c) == 0) TS(1); if ((c) == 6) TS(2); } while (0)
#define A_WP(c, ks) ((((c) & 2) ? wb1h : wb1) + ((c) & 1) * A_WS + (ks) * 2048)
        if (w < 4) {
            f32x16 ak = {}, av = {};
            f32x4 rv[4];
            float rs = 0.f;
#pragma unroll
            for (int n = 0; n < 30; ++n) {
                const int c = n >> 2, ks = n & 3, km = (n - 2) & 3;
                if (n < 28 && ks == 0) A_WAIT(c);
                if (n >= 2) { ak = MFMA(f1[km], fx[km], ak); PIN(ak); } SB();
                if (n < 28) { fx[ks] = lds8(xb[ks] + c * 4096); f1[ks] = cat(trd(A_WP(c, ks)), trd(A_WP(c, ks) + 512)); } SB();
                if (n >= 2) { av = MFMA(fx[km], f2[km], av); PIN(av); } SB();
                if (n < 28) f2[ks] = cat(trd(A_WP(c, ks) + 8192), trd(A_WP(c, ks) + 8192 + 512));
                if (c == 0 && ks < 3) A_PIECE(3, ks); if (c >= 2 && c < 5 && ks < 3) A_PIECE(c + 2, ks);
                if (n == 21) {
                    rs = rst[tt * 32 + r];
#pragma unroll
                    for (int g = 0; g < 4; ++g) rv[g] = *(LAS f32x4*)(rst + tt * 32 + 8 * g + 4 * hf);
                }
                if (n == 24) { PIN(epoch); tagv = epoch + 1; PIN(tagv); }
                if (n == 24) {
                    const unsigned pv = vd == 0 ? ones : 0u, po = vd < 4 ? vo + 448 : OOB;
                    st16_dev((u32x4){pv, pv, pv, pv}, rws, po, 0); st16_dev((u32x4){pv, pv, pv, pv}, rws, po + 1024, 0);
                }
                SB();
            }
            TS(3);
#pragma unroll
            for (int i = 0; i < 16; ++i) { ak[i] *= rs; av[i] *= rv[i >> 2][i & 3]; }
#pragma unroll
            for (int p = 0; p < 2; ++p) {
                const u32x4 f = pack8(ak, p);
                const auto s0 = __builtin_amdgcn_permlane32_swap(f[0], f[2], false, false), s1 = __builtin_amdgcn_permlane32_swap(f[1], f[3], false, false);
                const u32x4 c = {s0[0], s1[0], s0[1], s1[1]};
                st16_dev(c, rws, go1[p], 0); st16_dev(c, rws, go2[p], 0);
            }
            st16_dev(pack8(av, 0), rws, vo, 0); st16_dev(pack8(av, 1), rws, vo + 1024, 0);
        } else {
            f32x16 aq = {};
            __builtin_amdgcn_s_setprio(1);
#pragma unroll
            for (int n = 0; n < 30; ++n) {
                const int c = n >> 2, ks = n & 3, km = (n - 2) & 3;
                if (n < 28 && ks == 0) A_WAIT(c);
                if (n == 24) { PIN(epoch); tagv = epoch + 1; PIN(tagv); }
                if (n >= 2) { aq = MFMA(f1[km], fx[km], aq); PIN(aq); } SB();
                if (n < 28) { fx[ks] = lds8(xb[ks] + c * 4096); f1[ks] = cat(trd(A_WP(c, ks)), trd(A_WP(c, ks) + 512)); }
                if (c == 0 && ks < 3) A_PIECE(3, ks); if (c >= 2 && c < 5 && ks < 3) A_PIECE(c + 2, ks);
                SB();
            }
            __builtin_amdgcn_s_setprio(0);
            const float QS = rst[tt * 32 + r] * (0.18898223650461362f * 1.4426950408889634f);
#pragma unroll
            for (int i = 0; i < 16; ++i) aq[i] *= QS;
#pragma unroll
            for (int p = 0; p < 2; ++p) {
                const u32x4 f = pack8(aq, p);
                const auto s0 = __builtin_amdgcn_permlane32_swap(f[0], f[2], false, false), s1 = __builtin_amdgcn_permlane32_swap(f[1], f[3], false, false);
                const bool sp = gi[p] == 3;
                st16_dev((u32x4){s0[0], s1[0], sp ? 0u : s0[1], sp ? 0u : s1[1]}, rws, go1[p], 0);
                st16_dev((u32x4){0u, 0u, s0[1], s1[1]}, rws, go2[p], 0);
            }
        }
    } else {
        epoch = peek_flag(ws, FL_EPOCH * 128); fl1 = (FL_RT + xcd * 4 + loc - 28) * 128;
        const int ri = xcd * 4 + loc - 28, nu = (w == 0 && ri < 16) ? 4 : 3;
        const unsigned vo = (unsigned)(row8 * 3584 + (lane & 7) * 16);
#pragma unroll 1
        for (int i = 0; i < nu; ++i) {
            const int u = i < 3 ? i * 256 + ri * 8 + w : 768 + ri;
            const size_t so = (size_t)(u / 28) * 16 * 3584 + (u % 28) * 128;
            const unsigned d_ = lds0 + w * 16384 + i * 4096;
            glds(vo, wg + so, d_); glds(vo, wg + so + 8 * 3584, d_ + 1024); glds(vo, wu + so, d_ + 2048); glds(vo, wu + so + 8 * 3584, d_ + 3072);
        }
#pragma unroll 1
        for (int i = 0; i < nu; ++i) {
            const int u = i < 3 ? i * 256 + ri * 8 + w : 768 + ri;
            waitv<8>();
            lds_ptr sg = lds + w * 16384 + i * 4096 + g1 * 2048 + (4 * hf + q4) * 128 + p4 * 8;
#pragma unroll
            for (int e = 0; e < 4; ++e) st16_dev(U4(cat(trd(sg + e * 32), trd(sg + e * 32 + 1024))), rws, l16, WS_WGU + (unsigned)(((u % 28) * 4 + e) * 28 + u / 28) * 1024);
            FENCE();
        }
        tagv = epoch + 1;
    }
    TS(24);
    const int head = xcd * 2 + (loc & 1), rb = loc >> 1, qt = w & 3;
    const char* kvs = ws + WS_KV + (size_t)head * 262144 + w * 2048;
    const char* qs = ws + WS_QF + (size_t)(head * 64 + rb * 4 + qt) * 2048 + kh * 1024;
    unsigned kvd = lds0 + w * 2048, qd = lds0 + 147456 + qt * 2048 + kh * 1024;
    unsigned pf1 = (FL_KV + (lane >> 1) * 8 + (head * 28 + (lane & 1) * 27) / 64) * 128;
    PINS(kvs); PINS(qs); PINS(kvd); PINS(qd); PIN2(pf1, fl1);
    waitv_bar<0>();
    if (w == 0) set_flag(ws, fl1, tagv);
    const u64 tag = ((u64)(unsigned)__builtin_amdgcn_readfirstlane((unsigned)(tagv >> 32)) << 32 | (u64)(unsigned)__builtin_amdgcn_readfirstlane((unsigned)tagv));
    TS(4);

    const int drb = (xcd >> 1) * 4 + l7, dfg = (xcd & 1) * 7 + m7;
    bf16x8 wf[28];
    u32x2 xa, xb;
    f32x16 cs = {}, acc = {};
    const u32x4 one4 = {ones, ones, ones, ones};
    const unsigned hi_ = (unsigned)((tb * 2 + tt) * 28 + m7 * 4 + ct * 2 + kh), ofo = (unsigned)(tt * 32768 + kh * 16384 + ct * 4096);
    unsigned ho = WS_HF + hi_ * 1024 + l16, so = hf ? OOB : WS_HS + hi_ * 256 + r * 8;
    const char* os = ws + WS_OF + (size_t)tb * 65536 + ofo;
    unsigned od = lds0 + 65536 + ofo;
    unsigned pf2 = (FL_O + (tb >> 1) * 16 + kh * 8 + ct * 2 + ((lane >> 1) & 1) + (lane & 4)) * 128 + ((lane & 1) * 4 + (tb & 1) * 2 + tt) * 8;
    unsigned go = ffn ? WS_GF + (unsigned)(drb * 4 * 112 + dfg * 8 + w) * 1024 + l16 : OOB;
    unsigned fo3 = (FL_H + tb * 8 + m7) * 128;
    {
        lds_ptr ap = opq(lds + kh * 8192 + l16), apB = opq(lds + 65536 + kh * 8192 + l16), apC = opq(lds + 131072 + kh * 8192 + l16);
        const unsigned lv = kh * 8192 + (l16 ^ (kh * 256));
        lds_ptr vp = opq(lds + lv), vpB = opq(lds + 65536 + lv), vpC = opq(lds + 131072 + lv);
#define AP(off) ((off) < 65536 ? ap + (off) : (off) < 131072 ? apB + ((off) - 65536) : apC + ((off) - 131072))
#define VP(off) ((off) < 65536 ? vp + (off) : (off) < 131072 ? vpB + ((off) - 65536) : vpC + ((off) - 131072))
#define KV_ISSUE(t) glds2_dev(l16, kvs + (t) * 16384, kvd + phys(t))
        unsigned wgo = WS_WGU + (unsigned)((ffn ? dfg : 0) * 8 + w) * 28672;
        const int ri = umin(xcd * 4 + loc - 28, 27);
        const char* wos = (ffn ? wo + m7 * 128 : wd + (ri % 7) * 128 + (size_t)(ri / 7) * 448 * 896) + (size_t)w * 56 * 896;
        unsigned wov = (unsigned)(row8 * 896) + swz;
        const rsrc_t rx = __builtin_amdgcn_make_buffer_rsrc((void*)x, 0, 2048 * 448 * 2, 0x00020000);
        unsigned xo_ = (unsigned)(((tb * 2 + tt) * 32 + r) * 896 + m7 * 128 + ct * 64 + kh * 32 + hf * 8);
        unsigned rto = (FL_RT + r) * 128;
        lds_ptr qp = opq(lds + 147456 + qt * 2048 + l16);
        PINS(wgo); PINS(wos); PIN2(wov, xo_); PIN(rto); PIN2(ho, so); PIN2(pf2, fo3); PIN(go);
        lds_ptr xw = opq(lds + 147456 + w * 2048 + l16), xr = opq(lds + 147456 + (w ^ 4) * 2048 + l16);
        lds_ptr lw = opq(lds + 58368 + w * 256 + lane * 4), lr = opq(lds + 58368 + qt * 256 + 128 + r * 4);
        lds_ptr bw = opq(lds + 60416 + w * 4), br = opq(lds + 60416 + (lane & 7) * 4);
        unsigned oo = WS_OF + (unsigned)((rb * 4 + qt) * 32 + head * 2 + kh) * 1024 + l16;
        unsigned fo2 = (FL_O + rb * 16 + head) * 128 + w * 8;
        PIN2(oo, fo2);
        if (w == HOP3_POLLER) poll_flag(ws, pf1, tag);
        BAR();
        TS(5);
        glds_dev(l16, qs, qd);
        KV_ISSUE(0); KV_ISSUE(1);
        asm volatile("s_barrier" ::: "memory");
        KV_ISSUE(2); KV_ISSUE(3);
        waitv_bar<4>();
        TS(6);
        const bf16x8 q0 = lds8(qp), q1 = lds8(qp + 1024);
        f32x16 o = {}, sa = {}, sb;
        bf16x8 k0 = lds8(ap + phys(0)), k1 = lds8(ap + phys(0) + 1024), v0 = lds8(vp + phys(0) + 2048), v1 = {};
        u32x4 p0, p1 = {};
        sa = MFMA(k0, q0, sa); sa = MFMA(k1, q1, sa);
        k0 = lds8(ap + phys(0) + 4096); k1 = lds8(ap + phys(0) + 5120);
        PIN(sa); SB();
        u64 rtf = 0;
#define EXP4(S, o4) _Pragma("unroll") for (int i_ = o4; i_ < o4 + 4; ++i_) S[i_] = __builtin_amdgcn_exp2f(S[i_]);
#define WLD(s) wf[s] = BF8(ld16_dev(rws, l16 + ((s) & 3) * 1024, wgo + ((s) >> 2) * 4096))
#define SIDE(t, g) do { \
            if ((t) == 0 && (g) == 0) KV_ISSUE(4); if ((t) == 0 && (g) == 2) KV_ISSUE(5); if ((t) == 1 && (g) == 0) KV_ISSUE(6); if ((t) == 1 && (g) == 2) KV_ISSUE(7); \
            if ((t) == 2 && (g) == 0) KV_ISSUE(8); if ((t) == 1 && (g) == 3) rtf = peek_flag(ws, rto); \
            if ((t) == 3 && (g) < 3) KV_ISSUE(9 + (g)); \
            if ((t) == 7 && (g) == 0) KV_ISSUE(12); if ((t) == 7 && (g) == 2) KV_ISSUE(13); if ((t) == 8 && (g) == 0) KV_ISSUE(14); if ((t) == 8 && (g) == 2) KV_ISSUE(15); \
            if ((t) >= 4 && (t) <= 6) WLD(((t) - 4) * 4 + (g)); \
            if (((t) == 7 || (t) == 8) && ((g) & 1)) WLD(12 + ((t) - 7) * 2 + ((g) >> 1)); \
            if ((t) >= 9 && (t) <= 11) WLD(((t) - 5) * 4 + (g)); \
            if ((t) == 12 || ((t) == 13 && (g) < 3)) glds(wov, wos + (((t) - 12) * 4 + (g)) * 8 * 896, lds0 + (w * 7 + ((t) - 12) * 4 + (g)) * 1024); \
            if ((t) == 13 && (g) == 3) { xa = ld8(rx, xo_, 0); xb = ld8(rx, xo_, 16); } \
            if ((t) == 14 && (g) == 1) { if (w >= 6) touch(lane * (w == 6 ? (WS_END / 64 & ~127u) : 28672), w == 6 ? ws : out, lds0 + 61440); } \
            if ((t) == 14 && (g) == 0) { if (tid < 32) *(LAS u32x4*)(lds + 57344 + tid * 16) = (u32x4){0, 0, 0, 0}; } \
        } while (0)
#define TOFF(i) (phys((i) >> 1) + ((i) & 1) * 4096)
#define TILE(i, SC, SN) do { \
            __builtin_amdgcn_s_setprio(((i) & 1) ? 1 : 3); \
            if ((i) < 31) { f32x16 z_ = {}; SN = MFMA(k0, q0, z_); PIN(SN); } if ((i) < 30) k0 = lds8(AP(TOFF((i) + 2))); SB(); \
            EXP4(SC, 0); PIN(SC); SB(); \
            o = MFMA(v1, BF8(p1), o); PIN(o); v1 = lds8(VP(TOFF(i) + 3072)); SIDE((i) >> 1, ((i) & 1) * 2); SB(); \
            EXP4(SC, 4); p0 = pack8(SC, 0); PIN(p0); SB(); \
            __builtin_amdgcn_s_setprio(((i) & 1) ? 0 : 2); \
            if ((i) < 31) { SN = MFMA(k1, q1, SN); PIN(SN); } if ((i) < 30) k1 = lds8(AP(TOFF((i) + 2) + 1024)); SB(); \
            EXP4(SC, 8); PIN(SC); SB(); \
            o = MFMA(v0, BF8(p0), o); PIN(o); if ((i) < 31) v0 = lds8(VP(TOFF((i) + 1) + 2048)); SIDE((i) >> 1, ((i) & 1) * 2 + 1); SB(); \
            EXP4(SC, 12); p1 = pack8(SC, 1); PIN(p1); SB(); } while (0)
#define STEP(t) do { TILE(2 * (t), sa, sb); TILE(2 * (t) + 1, sb, sa); } while (0)
        STEP(0);
        waitv_barx<4>();
        STEP(1); STEP(2);
        waitv_bar2<3>();
        TS(21);
        if (__builtin_expect(!wave_all(rtf == tag), 0)) poll_flag(ws, rto, tag);
        FENCE();
        STEP(3); STEP(4); STEP(5); STEP(6);
        waitv_bar2<12>();
        TS(22);
        STEP(7); STEP(8); STEP(9); STEP(10);
        waitv_bar2<9>();
        TS(23);
        STEP(11); STEP(12); STEP(13); STEP(14); STEP(15);
        o = MFMA(v1, BF8(p1), o);
        TS(7);
        u32x4 ov;
        {
            *(LAS f32x4*)xw = (f32x4){o[8], o[9], o[10], o[11]}; *(LAS f32x4*)(xw + 1024) = (f32x4){o[12], o[13], o[14], o[15]};
            const float l = kh ? o[4] : o[12];
            *(LAS float*)lw = l;
            float sa_ = 0.f;
#pragma unroll
            for (int i = 0; i < 16; ++i) sa_ += __builtin_fabsf(o[i]);
            const bool ok = sa_ < 0x1p100f && (hf == 0 || l > 0x1p-80f);
            *(LAS unsigned*)bw = wave_all(ok) ? 0u : 1u;
            BAR();
            const f32x4 a = *(LAS f32x4*)xr, c = *(LAS f32x4*)(xr + 1024);
            const float inv = __builtin_amdgcn_rcpf(*(LAS float*)lr + *(LAS float*)(lr + 1024));
            const unsigned bad = *(LAS unsigned*)br;
            const float iz = (kh && hf) ? 0.f : inv;
            ov = (u32x4){cvtpk((o[0] + a[0]) * inv, (o[1] + a[1]) * inv), cvtpk((o[2] + a[2]) * inv, (o[3] + a[3]) * inv),
                         cvtpk((o[4] + c[0]) * iz, (o[5] + c[1]) * iz), cvtpk((o[6] + c[2]) * iz, (o[7] + c[3]) * iz)};
            if (__builtin_expect(wave_any(bad != 0), 0)) {
                f32x16 of = {};
                float m = -0x1p126f;
#pragma unroll 1
                for (int t = 0; t < 16; ++t) {
                    BAR();
                    glds2_dev(l16, kvs + t * 16384, lds0 + 65536 + w * 2048);
                    waitv_bar<0>();
#pragma unroll 1
                    for (int u = 0; u < 2; ++u) {
                        f32x16 s = {};
                        s = MFMA(lds8(apB + u * 4096), q0, s); s = MFMA(lds8(apB + u * 4096 + 1024), q1, s);
                        float mx = s[0];
#pragma unroll
                        for (int i = 1; i < 16; ++i) mx = __builtin_fmaxf(mx, s[i]);
                        mx = __builtin_fmaxf(swap_max(mx), m);
                        const float alpha = __builtin_amdgcn_exp2f(m - mx);
                        m = mx;
#pragma unroll
                        for (int i = 0; i < 16; ++i) { s[i] = __builtin_amdgcn_exp2f(s[i] - mx); of[i] *= alpha; }
                        of = MFMA(lds8(vpB + u * 4096 + 2048), BF8(pack8(s, 0)), of);
                        of = MFMA(lds8(vpB + u * 4096 + 3072), BF8(pack8(s, 1)), of);
                    }
                }
                *(LAS f32x4*)xw = (f32x4){of[8], of[9], of[10], of[11]}; *(LAS f32x4*)(xw + 1024) = (f32x4){of[12], of[13], of[14], of[15]};
                *(LAS float*)lw = hf ? (kh ? of[4] : of[12]) : m;
                BAR();
                const f32x4 a2 = *(LAS f32x4*)xr, c2 = *(LAS f32x4*)(xr + 1024);
                const float m0 = *(LAS float*)(lr - 128), m1 = *(LAS float*)(lr + 1024 - 128), mm = __builtin_fmaxf(m0, m1);
                const float e0 = __builtin_amdgcn_exp2f(m0 - mm), e1 = __builtin_amdgcn_exp2f(m1 - mm);
                const float iv = __builtin_amdgcn_rcpf(*(LAS float*)lr * e0 + *(LAS float*)(lr + 1024) * e1);
                const float im = (kh ? e1 : e0) * iv, io = (kh ? e0 : e1) * iv;
                const bool z = kh && hf;
                ov = (u32x4){cvtpk(of[0] * im + a2[0] * io, of[1] * im + a2[1] * io), cvtpk(of[2] * im + a2[2] * io, of[3] * im + a2[3] * io),
                             z ? 0u : cvtpk(of[4] * im + c2[0] * io, of[5] * im + c2[1] * io), z ? 0u : cvtpk(of[6] * im + c2[2] * io, of[7] * im + c2[3] * io)};
            }
        }
        st16_dev(ov, rws, oo, 0);
        TS(25);
        FENCE();
        PIN2(xa, xb);
#pragma unroll
        for (int s = 27; s >= 0; --s) PIN(wf[s]);
#pragma unroll
        for (int s = 0; s < 8; ++s) cs = MFMA(wf[s], BF8(one4), cs);
        acc[0] = bflo(xa[0]); acc[1] = bfhi(xa[0]); acc[2] = bflo(xa[1]); acc[3] = bfhi(xa[1]); acc[4] = bflo(xb[0]); acc[5] = bfhi(xb[0]); acc[6] = bflo(xb[1]); acc[7] = bfhi(xb[1]);
        PIN(acc); PIN2(ho, so); PIN2(pf2, fo3); PINS(os); PINS(od); PIN(cs);
        waitv<0>();
        set_flag(ws, fo2, tagv);
        BAR();
        TS(8);
    }
    if (__builtin_expect(!ffn, 0)) {
        const int ri = xcd * 4 + loc - 28;
        if (ri < 28) {
#pragma unroll 1
            for (int u = w; u < 28; u += 8) {
                lds_ptr sd = lds + u * 2048 + (4 * hf + q4) * 128 + g1 * 32 + p4 * 8;
#pragma unroll
                for (int e = 0; e < 2; ++e) { lds_ptr p_ = sd + ((e ^ (q4 >> 1)) * 64);
                    st16_dev(U4(cat(trd(p_), trd(p_ + 1024))), rws, l16, WS_WD + (unsigned)(((ri % 7) * 112 + (ri / 7) * 28 + u) * 2 + e) * 1024); }
            }
        }
        waitv_bar<0>();
        if (w == 0) set_flag(ws, (FL_RD + ri) * 128, tagv);
        __builtin_amdgcn_s_sleep(CLR_SLEEP);
#pragma unroll 1
        for (int i = 0; i < 14; ++i) st16_dev((u32x4){~0u, ~0u, ~0u, ~0u}, rws, l16, WS_GF + (unsigned)(((ri >> 1) * 4 + 2 + (ri & 1)) * 112 + w * 14 + i) * 1024);
        waitv_bar<0>();
        if (w == 0) set_flag(ws, (FL_CL + ri) * 128, tagv);
        return;
    }

    const int erb = tb >> 1;
    const char* hs = ws + WS_HF + (size_t)drb * 114688 + w * 1024;
    const char* hss = ws + WS_HS + (size_t)((drb * 4 + (w & 3)) * 28 + kh * 14) * 256;
    unsigned pf3, fo4 = (FL_G + drb * 64 + dfg) * 128 + w * 8;
    {
        lds_ptr wb = lds + kh * (8 * 28 * 128) + (4 * hf + q4) * 128 + ((ct ^ (q4 >> 1)) * 64) + (g1 ^ kh) * 32 + p4 * 8;
        bf16x8 wr[16];
#pragma unroll
        for (int s = 0; s < 12; ++s) { const int k0 = ((s >> 1) * 28 + (s & 1) * 16) * 128; wr[s] = cat(trd(wb + k0), trd(wb + k0 + 1024)); }
        lds_ptr cw = opq(lds + 131072 + w * 2048 + l16), cr = opq(lds + 131072 + (w ^ 4) * 2048 + l16);
        poll_flag(ws, pf2, tag);
        TS(9);
        glds4_dev(l16, os, od);
        asm volatile("s_barrier" ::: "memory");
        glds4_dev(l16, os + 8192, od + 8192);
        lds_ptr ob = opq(lds + 65536 + tt * 32768 + kh * 16384 + l16);
        bf16x8 of[8];
        waitv_bar<4>();
        TS(26);
#pragma unroll
        for (int s = 0; s < 8; ++s) of[s] = lds8(ob + s * 1024);
        SB();
#pragma unroll
        for (int s = 0; s < 8; ++s) acc = MFMA(wr[s], of[s], acc);
        PIN(acc); SB();
#pragma unroll
        for (int s = 12; s < 16; ++s) { const int k0 = ((s >> 1) * 28 + (s & 1) * 16) * 128; wr[s] = cat(trd(wb + k0), trd(wb + k0 + 1024)); }
        waitv_bar<0>();
#pragma unroll
        for (int s = 0; s < 8; ++s) of[s] = lds8(ob + (8 + s) * 1024);
        SB();
#pragma unroll
        for (int s = 0; s < 8; ++s) acc = MFMA(wr[8 + s], of[s], acc);
        TS(10);
        *(LAS f32x4*)cw = (f32x4){acc[8], acc[9], acc[10], acc[11]}; *(LAS f32x4*)(cw + 1024) = (f32x4){acc[12], acc[13], acc[14], acc[15]};
        BAR();
        const f32x4 a = *(LAS f32x4*)cr + (f32x4){acc[0], acc[1], acc[2], acc[3]}, b = *(LAS f32x4*)(cr + 1024) + (f32x4){acc[4], acc[5], acc[6], acc[7]};
        const u32x4 hv = {cvtpk(a[0], a[1]), cvtpk(a[2], a[3]), cvtpk(b[0], b[1]), cvtpk(b[2], b[3])};
        st16_dev(hv, rws, ho, 0);
        float s1 = 0.f, s2 = 0.f;
        acc_stats(hv, s1, s2);
        s1 = swap_add(s1); s2 = swap_add(s2);
        st8_dev((u32x2){f2u(s1), f2u(s2)}, rws, so, 0);
        FENCE();
#pragma unroll
        for (int s = 8; s < 14; ++s) cs = MFMA(wf[s], BF8(one4), cs);
        pf3 = (FL_H + drb * 16 + (lane & 8) + umin(lane & 7, 6)) * 128;
        PIN(go); PIN2(pf3, fo4); PINS(hs); PINS(hss); PIN(cs);
        waitv_bar<0>();
        if (w == 0) set_flag(ws, fo3, tagv);
        if (bid == 0 && w == 0) set_flag(ws, FL_EPOCH * 128, tagv);
        TS(11);
    }

    const unsigned wdo = WS_WD + (unsigned)(m7 * 112 + w * 14) * 2048;
    bf16x8 wdf[28];
    bf16x8 gf[8];
    const unsigned erot = EROT * m7;
#define E_KS(s) (erot + (s) >= 14 ? erot + (s) - 14 : erot + (s))
    const int ta = erb * 4 + (tb & 1);
    const unsigned gxo = WS_GF + (unsigned)(ta * 112 + w * 14) * 1024;
    u64 gfl = 0;
    const unsigned gks = w * 14 + umin(lane & 15, 13), gfi = (FL_G + (erb * 4 + (tb & 1)) * 16 + (gks >> 3)) * 128 + (gks & 7) * 8;
    {
#pragma unroll
        for (int s = 14; s < 28; ++s) cs = MFMA(wf[s], BF8(one4), cs);
        lds_ptr csp = opq(lds + 161792 + w * 128 + hf * 64);
        if (r == 0) {
#pragma unroll
            for (int g = 0; g < 4; ++g) *(LAS f32x4*)(csp + g * 16) = (f32x4){cs[4 * g], cs[4 * g + 1], cs[4 * g + 2], cs[4 * g + 3]};
        }
        const unsigned hd = lds0 + w * 1024;
        const char* wds = ws + wdo;
        lds_ptr hb = opq(lds + l16), hb2 = opq(lds + 57344 + l16);
        lds_ptr wl = opq(lds + 114688 + w * 5120 + l16), wl2 = opq(lds + w * 7168 + l16);
        LAS f32x2* stp = (LAS f32x2*)opq(lds + 159744 + r * 8);
        if (w == HOP3_POLLER) poll_flag(ws, pf3, tag);
        BAR();
        TS(12);
#define H_PIECE(j) glds_dev(l16, hs + (j) * 8192, hd + (j) * 8192)
#define WDO(s) (E_KS((s) >> 1) * 2048 + ((s) & 1) * 1024)
#define WDLD(s) wdf[s] = BF8(ld16_dev(rws, l16, wdo + WDO(s)))
#define HB(n) ((n) < 56 ? hb + (n) * 1024 : hb2 + ((n) - 56) * 1024)
        H_PIECE(0);
        asm volatile("s_barrier" ::: "memory");
        H_PIECE(1); H_PIECE(2); H_PIECE(3);
        constexpr int PF = 5;
        bf16x8 bf[PF + 1];
        f32x16 acc[2];
        f32x2 sp[7], sv0, sv1;
        f32x4 c4[4];
        float mean = 0.f, r2 = 0.f, hp = 0.f;
        u32x4 hv;
        u64 rdf = 0;
        const unsigned rdo = (FL_RD + (w >> 1) * 7 + m7) * 128, clo = (FL_CL + (hf ? tb : drb * 2 + (lane & 1))) * 128;
        waitv_barx<3>();
        TS(27);
#pragma unroll
        for (int n = 0; n < PF; ++n) bf[n] = lds8(HB(n));
        SB();
        sfor<0, 125>([&](auto N_) __attribute__((always_inline)) {
            constexpr int n = decltype(N_)::v, p = n / 28, s = n % 28;
            if constexpr (n == 3) waitv_barx<3>();
            if constexpr (n == 11) waitv_barx<7>();
            if constexpr (n == 27) { waitv_barx<3>(); TS(13); }
            if constexpr (n == 64) { waitv_barx<10>(); set_flag(ws, fo4, tagv); TS(14); }
            if constexpr (n == 81) { waitv<7>(); set_flag(ws, fo4 + 2048, tagv); }
            if constexpr (n == 39) { if (__builtin_expect(!wave_all(rdf == tag), 0)) poll_flag(ws, rdo, tag); FENCE(); }
            if constexpr (d_prio(n) != d_prio(n - 1)) __builtin_amdgcn_s_setprio(d_prio(n));
            if constexpr (n < 112) { if constexpr (s == 0) { f32x16 z = {}; acc[p & 1] = MFMA(wf[s], bf[n % (PF + 1)], z); } else acc[p & 1] = MFMA(wf[s], bf[n % (PF + 1)], acc[p & 1]); PIN(acc[p & 1]); }
            SB();
            if constexpr (n + PF < 112) bf[(n + PF) % (PF + 1)] = lds8(HB(n + PF));
            if constexpr (n % 3 == 2 && n < 30) H_PIECE(4 + n / 3);
            if constexpr (n == 4) glds4_dev(l16, hss, lds0 + 114688 + w * 5120);
            if constexpr (n == 16) { waitv<4>();
#pragma unroll
                for (int j = 0; j < 7; ++j) sp[j] = *(LAS f32x2*)(lds + 114688 + w * 5120 + (hf * 7 + j) * 256 + r * 8);
            }
            if constexpr (n == 20) { const f32x2 t = ((sp[0] + sp[1]) + (sp[2] + sp[3])) + ((sp[4] + sp[5]) + sp[6]); stp[w * 32] = (f32x2){swap_add(t[0]), swap_add(t[1])}; }
            if constexpr (n >= 39 && n < 48 && n % 2 == 1) glds_dev(l16, wds + WDO((n - 39) / 2), lds0 + 114688 + w * 5120 + (n - 39) / 2 * 1024);
            if constexpr (n >= 48 && n < 64 && n % 3 == 0) WDLD(12 + (n - 48) / 3);
            if constexpr (n >= 70 && n < 84 && n % 2 == 0) glds_dev(l16, wds + WDO(5 + (n - 70) / 2), lds0 + w * 7168 + (n - 70) / 2 * 1024);
            if constexpr (n >= 85 && n < 105 && n % 2 == 1) WDLD(18 + (n - 85) / 2);
            if constexpr (n == 106) { waitv<4>(); set_flag(ws, fo4 + 4096, tagv); }
            if constexpr (n == 107) gfl = peek_flag(ws, gfi);
            if constexpr (n == 30) rdf = peek_flag(ws, rdo);
            if constexpr (n == 79) rdf = peek_flag(ws, clo);
            if constexpr (n == 95) { if (__builtin_expect(!wave_all(rdf == tag), 0)) poll_flag(ws, clo, tag); FENCE(); }
            if constexpr (p >= 1) {
                const f32x16& a = acc[(p - 1) & 1];
                if constexpr (n == 100) { sv0 = stp[3 * 32]; sv1 = stp[7 * 32]; }
                if constexpr (s == 0 && p < 4) { sv0 = stp[(p - 1) * 32]; sv1 = stp[(p + 3) * 32];
#pragma unroll
                    for (int g = 0; g < 4; ++g) c4[g] = *(LAS f32x4*)(csp + g * 16); }
                if constexpr ((s == 3 && p < 4) || n == 104) { const f32x2 sv = sv0 + sv1; mean = sv[0] * (1.f / D); r2 = __builtin_amdgcn_rcpf(__builtin_fmaxf(sv[1] * (1.f / D) - mean * mean, 0.f) + EPS); PIN2(mean, r2); }
                if constexpr (s >= 4 && s < 12) { constexpr int i = s - 4;
                    const float h0 = __builtin_fmaxf(a[i] - mean * c4[i >> 2][i & 3], 0.f) * r2 * (a[8 + i] - mean * c4[2 + (i >> 2)][i & 3]);
                    if constexpr (i & 1) { hv[i >> 1] = p >= 3 ? umin(cvtpk(hp, h0), ~1u) : cvtpk(hp, h0); PIN(hv); } else { hp = h0; PIN(hp); } }
                if constexpr (s == 12) { st16_dev(hv, rws, go + (p - 1) * 114688, 0); FENCE(); }
            }
            SB();
        });
        TS(15);
        if (__builtin_expect(!wave_all(gfl == tag), 0)) poll_flag(ws, gfi, tag);
        FENCE();
#pragma unroll
        for (int s = 0; s < 8; ++s) { gf[s] = BF8(ld16_dev(rws, l16, gxo + E_KS(s) * 1024)); SB(); }
        FENCE();
#pragma unroll
        for (int s = 0; s < 12; ++s) wdf[s] = lds8(s < 5 ? wl + s * 1024 : wl2 + (s - 5) * 1024);
    }

    {
        const int etok = tid >> 4, eblk = tid & 15;
        const unsigned hro = (unsigned)((((eblk >> 2) & 3) * 64 + (eblk & 1) * 32 + etok) * 16 + ((eblk >> 1) & 1) * 8) + WS_HF + (unsigned)(ta * 28 + m7 * 4) * 1024;
        u32x2 hres[2];
        GAS u64* op = (GAS u64*)(out + (size_t)(ta * 32 + etok) * 896 + m7 * 128 + eblk * 8);
        lds_ptr rp = opq(lds + w * 8448 + hf * 528 + r * 16), rq = opq(lds + eblk * 528 + etok * 16);
        lds_ptr rp2 = opq(lds + 67584 + w * 8448 + hf * 528 + r * 16), rq2 = opq(lds + 67584 + eblk * 528 + etok * 16);
#define G_LD(s) gf[(s) & 7] = BF8(ld16_dev(rws, l16, gxo + ((s) / 14) * 229376 + E_KS((s) % 14) * 1024))
#define G_PUT(RP) do { \
            _Pragma("unroll") for (int g = 0; g < 4; ++g) *(LAS f32x4*)(RP + g * 1056) = (f32x4){c0[4 * g], c0[4 * g + 1], c0[4 * g + 2], c0[4 * g + 3]}; \
            _Pragma("unroll") for (int g = 0; g < 4; ++g) *(LAS f32x4*)(RP + 4224 + g * 1056) = (f32x4){c1[4 * g], c1[4 * g + 1], c1[4 * g + 2], c1[4 * g + 3]}; } while (0)
#define G_OUT(hh, RQ) do { f32x4 pa[8]; \
            _Pragma("unroll") for (int ww = 0; ww < 8; ++ww) pa[ww] = *(LAS f32x4*)(RQ + ww * 8448); SB(); \
            const f32x4 a = ((pa[0] + pa[1]) + (pa[2] + pa[3])) + ((pa[4] + pa[5]) + (pa[6] + pa[7])); \
            const u32x2 ov = {cvtpk(a[0] + bflo(hres[hh][0]), a[1] + bfhi(hres[hh][0])), cvtpk(a[2] + bflo(hres[hh][1]), a[3] + bfhi(hres[hh][1]))}; \
            __hip_atomic_store(op + (hh) * (64 * 896 / 8), __builtin_bit_cast(u64, ov), __ATOMIC_RELAXED, __HIP_MEMORY_SCOPE_AGENT); } while (0)
        f32x16 c0 = {}, c1 = {};
        TS(16);
        unsigned seen = 0;
#pragma unroll
        for (int s = 0; s < 28; ++s) {
            if (s == 1) { hres[0] = ld8_dev(rws, hro, 0); hres[1] = ld8_dev(rws, hro, 57344); FENCE(); }
            if (s == 10) BAR();
            if (s == 14) { G_PUT(rp); c0 = (f32x16){}; c1 = (f32x16){}; TS(17); }
            if (s == 20) { waitv<8>(); set_flag(ws, fo4 + 6144, tagv); PIN2(hres[0], hres[1]); BAR(); G_OUT(0, rq); FENCE(); TS(18); }
            SB();
            c0 = MFMA(wdf[2 * (s % 14)], gf[s & 7], c0); PIN(c0); c1 = MFMA(wdf[2 * (s % 14) + 1], gf[s & 7], c1); PIN(c1);
            if (s >= 14) { const u32x4 u = U4(gf[s & 7]); seen = umax3(umax3(seen, u[0], u[1]), u[2], u[3]); PIN(seen); }
            SB();
            if (s < 20) G_LD(s + 8);
        }
        if (__builtin_expect(wave_any(seen == ~0u), 0)) {
            poll_flag(ws, gfi + 4096, tag);
            c0 = (f32x16){}; c1 = (f32x16){};
#pragma unroll
            for (int s = 14; s < 22; ++s) G_LD(s);
#pragma unroll
            for (int s = 14; s < 28; ++s) { c0 = MFMA(wdf[2 * (s % 14)], gf[s & 7], c0); c1 = MFMA(wdf[2 * (s % 14) + 1], gf[s & 7], c1); PIN2(c0, c1); SB(); if (s < 20) G_LD(s + 8); SB(); }
        }
        G_PUT(rp2);
        TS(19);
        BAR();
        G_OUT(1, rq2);
    }
    TS(20);
}

#define WS_OFF 0
extern "C" void kernel_launch(void* const* d_in, const int* in_sizes, int n_in, void* d_out, int out_size, void* d_ws, size_t ws_size, hipStream_t stream) {
    (void)hipFuncSetAttribute((const void*)block_fwd, hipFuncAttributeMaxDynamicSharedMemorySize, LDS_BYTES);
    block_fwd<<<256, 512, LDS_BYTES, stream>>>((const char*)d_in[0], (const char*)d_in[1], (const char*)d_in[2], (const char*)d_in[4], (const char*)d_in[5], (const char*)d_in[6], (char*)d_out, (char*)d_ws + WS_OFF);
}
```

```cpp
#include <hip/hip_runtime.h>
#include <hip/hip_bf16.h>
#include <stdint.h>

typedef __attribute__((ext_vector_type(8))) short bf16x8;
typedef __attribute__((ext_vector_type(4))) short s16x4;
typedef __attribute__((ext_vector_type(2))) __bf16 bf16x2;
typedef __attribute__((ext_vector_type(16))) float f32x16;
typedef __attribute__((ext_vector_type(4))) float f32x4;
typedef __attribute__((ext_vector_type(2))) float f32x2;
typedef __attribute__((ext_vector_type(4))) unsigned u32x4;
typedef __attribute__((ext_vector_type(2))) unsigned u32x2;
typedef short v4i16_t __attribute__((ext_vector_type(4)));
typedef unsigned long long u64;
#define LAS __attribute__((address_space(3)))
#define GAS __attribute__((address_space(1)))
typedef LAS char* lds_ptr;
#if defined(__HIP_DEVICE_COMPILE__)
#define NOPK __attribute__((target("no-packed-fp32-ops")))
#else
#define NOPK
#endif
#define DI __device__ __forceinline__
#define MFMA(a, b, c) __builtin_amdgcn_mfma_f32_32x32x16_bf16(a, b, c, 0, 0, 0)
#define MFMA16(a, b, c) __builtin_amdgcn_mfma_f32_16x16x32_bf16(a, b, c, 0, 0, 0)
#define BF8(x) __builtin_bit_cast(bf16x8, x)
#define U4(x) __builtin_bit_cast(u32x4, x)

constexpr int D = 448;
constexpr float EPS = 1e-5f;
constexpr unsigned WS_FLAG = 0;
constexpr unsigned FL_EPOCH = 0, FL_KV = 1, FL_RT = FL_KV + 256, FL_RD = FL_RT + 32, FL_CL = FL_RD + 32, FL_O = FL_CL + 32, FL_H = FL_O + 256, FL_G = FL_H + 256;
constexpr unsigned WS_KV = 1u << 20;
constexpr unsigned WS_QF = WS_KV + 4194304;
constexpr unsigned WS_OF = WS_QF + 2097152;
constexpr unsigned WS_HF = WS_OF + 2097152;
constexpr unsigned WS_GF = WS_HF + 64 * 28 * 1024;
constexpr unsigned WS_WGU = WS_GF + 64 * 112 * 1024;
constexpr unsigned WS_WD = WS_WGU + 112 * 28 * 1024;
constexpr unsigned WS_HS = WS_WD + 7 * 112 * 2 * 1024;
constexpr unsigned WS_END = WS_HS + 64 * 28 * 256 + 1024;
constexpr unsigned OOB = 0x40000000u;
constexpr int LDS_BYTES = 163840;

DI float u2f(unsigned u) { return __builtin_bit_cast(float, u); }
DI unsigned f2u(float f) { return __builtin_bit_cast(unsigned, f); }
DI unsigned umin(unsigned a, unsigned b) { return a < b ? a : b; }
DI unsigned umax3(unsigned a, unsigned b, unsigned c) { const unsigned m = a > b ? a : b; return m > c ? m : c; }
DI bool wave_any(bool p) { return __builtin_amdgcn_ballot_w64(p) != 0; }
DI bool wave_all(bool p) { return __builtin_amdgcn_ballot_w64(!p) == 0; }
DI unsigned cvtpk(float lo, float hi) { return __builtin_bit_cast(unsigned, __builtin_convertvector((f32x2){lo, hi}, bf16x2)); }
DI float bflo(unsigned u) { return u2f(u << 16); }
DI float bfhi(unsigned u) { return u2f(u & 0xffff0000u); }
DI void glds(unsigned voff, const void* sbase, unsigned dst) {
    asm volatile("s_mov_b32 m0, %2\n\ts_nop 0\n\tglobal_load_lds_dwordx4 %0, %1" :: "v"(voff), "s"(sbase), "s"(dst) : "memory");
}
DI void glds_dev(unsigned voff, const void* sbase, unsigned dst) {
    asm volatile("s_mov_b32 m0, %2\n\ts_nop 0\n\tglobal_load_lds_dwordx4 %0, %1 sc1" :: "v"(voff), "s"(sbase), "s"(dst) : "memory");
}
DI void glds2_dev(unsigned voff, const void* sbase, unsigned dst) {
    asm volatile("s_mov_b32 m0, %2\n\ts_nop 0\n\tglobal_load_lds_dwordx4 %0, %1 sc1\n\tglobal_load_lds_dwordx4 %0, %1 offset:1024 sc1" :: "v"(voff), "s"(sbase), "s"(dst) : "memory");
}
DI void glds4_dev(unsigned voff, const void* sbase, unsigned dst) {
    asm volatile("s_mov_b32 m0, %2\n\ts_nop 0\n\tglobal_load_lds_dwordx4 %0, %1 sc1\n\tglobal_load_lds_dwordx4 %0, %1 offset:1024 sc1\n\t"
                 "global_load_lds_dwordx4 %0, %1 offset:2048 sc1\n\tglobal_load_lds_dwordx4 %0, %1 offset:3072 sc1" :: "v"(voff), "s"(sbase), "s"(dst) : "memory");
}
DI s16x4 trd(lds_ptr p) { return __builtin_bit_cast(s16x4, __builtin_amdgcn_ds_read_tr16_b64_v4i16((LAS v4i16_t*)p)); }
DI bf16x8 cat(s16x4 a, s16x4 b) { return __builtin_shufflevector(a, b, 0, 1, 2, 3, 4, 5, 6, 7); }
DI bf16x8 lds8(lds_ptr p) { return *(LAS bf16x8*)p; }
DI float swap_add(float v) { auto r = __builtin_amdgcn_permlane32_swap(f2u(v), f2u(v), false, false); return u2f(r[0]) + u2f(r[1]); }
DI float swap_max(float v) { auto r = __builtin_amdgcn_permlane32_swap(f2u(v), f2u(v), false, false); return __builtin_fmaxf(u2f(r[0]), u2f(r[1])); }
DI u32x4 pack8(const f32x16& a, int s) { return (u32x4){cvtpk(a[8 * s], a[8 * s + 1]), cvtpk(a[8 * s + 2], a[8 * s + 3]), cvtpk(a[8 * s + 4], a[8 * s + 5]), cvtpk(a[8 * s + 6], a[8 * s + 7])}; }
DI float dot2(unsigned a, unsigned b, float c) { return __builtin_amdgcn_fdot2_f32_bf16(__builtin_bit_cast(bf16x2, a), __builtin_bit_cast(bf16x2, b), c, false); }
DI void acc_stats(u32x4 u, float& s1, float& s2) {
#pragma unroll
    for (int i = 0; i < 4; ++i) { const float a = bflo(u[i]), b = bfhi(u[i]); s1 += a; s2 = __builtin_fmaf(a, a, s2); s1 += b; s2 = __builtin_fmaf(b, b, s2); }
}
DI void acc_stats2(unsigned u0, unsigned u1, f32x4& s) {
    const float a = bflo(u0), b = bfhi(u0), c = bflo(u1), d = bfhi(u1);
    s[0] += a; s[1] += b; s[2] = __builtin_fmaf(a, a, s[2]); s[3] = __builtin_fmaf(b, b, s[3]); s[0] += c; s[1] += d; s[2] = __builtin_fmaf(c, c, s[2]); s[3] = __builtin_fmaf(d, d, s[3]);
}
DI float sum8(float v) {
    v += u2f(__builtin_amdgcn_update_dpp(0u, f2u(v), 0xB1, 0xf, 0xf, true));
    v += u2f(__builtin_amdgcn_update_dpp(0u, f2u(v), 0x4E, 0xf, 0xf, true));
    v += u2f(__builtin_amdgcn_update_dpp(0u, f2u(v), 0x141, 0xf, 0xf, true));
    return v;
}
DI u32x4 shift8(u32x4 x, float c) {
    u32x4 o; float t0, t1, t2, t3, t4, t5, t6, t7;
    asm("v_dot2_f32_bf16 %4, %12, %16, %18\n\tv_dot2_f32_bf16 %5, %12, %17, %18\n\tv_dot2_f32_bf16 %6, %13, %16, %18\n\tv_dot2_f32_bf16 %7, %13, %17, %18\n\t"
        "v_dot2_f32_bf16 %8, %14, %16, %18\n\tv_dot2_f32_bf16 %9, %14, %17, %18\n\tv_dot2_f32_bf16 %10, %15, %16, %18\n\tv_dot2_f32_bf16 %11, %15, %17, %18\n\t"
        "v_cvt_pk_bf16_f32 %0, %4, %5\n\tv_cvt_pk_bf16_f32 %1, %6, %7\n\tv_cvt_pk_bf16_f32 %2, %8, %9\n\tv_cvt_pk_bf16_f32 %3, %10, %11"
        : "=&v"(o[0]), "=&v"(o[1]), "=&v"(o[2]), "=&v"(o[3]), "=&v"(t0), "=&v"(t1), "=&v"(t2), "=&v"(t3), "=&v"(t4), "=&v"(t5), "=&v"(t6), "=&v"(t7)
        : "v"(x[0]), "v"(x[1]), "v"(x[2]), "v"(x[3]), "s"(0x00003f80u), "s"(0x3f800000u), "v"(c));
    return o;
}
template <int N> struct IC { static constexpr int v = N; };
template <int B, int E, class F> DI void sfor(F&& f) { if constexpr (B < E) { f(IC<B>{}); sfor<B + 1, E>(f); } }
template <int N> DI void waitv() { asm volatile("s_waitcnt vmcnt(%0)" :: "i"(N) : "memory"); }
template <int N> DI void waitv_bar() { asm volatile("s_waitcnt vmcnt(%0) lgkmcnt(0)\n\ts_barrier" :: "i"(N) : "memory"); }
template <int N> DI void waitv_barx() { asm volatile("s_waitcnt vmcnt(%0)\n\ts_barrier" :: "i"(N) : "memory"); }
template <int N> DI void waitv_bar2() { asm volatile("s_waitcnt vmcnt(%0) lgkmcnt(2)\n\ts_barrier" :: "i"(N) : "memory"); }
DI void touch(unsigned voff, const void* sbase, unsigned junk) {
    asm volatile("s_mov_b32 m0, %2\n\ts_nop 0\n\tglobal_load_lds_dword %0, %1 sc1" :: "v"(voff), "s"(sbase), "s"(junk) : "memory");
}
#define BAR() asm volatile("s_waitcnt lgkmcnt(0)\n\ts_barrier" ::: "memory")
#define FENCE() asm volatile("" ::: "memory")
typedef __amdgpu_buffer_rsrc_t rsrc_t;
DI u32x4 ld16(rsrc_t rs, unsigned voff, unsigned soff) { return U4(__builtin_amdgcn_raw_buffer_load_b128(rs, voff, soff, 0)); }
DI u32x4 ld16_dev(rsrc_t rs, unsigned voff, unsigned soff) { return U4(__builtin_amdgcn_raw_buffer_load_b128(rs, voff, soff, 16)); }
DI u32x2 ld8(rsrc_t rs, unsigned voff, unsigned soff) { return __builtin_bit_cast(u32x2, __builtin_amdgcn_raw_buffer_load_b64(rs, voff, soff, 0)); }
DI u32x2 ld8_dev(rsrc_t rs, unsigned voff, unsigned soff) { return __builtin_bit_cast(u32x2, __builtin_amdgcn_raw_buffer_load_b64(rs, voff, soff, 16)); }
DI void st16_dev(u32x4 v, rsrc_t rs, unsigned voff, unsigned soff) { __builtin_amdgcn_raw_buffer_store_b128(v, rs, voff + soff, 0, 16); }
DI void st8_dev(u32x2 v, rsrc_t rs, unsigned voff, unsigned soff) { __builtin_amdgcn_raw_buffer_store_b64(v, rs, voff, soff, 16); }
#define POLL_SLEEP 1
DI void set_flag(char* ws, unsigned fo, u64 tagv) {
    asm volatile("s_mov_b64 exec, 1\n\tglobal_store_dwordx2 %0, %1, %2 sc1\n\ts_mov_b64 exec, -1" :: "v"(fo), "v"(tagv), "s"(ws) : "memory");
}
DI u64 peek_flag(char* ws, unsigned fo) { return __hip_atomic_load((GAS u64*)(ws + fo), __ATOMIC_RELAXED, __HIP_MEMORY_SCOPE_AGENT); }
DI void peek_lds(char* ws, unsigned fo, unsigned dst) {
    asm volatile("s_mov_b32 m0, %2\n\ts_nop 0\n\tglobal_load_lds_dword %0, %1 sc1\n\ts_add_u32 m0, %2, 252\n\ts_nop 0\n\tglobal_load_lds_dword %0, %1 offset:4 sc1" :: "v"(fo), "s"(ws), "s"(dst) : "memory", "scc");
}
DI u64 peeked(lds_ptr p) { return (u64)*(LAS unsigned*)(p + 256) << 32 | *(LAS unsigned*)p; }
DI void poll_flag(char* ws, unsigned fo, u64 tag) {
#pragma unroll 1
    for (int spin = 0; spin < (1 << 16); ++spin) {
        if (wave_all(peek_flag(ws, fo) == tag)) break;
        __builtin_amdgcn_s_sleep(POLL_SLEEP);
    }
    FENCE();
}
#define SB() __builtin_amdgcn_sched_barrier(0)
#define PINS(a) asm volatile("" : "+s"(a))
#define PIN(a) asm volatile("" : "+v"(a))
#define PIN2(a, b) asm volatile("" : "+v"(a), "+v"(b))
DI lds_ptr opq(lds_ptr p) { unsigned u = (unsigned)(uintptr_t)p; asm volatile("" : "+v"(u)); return (lds_ptr)(uintptr_t)u; }
#define TS(i) do {} while (0)

DI constexpr int wd_slot(int s) { return (s * 17 + 27) / 28 != ((s + 1) * 17 + 27) / 28 ? (s * 17 + 27) / 28 : -1; }
#define EROT 0
#define CLR_SLEEP 90
#define ROTMODE 0
#define EARLYG 0
#define GWG 1
#define GW 8
#define ENL 4
#define HOP3_POLLER 1
DI constexpr int d_prio(int n) { return n < 0 ? -1 : n < 27 ? 3 - n / 7 : n < 64 ? 3 - (n - 27) / 10 : n < 84 ? 3 - (n - 64) / 5 : n < 112 ? 3 - (n - 84) / 7 : 0; }
DI constexpr int phys(int t) { return ((t % 9) + 2) % 9 * 16384; }

__global__ NOPK __launch_bounds__(512, 2) void block_fwd(const char* x, const char* wqkv, const char* wo, const char* wg, const char* wu, const char* wd, char* out, char* ws) {
    extern __shared__ __attribute__((aligned(16))) char smem[];
    lds_ptr lds = (lds_ptr)smem;
    const int tid = __builtin_amdgcn_workitem_id_x(), lane = tid & 63, w = __builtin_amdgcn_readfirstlane(tid >> 6);
    const int bid = __builtin_amdgcn_workgroup_id_x(), xcd = bid & 7, loc = bid >> 3;
    const int r = lane & 31, hf = lane >> 5, q4 = (lane & 15) >> 2, g1 = (lane >> 4) & 1, p4 = lane & 3;
    const unsigned lds0 = (unsigned)(uintptr_t)smem;
    const unsigned l16 = lane * 16;
    TS(0);
    const int sp3 = (loc + 8) >> 5;
    const int atb = (xcd >> 1) * 8 + (loc & 7) + sp3 * (xcd & 1) * 4, acg = (loc >> 3) + (1 - sp3) * (xcd & 1) * 4;
    const int row8 = lane >> 3;
    const int rot8 = ((loc & 7) << sp3) + sp3 * (xcd & 1), rot = rot8 == 7 ? 0 : rot8;
    if (loc < 28) {
        const unsigned xo = (unsigned)((atb * 64 + (tid >> 3)) * 896 + (((tid ^ (tid >> 4)) & 7) * 16));
        const unsigned xd0 = lds0 + 1024 + (w >> 2) * 28672 + (w & 3) * 1024;
        unsigned xd = xd0 + (7 - rot) * 4096;
        const unsigned w0 = (unsigned)((rot * 64 + (tid >> 3)) * 2688 + acg * 128 + (((tid & 7) ^ ((tid >> 2) & 4)) * 16));
        asm volatile("s_cmp_eq_u32 %0, %3\n\ts_cselect_b32 %0, %2, %0\n\ts_mov_b32 m0, %0\n\ts_add_u32 %0, %0, 0x1000\n\tglobal_load_lds_dwordx4 %1, %4\n\t"
#define XP(o) "s_cmp_eq_u32 %0, %3\n\ts_cselect_b32 %0, %2, %0\n\ts_sub_u32 m0, %0, " #o "\n\ts_add_u32 %0, %0, 0x1000\n\tglobal_load_lds_dwordx4 %1, %4 offset:" #o "\n\t"
                     XP(128) XP(256) XP(384) XP(512) XP(640) XP(768)
                     "s_add_u32 m0, %6, 8192-896\n\ts_barrier\n\tglobal_load_lds_dwordx4 %5, %7 offset:896\n\t"
                     "s_add_u32 m0, %6, 16384-1792\n\ts_nop 0\n\tglobal_load_lds_dwordx4 %5, %7 offset:1792\n\t"
                     "s_mov_b32 m0, %6\n\ts_nop 0\n\tglobal_load_lds_dwordx4 %5, %7"
                     : "+s"(xd) : "v"(xo), "s"(xd0), "s"(xd0 + 7 * 4096), "s"(x), "v"(w0), "s"(lds0 + 58368 + w * 1024), "s"(wqkv) : "memory", "scc");
    }
    SB();
    const rsrc_t rws = __builtin_amdgcn_make_buffer_rsrc((void*)ws, 0, WS_END, 0x00020000);
    u64 epoch, tagv;
    unsigned fl1;
    const unsigned ones = 0x3f803f80u;
    const bool ffn = loc < 28;
    const int l7 = loc / 7, m7 = loc % 7;
    const int tb = xcd * 4 + l7;
    const int tt = w & 1, ct = (w >> 1) & 1, kh = w >> 2;
    const unsigned swz = (unsigned)(((lane & 7) ^ (((row8 >> 1) & 1) << 2)) * 16);

    if (__builtin_expect(ffn, 1)) {
        constexpr int A_W = 58368, A_WS = 24576;
#define A_KC(c) (rot + (c) >= 7 ? rot + (c) - 7 : rot + (c))
        const unsigned wvo = (unsigned)(row8 * 2688) + swz;
        const char* wsrc = wqkv + acg * 128 + (size_t)w * 8 * 2688;
        const unsigned wdst = lds0 + A_W + w * 1024;
#define A_PIECE(c, i) glds(wvo, wsrc + A_KC(c) * (64 * 2688) + (i) * 896, wdst + ((c) & 3) * A_WS + (i) * 8192)
#define A_GROUP(c) do { A_PIECE(c, 1); A_PIECE(c, 2); A_PIECE(c, 0); } while (0)
        epoch = peek_flag(ws, FL_EPOCH * 128); FENCE(); fl1 = (FL_KV + atb * 8 + acg) * 128;
        if (EARLYG) A_GROUP(1);
        lds_ptr xb[4], wb1, wb1h;
#pragma unroll
        for (int j = 0; j < 4; ++j) xb[j] = lds + 1024 + tt * 28672 + (r >> 3) * 1024 + (r & 7) * 128 + (((2 * j + hf) ^ ((r >> 1) & 7)) * 16);
        wb1 = opq(lds + A_W + (kh ? 0 : 8192) + (8 * hf + q4) * 128 + ((ct ^ (q4 >> 1)) * 64) + g1 * 32 + p4 * 8);
        wb1h = opq(wb1 + 2 * A_WS);
        unsigned go1[2], go2[2]; int gi[2];
#pragma unroll
        for (int p = 0; p < 2; ++p) {
            const int G = acg * 8 + ct * 4 + 2 * p + hf, hp = G / 7, i = G - hp * 7;
            const unsigned hs = kh ? 131072u : 262144u;
            const unsigned to = (kh ? WS_QF + (atb * 2 + tt) * 2048 : WS_KV + (atb * 2 + tt) * 4096) + r * 16;
            go1[p] = (unsigned)(2 * hp + (i > 3)) * hs + (unsigned)(i > 3 ? i - 3 : i) * 512 + to;
            go2[p] = i == 3 ? (unsigned)(2 * hp + 1) * hs + to : OOB;
            gi[p] = i;
        }
        const int vcol = acg * 64 + ct * 32 + r, vh = vcol / 28, vd = vcol - vh * 28;
        unsigned vo = WS_KV + (atb * 2 + tt) * 4096 + (unsigned)vh * 262144 + 2048 + hf * 512 + vd * 16;
        PIN2(go1[0], go1[1]); PIN2(go2[0], go2[1]); PIN(vo);
        lds_ptr xw = opq(lds + 1024 + kh * 28672 + (w & 3) * 1024 + l16);
        waitv<EARLYG ? 7 : 4>();
        TS(28);
        u32x4 xv[7];
#pragma unroll
        for (int c = 0; c < 7; ++c) xv[c] = *(LAS u32x4*)(xw + c * 4096);
        SB();
        f32x4 sm = {};
#pragma unroll
        for (int c = 0; c < 7; ++c)
#pragma unroll
            for (int j = 0; j < 4; ++j) sm[j] = dot2(xv[c][j], ones, sm[j]);
        float nm = sum8((sm[0] + sm[1]) + (sm[2] + sm[3])) * (-1.f / D);
        PIN(nm);
#pragma unroll
        for (int c = 0; c < 7; ++c) { xv[c] = shift8(xv[c], nm); *(LAS u32x4*)(xw + c * 4096) = xv[c]; }
        if (EARLYG) A_GROUP(2); else A_GROUP(1);
        SB();
        sm = (f32x4){};
#pragma unroll
        for (int c = 0; c < 7; ++c)
#pragma unroll
            for (int j = 0; j < 4; ++j) sm[j] = dot2(xv[c][j], xv[c][j], sm[j]);
        LAS float* rst = (LAS float*)lds;
        rst[w * 8 + row8] = __builtin_amdgcn_rsqf(sum8((sm[0] + sm[1]) + (sm[2] + sm[3])) * (1.f / D) + EPS);
        if (EARLYG) A_GROUP(3); else A_GROUP(2);
        TS(31);
        bf16x8 fx[4], f1[4], f2[4];
#define A_WAIT(c) do { if ((c) == 0) waitv_bar<EARLYG ? 9 : 6>(); else if ((c) == 1) waitv_barx<6>(); else if ((c) < 6) waitv_barx<3>(); else waitv_barx<0>(); \
                       if ((c) == 0) TS(1); if ((c) == 6) TS(2); } while (0)
#define A_WP(c, ks) ((((c) & 2) ? wb1h : wb1) + ((c) & 1) * A_WS + (ks) * 2048)
        if (w < 4) {
            f32x16 ak = {}, av = {};
            f32x4 rv[4];
            float rs = 0.f;
#pragma unroll
            for (int n = 0; n < 30; ++n) {
                const int c = n >> 2, ks = n & 3, km = (n - 2) & 3;
                if (n < 28 && ks == 0) A_WAIT(c);
                if (n >= 2) { ak = MFMA(f1[km], fx[km], ak); PIN(ak); } SB();
                if (n < 28) { fx[ks] = lds8(xb[ks] + c * 4096); f1[ks] = cat(trd(A_WP(c, ks)), trd(A_WP(c, ks) + 512)); } SB();
                if (n >= 2) { av = MFMA(fx[km], f2[km], av); PIN(av); } SB();
                if (n < 28) f2[ks] = cat(trd(A_WP(c, ks) + 8192), trd(A_WP(c, ks) + 8192 + 512));
                if (!EARLYG && c == 0 && ks < 3) A_PIECE(3, ks); if (c >= 2 && c < 5 && ks < 3) A_PIECE(c + 2, ks);
                if (n == 21) {
                    rs = rst[tt * 32 + r];
#pragma unroll
                    for (int g = 0; g < 4; ++g) rv[g] = *(LAS f32x4*)(rst + tt * 32 + 8 * g + 4 * hf);
                }
                if (n == 24) { PIN(epoch); tagv = epoch + 1; PIN(tagv); }
                if (n == 24) {
                    const unsigned pv = vd == 0 ? ones : 0u, po = vd < 4 ? vo + 448 : OOB;
                    st16_dev((u32x4){pv, pv, pv, pv}, rws, po, 0); st16_dev((u32x4){pv, pv, pv, pv}, rws, po + 1024, 0);
                }
                SB();
            }
            TS(3);
#pragma unroll
            for (int i = 0; i < 16; ++i) { ak[i] *= rs; av[i] *= rv[i >> 2][i & 3]; }
#pragma unroll
            for (int p = 0; p < 2; ++p) {
                const u32x4 f = pack8(ak, p);
                const auto s0 = __builtin_amdgcn_permlane32_swap(f[0], f[2], false, false), s1 = __builtin_amdgcn_permlane32_swap(f[1], f[3], false, false);
                const u32x4 c = {s0[0], s1[0], s0[1], s1[1]};
                st16_dev(c, rws, go1[p], 0); st16_dev(c, rws, go2[p], 0);
            }
            st16_dev(pack8(av, 0), rws, vo, 0); st16_dev(pack8(av, 1), rws, vo + 1024, 0);
        } else {
            f32x16 aq = {};
            __builtin_amdgcn_s_setprio(1);
#pragma unroll
            for (int n = 0; n < 30; ++n) {
                const int c = n >> 2, ks = n & 3, km = (n - 2) & 3;
                if (n < 28 && ks == 0) A_WAIT(c);
                if (n == 24) { PIN(epoch); tagv = epoch + 1; PIN(tagv); }
                if (n >= 2) { aq = MFMA(f1[km], fx[km], aq); PIN(aq); } SB();
                if (n < 28) { fx[ks] = lds8(xb[ks] + c * 4096); f1[ks] = cat(trd(A_WP(c, ks)), trd(A_WP(c, ks) + 512)); }
                if (!EARLYG && c == 0 && ks < 3) A_PIECE(3, ks); if (c >= 2 && c < 5 && ks < 3) A_PIECE(c + 2, ks);
                SB();
            }
            __builtin_amdgcn_s_setprio(0);
            const float QS = rst[tt * 32 + r] * (0.18898223650461362f * 1.4426950408889634f);
#pragma unroll
            for (int i = 0; i < 16; ++i) aq[i] *= QS;
#pragma unroll
            for (int p = 0; p < 2; ++p) {
                const u32x4 f = pack8(aq, p);
                const auto s0 = __builtin_amdgcn_permlane32_swap(f[0], f[2], false, false), s1 = __builtin_amdgcn_permlane32_swap(f[1], f[3], false, false);
                const bool sp = gi[p] == 3;
                st16_dev((u32x4){s0[0], s1[0], sp ? 0u : s0[1], sp ? 0u : s1[1]}, rws, go1[p], 0);
                st16_dev((u32x4){0u, 0u, s0[1], s1[1]}, rws, go2[p], 0);
            }
        }
    } else {
        epoch = peek_flag(ws, FL_EPOCH * 128); fl1 = (FL_RT + xcd * 4 + loc - 28) * 128;
        const int ri = xcd * 4 + loc - 28, nu = (w == 0 && ri < 16) ? 4 : 3;
        const unsigned vo = (unsigned)(row8 * 3584 + (lane & 7) * 16);
#pragma unroll 1
        for (int i = 0; i < nu; ++i) {
            const int u = i < 3 ? i * 256 + ri * 8 + w : 768 + ri;
            const size_t so = (size_t)(u / 28) * 16 * 3584 + (u % 28) * 128;
            const unsigned d_ = lds0 + w * 16384 + i * 4096;
            glds(vo, wg + so, d_); glds(vo, wg + so + 8 * 3584, d_ + 1024); glds(vo, wu + so, d_ + 2048); glds(vo, wu + so + 8 * 3584, d_ + 3072);
        }
#pragma unroll 1
        for (int i = 0; i < nu; ++i) {
            const int u = i < 3 ? i * 256 + ri * 8 + w : 768 + ri;
            waitv<8>();
            lds_ptr sg = lds + w * 16384 + i * 4096 + g1 * 2048 + (4 * hf + q4) * 128 + p4 * 8;
#pragma unroll
            for (int e = 0; e < 4; ++e) st16_dev(U4(cat(trd(sg + e * 32), trd(sg + e * 32 + 1024))), rws, l16, WS_WGU + (unsigned)(((u % 28) * 4 + e) * 28 + u / 28) * 1024);
            FENCE();
        }
        tagv = epoch + 1;
    }
    TS(24);
    const int head = xcd * 2 + (loc & 1), rb = loc >> 1, qt = w & 3;
    const char* kvs = ws + WS_KV + (size_t)head * 262144 + w * 2048;
    const char* qs = ws + WS_QF + (size_t)(head * 64 + rb * 4 + qt) * 2048 + kh * 1024;
    unsigned kvd = lds0 + w * 2048, qd = lds0 + 147456 + qt * 2048 + kh * 1024;
    unsigned pf1 = (FL_KV + (lane >> 1) * 8 + (head * 28 + (lane & 1) * 27) / 64) * 128;
    PINS(kvs); PINS(qs); PINS(kvd); PINS(qd); PIN2(pf1, fl1);
    waitv_bar<0>();
    if (w == 0) set_flag(ws, fl1, tagv);
    const u64 tag = ((u64)(unsigned)__builtin_amdgcn_readfirstlane((unsigned)(tagv >> 32)) << 32 | (u64)(unsigned)__builtin_amdgcn_readfirstlane((unsigned)tagv));
    TS(4);

    const int drb = (xcd >> 1) * 4 + l7, dfg = (xcd & 1) * 7 + m7;
    bf16x8 wf[28];
    u32x2 xa, xb;
    f32x16 cs = {}, acc = {};
    const u32x4 one4 = {ones, ones, ones, ones};
    const unsigned hi_ = (unsigned)((tb * 2 + tt) * 28 + m7 * 4 + ct * 2 + kh), ofo = (unsigned)(tt * 32768 + kh * 16384 + ct * 4096);
    unsigned ho = WS_HF + hi_ * 1024 + l16, so = hf ? OOB : WS_HS + hi_ * 256 + r * 8;
    const char* os = ws + WS_OF + (size_t)tb * 65536 + ofo;
    unsigned od = lds0 + 65536 + ofo;
    unsigned pf2 = (FL_O + (tb >> 1) * 16 + kh * 8 + ct * 2 + ((lane >> 1) & 1) + (lane & 4)) * 128 + ((lane & 1) * 4 + (tb & 1) * 2 + tt) * 8;
    pf2 &= ~127u;
    unsigned go = ffn ? WS_GF + (unsigned)(drb * 4 * 112 + dfg * 8 + w) * 1024 + l16 : OOB;
    unsigned fo3 = (FL_H + tb * 8 + m7) * 128;
    {
        lds_ptr ap = opq(lds + kh * 8192 + l16), apB = opq(lds + 65536 + kh * 8192 + l16), apC = opq(lds + 131072 + kh * 8192 + l16);
        const unsigned lv = kh * 8192 + (l16 ^ (kh * 256));
        lds_ptr vp = opq(lds + lv), vpB = opq(lds + 65536 + lv), vpC = opq(lds + 131072 + lv);
#define AP(off) ((off) < 65536 ? ap + (off) : (off) < 131072 ? apB + ((off) - 65536) : apC + ((off) - 131072))
#define VP(off) ((off) < 65536 ? vp + (off) : (off) < 131072 ? vpB + ((off) - 65536) : vpC + ((off) - 131072))
#define KV_ISSUE(t) glds2_dev(l16, kvs + (t) * 16384, kvd + phys(t))
        unsigned wgo = ffn ? WS_WGU + (unsigned)(dfg * 8 + w) * 28672 : OOB;
        unsigned clro = WS_GF + (unsigned)((((xcd * 4 + loc - 28) >> 1) * 4 + 2 + (loc & 1)) * 112 + w * 14) * 1024 + l16;
        const int ri = umin(xcd * 4 + loc - 28, 27);
        const char* wos = (ffn ? wo + m7 * 128 : wd + (ri % 7) * 128 + (size_t)(ri / 7) * 448 * 896) + (size_t)w * 56 * 896;
        unsigned wov = (unsigned)(row8 * 896) + swz;
        const rsrc_t rx = __builtin_amdgcn_make_buffer_rsrc((void*)x, 0, 2048 * 448 * 2, 0x00020000);
        unsigned xo_ = (unsigned)(((tb * 2 + tt) * 32 + r) * 896 + m7 * 128 + ct * 64 + kh * 32 + hf * 8);
        unsigned rto = (FL_RT + r) * 128;
        lds_ptr qp = opq(lds + 147456 + qt * 2048 + l16);
        PINS(wgo); PINS(wos); PIN2(wov, xo_); PIN(rto); PIN2(ho, so); PIN2(pf2, fo3); PIN(go);
        lds_ptr xw = opq(lds + 147456 + w * 2048 + l16), xr = opq(lds + 147456 + (w ^ 4) * 2048 + l16);
        lds_ptr lw = opq(lds + 58368 + w * 256 + lane * 4), lr = opq(lds + 58368 + qt * 256 + 128 + r * 4);
        lds_ptr bw = opq(lds + 60416 + w * 4), br = opq(lds + 60416 + (lane & 7) * 4);
        unsigned oo = WS_OF + (unsigned)((rb * 4 + qt) * 32 + head * 2 + kh) * 1024 + l16;
        unsigned fo2 = (FL_O + rb * 16 + head) * 128 + w * 8;
        PIN2(oo, fo2);
        if (w == HOP3_POLLER) poll_flag(ws, pf1, tag);
        BAR();
        TS(5);
        glds_dev(l16, qs, qd);
        KV_ISSUE(0); KV_ISSUE(1);
        asm volatile("s_barrier" ::: "memory");
        KV_ISSUE(2); KV_ISSUE(3);
        waitv_bar<4>();
        TS(6);
        const bf16x8 q0 = lds8(qp), q1 = lds8(qp + 1024);
        f32x16 o = {}, sa = {}, sb;
        bf16x8 k0 = lds8(ap + phys(0)), k1 = lds8(ap + phys(0) + 1024), v0 = lds8(vp + phys(0) + 2048), v1 = {};
        u32x4 p0, p1 = {};
        sa = MFMA(k0, q0, sa); sa = MFMA(k1, q1, sa);
        k0 = lds8(ap + phys(0) + 4096); k1 = lds8(ap + phys(0) + 5120);
        PIN(sa); SB();
        u64 rtf = 0;
#define EXP4(S, o4) _Pragma("unroll") for (int i_ = o4; i_ < o4 + 4; ++i_) S[i_] = __builtin_amdgcn_exp2f(S[i_]);
#define WLD(s) wf[s] = BF8(ld16_dev(rws, l16 + ((s) & 3) * 1024, wgo + ((s) >> 2) * 4096))
#define SIDE(t, g) do { \
            if ((t) == 0 && (g) == 0) KV_ISSUE(4); if ((t) == 0 && (g) == 2) KV_ISSUE(5); if ((t) == 1 && (g) == 0) KV_ISSUE(6); if ((t) == 1 && (g) == 2) KV_ISSUE(7); \
            if ((t) == 2 && (g) == 0) KV_ISSUE(8); if ((t) == 1 && (g) == 3) rtf = peek_flag(ws, rto); \
            if ((t) == 3 && (g) < 3) KV_ISSUE(9 + (g)); \
            if ((t) >= 4 && (t) <= 10 && ((g) & 1)) { if (__builtin_expect(!ffn, 0)) st16_dev((u32x4){~0u, ~0u, ~0u, ~0u}, rws, clro + (((t) - 4) * 2 + ((g) >> 1)) * 1024, 0); } \
            if ((t) == 7 && (g) == 0) KV_ISSUE(12); if ((t) == 7 && (g) == 2) KV_ISSUE(13); if ((t) == 8 && (g) == 0) KV_ISSUE(14); if ((t) == 8 && (g) == 2) KV_ISSUE(15); \
            if ((t) >= 4 && (t) <= 6) WLD(((t) - 4) * 4 + (g)); \
            if (((t) == 7 || (t) == 8) && ((g) & 1)) WLD(12 + ((t) - 7) * 2 + ((g) >> 1)); \
            if ((t) >= 9 && (t) <= 11) WLD(((t) - 5) * 4 + (g)); \
            if ((t) == 12 || ((t) == 13 && (g) < 3)) glds(wov, wos + (((t) - 12) * 4 + (g)) * 8 * 896, lds0 + (w * 7 + ((t) - 12) * 4 + (g)) * 1024); \
            if ((t) == 13 && (g) == 3) { xa = ld8(rx, xo_, 0); xb = ld8(rx, xo_, 16); } \
            if ((t) == 14 && (g) == 1) { if (w >= 6) touch(lane * (w == 6 ? (WS_END / 64 & ~127u) : 28672), w == 6 ? ws : out, lds0 + 61440); } \
            if ((t) == 14 && (g) == 0) { if (tid < 32) *(LAS u32x4*)(lds + 57344 + tid * 16) = (u32x4){0, 0, 0, 0}; } \
        } while (0)
#define TOFF(i) (phys((i) >> 1) + ((i) & 1) * 4096)
#define TILE(i, SC, SN) do { \
            __builtin_amdgcn_s_setprio(((i) & 1) ? 1 : 3); \
            if ((i) < 31) { f32x16 z_ = {}; SN = MFMA(k0, q0, z_); PIN(SN); } if ((i) < 30) k0 = lds8(AP(TOFF((i) + 2))); SB(); \
            EXP4(SC, 0); PIN(SC); SB(); \
            o = MFMA(v1, BF8(p1), o); PIN(o); v1 = lds8(VP(TOFF(i) + 3072)); SIDE((i) >> 1, ((i) & 1) * 2); SB(); \
            EXP4(SC, 4); p0 = pack8(SC, 0); PIN(p0); SB(); \
            __builtin_amdgcn_s_setprio(((i) & 1) ? 0 : 2); \
            if ((i) < 31) { SN = MFMA(k1, q1, SN); PIN(SN); } if ((i) < 30) k1 = lds8(AP(TOFF((i) + 2) + 1024)); SB(); \
            EXP4(SC, 8); PIN(SC); SB(); \
            o = MFMA(v0, BF8(p0), o); PIN(o); if ((i) < 31) v0 = lds8(VP(TOFF((i) + 1) + 2048)); SIDE((i) >> 1, ((i) & 1) * 2 + 1); SB(); \
            EXP4(SC, 12); p1 = pack8(SC, 1); PIN(p1); SB(); } while (0)
#define STEP(t) do { TILE(2 * (t), sa, sb); TILE(2 * (t) + 1, sb, sa); } while (0)
        STEP(0);
        waitv_barx<4>();
        STEP(1); STEP(2);
        waitv_bar2<3>();
        TS(21);
        if (__builtin_expect(!wave_all(rtf == tag), 0)) poll_flag(ws, rto, tag);
        FENCE();
        STEP(3); STEP(4); STEP(5); STEP(6);
        waitv_bar2<12>();
        TS(22);
        STEP(7); STEP(8); STEP(9); STEP(10);
        waitv_bar2<9>();
        TS(23);
        STEP(11); STEP(12); STEP(13); STEP(14); STEP(15);
        o = MFMA(v1, BF8(p1), o);
        TS(7);
        u32x4 ov;
        {
            *(LAS f32x4*)xw = (f32x4){o[8], o[9], o[10], o[11]}; *(LAS f32x4*)(xw + 1024) = (f32x4){o[12], o[13], o[14], o[15]};
            const float l = kh ? o[4] : o[12];
            *(LAS float*)lw = l;
            float sa_ = 0.f;
#pragma unroll
            for (int i = 0; i < 16; ++i) sa_ += __builtin_fabsf(o[i]);
            const bool ok = sa_ < 0x1p100f && (hf == 0 || l > 0x1p-80f);
            *(LAS unsigned*)bw = wave_all(ok) ? 0u : 1u;
            BAR();
            const f32x4 a = *(LAS f32x4*)xr, c = *(LAS f32x4*)(xr + 1024);
            const float inv = __builtin_amdgcn_rcpf(*(LAS float*)lr + *(LAS float*)(lr + 1024));
            const unsigned bad = *(LAS unsigned*)br;
            const float iz = (kh && hf) ? 0.f : inv;
            ov = (u32x4){cvtpk((o[0] + a[0]) * inv, (o[1] + a[1]) * inv), cvtpk((o[2] + a[2]) * inv, (o[3] + a[3]) * inv),
                         cvtpk((o[4] + c[0]) * iz, (o[5] + c[1]) * iz), cvtpk((o[6] + c[2]) * iz, (o[7] + c[3]) * iz)};
            if (__builtin_expect(wave_any(bad != 0), 0)) {
                f32x16 of = {};
                float m = -0x1p126f;
#pragma unroll 1
                for (int t = 0; t < 16; ++t) {
                    BAR();
                    glds2_dev(l16, kvs + t * 16384, lds0 + 65536 + w * 2048);
                    waitv_bar<0>();
#pragma unroll 1
                    for (int u = 0; u < 2; ++u) {
                        f32x16 s = {};
                        s = MFMA(lds8(apB + u * 4096), q0, s); s = MFMA(lds8(apB + u * 4096 + 1024), q1, s);
                        float mx = s[0];
#pragma unroll
                        for (int i = 1; i < 16; ++i) mx = __builtin_fmaxf(mx, s[i]);
                        mx = __builtin_fmaxf(swap_max(mx), m);
                        const float alpha = __builtin_amdgcn_exp2f(m - mx);
                        m = mx;
#pragma unroll
                        for (int i = 0; i < 16; ++i) { s[i] = __builtin_amdgcn_exp2f(s[i] - mx); of[i] *= alpha; }
                        of = MFMA(lds8(vpB + u * 4096 + 2048), BF8(pack8(s, 0)), of);
                        of = MFMA(lds8(vpB + u * 4096 + 3072), BF8(pack8(s, 1)), of);
                    }
                }
                *(LAS f32x4*)xw = (f32x4){of[8], of[9], of[10], of[11]}; *(LAS f32x4*)(xw + 1024) = (f32x4){of[12], of[13], of[14], of[15]};
                *(LAS float*)lw = hf ? (kh ? of[4] : of[12]) : m;
                BAR();
                const f32x4 a2 = *(LAS f32x4*)xr, c2 = *(LAS f32x4*)(xr + 1024);
                const float m0 = *(LAS float*)(lr - 128), m1 = *(LAS float*)(lr + 1024 - 128), mm = __builtin_fmaxf(m0, m1);
                const float e0 = __builtin_amdgcn_exp2f(m0 - mm), e1 = __builtin_amdgcn_exp2f(m1 - mm);
                const float iv = __builtin_amdgcn_rcpf(*(LAS float*)lr * e0 + *(LAS float*)(lr + 1024) * e1);
                const float im = (kh ? e1 : e0) * iv, io = (kh ? e0 : e1) * iv;
                const bool z = kh && hf;
                ov = (u32x4){cvtpk(of[0] * im + a2[0] * io, of[1] * im + a2[1] * io), cvtpk(of[2] * im + a2[2] * io, of[3] * im + a2[3] * io),
                             z ? 0u : cvtpk(of[4] * im + c2[0] * io, of[5] * im + c2[1] * io), z ? 0u : cvtpk(of[6] * im + c2[2] * io, of[7] * im + c2[3] * io)};
            }
        }
        st16_dev(ov, rws, oo, 0);
        TS(25);
        FENCE();
        PIN2(xa, xb);
#pragma unroll
        for (int s = 27; s >= 0; --s) PIN(wf[s]);
#pragma unroll
        for (int s = 0; s < 8; ++s) cs = MFMA(wf[s], BF8(one4), cs);
        acc[0] = bflo(xa[0]); acc[1] = bfhi(xa[0]); acc[2] = bflo(xa[1]); acc[3] = bfhi(xa[1]); acc[4] = bflo(xb[0]); acc[5] = bfhi(xb[0]); acc[6] = bflo(xb[1]); acc[7] = bfhi(xb[1]);
        PIN(acc); PIN2(ho, so); PIN2(pf2, fo3); PINS(os); PINS(od); PIN(cs);
        waitv_bar<0>();
        if (w == 0) set_flag(ws, fo2, tagv);
        TS(8);
    }
    if (__builtin_expect(!ffn, 0)) {
        const int ri = xcd * 4 + loc - 28;
        if (ri < 28) {
#pragma unroll 1
            for (int u = w; u < 28; u += 8) {
                lds_ptr sd = lds + u * 2048 + (4 * hf + q4) * 128 + g1 * 32 + p4 * 8;
#pragma unroll
                for (int e = 0; e < 2; ++e) { lds_ptr p_ = sd + ((e ^ (q4 >> 1)) * 64);
                    st16_dev(U4(cat(trd(p_), trd(p_ + 1024))), rws, l16, WS_WD + (unsigned)(((ri % 7) * 112 + (ri / 7) * 28 + u) * 2 + e) * 1024); }
            }
        }
        waitv_bar<0>();
        if (w == 0) set_flag(ws, (FL_RD + ri) * 128, tagv);
        return;
    }

    const int erb = tb >> 1;
    const char* hs = ws + WS_HF + (size_t)drb * 114688 + w * 1024;
    const char* hss = ws + WS_HS + (size_t)((drb * 4 + (w & 3)) * 28 + kh * 14) * 256;
    unsigned pf3, fo4 = (FL_G + drb * 64 + dfg) * 128 + w * 8;
    {
        lds_ptr wb = lds + kh * (8 * 28 * 128) + (4 * hf + q4) * 128 + ((ct ^ (q4 >> 1)) * 64) + (g1 ^ kh) * 32 + p4 * 8;
        bf16x8 wr[16];
#pragma unroll
        for (int s = 0; s < 12; ++s) { const int k0 = ((s >> 1) * 28 + (s & 1) * 16) * 128; wr[s] = cat(trd(wb + k0), trd(wb + k0 + 1024)); }
        lds_ptr cw = opq(lds + 131072 + w * 2048 + l16), cr = opq(lds + 131072 + (w ^ 4) * 2048 + l16);
        poll_flag(ws, pf2, tag);
        TS(9);
        glds4_dev(l16, os, od);
        asm volatile("s_barrier" ::: "memory");
        glds4_dev(l16, os + 8192, od + 8192);
        lds_ptr ob = opq(lds + 65536 + tt * 32768 + kh * 16384 + l16);
        bf16x8 of[8];
        waitv_bar<4>();
        TS(26);
#pragma unroll
        for (int s = 0; s < 8; ++s) of[s] = lds8(ob + s * 1024);
        SB();
#pragma unroll
        for (int s = 0; s < 8; ++s) acc = MFMA(wr[s], of[s], acc);
        PIN(acc); SB();
#pragma unroll
        for (int s = 12; s < 16; ++s) { const int k0 = ((s >> 1) * 28 + (s & 1) * 16) * 128; wr[s] = cat(trd(wb + k0), trd(wb + k0 + 1024)); }
        waitv_bar<0>();
#pragma unroll
        for (int s = 0; s < 8; ++s) of[s] = lds8(ob + (8 + s) * 1024);
        SB();
#pragma unroll
        for (int s = 0; s < 8; ++s) acc = MFMA(wr[8 + s], of[s], acc);
        TS(10);
        *(LAS f32x4*)cw = (f32x4){acc[8], acc[9], acc[10], acc[11]}; *(LAS f32x4*)(cw + 1024) = (f32x4){acc[12], acc[13], acc[14], acc[15]};
        BAR();
        const f32x4 a = *(LAS f32x4*)cr + (f32x4){acc[0], acc[1], acc[2], acc[3]}, b = *(LAS f32x4*)(cr + 1024) + (f32x4){acc[4], acc[5], acc[6], acc[7]};
        const u32x4 hv = {cvtpk(a[0], a[1]), cvtpk(a[2], a[3]), cvtpk(b[0], b[1]), cvtpk(b[2], b[3])};
        st16_dev(hv, rws, ho, 0);
        float s1 = 0.f, s2 = 0.f;
        acc_stats(hv, s1, s2);
        s1 = swap_add(s1); s2 = swap_add(s2);
        st8_dev((u32x2){f2u(s1), f2u(s2)}, rws, so, 0);
        FENCE();
#pragma unroll
        for (int s = 8; s < 14; ++s) cs = MFMA(wf[s], BF8(one4), cs);
        pf3 = (FL_H + drb * 16 + (lane & 8) + umin(lane & 7, 6)) * 128;
        PIN(go); PIN2(pf3, fo4); PINS(hs); PINS(hss); PIN(cs);
        waitv_bar<0>();
        if (w == 0) set_flag(ws, fo3, tagv);
        if (bid == 0 && w == 0) set_flag(ws, FL_EPOCH * 128, tagv);
        TS(11);
    }

    const unsigned wdo = WS_WD + (unsigned)(m7 * 112 + w * 14) * 2048;
    bf16x8 wdf[28];
    bf16x8 gf[GW];
    const unsigned erot = EROT * m7;
#define E_KS(s) (erot + (s) >= 14 ? erot + (s) - 14 : erot + (s))
    const int ta = erb * 4 + (tb & 1);
    const unsigned gxo = WS_GF + (unsigned)(ta * 112 + w * 14) * 1024;
    u64 gfl = 0;
    const unsigned gks = w * 14 + umin(lane & 15, 13), gfi = (FL_G + (erb * 4 + (tb & 1)) * 16 + (gks >> 3)) * 128 + (GWG ? 0 : (gks & 7) * 8);
    {
#pragma unroll
        for (int s = 14; s < 28; ++s) cs = MFMA(wf[s], BF8(one4), cs);
        lds_ptr csp = opq(lds + 161792 + w * 128 + hf * 64);
        if (r == 0) {
#pragma unroll
            for (int g = 0; g < 4; ++g) *(LAS f32x4*)(csp + g * 16) = (f32x4){cs[4 * g], cs[4 * g + 1], cs[4 * g + 2], cs[4 * g + 3]};
        }
        const unsigned hd = lds0 + w * 1024;
        const char* wds = ws + wdo;
        lds_ptr hb = opq(lds + l16), hb2 = opq(lds + 57344 + l16);
        lds_ptr wl = opq(lds + 114688 + w * 5120 + l16), wl2 = opq(lds + w * 7168 + l16);
        LAS f32x2* stp = (LAS f32x2*)opq(lds + 159744 + r * 8);
        if (w == HOP3_POLLER) poll_flag(ws, pf3, tag);
        BAR();
        TS(12);
#define H_PIECE(j) glds_dev(l16, hs + (j) * 8192, hd + (j) * 8192)
#define WDO(s) (E_KS((s) >> 1) * 2048 + ((s) & 1) * 1024)
#define WDLD(s) wdf[s] = BF8(ld16_dev(rws, l16, wdo + WDO(s)))
#define HB(n) ((n) < 56 ? hb + (n) * 1024 : hb2 + ((n) - 56) * 1024)
        H_PIECE(0);
        asm volatile("s_barrier" ::: "memory");
        H_PIECE(1); H_PIECE(2); H_PIECE(3);
        constexpr int PF = 5;
        bf16x8 bf[PF + 1];
        f32x16 acc[2];
        f32x2 sp[7], sv0, sv1;
        f32x4 c4[4];
        float mean = 0.f, r2 = 0.f, hp = 0.f;
        u32x4 hv;
        lds_ptr pk = opq(lds + 155648 + w * 512 + lane * 4);
        u64 pv = 0;
        const unsigned pkd = lds0 + 155648 + w * 512;
        const unsigned rdo = (FL_RD + ((lane & 2) ? drb * 2 + (lane & 1) : (lane & 1) ? tb : (w >> 1) * 7 + m7)) * 128;
        waitv_barx<3>();
        TS(27);
#pragma unroll
        for (int n = 0; n < PF; ++n) bf[n] = lds8(HB(n));
        SB();
        sfor<0, 125>([&](auto N_) __attribute__((always_inline)) {
            constexpr int n = decltype(N_)::v, p = n / 28, s = n % 28;
            if constexpr (n == 3) waitv_barx<3>();
            if constexpr (n == 11) waitv_barx<7>();
            if constexpr (n == 27) { waitv_barx<3>(); TS(13); }
            if constexpr (n == 64) { waitv_barx<10>(); if (!GWG || w == 0) set_flag(ws, fo4, tagv); TS(14); }
            if constexpr (n == 85) { if (GWG) waitv_barx<7>(); else waitv<7>(); if (!GWG || w == 0) set_flag(ws, fo4 + 2048, tagv); }
            if constexpr (n == 38) { if (__builtin_expect(!wave_all(pv == tag), 0)) poll_flag(ws, rdo, tag); FENCE(); }
            if constexpr (d_prio(n) != d_prio(n - 1)) __builtin_amdgcn_s_setprio(d_prio(n));
            if constexpr (n < 112) { if constexpr (s == 0) { f32x16 z = {}; acc[p & 1] = MFMA(wf[s], bf[n % (PF + 1)], z); } else acc[p & 1] = MFMA(wf[s], bf[n % (PF + 1)], acc[p & 1]); PIN(acc[p & 1]); }
            SB();
            if constexpr (n + PF < 112) bf[(n + PF) % (PF + 1)] = lds8(HB(n + PF));
            if constexpr (n % 3 == 2 && n < 30) H_PIECE(4 + n / 3);
            if constexpr (n == 4) glds4_dev(l16, hss, lds0 + 114688 + w * 5120);
            if constexpr (n == 16) { waitv<4>();
#pragma unroll
                for (int j = 0; j < 7; ++j) sp[j] = *(LAS f32x2*)(lds + 114688 + w * 5120 + (hf * 7 + j) * 256 + r * 8);
            }
            if constexpr (n == 20) { const f32x2 t = ((sp[0] + sp[1]) + (sp[2] + sp[3])) + ((sp[4] + sp[5]) + sp[6]); stp[w * 32] = (f32x2){swap_add(t[0]), swap_add(t[1])}; }
            if constexpr (n >= 39 && n < 48 && n % 2 == 1) glds_dev(l16, wds + WDO((n - 39) / 2), lds0 + 114688 + w * 5120 + (n - 39) / 2 * 1024);
            if constexpr (n >= 48 && n < 64 && n % 3 == 0) WDLD(12 + (n - 48) / 3);
            if constexpr (n >= 70 && n < 84 && n % 2 == 0) glds_dev(l16, wds + WDO(5 + (n - 70) / 2), lds0 + w * 7168 + (n - 70) / 2 * 1024);
            if constexpr (n >= 85 && n < 105 && n % 2 == 1) WDLD(18 + (n - 85) / 2);
            if constexpr (n == 107) gfl = peek_flag(ws, gfi);
            if constexpr (n == 32) pv = peeked(pk);
            if constexpr (n == 18) peek_lds(ws, rdo, pkd);
            if constexpr (p >= 1) {
                const f32x16& a = acc[(p - 1) & 1];
                if constexpr (n == 100) { sv0 = stp[3 * 32]; sv1 = stp[7 * 32]; }
                if constexpr (s == 0 && p < 4) { sv0 = stp[(p - 1) * 32]; sv1 = stp[(p + 3) * 32];
#pragma unroll
                    for (int g = 0; g < 4; ++g) c4[g] = *(LAS f32x4*)(csp + g * 16); }
                if constexpr ((s == 3 && p < 4) || n == 104) { const f32x2 sv = sv0 + sv1; mean = sv[0] * (1.f / D); r2 = __builtin_amdgcn_rcpf(__builtin_fmaxf(sv[1] * (1.f / D) - mean * mean, 0.f) + EPS); PIN2(mean, r2); }
                if constexpr (s >= 4 && s < 12) { constexpr int i = s - 4;
                    const float h0 = __builtin_fmaxf(a[i] - mean * c4[i >> 2][i & 3], 0.f) * r2 * (a[8 + i] - mean * c4[2 + (i >> 2)][i & 3]);
                    if constexpr (i & 1) { hv[i >> 1] = p >= 3 ? umin(cvtpk(hp, h0), ~1u) : cvtpk(hp, h0); PIN(hv); } else { hp = h0; PIN(hp); } }
                if constexpr (s == 12) { st16_dev(hv, rws, go + (p - 1) * 114688, 0); FENCE(); }
            }
            SB();
        });
        TS(15);
        if (__builtin_expect(!wave_all(gfl == tag), 0)) poll_flag(ws, gfi, tag);
        FENCE();
#pragma unroll
        for (int s = 0; s < GW; ++s) { gf[s] = BF8(ld16_dev(rws, l16, gxo + E_KS(s) * 1024)); SB(); }
        FENCE();
#pragma unroll
        for (int s = 0; s < 12; ++s) wdf[s] = lds8(s < 5 ? wl + s * 1024 : wl2 + (s - 5) * 1024);
    }

    {
        const int etok = tid >> 4, eblk = tid & 15;
        const unsigned hro = (unsigned)((((eblk >> 2) & 3) * 64 + (eblk & 1) * 32 + etok) * 16 + ((eblk >> 1) & 1) * 8) + WS_HF + (unsigned)(ta * 28 + m7 * 4) * 1024;
        u32x2 hres[2];
        GAS u64* op = (GAS u64*)(out + (size_t)(ta * 32 + etok) * 896 + m7 * 128 + eblk * 8);
        lds_ptr rp = opq(lds + w * 8448 + hf * 528 + r * 16), rq = opq(lds + eblk * 528 + etok * 16);
        lds_ptr rp2 = opq(lds + 67584 + w * 8448 + hf * 528 + r * 16), rq2 = opq(lds + 67584 + eblk * 528 + etok * 16);
#define G_LD(s) gf[(s) % GW] = BF8(ld16_dev(rws, l16, gxo + ((s) / 14) * 229376 + E_KS((s) % 14) * 1024))
#define G_PUT(RP) do { \
            _Pragma("unroll") for (int g = 0; g < 4; ++g) *(LAS f32x4*)(RP + g * 1056) = (f32x4){c0[4 * g], c0[4 * g + 1], c0[4 * g + 2], c0[4 * g + 3]}; \
            _Pragma("unroll") for (int g = 0; g < 4; ++g) *(LAS f32x4*)(RP + 4224 + g * 1056) = (f32x4){c1[4 * g], c1[4 * g + 1], c1[4 * g + 2], c1[4 * g + 3]}; } while (0)
#define G_OUT(hh, RQ) do { f32x4 pa[8]; \
            _Pragma("unroll") for (int ww = 0; ww < 8; ++ww) pa[ww] = *(LAS f32x4*)(RQ + ww * 8448); SB(); \
            const f32x4 a = ((pa[0] + pa[1]) + (pa[2] + pa[3])) + ((pa[4] + pa[5]) + (pa[6] + pa[7])); \
            const u32x2 ov = {cvtpk(a[0] + bflo(hres[hh][0]), a[1] + bfhi(hres[hh][0])), cvtpk(a[2] + bflo(hres[hh][1]), a[3] + bfhi(hres[hh][1]))}; \
            __hip_atomic_store(op + (hh) * (64 * 896 / 8), __builtin_bit_cast(u64, ov), __ATOMIC_RELAXED, __HIP_MEMORY_SCOPE_AGENT); } while (0)
        f32x16 c0 = {}, c1 = {};
        TS(16);
        unsigned seen = 0;
#pragma unroll
        for (int s = 0; s < 28; ++s) {
            if (s == 1) { hres[0] = ld8_dev(rws, hro, 0); hres[1] = ld8_dev(rws, hro, 57344); FENCE(); }
            if (s == 10) BAR();
            if (s == 14) { G_PUT(rp); c0 = (f32x16){}; c1 = (f32x16){}; TS(17); }
            if (s == 20) { waitv<8>(); PIN2(hres[0], hres[1]); BAR(); if (!GWG || w == 0) { set_flag(ws, fo4 + 4096, tagv); set_flag(ws, fo4 + 6144, tagv); } G_OUT(0, rq); FENCE(); TS(18); }
            SB();
            c0 = MFMA(wdf[2 * (s % 14)], gf[s % GW], c0); PIN(c0); c1 = MFMA(wdf[2 * (s % 14) + 1], gf[s % GW], c1); PIN(c1);
            if (s >= 14) { const u32x4 u = U4(gf[s % GW]); seen = umax3(umax3(seen, u[0], u[1]), u[2], u[3]); PIN(seen); }
            SB();
            if (s + GW < 28) G_LD(s + GW);
        }
        if (__builtin_expect(wave_any(seen == ~0u), 0)) {
            poll_flag(ws, gfi + 4096, tag);
            c0 = (f32x16){}; c1 = (f32x16){};
#pragma unroll
            for (int s = 14; s < 14 + GW; ++s) G_LD(s);
#pragma unroll
            for (int s = 14; s < 28; ++s) { c0 = MFMA(wdf[2 * (s % 14)], gf[s % GW], c0); c1 = MFMA(wdf[2 * (s % 14) + 1], gf[s % GW], c1); PIN2(c0, c1); SB(); if (s + GW < 28) G_LD(s + GW); SB(); }
        }
        G_PUT(rp2);
        TS(19);
        BAR();
        G_OUT(1, rq2);
    }
    TS(20);
}

#define WS_OFF 0
extern "C" void kernel_launch(void* const* d_in, const int* in_sizes, int n_in, void* d_out, int out_size, void* d_ws, size_t ws_size, hipStream_t stream) {
    (void)hipFuncSetAttribute((const void*)block_fwd, hipFuncAttributeMaxDynamicSharedMemorySize, LDS_BYTES);
    block_fwd<<<256, 512, LDS_BYTES, stream>>>((const char*)d_in[0], (const char*)d_in[1], (const char*)d_in[2], (const char*)d_in[4], (const char*)d_in[5], (const char*)d_in[6], (char*)d_out, (char*)d_ws + WS_OFF);
}
```

```cpp
#define PROBE 4
#include <hip/hip_runtime.h>
#include <hip/hip_bf16.h>
#include <stdint.h>

typedef __attribute__((ext_vector_type(8))) short bf16x8;
typedef __attribute__((ext_vector_type(4))) short s16x4;
typedef __attribute__((ext_vector_type(2))) __bf16 bf16x2;
typedef __attribute__((ext_vector_type(16))) float f32x16;
typedef __attribute__((ext_vector_type(4))) float f32x4;
typedef __attribute__((ext_vector_type(2))) float f32x2;
typedef __attribute__((ext_vector_type(4))) unsigned u32x4;
typedef __attribute__((ext_vector_type(2))) unsigned u32x2;
typedef short v4i16_t __attribute__((ext_vector_type(4)));
typedef unsigned long long u64;
#define LAS __attribute__((address_space(3)))
#define GAS __attribute__((address_space(1)))
typedef LAS char* lds_ptr;
#if defined(__HIP_DEVICE_COMPILE__)
#define NOPK __attribute__((target("no-packed-fp32-ops")))
#else
#define NOPK
#endif
#define DI __device__ __forceinline__
#define MFMA(a, b, c) __builtin_amdgcn_mfma_f32_32x32x16_bf16(a, b, c, 0, 0, 0)
#define MFMA16(a, b, c) __builtin_amdgcn_mfma_f32_16x16x32_bf16(a, b, c, 0, 0, 0)
#define BF8(x) __builtin_bit_cast(bf16x8, x)
#define U4(x) __builtin_bit_cast(u32x4, x)

constexpr int D = 448;
constexpr float EPS = 1e-5f;
constexpr unsigned WS_FLAG = 0;
constexpr unsigned FL_EPOCH = 0, FL_KV = 1, FL_RT = FL_KV + 256, FL_RD = FL_RT + 32, FL_CL = FL_RD + 32, FL_O = FL_CL + 32, FL_H = FL_O + 256, FL_G = FL_H + 256;
constexpr unsigned WS_KV = 1u << 20;
constexpr unsigned WS_QF = WS_KV + 4194304;
constexpr unsigned WS_OF = WS_QF + 2097152;
constexpr unsigned WS_HF = WS_OF + 2097152;
constexpr unsigned WS_GF = WS_HF + 64 * 28 * 1024;
constexpr unsigned WS_WGU = WS_GF + 64 * 112 * 1024;
constexpr unsigned WS_WD = WS_WGU + 112 * 28 * 1024;
constexpr unsigned WS_HS = WS_WD + 7 * 112 * 2 * 1024;
constexpr unsigned WS_END = WS_HS + 64 * 28 * 256 + 1024;
constexpr unsigned OOB = 0x40000000u;
constexpr int LDS_BYTES = 163840;

DI float u2f(unsigned u) { return __builtin_bit_cast(float, u); }
DI unsigned f2u(float f) { return __builtin_bit_cast(unsigned, f); }
DI unsigned umin(unsigned a, unsigned b) { return a < b ? a : b; }
DI unsigned umax3(unsigned a, unsigned b, unsigned c) { const unsigned m = a > b ? a : b; return m > c ? m : c; }
DI bool wave_any(bool p) { return __builtin_amdgcn_ballot_w64(p) != 0; }
DI bool wave_all(bool p) { return __builtin_amdgcn_ballot_w64(!p) == 0; }
DI unsigned cvtpk(float lo, float hi) { return __builtin_bit_cast(unsigned, __builtin_convertvector((f32x2){lo, hi}, bf16x2)); }
DI float bflo(unsigned u) { return u2f(u << 16); }
DI float bfhi(unsigned u) { return u2f(u & 0xffff0000u); }
DI void glds(unsigned voff, const void* sbase, unsigned dst) {
    asm volatile("s_mov_b32 m0, %2\n\ts_nop 0\n\tglobal_load_lds_dwordx4 %0, %1" :: "v"(voff), "s"(sbase), "s"(dst) : "memory");
}
DI void glds_dev(unsigned voff, const void* sbase, unsigned dst) {
    asm volatile("s_mov_b32 m0, %2\n\ts_nop 0\n\tglobal_load_lds_dwordx4 %0, %1 sc1" :: "v"(voff), "s"(sbase), "s"(dst) : "memory");
}
DI void glds2_dev(unsigned voff, const void* sbase, unsigned dst) {
    asm volatile("s_mov_b32 m0, %2\n\ts_nop 0\n\tglobal_load_lds_dwordx4 %0, %1 sc1\n\tglobal_load_lds_dwordx4 %0, %1 offset:1024 sc1" :: "v"(voff), "s"(sbase), "s"(dst) : "memory");
}
DI void glds4_dev(unsigned voff, const void* sbase, unsigned dst) {
    asm volatile("s_mov_b32 m0, %2\n\ts_nop 0\n\tglobal_load_lds_dwordx4 %0, %1 sc1\n\tglobal_load_lds_dwordx4 %0, %1 offset:1024 sc1\n\t"
                 "global_load_lds_dwordx4 %0, %1 offset:2048 sc1\n\tglobal_load_lds_dwordx4 %0, %1 offset:3072 sc1" :: "v"(voff), "s"(sbase), "s"(dst) : "memory");
}
DI s16x4 trd(lds_ptr p) { return __builtin_bit_cast(s16x4, __builtin_amdgcn_ds_read_tr16_b64_v4i16((LAS v4i16_t*)p)); }
DI bf16x8 cat(s16x4 a, s16x4 b) { return __builtin_shufflevector(a, b, 0, 1, 2, 3, 4, 5, 6, 7); }
DI bf16x8 lds8(lds_ptr p) { return *(LAS bf16x8*)p; }
DI float swap_add(float v) { auto r = __builtin_amdgcn_permlane32_swap(f2u(v), f2u(v), false, false); return u2f(r[0]) + u2f(r[1]); }
DI float swap_max(float v) { auto r = __builtin_amdgcn_permlane32_swap(f2u(v), f2u(v), false, false); return __builtin_fmaxf(u2f(r[0]), u2f(r[1])); }
DI u32x4 pack8(const f32x16& a, int s) { return (u32x4){cvtpk(a[8 * s], a[8 * s + 1]), cvtpk(a[8 * s + 2], a[8 * s + 3]), cvtpk(a[8 * s + 4], a[8 * s + 5]), cvtpk(a[8 * s + 6], a[8 * s + 7])}; }
DI float dot2(unsigned a, unsigned b, float c) { return __builtin_amdgcn_fdot2_f32_bf16(__builtin_bit_cast(bf16x2, a), __builtin_bit_cast(bf16x2, b), c, false); }
DI void acc_stats(u32x4 u, float& s1, float& s2) {
#pragma unroll
    for (int i = 0; i < 4; ++i) { const float a = bflo(u[i]), b = bfhi(u[i]); s1 += a; s2 = __builtin_fmaf(a, a, s2); s1 += b; s2 = __builtin_fmaf(b, b, s2); }
}
DI void acc_stats2(unsigned u0, unsigned u1, f32x4& s) {
    const float a = bflo(u0), b = bfhi(u0), c = bflo(u1), d = bfhi(u1);
    s[0] += a; s[1] += b; s[2] = __builtin_fmaf(a, a, s[2]); s[3] = __builtin_fmaf(b, b, s[3]); s[0] += c; s[1] += d; s[2] = __builtin_fmaf(c, c, s[2]); s[3] = __builtin_fmaf(d, d, s[3]);
}
DI float sum8(float v) {
    v += u2f(__builtin_amdgcn_update_dpp(0u, f2u(v), 0xB1, 0xf, 0xf, true));
    v += u2f(__builtin_amdgcn_update_dpp(0u, f2u(v), 0x4E, 0xf, 0xf, true));
    v += u2f(__builtin_amdgcn_update_dpp(0u, f2u(v), 0x141, 0xf, 0xf, true));
    return v;
}
DI u32x4 shift8(u32x4 x, float c) {
    u32x4 o; float t0, t1, t2, t3, t4, t5, t6, t7;
    asm("v_dot2_f32_bf16 %4, %12, %16, %18\n\tv_dot2_f32_bf16 %5, %12, %17, %18\n\tv_dot2_f32_bf16 %6, %13, %16, %18\n\tv_dot2_f32_bf16 %7, %13, %17, %18\n\t"
        "v_dot2_f32_bf16 %8, %14, %16, %18\n\tv_dot2_f32_bf16 %9, %14, %17, %18\n\tv_dot2_f32_bf16 %10, %15, %16, %18\n\tv_dot2_f32_bf16 %11, %15, %17, %18\n\t"
        "v_cvt_pk_bf16_f32 %0, %4, %5\n\tv_cvt_pk_bf16_f32 %1, %6, %7\n\tv_cvt_pk_bf16_f32 %2, %8, %9\n\tv_cvt_pk_bf16_f32 %3, %10, %11"
        : "=&v"(o[0]), "=&v"(o[1]), "=&v"(o[2]), "=&v"(o[3]), "=&v"(t0), "=&v"(t1), "=&v"(t2), "=&v"(t3), "=&v"(t4), "=&v"(t5), "=&v"(t6), "=&v"(t7)
        : "v"(x[0]), "v"(x[1]), "v"(x[2]), "v"(x[3]), "s"(0x00003f80u), "s"(0x3f800000u), "v"(c));
    return o;
}
template <int N> struct IC { static constexpr int v = N; };
template <int B, int E, class F> DI void sfor(F&& f) { if constexpr (B < E) { f(IC<B>{}); sfor<B + 1, E>(f); } }
template <int N> DI void waitv() { asm volatile("s_waitcnt vmcnt(%0)" :: "i"(N) : "memory"); }
template <int N> DI void waitv_bar() { asm volatile("s_waitcnt vmcnt(%0) lgkmcnt(0)\n\ts_barrier" :: "i"(N) : "memory"); }
template <int N> DI void waitv_barx() { asm volatile("s_waitcnt vmcnt(%0)\n\ts_barrier" :: "i"(N) : "memory"); }
template <int N> DI void waitv_bar2() { asm volatile("s_waitcnt vmcnt(%0) lgkmcnt(2)\n\ts_barrier" :: "i"(N) : "memory"); }
DI void touch(unsigned voff, const void* sbase, unsigned junk) {
    asm volatile("s_mov_b32 m0, %2\n\ts_nop 0\n\tglobal_load_lds_dword %0, %1 sc1" :: "v"(voff), "s"(sbase), "s"(junk) : "memory");
}
#define BAR() asm volatile("s_waitcnt lgkmcnt(0)\n\ts_barrier" ::: "memory")
#define FENCE() asm volatile("" ::: "memory")
typedef __amdgpu_buffer_rsrc_t rsrc_t;
DI u32x4 ld16(rsrc_t rs, unsigned voff, unsigned soff) { return U4(__builtin_amdgcn_raw_buffer_load_b128(rs, voff, soff, 0)); }
DI u32x4 ld16_dev(rsrc_t rs, unsigned voff, unsigned soff) { return U4(__builtin_amdgcn_raw_buffer_load_b128(rs, voff, soff, 16)); }
DI u32x2 ld8(rsrc_t rs, unsigned voff, unsigned soff) { return __builtin_bit_cast(u32x2, __builtin_amdgcn_raw_buffer_load_b64(rs, voff, soff, 0)); }
DI u32x2 ld8_dev(rsrc_t rs, unsigned voff, unsigned soff) { return __builtin_bit_cast(u32x2, __builtin_amdgcn_raw_buffer_load_b64(rs, voff, soff, 16)); }
DI void st16_dev(u32x4 v, rsrc_t rs, unsigned voff, unsigned soff) { __builtin_amdgcn_raw_buffer_store_b128(v, rs, voff + soff, 0, 16); }
DI void st8_dev(u32x2 v, rsrc_t rs, unsigned voff, unsigned soff) { __builtin_amdgcn_raw_buffer_store_b64(v, rs, voff, soff, 16); }
#define POLL_SLEEP 1
DI void set_flag(char* ws, unsigned fo, u64 tagv) {
    asm volatile("s_mov_b64 exec, 1\n\tglobal_store_dwordx2 %0, %1, %2 sc1\n\ts_mov_b64 exec, -1" :: "v"(fo), "v"(tagv), "s"(ws) : "memory");
}
DI u64 peek_flag(char* ws, unsigned fo) { return __hip_atomic_load((GAS u64*)(ws + fo), __ATOMIC_RELAXED, __HIP_MEMORY_SCOPE_AGENT); }
DI void peek_lds(char* ws, unsigned fo, unsigned dst) {
    asm volatile("s_mov_b32 m0, %2\n\ts_nop 0\n\tglobal_load_lds_dword %0, %1 sc1\n\ts_add_u32 m0, %2, 252\n\ts_nop 0\n\tglobal_load_lds_dword %0, %1 offset:4 sc1" :: "v"(fo), "s"(ws), "s"(dst) : "memory", "scc");
}
DI u64 peeked(lds_ptr p) { return (u64)*(LAS unsigned*)(p + 256) << 32 | *(LAS unsigned*)p; }
DI void poll_flag(char* ws, unsigned fo, u64 tag) {
#pragma unroll 1
    for (int spin = 0; spin < (1 << 16); ++spin) {
        if (wave_all(peek_flag(ws, fo) == tag)) break;
        __builtin_amdgcn_s_sleep(POLL_SLEEP);
    }
    FENCE();
}
#define SB() __builtin_amdgcn_sched_barrier(0)
#define PINS(a) asm volatile("" : "+s"(a))
#define PIN(a) asm volatile("" : "+v"(a))
#define PIN2(a, b) asm volatile("" : "+v"(a), "+v"(b))
DI lds_ptr opq(lds_ptr p) { unsigned u = (unsigned)(uintptr_t)p; asm volatile("" : "+v"(u)); return (lds_ptr)(uintptr_t)u; }
#define TS(i) do { if ((i) == 0) pt0 = __builtin_amdgcn_s_memrealtime(); if ((i) == PROBE) pt1 = __builtin_amdgcn_s_memrealtime(); \
    if ((i) == 99) { const u64 e_ = __builtin_amdgcn_s_memrealtime() + (pt1 - pt0); while (__builtin_amdgcn_s_memrealtime() < e_) __builtin_amdgcn_s_sleep(1); } } while (0)

DI constexpr int wd_slot(int s) { return (s * 17 + 27) / 28 != ((s + 1) * 17 + 27) / 28 ? (s * 17 + 27) / 28 : -1; }
#define EROT 0
#define CLR_SLEEP 90
#define ROTMODE 0
#define EARLYG 0
#define GWG 1
#define GW 8
#define ENL 4
#define HOP3_POLLER 1
DI constexpr int d_prio(int n) { return n < 0 ? -1 : n < 27 ? 3 - n / 7 : n < 64 ? 3 - (n - 27) / 10 : n < 84 ? 3 - (n - 64) / 5 : n < 112 ? 3 - (n - 84) / 7 : 0; }
DI constexpr int phys(int t) { return ((t % 9) + 2) % 9 * 16384; }

__global__ NOPK __launch_bounds__(512, 2) void block_fwd(const char* x, const char* wqkv, const char* wo, const char* wg, const char* wu, const char* wd, char* out, char* ws) {
    extern __shared__ __attribute__((aligned(16))) char smem[];
    lds_ptr lds = (lds_ptr)smem;
    const int tid = __builtin_amdgcn_workitem_id_x(), lane = tid & 63, w = __builtin_amdgcn_readfirstlane(tid >> 6);
    const int bid = __builtin_amdgcn_workgroup_id_x(), xcd = bid & 7, loc = bid >> 3;
    const int r = lane & 31, hf = lane >> 5, q4 = (lane & 15) >> 2, g1 = (lane >> 4) & 1, p4 = lane & 3;
    const unsigned lds0 = (unsigned)(uintptr_t)smem;
    const unsigned l16 = lane * 16;
    u64 pt0 = 0, pt1 = 0;
    TS(0);
    const int sp3 = (loc + 8) >> 5;
    const int atb = (xcd >> 1) * 8 + (loc & 7) + sp3 * (xcd & 1) * 4, acg = (loc >> 3) + (1 - sp3) * (xcd & 1) * 4;
    const int row8 = lane >> 3;
    const int rot8 = ((loc & 7) << sp3) + sp3 * (xcd & 1), rot = rot8 == 7 ? 0 : rot8;
    if (loc < 28) {
        const unsigned xo = (unsigned)((atb * 64 + (tid >> 3)) * 896 + (((tid ^ (tid >> 4)) & 7) * 16));
        const unsigned xd0 = lds0 + 1024 + (w >> 2) * 28672 + (w & 3) * 1024;
        unsigned xd = xd0 + (7 - rot) * 4096;
        const unsigned w0 = (unsigned)((rot * 64 + (tid >> 3)) * 2688 + acg * 128 + (((tid & 7) ^ ((tid >> 2) & 4)) * 16));
        asm volatile("s_cmp_eq_u32 %0, %3\n\ts_cselect_b32 %0, %2, %0\n\ts_mov_b32 m0, %0\n\ts_add_u32 %0, %0, 0x1000\n\tglobal_load_lds_dwordx4 %1, %4\n\t"
#define XP(o) "s_cmp_eq_u32 %0, %3\n\ts_cselect_b32 %0, %2, %0\n\ts_sub_u32 m0, %0, " #o "\n\ts_add_u32 %0, %0, 0x1000\n\tglobal_load_lds_dwordx4 %1, %4 offset:" #o "\n\t"
                     XP(128) XP(256) XP(384) XP(512) XP(640) XP(768)
                     "s_add_u32 m0, %6, 8192-896\n\ts_barrier\n\tglobal_load_lds_dwordx4 %5, %7 offset:896\n\t"
                     "s_add_u32 m0, %6, 16384-1792\n\ts_nop 0\n\tglobal_load_lds_dwordx4 %5, %7 offset:1792\n\t"
                     "s_mov_b32 m0, %6\n\ts_nop 0\n\tglobal_load_lds_dwordx4 %5, %7"
                     : "+s"(xd) : "v"(xo), "s"(xd0), "s"(xd0 + 7 * 4096), "s"(x), "v"(w0), "s"(lds0 + 58368 + w * 1024), "s"(wqkv) : "memory", "scc");
    }
    SB();
    const rsrc_t rws = __builtin_amdgcn_make_buffer_rsrc((void*)ws, 0, WS_END, 0x00020000);
    u64 epoch, tagv;
    unsigned fl1;
    const unsigned ones = 0x3f803f80u;
    const bool ffn = loc < 28;
    const int l7 = loc / 7, m7 = loc % 7;
    const int tb = xcd * 4 + l7;
    const int tt = w & 1, ct = (w >> 1) & 1, kh = w >> 2;
    const unsigned swz = (unsigned)(((lane & 7) ^ (((row8 >> 1) & 1) << 2)) * 16);

    if (__builtin_expect(ffn, 1)) {
        constexpr int A_W = 58368, A_WS = 24576;
#define A_KC(c) (rot + (c) >= 7 ? rot + (c) - 7 : rot + (c))
        const unsigned wvo = (unsigned)(row8 * 2688) + swz;
        const char* wsrc = wqkv + acg * 128 + (size_t)w * 8 * 2688;
        const unsigned wdst = lds0 + A_W + w * 1024;
#define A_PIECE(c, i) glds(wvo, wsrc + A_KC(c) * (64 * 2688) + (i) * 896, wdst + ((c) & 3) * A_WS + (i) * 8192)
#define A_GROUP(c) do { A_PIECE(c, 1); A_PIECE(c, 2); A_PIECE(c, 0); } while (0)
        epoch = peek_flag(ws, FL_EPOCH * 128); FENCE(); fl1 = (FL_KV + atb * 8 + acg) * 128;
        if (EARLYG) A_GROUP(1);
        lds_ptr xb[4], wb1, wb1h;
#pragma unroll
        for (int j = 0; j < 4; ++j) xb[j] = lds + 1024 + tt * 28672 + (r >> 3) * 1024 + (r & 7) * 128 + (((2 * j + hf) ^ ((r >> 1) & 7)) * 16);
        wb1 = opq(lds + A_W + (kh ? 0 : 8192) + (8 * hf + q4) * 128 + ((ct ^ (q4 >> 1)) * 64) + g1 * 32 + p4 * 8);
        wb1h = opq(wb1 + 2 * A_WS);
        unsigned go1[2], go2[2]; int gi[2];
#pragma unroll
        for (int p = 0; p < 2; ++p) {
            const int G = acg * 8 + ct * 4 + 2 * p + hf, hp = G / 7, i = G - hp * 7;
            const unsigned hs = kh ? 131072u : 262144u;
            const unsigned to = (kh ? WS_QF + (atb * 2 + tt) * 2048 : WS_KV + (atb * 2 + tt) * 4096) + r * 16;
            go1[p] = (unsigned)(2 * hp + (i > 3)) * hs + (unsigned)(i > 3 ? i - 3 : i) * 512 + to;
            go2[p] = i == 3 ? (unsigned)(2 * hp + 1) * hs + to : OOB;
            gi[p] = i;
        }
        const int vcol = acg * 64 + ct * 32 + r, vh = vcol / 28, vd = vcol - vh * 28;
        unsigned vo = WS_KV + (atb * 2 + tt) * 4096 + (unsigned)vh * 262144 + 2048 + hf * 512 + vd * 16;
        PIN2(go1[0], go1[1]); PIN2(go2[0], go2[1]); PIN(vo);
        lds_ptr xw = opq(lds + 1024 + kh * 28672 + (w & 3) * 1024 + l16);
        waitv<EARLYG ? 7 : 4>();
        TS(28);
        u32x4 xv[7];
#pragma unroll
        for (int c = 0; c < 7; ++c) xv[c] = *(LAS u32x4*)(xw + c * 4096);
        SB();
        f32x4 sm = {};
#pragma unroll
        for (int c = 0; c < 7; ++c)
#pragma unroll
            for (int j = 0; j < 4; ++j) sm[j] = dot2(xv[c][j], ones, sm[j]);
        float nm = sum8((sm[0] + sm[1]) + (sm[2] + sm[3])) * (-1.f / D);
        PIN(nm);
#pragma unroll
        for (int c = 0; c < 7; ++c) { xv[c] = shift8(xv[c], nm); *(LAS u32x4*)(xw + c * 4096) = xv[c]; }
        if (EARLYG) A_GROUP(2); else A_GROUP(1);
        SB();
        sm = (f32x4){};
#pragma unroll
        for (int c = 0; c < 7; ++c)
#pragma unroll
            for (int j = 0; j < 4; ++j) sm[j] = dot2(xv[c][j], xv[c][j], sm[j]);
        LAS float* rst = (LAS float*)lds;
        rst[w * 8 + row8] = __builtin_amdgcn_rsqf(sum8((sm[0] + sm[1]) + (sm[2] + sm[3])) * (1.f / D) + EPS);
        if (EARLYG) A_GROUP(3); else A_GROUP(2);
        TS(31);
        bf16x8 fx[4], f1[4], f2[4];
#define A_WAIT(c) do { if ((c) == 0) waitv_bar<EARLYG ? 9 : 6>(); else if ((c) == 1) waitv_barx<6>(); else if ((c) < 6) waitv_barx<3>(); else waitv_barx<0>(); \
                       if ((c) == 0) TS(1); if ((c) == 6) TS(2); } while (0)
#define A_WP(c, ks) ((((c) & 2) ? wb1h : wb1) + ((c) & 1) * A_WS + (ks) * 2048)
        if (w < 4) {
            f32x16 ak = {}, av = {};
            f32x4 rv[4];
            float rs = 0.f;
#pragma unroll
            for (int n = 0; n < 30; ++n) {
                const int c = n >> 2, ks = n & 3, km = (n - 2) & 3;
                if (n < 28 && ks == 0) A_WAIT(c);
                if (n >= 2) { ak = MFMA(f1[km], fx[km], ak); PIN(ak); } SB();
                if (n < 28) { fx[ks] = lds8(xb[ks] + c * 4096); f1[ks] = cat(trd(A_WP(c, ks)), trd(A_WP(c, ks) + 512)); } SB();
                if (n >= 2) { av = MFMA(fx[km], f2[km], av); PIN(av); } SB();
                if (n < 28) f2[ks] = cat(trd(A_WP(c, ks) + 8192), trd(A_WP(c, ks) + 8192 + 512));
                if (!EARLYG && c == 0 && ks < 3) A_PIECE(3, ks); if (c >= 2 && c < 5 && ks < 3) A_PIECE(c + 2, ks);
                if (n == 21) {
                    rs = rst[tt * 32 + r];
#pragma unroll
                    for (int g = 0; g < 4; ++g) rv[g] = *(LAS f32x4*)(rst + tt * 32 + 8 * g + 4 * hf);
                }
                if (n == 24) { PIN(epoch); tagv = epoch + 1; PIN(tagv); }
                if (n == 24) {
                    const unsigned pv = vd == 0 ? ones : 0u, po = vd < 4 ? vo + 448 : OOB;
                    st16_dev((u32x4){pv, pv, pv, pv}, rws, po, 0); st16_dev((u32x4){pv, pv, pv, pv}, rws, po + 1024, 0);
                }
                SB();
            }
            TS(3);
#pragma unroll
            for (int i = 0; i < 16; ++i) { ak[i] *= rs; av[i] *= rv[i >> 2][i & 3]; }
#pragma unroll
            for (int p = 0; p < 2; ++p) {
                const u32x4 f = pack8(ak, p);
                const auto s0 = __builtin_amdgcn_permlane32_swap(f[0], f[2], false, false), s1 = __builtin_amdgcn_permlane32_swap(f[1], f[3], false, false);
                const u32x4 c = {s0[0], s1[0], s0[1], s1[1]};
                st16_dev(c, rws, go1[p], 0); st16_dev(c, rws, go2[p], 0);
            }
            st16_dev(pack8(av, 0), rws, vo, 0); st16_dev(pack8(av, 1), rws, vo + 1024, 0);
        } else {
            f32x16 aq = {};
            __builtin_amdgcn_s_setprio(1);
#pragma unroll
            for (int n = 0; n < 30; ++n) {
                const int c = n >> 2, ks = n & 3, km = (n - 2) & 3;
                if (n < 28 && ks == 0) A_WAIT(c);
                if (n == 24) { PIN(epoch); tagv = epoch + 1; PIN(tagv); }
                if (n >= 2) { aq = MFMA(f1[km], fx[km], aq); PIN(aq); } SB();
                if (n < 28) { fx[ks] = lds8(xb[ks] + c * 4096); f1[ks] = cat(trd(A_WP(c, ks)), trd(A_WP(c, ks) + 512)); }
                if (!EARLYG && c == 0 && ks < 3) A_PIECE(3, ks); if (c >= 2 && c < 5 && ks < 3) A_PIECE(c + 2, ks);
                SB();
            }
            __builtin_amdgcn_s_setprio(0);
            const float QS = rst[tt * 32 + r] * (0.18898223650461362f * 1.4426950408889634f);
#pragma unroll
            for (int i = 0; i < 16; ++i) aq[i] *= QS;
#pragma unroll
            for (int p = 0; p < 2; ++p) {
                const u32x4 f = pack8(aq, p);
                const auto s0 = __builtin_amdgcn_permlane32_swap(f[0], f[2], false, false), s1 = __builtin_amdgcn_permlane32_swap(f[1], f[3], false, false);
                const bool sp = gi[p] == 3;
                st16_dev((u32x4){s0[0], s1[0], sp ? 0u : s0[1], sp ? 0u : s1[1]}, rws, go1[p], 0);
                st16_dev((u32x4){0u, 0u, s0[1], s1[1]}, rws, go2[p], 0);
            }
        }
    } else {
        epoch = peek_flag(ws, FL_EPOCH * 128); fl1 = (FL_RT + xcd * 4 + loc - 28) * 128;
        const int ri = xcd * 4 + loc - 28, nu = (w == 0 && ri < 16) ? 4 : 3;
        const unsigned vo = (unsigned)(row8 * 3584 + (lane & 7) * 16);
#pragma unroll 1
        for (int i = 0; i < nu; ++i) {
            const int u = i < 3 ? i * 256 + ri * 8 + w : 768 + ri;
            const size_t so = (size_t)(u / 28) * 16 * 3584 + (u % 28) * 128;
            const unsigned d_ = lds0 + w * 16384 + i * 4096;
            glds(vo, wg + so, d_); glds(vo, wg + so + 8 * 3584, d_ + 1024); glds(vo, wu + so, d_ + 2048); glds(vo, wu + so + 8 * 3584, d_ + 3072);
        }
#pragma unroll 1
        for (int i = 0; i < nu; ++i) {
            const int u = i < 3 ? i * 256 + ri * 8 + w : 768 + ri;
            waitv<8>();
            lds_ptr sg = lds + w * 16384 + i * 4096 + g1 * 2048 + (4 * hf + q4) * 128 + p4 * 8;
#pragma unroll
            for (int e = 0; e < 4; ++e) st16_dev(U4(cat(trd(sg + e * 32), trd(sg + e * 32 + 1024))), rws, l16, WS_WGU + (unsigned)(((u % 28) * 4 + e) * 28 + u / 28) * 1024);
            FENCE();
        }
        tagv = epoch + 1;
    }
    TS(24);
    const int head = xcd * 2 + (loc & 1), rb = loc >> 1, qt = w & 3;
    const char* kvs = ws + WS_KV + (size_t)head * 262144 + w * 2048;
    const char* qs = ws + WS_QF + (size_t)(head * 64 + rb * 4 + qt) * 2048 + kh * 1024;
    unsigned kvd = lds0 + w * 2048, qd = lds0 + 147456 + qt * 2048 + kh * 1024;
    unsigned pf1 = (FL_KV + (lane >> 1) * 8 + (head * 28 + (lane & 1) * 27) / 64) * 128;
    PINS(kvs); PINS(qs); PINS(kvd); PINS(qd); PIN2(pf1, fl1);
    waitv_bar<0>();
    if (w == 0) set_flag(ws, fl1, tagv);
    const u64 tag = ((u64)(unsigned)__builtin_amdgcn_readfirstlane((unsigned)(tagv >> 32)) << 32 | (u64)(unsigned)__builtin_amdgcn_readfirstlane((unsigned)tagv));
    TS(4);

    const int drb = (xcd >> 1) * 4 + l7, dfg = (xcd & 1) * 7 + m7;
    bf16x8 wf[28];
    u32x2 xa, xb;
    f32x16 cs = {}, acc = {};
    const u32x4 one4 = {ones, ones, ones, ones};
    const unsigned hi_ = (unsigned)((tb * 2 + tt) * 28 + m7 * 4 + ct * 2 + kh), ofo = (unsigned)(tt * 32768 + kh * 16384 + ct * 4096);
    unsigned ho = WS_HF + hi_ * 1024 + l16, so = hf ? OOB : WS_HS + hi_ * 256 + r * 8;
    const char* os = ws + WS_OF + (size_t)tb * 65536 + ofo;
    unsigned od = lds0 + 65536 + ofo;
    unsigned pf2 = (FL_O + (tb >> 1) * 16 + kh * 8 + ct * 2 + ((lane >> 1) & 1) + (lane & 4)) * 128 + ((lane & 1) * 4 + (tb & 1) * 2 + tt) * 8;
    pf2 &= ~127u;
    unsigned go = ffn ? WS_GF + (unsigned)(drb * 4 * 112 + dfg * 8 + w) * 1024 + l16 : OOB;
    unsigned fo3 = (FL_H + tb * 8 + m7) * 128;
    {
        lds_ptr ap = opq(lds + kh * 8192 + l16), apB = opq(lds + 65536 + kh * 8192 + l16), apC = opq(lds + 131072 + kh * 8192 + l16);
        const unsigned lv = kh * 8192 + (l16 ^ (kh * 256));
        lds_ptr vp = opq(lds + lv), vpB = opq(lds + 65536 + lv), vpC = opq(lds + 131072 + lv);
#define AP(off) ((off) < 65536 ? ap + (off) : (off) < 131072 ? apB + ((off) - 65536) : apC + ((off) - 131072))
#define VP(off) ((off) < 65536 ? vp + (off) : (off) < 131072 ? vpB + ((off) - 65536) : vpC + ((off) - 131072))
#define KV_ISSUE(t) glds2_dev(l16, kvs + (t) * 16384, kvd + phys(t))
        unsigned wgo = ffn ? WS_WGU + (unsigned)(dfg * 8 + w) * 28672 : OOB;
        unsigned clro = WS_GF + (unsigned)((((xcd * 4 + loc - 28) >> 1) * 4 + 2 + (loc & 1)) * 112 + w * 14) * 1024 + l16;
        const int ri = umin(xcd * 4 + loc - 28, 27);
        const char* wos = (ffn ? wo + m7 * 128 : wd + (ri % 7) * 128 + (size_t)(ri / 7) * 448 * 896) + (size_t)w * 56 * 896;
        unsigned wov = (unsigned)(row8 * 896) + swz;
        const rsrc_t rx = __builtin_amdgcn_make_buffer_rsrc((void*)x, 0, 2048 * 448 * 2, 0x00020000);
        unsigned xo_ = (unsigned)(((tb * 2 + tt) * 32 + r) * 896 + m7 * 128 + ct * 64 + kh * 32 + hf * 8);
        unsigned rto = (FL_RT + r) * 128;
        lds_ptr qp = opq(lds + 147456 + qt * 2048 + l16);
        PINS(wgo); PINS(wos); PIN2(wov, xo_); PIN(rto); PIN2(ho, so); PIN2(pf2, fo3); PIN(go);
        lds_ptr xw = opq(lds + 147456 + w * 2048 + l16), xr = opq(lds + 147456 + (w ^ 4) * 2048 + l16);
        lds_ptr lw = opq(lds + 58368 + w * 256 + lane * 4), lr = opq(lds + 58368 + qt * 256 + 128 + r * 4);
        lds_ptr bw = opq(lds + 60416 + w * 4), br = opq(lds + 60416 + (lane & 7) * 4);
        unsigned oo = WS_OF + (unsigned)((rb * 4 + qt) * 32 + head * 2 + kh) * 1024 + l16;
        unsigned fo2 = (FL_O + rb * 16 + head) * 128 + w * 8;
        PIN2(oo, fo2);
        if (w == HOP3_POLLER) poll_flag(ws, pf1, tag);
        BAR();
        TS(5);
        glds_dev(l16, qs, qd);
        KV_ISSUE(0); KV_ISSUE(1);
        asm volatile("s_barrier" ::: "memory");
        KV_ISSUE(2); KV_ISSUE(3);
        waitv_bar<4>();
        TS(6);
        const bf16x8 q0 = lds8(qp), q1 = lds8(qp + 1024);
        f32x16 o = {}, sa = {}, sb;
        bf16x8 k0 = lds8(ap + phys(0)), k1 = lds8(ap + phys(0) + 1024), v0 = lds8(vp + phys(0) + 2048), v1 = {};
        u32x4 p0, p1 = {};
        sa = MFMA(k0, q0, sa); sa = MFMA(k1, q1, sa);
        k0 = lds8(ap + phys(0) + 4096); k1 = lds8(ap + phys(0) + 5120);
        PIN(sa); SB();
        u64 rtf = 0;
#define EXP4(S, o4) _Pragma("unroll") for (int i_ = o4; i_ < o4 + 4; ++i_) S[i_] = __builtin_amdgcn_exp2f(S[i_]);
#define WLD(s) wf[s] = BF8(ld16_dev(rws, l16 + ((s) & 3) * 1024, wgo + ((s) >> 2) * 4096))
#define SIDE(t, g) do { \
            if ((t) == 0 && (g) == 0) KV_ISSUE(4); if ((t) == 0 && (g) == 2) KV_ISSUE(5); if ((t) == 1 && (g) == 0) KV_ISSUE(6); if ((t) == 1 && (g) == 2) KV_ISSUE(7); \
            if ((t) == 2 && (g) == 0) KV_ISSUE(8); if ((t) == 1 && (g) == 3) rtf = peek_flag(ws, rto); \
            if ((t) == 3 && (g) < 3) KV_ISSUE(9 + (g)); \
            if ((t) >= 4 && (t) <= 10 && ((g) & 1)) { if (__builtin_expect(!ffn, 0)) st16_dev((u32x4){~0u, ~0u, ~0u, ~0u}, rws, clro + (((t) - 4) * 2 + ((g) >> 1)) * 1024, 0); } \
            if ((t) == 7 && (g) == 0) KV_ISSUE(12); if ((t) == 7 && (g) == 2) KV_ISSUE(13); if ((t) == 8 && (g) == 0) KV_ISSUE(14); if ((t) == 8 && (g) == 2) KV_ISSUE(15); \
            if ((t) >= 4 && (t) <= 6) WLD(((t) - 4) * 4 + (g)); \
            if (((t) == 7 || (t) == 8) && ((g) & 1)) WLD(12 + ((t) - 7) * 2 + ((g) >> 1)); \
            if ((t) >= 9 && (t) <= 11) WLD(((t) - 5) * 4 + (g)); \
            if ((t) == 12 || ((t) == 13 && (g) < 3)) glds(wov, wos + (((t) - 12) * 4 + (g)) * 8 * 896, lds0 + (w * 7 + ((t) - 12) * 4 + (g)) * 1024); \
            if ((t) == 13 && (g) == 3) { xa = ld8(rx, xo_, 0); xb = ld8(rx, xo_, 16); } \
            if ((t) == 14 && (g) == 1) { if (w >= 6) touch(lane * (w == 6 ? (WS_END / 64 & ~127u) : 28672), w == 6 ? ws : out, lds0 + 61440); } \
            if ((t) == 14 && (g) == 0) { if (tid < 32) *(LAS u32x4*)(lds + 57344 + tid * 16) = (u32x4){0, 0, 0, 0}; } \
        } while (0)
#define TOFF(i) (phys((i) >> 1) + ((i) & 1) * 4096)
#define TILE(i, SC, SN) do { \
            __builtin_amdgcn_s_setprio(((i) & 1) ? 1 : 3); \
            if ((i) < 31) { f32x16 z_ = {}; SN = MFMA(k0, q0, z_); PIN(SN); } if ((i) < 30) k0 = lds8(AP(TOFF((i) + 2))); SB(); \
            EXP4(SC, 0); PIN(SC); SB(); \
            o = MFMA(v1, BF8(p1), o); PIN(o); v1 = lds8(VP(TOFF(i) + 3072)); SIDE((i) >> 1, ((i) & 1) * 2); SB(); \
            EXP4(SC, 4); p0 = pack8(SC, 0); PIN(p0); SB(); \
            __builtin_amdgcn_s_setprio(((i) & 1) ? 0 : 2); \
            if ((i) < 31) { SN = MFMA(k1, q1, SN); PIN(SN); } if ((i) < 30) k1 = lds8(AP(TOFF((i) + 2) + 1024)); SB(); \
            EXP4(SC, 8); PIN(SC); SB(); \
            o = MFMA(v0, BF8(p0), o); PIN(o); if ((i) < 31) v0 = lds8(VP(TOFF((i) + 1) + 2048)); SIDE((i) >> 1, ((i) & 1) * 2 + 1); SB(); \
            EXP4(SC, 12); p1 = pack8(SC, 1); PIN(p1); SB(); } while (0)
#define STEP(t) do { TILE(2 * (t), sa, sb); TILE(2 * (t) + 1, sb, sa); } while (0)
        STEP(0);
        waitv_barx<4>();
        STEP(1); STEP(2);
        waitv_bar2<3>();
        TS(21);
        if (__builtin_expect(!wave_all(rtf == tag), 0)) poll_flag(ws, rto, tag);
        FENCE();
        STEP(3); STEP(4); STEP(5); STEP(6);
        waitv_bar2<12>();
        TS(22);
        STEP(7); STEP(8); STEP(9); STEP(10);
        waitv_bar2<9>();
        TS(23);
        STEP(11); STEP(12); STEP(13); STEP(14); STEP(15);
        o = MFMA(v1, BF8(p1), o);
        TS(7);
        u32x4 ov;
        {
            *(LAS f32x4*)xw = (f32x4){o[8], o[9], o[10], o[11]}; *(LAS f32x4*)(xw + 1024) = (f32x4){o[12], o[13], o[14], o[15]};
            const float l = kh ? o[4] : o[12];
            *(LAS float*)lw = l;
            float sa_ = 0.f;
#pragma unroll
            for (int i = 0; i < 16; ++i) sa_ += __builtin_fabsf(o[i]);
            const bool ok = sa_ < 0x1p100f && (hf == 0 || l > 0x1p-80f);
            *(LAS unsigned*)bw = wave_all(ok) ? 0u : 1u;
            BAR();
            const f32x4 a = *(LAS f32x4*)xr, c = *(LAS f32x4*)(xr + 1024);
            const float inv = __builtin_amdgcn_rcpf(*(LAS float*)lr + *(LAS float*)(lr + 1024));
            const unsigned bad = *(LAS unsigned*)br;
            const float iz = (kh && hf) ? 0.f : inv;
            ov = (u32x4){cvtpk((o[0] + a[0]) * inv, (o[1] + a[1]) * inv), cvtpk((o[2] + a[2]) * inv, (o[3] + a[3]) * inv),
                         cvtpk((o[4] + c[0]) * iz, (o[5] + c[1]) * iz), cvtpk((o[6] + c[2]) * iz, (o[7] + c[3]) * iz)};
            if (__builtin_expect(wave_any(bad != 0), 0)) {
                f32x16 of = {};
                float m = -0x1p126f;
#pragma unroll 1
                for (int t = 0; t < 16; ++t) {
                    BAR();
                    glds2_dev(l16, kvs + t * 16384, lds0 + 65536 + w * 2048);
                    waitv_bar<0>();
#pragma unroll 1
                    for (int u = 0; u < 2; ++u) {
                        f32x16 s = {};
                        s = MFMA(lds8(apB + u * 4096), q0, s); s = MFMA(lds8(apB + u * 4096 + 1024), q1, s);
                        float mx = s[0];
#pragma unroll
                        for (int i = 1; i < 16; ++i) mx = __builtin_fmaxf(mx, s[i]);
                        mx = __builtin_fmaxf(swap_max(mx), m);
                        const float alpha = __builtin_amdgcn_exp2f(m - mx);
                        m = mx;
#pragma unroll
                        for (int i = 0; i < 16; ++i) { s[i] = __builtin_amdgcn_exp2f(s[i] - mx); of[i] *= alpha; }
                        of = MFMA(lds8(vpB + u * 4096 + 2048), BF8(pack8(s, 0)), of);
                        of = MFMA(lds8(vpB + u * 4096 + 3072), BF8(pack8(s, 1)), of);
                    }
                }
                *(LAS f32x4*)xw = (f32x4){of[8], of[9], of[10], of[11]}; *(LAS f32x4*)(xw + 1024) = (f32x4){of[12], of[13], of[14], of[15]};
                *(LAS float*)lw = hf ? (kh ? of[4] : of[12]) : m;
                BAR();
                const f32x4 a2 = *(LAS f32x4*)xr, c2 = *(LAS f32x4*)(xr + 1024);
                const float m0 = *(LAS float*)(lr - 128), m1 = *(LAS float*)(lr + 1024 - 128), mm = __builtin_fmaxf(m0, m1);
                const float e0 = __builtin_amdgcn_exp2f(m0 - mm), e1 = __builtin_amdgcn_exp2f(m1 - mm);
                const float iv = __builtin_amdgcn_rcpf(*(LAS float*)lr * e0 + *(LAS float*)(lr + 1024) * e1);
                const float im = (kh ? e1 : e0) * iv, io = (kh ? e0 : e1) * iv;
                const bool z = kh && hf;
                ov = (u32x4){cvtpk(of[0] * im + a2[0] * io, of[1] * im + a2[1] * io), cvtpk(of[2] * im + a2[2] * io, of[3] * im + a2[3] * io),
                             z ? 0u : cvtpk(of[4] * im + c2[0] * io, of[5] * im + c2[1] * io), z ? 0u : cvtpk(of[6] * im + c2[2] * io, of[7] * im + c2[3] * io)};
            }
        }
        st16_dev(ov, rws, oo, 0);
        TS(25);
        FENCE();
        PIN2(xa, xb);
#pragma unroll
        for (int s = 27; s >= 0; --s) PIN(wf[s]);
#pragma unroll
        for (int s = 0; s < 8; ++s) cs = MFMA(wf[s], BF8(one4), cs);
        acc[0] = bflo(xa[0]); acc[1] = bfhi(xa[0]); acc[2] = bflo(xa[1]); acc[3] = bfhi(xa[1]); acc[4] = bflo(xb[0]); acc[5] = bfhi(xb[0]); acc[6] = bflo(xb[1]); acc[7] = bfhi(xb[1]);
        PIN(acc); PIN2(ho, so); PIN2(pf2, fo3); PINS(os); PINS(od); PIN(cs);
        waitv_bar<0>();
        if (w == 0) set_flag(ws, fo2, tagv);
        TS(8);
    }
    if (__builtin_expect(!ffn, 0)) {
        const int ri = xcd * 4 + loc - 28;
        if (ri < 28) {
#pragma unroll 1
            for (int u = w; u < 28; u += 8) {
                lds_ptr sd = lds + u * 2048 + (4 * hf + q4) * 128 + g1 * 32 + p4 * 8;
#pragma unroll
                for (int e = 0; e < 2; ++e) { lds_ptr p_ = sd + ((e ^ (q4 >> 1)) * 64);
                    st16_dev(U4(cat(trd(p_), trd(p_ + 1024))), rws, l16, WS_WD + (unsigned)(((ri % 7) * 112 + (ri / 7) * 28 + u) * 2 + e) * 1024); }
            }
        }
        waitv_bar<0>();
        if (w == 0) set_flag(ws, (FL_RD + ri) * 128, tagv);
        { const u64 e_ = pt1 + 4000; while (__builtin_amdgcn_s_memrealtime() < e_) __builtin_amdgcn_s_sleep(1); }
        return;
    }

    const int erb = tb >> 1;
    const char* hs = ws + WS_HF + (size_t)drb * 114688 + w * 1024;
    const char* hss = ws + WS_HS + (size_t)((drb * 4 + (w & 3)) * 28 + kh * 14) * 256;
    unsigned pf3, fo4 = (FL_G + drb * 64 + dfg) * 128 + w * 8;
    {
        lds_ptr wb = lds + kh * (8 * 28 * 128) + (4 * hf + q4) * 128 + ((ct ^ (q4 >> 1)) * 64) + (g1 ^ kh) * 32 + p4 * 8;
        bf16x8 wr[16];
#pragma unroll
        for (int s = 0; s < 12; ++s) { const int k0 = ((s >> 1) * 28 + (s & 1) * 16) * 128; wr[s] = cat(trd(wb + k0), trd(wb + k0 + 1024)); }
        lds_ptr cw = opq(lds + 131072 + w * 2048 + l16), cr = opq(lds + 131072 + (w ^ 4) * 2048 + l16);
        poll_flag(ws, pf2, tag);
        TS(9);
        glds4_dev(l16, os, od);
        asm volatile("s_barrier" ::: "memory");
        glds4_dev(l16, os + 8192, od + 8192);
        lds_ptr ob = opq(lds + 65536 + tt * 32768 + kh * 16384 + l16);
        bf16x8 of[8];
        waitv_bar<4>();
        TS(26);
#pragma unroll
        for (int s = 0; s < 8; ++s) of[s] = lds8(ob + s * 1024);
        SB();
#pragma unroll
        for (int s = 0; s < 8; ++s) acc = MFMA(wr[s], of[s], acc);
        PIN(acc); SB();
#pragma unroll
        for (int s = 12; s < 16; ++s) { const int k0 = ((s >> 1) * 28 + (s & 1) * 16) * 128; wr[s] = cat(trd(wb + k0), trd(wb + k0 + 1024)); }
        waitv_bar<0>();
#pragma unroll
        for (int s = 0; s < 8; ++s) of[s] = lds8(ob + (8 + s) * 1024);
        SB();
#pragma unroll
        for (int s = 0; s < 8; ++s) acc = MFMA(wr[8 + s], of[s], acc);
        TS(10);
        *(LAS f32x4*)cw = (f32x4){acc[8], acc[9], acc[10], acc[11]}; *(LAS f32x4*)(cw + 1024) = (f32x4){acc[12], acc[13], acc[14], acc[15]};
        BAR();
        const f32x4 a = *(LAS f32x4*)cr + (f32x4){acc[0], acc[1], acc[2], acc[3]}, b = *(LAS f32x4*)(cr + 1024) + (f32x4){acc[4], acc[5], acc[6], acc[7]};
        const u32x4 hv = {cvtpk(a[0], a[1]), cvtpk(a[2], a[3]), cvtpk(b[0], b[1]), cvtpk(b[2], b[3])};
        st16_dev(hv, rws, ho, 0);
        float s1 = 0.f, s2 = 0.f;
        acc_stats(hv, s1, s2);
        s1 = swap_add(s1); s2 = swap_add(s2);
        st8_dev((u32x2){f2u(s1), f2u(s2)}, rws, so, 0);
        FENCE();
#pragma unroll
        for (int s = 8; s < 14; ++s) cs = MFMA(wf[s], BF8(one4), cs);
        pf3 = (FL_H + drb * 16 + (lane & 8) + umin(lane & 7, 6)) * 128;
        PIN(go); PIN2(pf3, fo4); PINS(hs); PINS(hss); PIN(cs);
        waitv_bar<0>();
        if (w == 0) set_flag(ws, fo3, tagv);
        if (bid == 0 && w == 0) set_flag(ws, FL_EPOCH * 128, tagv);
        TS(11);
    }

    const unsigned wdo = WS_WD + (unsigned)(m7 * 112 + w * 14) * 2048;
    bf16x8 wdf[28];
    bf16x8 gf[GW];
    const unsigned erot = EROT * m7;
#define E_KS(s) (erot + (s) >= 14 ? erot + (s) - 14 : erot + (s))
    const int ta = erb * 4 + (tb & 1);
    const unsigned gxo = WS_GF + (unsigned)(ta * 112 + w * 14) * 1024;
    u64 gfl = 0;
    const unsigned gks = w * 14 + umin(lane & 15, 13), gfi = (FL_G + (erb * 4 + (tb & 1)) * 16 + (gks >> 3)) * 128 + (GWG ? 0 : (gks & 7) * 8);
    {
#pragma unroll
        for (int s = 14; s < 28; ++s) cs = MFMA(wf[s], BF8(one4), cs);
        lds_ptr csp = opq(lds + 161792 + w * 128 + hf * 64);
        if (r == 0) {
#pragma unroll
            for (int g = 0; g < 4; ++g) *(LAS f32x4*)(csp + g * 16) = (f32x4){cs[4 * g], cs[4 * g + 1], cs[4 * g + 2], cs[4 * g + 3]};
        }
        const unsigned hd = lds0 + w * 1024;
        const char* wds = ws + wdo;
        lds_ptr hb = opq(lds + l16), hb2 = opq(lds + 57344 + l16);
        lds_ptr wl = opq(lds + 114688 + w * 5120 + l16), wl2 = opq(lds + w * 7168 + l16);
        LAS f32x2* stp = (LAS f32x2*)opq(lds + 159744 + r * 8);
        if (w == HOP3_POLLER) poll_flag(ws, pf3, tag);
        BAR();
        TS(12);
#define H_PIECE(j) glds_dev(l16, hs + (j) * 8192, hd + (j) * 8192)
#define WDO(s) (E_KS((s) >> 1) * 2048 + ((s) & 1) * 1024)
#define WDLD(s) wdf[s] = BF8(ld16_dev(rws, l16, wdo + WDO(s)))
#define HB(n) ((n) < 56 ? hb + (n) * 1024 : hb2 + ((n) - 56) * 1024)
        H_PIECE(0);
        asm volatile("s_barrier" ::: "memory");
        H_PIECE(1); H_PIECE(2); H_PIECE(3);
        constexpr int PF = 5;
        bf16x8 bf[PF + 1];
        f32x16 acc[2];
        f32x2 sp[7], sv0, sv1;
        f32x4 c4[4];
        float mean = 0.f, r2 = 0.f, hp = 0.f;
        u32x4 hv;
        lds_ptr pk = opq(lds + 155648 + w * 512 + lane * 4);
        u64 pv = 0;
        const unsigned pkd = lds0 + 155648 + w * 512;
        const unsigned rdo = (FL_RD + ((lane & 2) ? drb * 2 + (lane & 1) : (lane & 1) ? tb : (w >> 1) * 7 + m7)) * 128;
        waitv_barx<3>();
        TS(27);
#pragma unroll
        for (int n = 0; n < PF; ++n) bf[n] = lds8(HB(n));
        SB();
        sfor<0, 125>([&](auto N_) __attribute__((always_inline)) {
            constexpr int n = decltype(N_)::v, p = n / 28, s = n % 28;
            if constexpr (n == 3) waitv_barx<3>();
            if constexpr (n == 11) waitv_barx<7>();
            if constexpr (n == 27) { waitv_barx<3>(); TS(13); }
            if constexpr (n == 64) { waitv_barx<10>(); if (!GWG || w == 0) set_flag(ws, fo4, tagv); TS(14); }
            if constexpr (n == 85) { if (GWG) waitv_barx<7>(); else waitv<7>(); if (!GWG || w == 0) set_flag(ws, fo4 + 2048, tagv); }
            if constexpr (n == 38) { if (__builtin_expect(!wave_all(pv == tag), 0)) poll_flag(ws, rdo, tag); FENCE(); }
            if constexpr (d_prio(n) != d_prio(n - 1)) __builtin_amdgcn_s_setprio(d_prio(n));
            if constexpr (n < 112) { if constexpr (s == 0) { f32x16 z = {}; acc[p & 1] = MFMA(wf[s], bf[n % (PF + 1)], z); } else acc[p & 1] = MFMA(wf[s], bf[n % (PF + 1)], acc[p & 1]); PIN(acc[p & 1]); }
            SB();
            if constexpr (n + PF < 112) bf[(n + PF) % (PF + 1)] = lds8(HB(n + PF));
            if constexpr (n % 3 == 2 && n < 30) H_PIECE(4 + n / 3);
            if constexpr (n == 4) glds4_dev(l16, hss, lds0 + 114688 + w * 5120);
            if constexpr (n == 16) { waitv<4>();
#pragma unroll
                for (int j = 0; j < 7; ++j) sp[j] = *(LAS f32x2*)(lds + 114688 + w * 5120 + (hf * 7 + j) * 256 + r * 8);
            }
            if constexpr (n == 20) { const f32x2 t = ((sp[0] + sp[1]) + (sp[2] + sp[3])) + ((sp[4] + sp[5]) + sp[6]); stp[w * 32] = (f32x2){swap_add(t[0]), swap_add(t[1])}; }
            if constexpr (n >= 39 && n < 48 && n % 2 == 1) glds_dev(l16, wds + WDO((n - 39) / 2), lds0 + 114688 + w * 5120 + (n - 39) / 2 * 1024);
            if constexpr (n >= 48 && n < 64 && n % 3 == 0) WDLD(12 + (n - 48) / 3);
            if constexpr (n >= 70 && n < 84 && n % 2 == 0) glds_dev(l16, wds + WDO(5 + (n - 70) / 2), lds0 + w * 7168 + (n - 70) / 2 * 1024);
            if constexpr (n >= 85 && n < 105 && n % 2 == 1) WDLD(18 + (n - 85) / 2);
            if constexpr (n == 107) gfl = peek_flag(ws, gfi);
            if constexpr (n == 32) pv = peeked(pk);
            if constexpr (n == 18) peek_lds(ws, rdo, pkd);
            if constexpr (p >= 1) {
                const f32x16& a = acc[(p - 1) & 1];
                if constexpr (n == 100) { sv0 = stp[3 * 32]; sv1 = stp[7 * 32]; }
                if constexpr (s == 0 && p < 4) { sv0 = stp[(p - 1) * 32]; sv1 = stp[(p + 3) * 32];
#pragma unroll
                    for (int g = 0; g < 4; ++g) c4[g] = *(LAS f32x4*)(csp + g * 16); }
                if constexpr ((s == 3 && p < 4) || n == 104) { const f32x2 sv = sv0 + sv1; mean = sv[0] * (1.f / D); r2 = __builtin_amdgcn_rcpf(__builtin_fmaxf(sv[1] * (1.f / D) - mean * mean, 0.f) + EPS); PIN2(mean, r2); }
                if constexpr (s >= 4 && s < 12) { constexpr int i = s - 4;
                    const float h0 = __builtin_fmaxf(a[i] - mean * c4[i >> 2][i & 3], 0.f) * r2 * (a[8 + i] - mean * c4[2 + (i >> 2)][i & 3]);
                    if constexpr (i & 1) { hv[i >> 1] = p >= 3 ? umin(cvtpk(hp, h0), ~1u) : cvtpk(hp, h0); PIN(hv); } else { hp = h0; PIN(hp); } }
                if constexpr (s == 12) { st16_dev(hv, rws, go + (p - 1) * 114688, 0); FENCE(); }
            }
            SB();
        });
        TS(15);
        if (__builtin_expect(!wave_all(gfl == tag), 0)) poll_flag(ws, gfi, tag);
        FENCE();
#pragma unroll
        for (int s = 0; s < GW; ++s) { gf[s] = BF8(ld16_dev(rws, l16, gxo + E_KS(s) * 1024)); SB(); }
        FENCE();
#pragma unroll
        for (int s = 0; s < 12; ++s) wdf[s] = lds8(s < 5 ? wl + s * 1024 : wl2 + (s - 5) * 1024);
    }

    {
        const int etok = tid >> 4, eblk = tid & 15;
        const unsigned hro = (unsigned)((((eblk >> 2) & 3) * 64 + (eblk & 1) * 32 + etok) * 16 + ((eblk >> 1) & 1) * 8) + WS_HF + (unsigned)(ta * 28 + m7 * 4) * 1024;
        u32x2 hres[2];
        GAS u64* op = (GAS u64*)(out + (size_t)(ta * 32 + etok) * 896 + m7 * 128 + eblk * 8);
        lds_ptr rp = opq(lds + w * 8448 + hf * 528 + r * 16), rq = opq(lds + eblk * 528 + etok * 16);
        lds_ptr rp2 = opq(lds + 67584 + w * 8448 + hf * 528 + r * 16), rq2 = opq(lds + 67584 + eblk * 528 + etok * 16);
#define G_LD(s) gf[(s) % GW] = BF8(ld16_dev(rws, l16, gxo + ((s) / 14) * 229376 + E_KS((s) % 14) * 1024))
#define G_PUT(RP) do { \
            _Pragma("unroll") for (int g = 0; g < 4; ++g) *(LAS f32x4*)(RP + g * 1056) = (f32x4){c0[4 * g], c0[4 * g + 1], c0[4 * g + 2], c0[4 * g + 3]}; \
            _Pragma("unroll") for (int g = 0; g < 4; ++g) *(LAS f32x4*)(RP + 4224 + g * 1056) = (f32x4){c1[4 * g], c1[4 * g + 1], c1[4 * g + 2], c1[4 * g + 3]}; } while (0)
#define G_OUT(hh, RQ) do { f32x4 pa[8]; \
            _Pragma("unroll") for (int ww = 0; ww < 8; ++ww) pa[ww] = *(LAS f32x4*)(RQ + ww * 8448); SB(); \
            const f32x4 a = ((pa[0] + pa[1]) + (pa[2] + pa[3])) + ((pa[4] + pa[5]) + (pa[6] + pa[7])); \
            const u32x2 ov = {cvtpk(a[0] + bflo(hres[hh][0]), a[1] + bfhi(hres[hh][0])), cvtpk(a[2] + bflo(hres[hh][1]), a[3] + bfhi(hres[hh][1]))}; \
            __hip_atomic_store(op + (hh) * (64 * 896 / 8), __builtin_bit_cast(u64, ov), __ATOMIC_RELAXED, __HIP_MEMORY_SCOPE_AGENT); } while (0)
        f32x16 c0 = {}, c1 = {};
        TS(16);
        unsigned seen = 0;
#pragma unroll
        for (int s = 0; s < 28; ++s) {
            if (s == 1) { hres[0] = ld8_dev(rws, hro, 0); hres[1] = ld8_dev(rws, hro, 57344); FENCE(); }
            if (s == 10) BAR();
            if (s == 14) { G_PUT(rp); c0 = (f32x16){}; c1 = (f32x16){}; TS(17); }
            if (s == 20) { waitv<8>(); PIN2(hres[0], hres[1]); BAR(); if (!GWG || w == 0) { set_flag(ws, fo4 + 4096, tagv); set_flag(ws, fo4 + 6144, tagv); } G_OUT(0, rq); FENCE(); TS(18); }
            SB();
            c0 = MFMA(wdf[2 * (s % 14)], gf[s % GW], c0); PIN(c0); c1 = MFMA(wdf[2 * (s % 14) + 1], gf[s % GW], c1); PIN(c1);
            if (s >= 14) { const u32x4 u = U4(gf[s % GW]); seen = umax3(umax3(seen, u[0], u[1]), u[2], u[3]); PIN(seen); }
            SB();
            if (s + GW < 28) G_LD(s + GW);
        }
        if (__builtin_expect(wave_any(seen == ~0u), 0)) {
            poll_flag(ws, gfi + 4096, tag);
            c0 = (f32x16){}; c1 = (f32x16){};
#pragma unroll
            for (int s = 14; s < 14 + GW; ++s) G_LD(s);
#pragma unroll
            for (int s = 14; s < 28; ++s) { c0 = MFMA(wdf[2 * (s % 14)], gf[s % GW], c0); c1 = MFMA(wdf[2 * (s % 14) + 1], gf[s % GW], c1); PIN2(c0, c1); SB(); if (s + GW < 28) G_LD(s + GW); SB(); }
        }
        G_PUT(rp2);
        TS(19);
        BAR();
        G_OUT(1, rq2);
    }
    TS(20);
}

#define WS_OFF 0
extern "C" void kernel_launch(void* const* d_in, const int* in_sizes, int n_in, void* d_out, int out_size, void* d_ws, size_t ws_size, hipStream_t stream) {
    (void)hipFuncSetAttribute((const void*)block_fwd, hipFuncAttributeMaxDynamicSharedMemorySize, LDS_BYTES);
    block_fwd<<<256, 512, LDS_BYTES, stream>>>((const char*)d_in[0], (const char*)d_in[1], (const char*)d_in[2], (const char*)d_in[4], (const char*)d_in[5], (const char*)d_in[6], (char*)d_out, (char*)d_ws + WS_OFF);
}
```

```cpp
#include <hip/hip_runtime.h>
#include <hip/hip_bf16.h>
#include <stdint.h>

typedef __attribute__((ext_vector_type(8))) short bf16x8;
typedef __attribute__((ext_vector_type(4))) short s16x4;
typedef __attribute__((ext_vector_type(2))) __bf16 bf16x2;
typedef __attribute__((ext_vector_type(16))) float f32x16;
typedef __attribute__((ext_vector_type(4))) float f32x4;
typedef __attribute__((ext_vector_type(2))) float f32x2;
typedef __attribute__((ext_vector_type(4))) unsigned u32x4;
typedef __attribute__((ext_vector_type(2))) unsigned u32x2;
typedef short v4i16_t __attribute__((ext_vector_type(4)));
typedef unsigned long long u64;
#define LAS __attribute__((address_space(3)))
#define GAS __attribute__((address_space(1)))
typedef LAS char* lds_ptr;
#if defined(__HIP_DEVICE_COMPILE__)
#define NOPK __attribute__((target("no-packed-fp32-ops")))
#else
#define NOPK
#endif
#define DI __device__ __forceinline__
#define MFMA(a, b, c) __builtin_amdgcn_mfma_f32_32x32x16_bf16(a, b, c, 0, 0, 0)
#define MFMA16(a, b, c) __builtin_amdgcn_mfma_f32_16x16x32_bf16(a, b, c, 0, 0, 0)
#define BF8(x) __builtin_bit_cast(bf16x8, x)
#define U4(x) __builtin_bit_cast(u32x4, x)

constexpr int D = 448;
constexpr float EPS = 1e-5f;
constexpr unsigned WS_FLAG = 0;
constexpr unsigned FL_EPOCH = 0, FL_KV = 1, FL_RT = FL_KV + 256, FL_RD = FL_RT + 32, FL_CL = FL_RD + 32, FL_O = FL_CL + 32, FL_H = FL_O + 256, FL_G = FL_H + 256;
constexpr unsigned WS_KV = 1u << 20;
constexpr unsigned WS_QF = WS_KV + 4194304;
constexpr unsigned WS_OF = WS_QF + 2097152;
constexpr unsigned WS_HF = WS_OF + 2097152;
constexpr unsigned WS_GF = WS_HF + 64 * 28 * 1024;
constexpr unsigned WS_WGU = WS_GF + 64 * 112 * 1024;
constexpr unsigned WS_WD = WS_WGU + 112 * 28 * 1024;
constexpr unsigned WS_HS = WS_WD + 7 * 112 * 2 * 1024;
constexpr unsigned WS_END = WS_HS + 64 * 28 * 256 + 1024;
constexpr unsigned OOB = 0x40000000u;
constexpr int LDS_BYTES = 163840;

DI float u2f(unsigned u) { return __builtin_bit_cast(float, u); }
DI unsigned f2u(float f) { return __builtin_bit_cast(unsigned, f); }
DI unsigned umin(unsigned a, unsigned b) { return a < b ? a : b; }
DI unsigned umax3(unsigned a, unsigned b, unsigned c) { const unsigned m = a > b ? a : b; return m > c ? m : c; }
DI bool wave_any(bool p) { return __builtin_amdgcn_ballot_w64(p) != 0; }
DI bool wave_all(bool p) { return __builtin_amdgcn_ballot_w64(!p) == 0; }
DI unsigned cvtpk(float lo, float hi) { return __builtin_bit_cast(unsigned, __builtin_convertvector((f32x2){lo, hi}, bf16x2)); }
DI float bflo(unsigned u) { return u2f(u << 16); }
DI float bfhi(unsigned u) { return u2f(u & 0xffff0000u); }
DI void glds(unsigned voff, const void* sbase, unsigned dst) {
    asm volatile("s_mov_b32 m0, %2\n\ts_nop 0\n\tglobal_load_lds_dwordx4 %0, %1" :: "v"(voff), "s"(sbase), "s"(dst) : "memory");
}
DI void glds_dev(unsigned voff, const void* sbase, unsigned dst) {
    asm volatile("s_mov_b32 m0, %2\n\ts_nop 0\n\tglobal_load_lds_dwordx4 %0, %1 sc1" :: "v"(voff), "s"(sbase), "s"(dst) : "memory");
}
DI void glds2_dev(unsigned voff, const void* sbase, unsigned dst) {
    asm volatile("s_mov_b32 m0, %2\n\ts_nop 0\n\tglobal_load_lds_dwordx4 %0, %1 sc1\n\tglobal_load_lds_dwordx4 %0, %1 offset:1024 sc1" :: "v"(voff), "s"(sbase), "s"(dst) : "memory");
}
DI void glds4_dev(unsigned voff, const void* sbase, unsigned dst) {
    asm volatile("s_mov_b32 m0, %2\n\ts_nop 0\n\tglobal_load_lds_dwordx4 %0, %1 sc1\n\tglobal_load_lds_dwordx4 %0, %1 offset:1024 sc1\n\t"
                 "global_load_lds_dwordx4 %0, %1 offset:2048 sc1\n\tglobal_load_lds_dwordx4 %0, %1 offset:3072 sc1" :: "v"(voff), "s"(sbase), "s"(dst) : "memory");
}
DI s16x4 trd(lds_ptr p) { return __builtin_bit_cast(s16x4, __builtin_amdgcn_ds_read_tr16_b64_v4i16((LAS v4i16_t*)p)); }
DI bf16x8 cat(s16x4 a, s16x4 b) { return __builtin_shufflevector(a, b, 0, 1, 2, 3, 4, 5, 6, 7); }
DI bf16x8 lds8(lds_ptr p) { return *(LAS bf16x8*)p; }
DI float swap_add(float v) { auto r = __builtin_amdgcn_permlane32_swap(f2u(v), f2u(v), false, false); return u2f(r[0]) + u2f(r[1]); }
DI float swap_max(float v) { auto r = __builtin_amdgcn_permlane32_swap(f2u(v), f2u(v), false, false); return __builtin_fmaxf(u2f(r[0]), u2f(r[1])); }
DI u32x4 pack8(const f32x16& a, int s) { return (u32x4){cvtpk(a[8 * s], a[8 * s + 1]), cvtpk(a[8 * s + 2], a[8 * s + 3]), cvtpk(a[8 * s + 4], a[8 * s + 5]), cvtpk(a[8 * s + 6], a[8 * s + 7])}; }
DI float dot2(unsigned a, unsigned b, float c) { return __builtin_amdgcn_fdot2_f32_bf16(__builtin_bit_cast(bf16x2, a), __builtin_bit_cast(bf16x2, b), c, false); }
DI void acc_stats(u32x4 u, float& s1, float& s2) {
#pragma unroll
    for (int i = 0; i < 4; ++i) { const float a = bflo(u[i]), b = bfhi(u[i]); s1 += a; s2 = __builtin_fmaf(a, a, s2); s1 += b; s2 = __builtin_fmaf(b, b, s2); }
}
DI void acc_stats2(unsigned u0, unsigned u1, f32x4& s) {
    const float a = bflo(u0), b = bfhi(u0), c = bflo(u1), d = bfhi(u1);
    s[0] += a; s[1] += b; s[2] = __builtin_fmaf(a, a, s[2]); s[3] = __builtin_fmaf(b, b, s[3]); s[0] += c; s[1] += d; s[2] = __builtin_fmaf(c, c, s[2]); s[3] = __builtin_fmaf(d, d, s[3]);
}
DI float sum8(float v) {
    v += u2f(__builtin_amdgcn_update_dpp(0u, f2u(v), 0xB1, 0xf, 0xf, true));
    v += u2f(__builtin_amdgcn_update_dpp(0u, f2u(v), 0x4E, 0xf, 0xf, true));
    v += u2f(__builtin_amdgcn_update_dpp(0u, f2u(v), 0x141, 0xf, 0xf, true));
    return v;
}
DI u32x4 shift8(u32x4 x, float c) {
    u32x4 o; float t0, t1, t2, t3, t4, t5, t6, t7;
    asm("v_dot2_f32_bf16 %4, %12, %16, %18\n\tv_dot2_f32_bf16 %5, %12, %17, %18\n\tv_dot2_f32_bf16 %6, %13, %16, %18\n\tv_dot2_f32_bf16 %7, %13, %17, %18\n\t"
        "v_dot2_f32_bf16 %8, %14, %16, %18\n\tv_dot2_f32_bf16 %9, %14, %17, %18\n\tv_dot2_f32_bf16 %10, %15, %16, %18\n\tv_dot2_f32_bf16 %11, %15, %17, %18\n\t"
        "v_cvt_pk_bf16_f32 %0, %4, %5\n\tv_cvt_pk_bf16_f32 %1, %6, %7\n\tv_cvt_pk_bf16_f32 %2, %8, %9\n\tv_cvt_pk_bf16_f32 %3, %10, %11"
        : "=&v"(o[0]), "=&v"(o[1]), "=&v"(o[2]), "=&v"(o[3]), "=&v"(t0), "=&v"(t1), "=&v"(t2), "=&v"(t3), "=&v"(t4), "=&v"(t5), "=&v"(t6), "=&v"(t7)
        : "v"(x[0]), "v"(x[1]), "v"(x[2]), "v"(x[3]), "s"(0x00003f80u), "s"(0x3f800000u), "v"(c));
    return o;
}
template <int N> struct IC { static constexpr int v = N; };
template <int B, int E, class F> DI void sfor(F&& f) { if constexpr (B < E) { f(IC<B>{}); sfor<B + 1, E>(f); } }
template <int N> DI void waitv() { asm volatile("s_waitcnt vmcnt(%0)" :: "i"(N) : "memory"); }
template <int N> DI void waitv_bar() { asm volatile("s_waitcnt vmcnt(%0) lgkmcnt(0)\n\ts_barrier" :: "i"(N) : "memory"); }
template <int N> DI void waitv_barx() { asm volatile("s_waitcnt vmcnt(%0)\n\ts_barrier" :: "i"(N) : "memory"); }
template <int N> DI void waitv_bar2() { asm volatile("s_waitcnt vmcnt(%0) lgkmcnt(2)\n\ts_barrier" :: "i"(N) : "memory"); }
DI void touch(unsigned voff, const void* sbase, unsigned junk) {
    asm volatile("s_mov_b32 m0, %2\n\ts_nop 0\n\tglobal_load_lds_dword %0, %1 sc1" :: "v"(voff), "s"(sbase), "s"(junk) : "memory");
}
#define BAR() asm volatile("s_waitcnt lgkmcnt(0)\n\ts_barrier" ::: "memory")
#define FENCE() asm volatile("" ::: "memory")
typedef __amdgpu_buffer_rsrc_t rsrc_t;
DI u32x4 ld16(rsrc_t rs, unsigned voff, unsigned soff) { return U4(__builtin_amdgcn_raw_buffer_load_b128(rs, voff, soff, 0)); }
DI u32x4 ld16_dev(rsrc_t rs, unsigned voff, unsigned soff) { return U4(__builtin_amdgcn_raw_buffer_load_b128(rs, voff, soff, 16)); }
DI u32x2 ld8(rsrc_t rs, unsigned voff, unsigned soff) { return __builtin_bit_cast(u32x2, __builtin_amdgcn_raw_buffer_load_b64(rs, voff, soff, 0)); }
DI u32x2 ld8_dev(rsrc_t rs, unsigned voff, unsigned soff) { return __builtin_bit_cast(u32x2, __builtin_amdgcn_raw_buffer_load_b64(rs, voff, soff, 16)); }
DI void st16_dev(u32x4 v, rsrc_t rs, unsigned voff, unsigned soff) { __builtin_amdgcn_raw_buffer_store_b128(v, rs, voff + soff, 0, 16); }
DI void st8_dev(u32x2 v, rsrc_t rs, unsigned voff, unsigned soff) { __builtin_amdgcn_raw_buffer_store_b64(v, rs, voff, soff, 16); }
#define POLL_SLEEP 1
DI void set_flag(char* ws, unsigned fo, u64 tagv) {
    asm volatile("s_mov_b64 exec, 1\n\tglobal_store_dwordx2 %0, %1, %2 sc1\n\ts_mov_b64 exec, -1" :: "v"(fo), "v"(tagv), "s"(ws) : "memory");
}
DI u64 peek_flag(char* ws, unsigned fo) { return __hip_atomic_load((GAS u64*)(ws + fo), __ATOMIC_RELAXED, __HIP_MEMORY_SCOPE_AGENT); }
DI void peek_lds(char* ws, unsigned fo, unsigned dst) {
    asm volatile("s_mov_b32 m0, %2\n\ts_nop 0\n\tglobal_load_lds_dword %0, %1 sc1\n\ts_add_u32 m0, %2, 252\n\ts_nop 0\n\tglobal_load_lds_dword %0, %1 offset:4 sc1" :: "v"(fo), "s"(ws), "s"(dst) : "memory", "scc");
}
DI u64 peeked(lds_ptr p) { return (u64)*(LAS unsigned*)(p + 256) << 32 | *(LAS unsigned*)p; }
DI void poll_flag(char* ws, unsigned fo, u64 tag) {
#pragma unroll 1
    for (int spin = 0; spin < (1 << 16); ++spin) {
        if (wave_all(peek_flag(ws, fo) == tag)) break;
        __builtin_amdgcn_s_sleep(POLL_SLEEP);
    }
    FENCE();
}
#define SB() __builtin_amdgcn_sched_barrier(0)
#define PINS(a) asm volatile("" : "+s"(a))
#define PIN(a) asm volatile("" : "+v"(a))
#define PIN2(a, b) asm volatile("" : "+v"(a), "+v"(b))
DI lds_ptr opq(lds_ptr p) { unsigned u = (unsigned)(uintptr_t)p; asm volatile("" : "+v"(u)); return (lds_ptr)(uintptr_t)u; }
#define TS(i) do {} while (0)

DI constexpr int wd_slot(int s) { return (s * 17 + 27) / 28 != ((s + 1) * 17 + 27) / 28 ? (s * 17 + 27) / 28 : -1; }
#define EROT 0
#define CLR_SLEEP 90
#define ROTMODE 0
#define EARLYG 0
#define GWG 1
#define GW 8
#define CLRMODE 0
#define ENL 4
#define HOP3_POLLER 1
DI constexpr int d_prio(int n) { return n < 0 ? -1 : n < 27 ? 3 - n / 7 : n < 64 ? 3 - (n - 27) / 10 : n < 84 ? 3 - (n - 64) / 5 : n < 112 ? 3 - (n - 84) / 7 : 0; }
DI constexpr int phys(int t) { return ((t % 9) + 2) % 9 * 16384; }

__global__ NOPK __launch_bounds__(512, 2) void block_fwd(const char* x, const char* wqkv, const char* wo, const char* wg, const char* wu, const char* wd, char* out, char* ws) {
    extern __shared__ __attribute__((aligned(16))) char smem[];
    lds_ptr lds = (lds_ptr)smem;
    const int tid = __builtin_amdgcn_workitem_id_x(), lane = tid & 63, w = __builtin_amdgcn_readfirstlane(tid >> 6);
    const int bid = __builtin_amdgcn_workgroup_id_x(), xcd = bid & 7, loc = bid >> 3;
    const int r = lane & 31, hf = lane >> 5, q4 = (lane & 15) >> 2, g1 = (lane >> 4) & 1, p4 = lane & 3;
    const unsigned lds0 = (unsigned)(uintptr_t)smem;
    const unsigned l16 = lane * 16;
    TS(0);
    const int sp3 = (loc + 8) >> 5;
    const int atb = (xcd >> 1) * 8 + (loc & 7) + sp3 * (xcd & 1) * 4, acg = (loc >> 3) + (1 - sp3) * (xcd & 1) * 4;
    const int row8 = lane >> 3;
    const int rot8 = ((loc & 7) << sp3) + sp3 * (xcd & 1), rot = rot8 == 7 ? 0 : rot8;
    if (loc < 28) {
        const unsigned xo = (unsigned)((atb * 64 + (tid >> 3)) * 896 + (((tid ^ (tid >> 4)) & 7) * 16));
        const unsigned xd0 = lds0 + 1024 + (w >> 2) * 28672 + (w & 3) * 1024;
        unsigned xd = xd0 + (7 - rot) * 4096;
        const unsigned w0 = (unsigned)((rot * 64 + (tid >> 3)) * 2688 + acg * 128 + (((tid & 7) ^ ((tid >> 2) & 4)) * 16));
        asm volatile("s_cmp_eq_u32 %0, %3\n\ts_cselect_b32 %0, %2, %0\n\ts_mov_b32 m0, %0\n\ts_add_u32 %0, %0, 0x1000\n\tglobal_load_lds_dwordx4 %1, %4\n\t"
#define XP(o) "s_cmp_eq_u32 %0, %3\n\ts_cselect_b32 %0, %2, %0\n\ts_sub_u32 m0, %0, " #o "\n\ts_add_u32 %0, %0, 0x1000\n\tglobal_load_lds_dwordx4 %1, %4 offset:" #o "\n\t"
                     XP(128) XP(256) XP(384) XP(512) XP(640) XP(768)
                     "s_add_u32 m0, %6, 8192-896\n\ts_barrier\n\tglobal_load_lds_dwordx4 %5, %7 offset:896\n\t"
                     "s_add_u32 m0, %6, 16384-1792\n\ts_nop 0\n\tglobal_load_lds_dwordx4 %5, %7 offset:1792\n\t"
                     "s_mov_b32 m0, %6\n\ts_nop 0\n\tglobal_load_lds_dwordx4 %5, %7"
                     : "+s"(xd) : "v"(xo), "s"(xd0), "s"(xd0 + 7 * 4096), "s"(x), "v"(w0), "s"(lds0 + 58368 + w * 1024), "s"(wqkv) : "memory", "scc");
    }
    SB();
    const rsrc_t rws = __builtin_amdgcn_make_buffer_rsrc((void*)ws, 0, WS_END, 0x00020000);
    u64 epoch, tagv;
    unsigned fl1;
    const unsigned ones = 0x3f803f80u;
    const bool ffn = loc < 28;
    const int l7 = loc / 7, m7 = loc % 7;
    const int tb = xcd * 4 + l7;
    const int tt = w & 1, ct = (w >> 1) & 1, kh = w >> 2;
    const unsigned swz = (unsigned)(((lane & 7) ^ (((row8 >> 1) & 1) << 2)) * 16);

    if (__builtin_expect(ffn, 1)) {
        constexpr int A_W = 58368, A_WS = 24576;
#define A_KC(c) (rot + (c) >= 7 ? rot + (c) - 7 : rot + (c))
        const unsigned wvo = (unsigned)(row8 * 2688) + swz;
        const char* wsrc = wqkv + acg * 128 + (size_t)w * 8 * 2688;
        const unsigned wdst = lds0 + A_W + w * 1024;
#define A_PIECE(c, i) glds(wvo, wsrc + A_KC(c) * (64 * 2688) + (i) * 896, wdst + ((c) & 3) * A_WS + (i) * 8192)
#define A_GROUP(c) do { A_PIECE(c, 1); A_PIECE(c, 2); A_PIECE(c, 0); } while (0)
        epoch = peek_flag(ws, FL_EPOCH * 128); FENCE(); fl1 = (FL_KV + atb * 8 + acg) * 128;
        if (EARLYG) A_GROUP(1);
        lds_ptr xb[4], wb1, wb1h;
#pragma unroll
        for (int j = 0; j < 4; ++j) xb[j] = lds + 1024 + tt * 28672 + (r >> 3) * 1024 + (r & 7) * 128 + (((2 * j + hf) ^ ((r >> 1) & 7)) * 16);
        wb1 = opq(lds + A_W + (kh ? 0 : 8192) + (8 * hf + q4) * 128 + ((ct ^ (q4 >> 1)) * 64) + g1 * 32 + p4 * 8);
        wb1h = opq(wb1 + 2 * A_WS);
        unsigned go1[2], go2[2]; int gi[2];
#pragma unroll
        for (int p = 0; p < 2; ++p) {
            const int G = acg * 8 + ct * 4 + 2 * p + hf, hp = G / 7, i = G - hp * 7;
            const unsigned hs = kh ? 131072u : 262144u;
            const unsigned to = (kh ? WS_QF + (atb * 2 + tt) * 2048 : WS_KV + (atb * 2 + tt) * 4096) + r * 16;
            go1[p] = (unsigned)(2 * hp + (i > 3)) * hs + (unsigned)(i > 3 ? i - 3 : i) * 512 + to;
            go2[p] = i == 3 ? (unsigned)(2 * hp + 1) * hs + to : OOB;
            gi[p] = i;
        }
        const int vcol = acg * 64 + ct * 32 + r, vh = vcol / 28, vd = vcol - vh * 28;
        unsigned vo = WS_KV + (atb * 2 + tt) * 4096 + (unsigned)vh * 262144 + 2048 + hf * 512 + vd * 16;
        PIN2(go1[0], go1[1]); PIN2(go2[0], go2[1]); PIN(vo);
        lds_ptr xw = opq(lds + 1024 + kh * 28672 + (w & 3) * 1024 + l16);
        waitv<EARLYG ? 7 : 4>();
        TS(28);
        u32x4 xv[7];
#pragma unroll
        for (int c = 0; c < 7; ++c) xv[c] = *(LAS u32x4*)(xw + c * 4096);
        SB();
        f32x4 sm = {};
#pragma unroll
        for (int c = 0; c < 7; ++c)
#pragma unroll
            for (int j = 0; j < 4; ++j) sm[j] = dot2(xv[c][j], ones, sm[j]);
        float nm = sum8((sm[0] + sm[1]) + (sm[2] + sm[3])) * (-1.f / D);
        PIN(nm);
#pragma unroll
        for (int c = 0; c < 7; ++c) { xv[c] = shift8(xv[c], nm); *(LAS u32x4*)(xw + c * 4096) = xv[c]; }
        if (EARLYG) A_GROUP(2); else A_GROUP(1);
        SB();
        sm = (f32x4){};
#pragma unroll
        for (int c = 0; c < 7; ++c)
#pragma unroll
            for (int j = 0; j < 4; ++j) sm[j] = dot2(xv[c][j], xv[c][j], sm[j]);
        LAS float* rst = (LAS float*)lds;
        rst[w * 8 + row8] = __builtin_amdgcn_rsqf(sum8((sm[0] + sm[1]) + (sm[2] + sm[3])) * (1.f / D) + EPS);
        if (EARLYG) A_GROUP(3); else A_GROUP(2);
        TS(31);
        bf16x8 fx[4], f1[4], f2[4];
#define A_WAIT(c) do { if ((c) == 0) waitv_bar<EARLYG ? 9 : 6>(); else if ((c) == 1) waitv_barx<6>(); else if ((c) < 6) waitv_barx<3>(); else waitv_barx<0>(); \
                       if ((c) == 0) TS(1); if ((c) == 6) TS(2); } while (0)
#define A_WP(c, ks) ((((c) & 2) ? wb1h : wb1) + ((c) & 1) * A_WS + (ks) * 2048)
        if (w < 4) {
            f32x16 ak = {}, av = {};
            f32x4 rv[4];
            float rs = 0.f;
#pragma unroll
            for (int n = 0; n < 30; ++n) {
                const int c = n >> 2, ks = n & 3, km = (n - 2) & 3;
                if (n < 28 && ks == 0) A_WAIT(c);
                if (n >= 2) { ak = MFMA(f1[km], fx[km], ak); PIN(ak); } SB();
                if (n < 28) { fx[ks] = lds8(xb[ks] + c * 4096); f1[ks] = cat(trd(A_WP(c, ks)), trd(A_WP(c, ks) + 512)); } SB();
                if (n >= 2) { av = MFMA(fx[km], f2[km], av); PIN(av); } SB();
                if (n < 28) f2[ks] = cat(trd(A_WP(c, ks) + 8192), trd(A_WP(c, ks) + 8192 + 512));
                if (!EARLYG && c == 0 && ks < 3) A_PIECE(3, ks); if (c >= 2 && c < 5 && ks < 3) A_PIECE(c + 2, ks);
                if (n == 21) {
                    rs = rst[tt * 32 + r];
#pragma unroll
                    for (int g = 0; g < 4; ++g) rv[g] = *(LAS f32x4*)(rst + tt * 32 + 8 * g + 4 * hf);
                }
                if (n == 24) { PIN(epoch); tagv = epoch + 1; PIN(tagv); }
                if (n == 24) {
                    const unsigned pv = vd == 0 ? ones : 0u, po = vd < 4 ? vo + 448 : OOB;
                    st16_dev((u32x4){pv, pv, pv, pv}, rws, po, 0); st16_dev((u32x4){pv, pv, pv, pv}, rws, po + 1024, 0);
                }
                SB();
            }
            TS(3);
#pragma unroll
            for (int i = 0; i < 16; ++i) { ak[i] *= rs; av[i] *= rv[i >> 2][i & 3]; }
#pragma unroll
            for (int p = 0; p < 2; ++p) {
                const u32x4 f = pack8(ak, p);
                const auto s0 = __builtin_amdgcn_permlane32_swap(f[0], f[2], false, false), s1 = __builtin_amdgcn_permlane32_swap(f[1], f[3], false, false);
                const u32x4 c = {s0[0], s1[0], s0[1], s1[1]};
                st16_dev(c, rws, go1[p], 0); st16_dev(c, rws, go2[p], 0);
            }
            st16_dev(pack8(av, 0), rws, vo, 0); st16_dev(pack8(av, 1), rws, vo + 1024, 0);
        } else {
            f32x16 aq = {};
            __builtin_amdgcn_s_setprio(1);
#pragma unroll
            for (int n = 0; n < 30; ++n) {
                const int c = n >> 2, ks = n & 3, km = (n - 2) & 3;
                if (n < 28 && ks == 0) A_WAIT(c);
                if (n == 24) { PIN(epoch); tagv = epoch + 1; PIN(tagv); }
                if (n >= 2) { aq = MFMA(f1[km], fx[km], aq); PIN(aq); } SB();
                if (n < 28) { fx[ks] = lds8(xb[ks] + c * 4096); f1[ks] = cat(trd(A_WP(c, ks)), trd(A_WP(c, ks) + 512)); }
                if (!EARLYG && c == 0 && ks < 3) A_PIECE(3, ks); if (c >= 2 && c < 5 && ks < 3) A_PIECE(c + 2, ks);
                SB();
            }
            __builtin_amdgcn_s_setprio(0);
            const float QS = rst[tt * 32 + r] * (0.18898223650461362f * 1.4426950408889634f);
#pragma unroll
            for (int i = 0; i < 16; ++i) aq[i] *= QS;
#pragma unroll
            for (int p = 0; p < 2; ++p) {
                const u32x4 f = pack8(aq, p);
                const auto s0 = __builtin_amdgcn_permlane32_swap(f[0], f[2], false, false), s1 = __builtin_amdgcn_permlane32_swap(f[1], f[3], false, false);
                const bool sp = gi[p] == 3;
                st16_dev((u32x4){s0[0], s1[0], sp ? 0u : s0[1], sp ? 0u : s1[1]}, rws, go1[p], 0);
                st16_dev((u32x4){0u, 0u, s0[1], s1[1]}, rws, go2[p], 0);
            }
        }
    } else {
        epoch = peek_flag(ws, FL_EPOCH * 128); fl1 = (FL_RT + xcd * 4 + loc - 28) * 128;
        const int ri = xcd * 4 + loc - 28, nu = (w == 0 && ri < 16) ? 4 : 3;
        const unsigned vo = (unsigned)(row8 * 3584 + (lane & 7) * 16);
#pragma unroll 1
        for (int i = 0; i < nu; ++i) {
            const int u = i < 3 ? i * 256 + ri * 8 + w : 768 + ri;
            const size_t so = (size_t)(u / 28) * 16 * 3584 + (u % 28) * 128;
            const unsigned d_ = lds0 + w * 16384 + i * 4096;
            glds(vo, wg + so, d_); glds(vo, wg + so + 8 * 3584, d_ + 1024); glds(vo, wu + so, d_ + 2048); glds(vo, wu + so + 8 * 3584, d_ + 3072);
        }
#pragma unroll 1
        for (int i = 0; i < nu; ++i) {
            const int u = i < 3 ? i * 256 + ri * 8 + w : 768 + ri;
            waitv<8>();
            lds_ptr sg = lds + w * 16384 + i * 4096 + g1 * 2048 + (4 * hf + q4) * 128 + p4 * 8;
#pragma unroll
            for (int e = 0; e < 4; ++e) st16_dev(U4(cat(trd(sg + e * 32), trd(sg + e * 32 + 1024))), rws, l16, WS_WGU + (unsigned)(((u % 28) * 4 + e) * 28 + u / 28) * 1024);
            FENCE();
        }
        if (CLRMODE >= 2) {
            const unsigned c_ = WS_GF + (unsigned)(((ri >> 1) * 4 + 2 + (ri & 1)) * 112 + w * 14) * 1024 + l16;
#pragma unroll 1
            for (int i = 0; i < (CLRMODE == 2 ? 7 : 14); ++i) st16_dev((u32x4){~0u, ~0u, ~0u, ~0u}, rws, c_ + i * 1024, 0);
        }
        tagv = epoch + 1;
    }
    TS(24);
    const int head = xcd * 2 + (loc & 1), rb = loc >> 1, qt = w & 3;
    const char* kvs = ws + WS_KV + (size_t)head * 262144 + w * 2048;
    const char* qs = ws + WS_QF + (size_t)(head * 64 + rb * 4 + qt) * 2048 + kh * 1024;
    unsigned kvd = lds0 + w * 2048, qd = lds0 + 147456 + qt * 2048 + kh * 1024;
    unsigned pf1 = (FL_KV + (lane >> 1) * 8 + (head * 28 + (lane & 1) * 27) / 64) * 128;
    PINS(kvs); PINS(qs); PINS(kvd); PINS(qd); PIN2(pf1, fl1);
    waitv_bar<0>();
    if (w == 0) set_flag(ws, fl1, tagv);
    const u64 tag = ((u64)(unsigned)__builtin_amdgcn_readfirstlane((unsigned)(tagv >> 32)) << 32 | (u64)(unsigned)__builtin_amdgcn_readfirstlane((unsigned)tagv));
    TS(4);

    const int drb = (xcd >> 1) * 4 + l7, dfg = (xcd & 1) * 7 + m7;
    bf16x8 wf[28];
    u32x2 xa, xb;
    f32x16 cs = {}, acc = {};
    const u32x4 one4 = {ones, ones, ones, ones};
    const unsigned hi_ = (unsigned)((tb * 2 + tt) * 28 + m7 * 4 + ct * 2 + kh), ofo = (unsigned)(tt * 32768 + kh * 16384 + ct * 4096);
    unsigned ho = WS_HF + hi_ * 1024 + l16, so = hf ? OOB : WS_HS + hi_ * 256 + r * 8;
    const char* os = ws + WS_OF + (size_t)tb * 65536 + ofo;
    unsigned od = lds0 + 65536 + ofo;
    unsigned pf2 = (FL_O + (tb >> 1) * 16 + kh * 8 + ct * 2 + ((lane >> 1) & 1) + (lane & 4)) * 128 + ((lane & 1) * 4 + (tb & 1) * 2 + tt) * 8;
    pf2 &= ~127u;
    unsigned go = ffn ? WS_GF + (unsigned)(drb * 4 * 112 + dfg * 8 + w) * 1024 + l16 : OOB;
    unsigned fo3 = (FL_H + tb * 8 + m7) * 128;
    {
        lds_ptr ap = opq(lds + kh * 8192 + l16), apB = opq(lds + 65536 + kh * 8192 + l16), apC = opq(lds + 131072 + kh * 8192 + l16);
        const unsigned lv = kh * 8192 + (l16 ^ (kh * 256));
        lds_ptr vp = opq(lds + lv), vpB = opq(lds + 65536 + lv), vpC = opq(lds + 131072 + lv);
#define AP(off) ((off) < 65536 ? ap + (off) : (off) < 131072 ? apB + ((off) - 65536) : apC + ((off) - 131072))
#define VP(off) ((off) < 65536 ? vp + (off) : (off) < 131072 ? vpB + ((off) - 65536) : vpC + ((off) - 131072))
#define KV_ISSUE(t) glds2_dev(l16, kvs + (t) * 16384, kvd + phys(t))
        unsigned wgo = ffn ? WS_WGU + (unsigned)(dfg * 8 + w) * 28672 : OOB;
        unsigned clro = WS_GF + (unsigned)((((xcd * 4 + loc - 28) >> 1) * 4 + 2 + (loc & 1)) * 112 + w * 14) * 1024 + l16;
        const int ri = umin(xcd * 4 + loc - 28, 27);
        const char* wos = (ffn ? wo + m7 * 128 : wd + (ri % 7) * 128 + (size_t)(ri / 7) * 448 * 896) + (size_t)w * 56 * 896;
        unsigned wov = (unsigned)(row8 * 896) + swz;
        const rsrc_t rx = __builtin_amdgcn_make_buffer_rsrc((void*)x, 0, 2048 * 448 * 2, 0x00020000);
        unsigned xo_ = (unsigned)(((tb * 2 + tt) * 32 + r) * 896 + m7 * 128 + ct * 64 + kh * 32 + hf * 8);
        unsigned rto = (FL_RT + r) * 128;
        lds_ptr qp = opq(lds + 147456 + qt * 2048 + l16);
        PINS(wgo); PINS(wos); PIN2(wov, xo_); PIN(rto); PIN2(ho, so); PIN2(pf2, fo3); PIN(go);
        lds_ptr xw = opq(lds + 147456 + w * 2048 + l16), xr = opq(lds + 147456 + (w ^ 4) * 2048 + l16);
        lds_ptr lw = opq(lds + 58368 + w * 256 + lane * 4), lr = opq(lds + 58368 + qt * 256 + 128 + r * 4);
        lds_ptr bw = opq(lds + 60416 + w * 4), br = opq(lds + 60416 + (lane & 7) * 4);
        unsigned oo = WS_OF + (unsigned)((rb * 4 + qt) * 32 + head * 2 + kh) * 1024 + l16;
        unsigned fo2 = (FL_O + rb * 16 + head) * 128 + w * 8;
        PIN2(oo, fo2);
        if (w == HOP3_POLLER) poll_flag(ws, pf1, tag);
        BAR();
        TS(5);
        glds_dev(l16, qs, qd);
        KV_ISSUE(0); KV_ISSUE(1);
        asm volatile("s_barrier" ::: "memory");
        KV_ISSUE(2); KV_ISSUE(3);
        waitv_bar<4>();
        TS(6);
        const bf16x8 q0 = lds8(qp), q1 = lds8(qp + 1024);
        f32x16 o = {}, sa = {}, sb;
        bf16x8 k0 = lds8(ap + phys(0)), k1 = lds8(ap + phys(0) + 1024), v0 = lds8(vp + phys(0) + 2048), v1 = {};
        u32x4 p0, p1 = {};
        sa = MFMA(k0, q0, sa); sa = MFMA(k1, q1, sa);
        k0 = lds8(ap + phys(0) + 4096); k1 = lds8(ap + phys(0) + 5120);
        PIN(sa); SB();
        u64 rtf = 0;
#define EXP4(S, o4) _Pragma("unroll") for (int i_ = o4; i_ < o4 + 4; ++i_) S[i_] = __builtin_amdgcn_exp2f(S[i_]);
#define WLD(s) wf[s] = BF8(ld16_dev(rws, l16 + ((s) & 3) * 1024, wgo + ((s) >> 2) * 4096))
#define SIDE(t, g) do { \
            if ((t) == 0 && (g) == 0) KV_ISSUE(4); if ((t) == 0 && (g) == 2) KV_ISSUE(5); if ((t) == 1 && (g) == 0) KV_ISSUE(6); if ((t) == 1 && (g) == 2) KV_ISSUE(7); \
            if ((t) == 2 && (g) == 0) KV_ISSUE(8); if ((t) == 1 && (g) == 3) rtf = peek_flag(ws, rto); \
            if ((t) == 3 && (g) < 3) KV_ISSUE(9 + (g)); \
            if (CLRMODE == 0 && (t) >= 4 && (t) <= 10 && ((g) & 1)) { if (__builtin_expect(!ffn, 0)) st16_dev((u32x4){~0u, ~0u, ~0u, ~0u}, rws, clro + (((t) - 4) * 2 + ((g) >> 1)) * 1024, 0); } \
            if (CLRMODE == 1 && (t) >= 11 && ((t) - 11) * 4 + (g) < 14) { if (__builtin_expect(!ffn, 0)) st16_dev((u32x4){~0u, ~0u, ~0u, ~0u}, rws, clro + (((t) - 11) * 4 + (g)) * 1024, 0); } \
            if (CLRMODE == 2 && (t) >= 11 && !((g) & 1) && ((t) - 11) * 2 + (g) / 2 < 7) { if (__builtin_expect(!ffn, 0)) st16_dev((u32x4){~0u, ~0u, ~0u, ~0u}, rws, clro + (7 + ((t) - 11) * 2 + (g) / 2) * 1024, 0); } \
            if ((t) == 7 && (g) == 0) KV_ISSUE(12); if ((t) == 7 && (g) == 2) KV_ISSUE(13); if ((t) == 8 && (g) == 0) KV_ISSUE(14); if ((t) == 8 && (g) == 2) KV_ISSUE(15); \
            if ((t) >= 4 && (t) <= 6) WLD(((t) - 4) * 4 + (g)); \
            if (((t) == 7 || (t) == 8) && ((g) & 1)) WLD(12 + ((t) - 7) * 2 + ((g) >> 1)); \
            if ((t) >= 9 && (t) <= 11) WLD(((t) - 5) * 4 + (g)); \
            if ((t) == 12 || ((t) == 13 && (g) < 3)) glds(wov, wos + (((t) - 12) * 4 + (g)) * 8 * 896, lds0 + (w * 7 + ((t) - 12) * 4 + (g)) * 1024); \
            if ((t) == 13 && (g) == 3) { xa = ld8(rx, xo_, 0); xb = ld8(rx, xo_, 16); } \
            if ((t) == 14 && (g) == 1) { if (w >= 6) touch(lane * (w == 6 ? (WS_END / 64 & ~127u) : 28672), w == 6 ? ws : out, lds0 + 61440); } \
            if ((t) == 14 && (g) == 0) { if (tid < 32) *(LAS u32x4*)(lds + 57344 + tid * 16) = (u32x4){0, 0, 0, 0}; } \
        } while (0)
#define TOFF(i) (phys((i) >> 1) + ((i) & 1) * 4096)
#define TILE(i, SC, SN) do { \
            __builtin_amdgcn_s_setprio(((i) & 1) ? 1 : 3); \
            if ((i) < 31) { f32x16 z_ = {}; SN = MFMA(k0, q0, z_); PIN(SN); } if ((i) < 30) k0 = lds8(AP(TOFF((i) + 2))); SB(); \
            EXP4(SC, 0); PIN(SC); SB(); \
            o = MFMA(v1, BF8(p1), o); PIN(o); v1 = lds8(VP(TOFF(i) + 3072)); SIDE((i) >> 1, ((i) & 1) * 2); SB(); \
            EXP4(SC, 4); p0 = pack8(SC, 0); PIN(p0); SB(); \
            __builtin_amdgcn_s_setprio(((i) & 1) ? 0 : 2); \
            if ((i) < 31) { SN = MFMA(k1, q1, SN); PIN(SN); } if ((i) < 30) k1 = lds8(AP(TOFF((i) + 2) + 1024)); SB(); \
            EXP4(SC, 8); PIN(SC); SB(); \
            o = MFMA(v0, BF8(p0), o); PIN(o); if ((i) < 31) v0 = lds8(VP(TOFF((i) + 1) + 2048)); SIDE((i) >> 1, ((i) & 1) * 2 + 1); SB(); \
            EXP4(SC, 12); p1 = pack8(SC, 1); PIN(p1); SB(); } while (0)
#define STEP(t) do { TILE(2 * (t), sa, sb); TILE(2 * (t) + 1, sb, sa); } while (0)
        STEP(0);
        waitv_barx<4>();
        STEP(1); STEP(2);
        waitv_bar2<3>();
        TS(21);
        if (__builtin_expect(!wave_all(rtf == tag), 0)) poll_flag(ws, rto, tag);
        FENCE();
        STEP(3); STEP(4); STEP(5); STEP(6);
        waitv_bar2<12>();
        TS(22);
        STEP(7); STEP(8); STEP(9); STEP(10);
        waitv_bar2<9>();
        TS(23);
        STEP(11); STEP(12); STEP(13); STEP(14); STEP(15);
        o = MFMA(v1, BF8(p1), o);
        TS(7);
        u32x4 ov;
        {
            *(LAS f32x4*)xw = (f32x4){o[8], o[9], o[10], o[11]}; *(LAS f32x4*)(xw + 1024) = (f32x4){o[12], o[13], o[14], o[15]};
            const float l = kh ? o[4] : o[12];
            *(LAS float*)lw = l;
            float sa_ = 0.f;
#pragma unroll
            for (int i = 0; i < 16; ++i) sa_ += __builtin_fabsf(o[i]);
            const bool ok = sa_ < 0x1p100f && (hf == 0 || l > 0x1p-80f);
            *(LAS unsigned*)bw = wave_all(ok) ? 0u : 1u;
            BAR();
            const f32x4 a = *(LAS f32x4*)xr, c = *(LAS f32x4*)(xr + 1024);
            const float inv = __builtin_amdgcn_rcpf(*(LAS float*)lr + *(LAS float*)(lr + 1024));
            const unsigned bad = *(LAS unsigned*)br;
            const float iz = (kh && hf) ? 0.f : inv;
            ov = (u32x4){cvtpk((o[0] + a[0]) * inv, (o[1] + a[1]) * inv), cvtpk((o[2] + a[2]) * inv, (o[3] + a[3]) * inv),
                         cvtpk((o[4] + c[0]) * iz, (o[5] + c[1]) * iz), cvtpk((o[6] + c[2]) * iz, (o[7] + c[3]) * iz)};
            if (__builtin_expect(wave_any(bad != 0), 0)) {
                f32x16 of = {};
                float m = -0x1p126f;
#pragma unroll 1
                for (int t = 0; t < 16; ++t) {
                    BAR();
                    glds2_dev(l16, kvs + t * 16384, lds0 + 65536 + w * 2048);
                    waitv_bar<0>();
#pragma unroll 1
                    for (int u = 0; u < 2; ++u) {
                        f32x16 s = {};
                        s = MFMA(lds8(apB + u * 4096), q0, s); s = MFMA(lds8(apB + u * 4096 + 1024), q1, s);
                        float mx = s[0];
#pragma unroll
                        for (int i = 1; i < 16; ++i) mx = __builtin_fmaxf(mx, s[i]);
                        mx = __builtin_fmaxf(swap_max(mx), m);
                        const float alpha = __builtin_amdgcn_exp2f(m - mx);
                        m = mx;
#pragma unroll
                        for (int i = 0; i < 16; ++i) { s[i] = __builtin_amdgcn_exp2f(s[i] - mx); of[i] *= alpha; }
                        of = MFMA(lds8(vpB + u * 4096 + 2048), BF8(pack8(s, 0)), of);
                        of = MFMA(lds8(vpB + u * 4096 + 3072), BF8(pack8(s, 1)), of);
                    }
                }
                *(LAS f32x4*)xw = (f32x4){of[8], of[9], of[10], of[11]}; *(LAS f32x4*)(xw + 1024) = (f32x4){of[12], of[13], of[14], of[15]};
                *(LAS float*)lw = hf ? (kh ? of[4] : of[12]) : m;
                BAR();
                const f32x4 a2 = *(LAS f32x4*)xr, c2 = *(LAS f32x4*)(xr + 1024);
                const float m0 = *(LAS float*)(lr - 128), m1 = *(LAS float*)(lr + 1024 - 128), mm = __builtin_fmaxf(m0, m1);
                const float e0 = __builtin_amdgcn_exp2f(m0 - mm), e1 = __builtin_amdgcn_exp2f(m1 - mm);
                const float iv = __builtin_amdgcn_rcpf(*(LAS float*)lr * e0 + *(LAS float*)(lr + 1024) * e1);
                const float im = (kh ? e1 : e0) * iv, io = (kh ? e0 : e1) * iv;
                const bool z = kh && hf;
                ov = (u32x4){cvtpk(of[0] * im + a2[0] * io, of[1] * im + a2[1] * io), cvtpk(of[2] * im + a2[2] * io, of[3] * im + a2[3] * io),
                             z ? 0u : cvtpk(of[4] * im + c2[0] * io, of[5] * im + c2[1] * io), z ? 0u : cvtpk(of[6] * im + c2[2] * io, of[7] * im + c2[3] * io)};
            }
        }
        st16_dev(ov, rws, oo, 0);
        TS(25);
        FENCE();
        PIN2(xa, xb);
#pragma unroll
        for (int s = 27; s >= 0; --s) PIN(wf[s]);
#pragma unroll
        for (int s = 0; s < 8; ++s) cs = MFMA(wf[s], BF8(one4), cs);
        acc[0] = bflo(xa[0]); acc[1] = bfhi(xa[0]); acc[2] = bflo(xa[1]); acc[3] = bfhi(xa[1]); acc[4] = bflo(xb[0]); acc[5] = bfhi(xb[0]); acc[6] = bflo(xb[1]); acc[7] = bfhi(xb[1]);
        PIN(acc); PIN2(ho, so); PIN2(pf2, fo3); PINS(os); PINS(od); PIN(cs);
        waitv_bar<0>();
        if (w == 0) set_flag(ws, fo2, tagv);
        TS(8);
    }
    if (__builtin_expect(!ffn, 0)) {
        const int ri = xcd * 4 + loc - 28;
        if (ri < 28) {
#pragma unroll 1
            for (int u = w; u < 28; u += 8) {
                lds_ptr sd = lds + u * 2048 + (4 * hf + q4) * 128 + g1 * 32 + p4 * 8;
#pragma unroll
                for (int e = 0; e < 2; ++e) { lds_ptr p_ = sd + ((e ^ (q4 >> 1)) * 64);
                    st16_dev(U4(cat(trd(p_), trd(p_ + 1024))), rws, l16, WS_WD + (unsigned)(((ri % 7) * 112 + (ri / 7) * 28 + u) * 2 + e) * 1024); }
            }
        }
        waitv_bar<0>();
        if (w == 0) set_flag(ws, (FL_RD + ri) * 128, tagv);
        return;
    }

    const int erb = tb >> 1;
    const char* hs = ws + WS_HF + (size_t)drb * 114688 + w * 1024;
    const char* hss = ws + WS_HS + (size_t)((drb * 4 + (w & 3)) * 28 + kh * 14) * 256;
    unsigned pf3, fo4 = (FL_G + drb * 64 + dfg) * 128 + w * 8;
    {
        lds_ptr wb = lds + kh * (8 * 28 * 128) + (4 * hf + q4) * 128 + ((ct ^ (q4 >> 1)) * 64) + (g1 ^ kh) * 32 + p4 * 8;
        bf16x8 wr[16];
#pragma unroll
        for (int s = 0; s < 12; ++s) { const int k0 = ((s >> 1) * 28 + (s & 1) * 16) * 128; wr[s] = cat(trd(wb + k0), trd(wb + k0 + 1024)); }
        lds_ptr cw = opq(lds + 131072 + w * 2048 + l16), cr = opq(lds + 131072 + (w ^ 4) * 2048 + l16);
        poll_flag(ws, pf2, tag);
        TS(9);
        glds4_dev(l16, os, od);
        asm volatile("s_barrier" ::: "memory");
        glds4_dev(l16, os + 8192, od + 8192);
        lds_ptr ob = opq(lds + 65536 + tt * 32768 + kh * 16384 + l16);
        bf16x8 of[8];
        waitv_bar<4>();
        TS(26);
#pragma unroll
        for (int s = 0; s < 8; ++s) of[s] = lds8(ob + s * 1024);
        SB();
#pragma unroll
        for (int s = 0; s < 8; ++s) acc = MFMA(wr[s], of[s], acc);
        PIN(acc); SB();
#pragma unroll
        for (int s = 12; s < 16; ++s) { const int k0 = ((s >> 1) * 28 + (s & 1) * 16) * 128; wr[s] = cat(trd(wb + k0), trd(wb + k0 + 1024)); }
        waitv_bar<0>();
#pragma unroll
        for (int s = 0; s < 8; ++s) of[s] = lds8(ob + (8 + s) * 1024);
        SB();
#pragma unroll
        for (int s = 0; s < 8; ++s) acc = MFMA(wr[8 + s], of[s], acc);
        TS(10);
        *(LAS f32x4*)cw = (f32x4){acc[8], acc[9], acc[10], acc[11]}; *(LAS f32x4*)(cw + 1024) = (f32x4){acc[12], acc[13], acc[14], acc[15]};
        BAR();
        const f32x4 a = *(LAS f32x4*)cr + (f32x4){acc[0], acc[1], acc[2], acc[3]}, b = *(LAS f32x4*)(cr + 1024) + (f32x4){acc[4], acc[5], acc[6], acc[7]};
        const u32x4 hv = {cvtpk(a[0], a[1]), cvtpk(a[2], a[3]), cvtpk(b[0], b[1]), cvtpk(b[2], b[3])};
        st16_dev(hv, rws, ho, 0);
        float s1 = 0.f, s2 = 0.f;
        acc_stats(hv, s1, s2);
        s1 = swap_add(s1); s2 = swap_add(s2);
        st8_dev((u32x2){f2u(s1), f2u(s2)}, rws, so, 0);
        FENCE();
#pragma unroll
        for (int s = 8; s < 14; ++s) cs = MFMA(wf[s], BF8(one4), cs);
        pf3 = (FL_H + drb * 16 + (lane & 8) + umin(lane & 7, 6)) * 128;
        PIN(go); PIN2(pf3, fo4); PINS(hs); PINS(hss); PIN(cs);
        waitv_bar<0>();
        if (w == 0) set_flag(ws, fo3, tagv);
        if (bid == 0 && w == 0) set_flag(ws, FL_EPOCH * 128, tagv);
        TS(11);
    }

    const unsigned wdo = WS_WD + (unsigned)(m7 * 112 + w * 14) * 2048;
    bf16x8 wdf[28];
    bf16x8 gf[GW];
    const unsigned erot = EROT * m7;
#define E_KS(s) (erot + (s) >= 14 ? erot + (s) - 14 : erot + (s))
    const int ta = erb * 4 + (tb & 1);
    const unsigned gxo = WS_GF + (unsigned)(ta * 112 + w * 14) * 1024;
    u64 gfl = 0;
    const unsigned gks = w * 14 + umin(lane & 15, 13), gfi = (FL_G + (erb * 4 + (tb & 1)) * 16 + (gks >> 3)) * 128 + (GWG ? 0 : (gks & 7) * 8);
    {
#pragma unroll
        for (int s = 14; s < 28; ++s) cs = MFMA(wf[s], BF8(one4), cs);
        lds_ptr csp = opq(lds + 161792 + w * 128 + hf * 64);
        if (r == 0) {
#pragma unroll
            for (int g = 0; g < 4; ++g) *(LAS f32x4*)(csp + g * 16) = (f32x4){cs[4 * g], cs[4 * g + 1], cs[4 * g + 2], cs[4 * g + 3]};
        }
        const unsigned hd = lds0 + w * 1024;
        const char* wds = ws + wdo;
        lds_ptr hb = opq(lds + l16), hb2 = opq(lds + 57344 + l16);
        lds_ptr wl = opq(lds + 114688 + w * 5120 + l16), wl2 = opq(lds + w * 7168 + l16);
        LAS f32x2* stp = (LAS f32x2*)opq(lds + 159744 + r * 8);
        if (w == HOP3_POLLER) poll_flag(ws, pf3, tag);
        BAR();
        TS(12);
#define H_PIECE(j) glds_dev(l16, hs + (j) * 8192, hd + (j) * 8192)
#define WDO(s) (E_KS((s) >> 1) * 2048 + ((s) & 1) * 1024)
#define WDLD(s) wdf[s] = BF8(ld16_dev(rws, l16, wdo + WDO(s)))
#define HB(n) ((n) < 56 ? hb + (n) * 1024 : hb2 + ((n) - 56) * 1024)
        H_PIECE(0);
        asm volatile("s_barrier" ::: "memory");
        H_PIECE(1); H_PIECE(2); H_PIECE(3);
        constexpr int PF = 5;
        bf16x8 bf[PF + 1];
        f32x16 acc[2];
        f32x2 sp[7], sv0, sv1;
        f32x4 c4[4];
        float mean = 0.f, r2 = 0.f, hp = 0.f;
        u32x4 hv;
        lds_ptr pk = opq(lds + 155648 + w * 512 + lane * 4);
        u64 pv = 0;
        const unsigned pkd = lds0 + 155648 + w * 512;
        const unsigned rdo = (FL_RD + ((lane & 2) ? drb * 2 + (lane & 1) : (lane & 1) ? tb : (w >> 1) * 7 + m7)) * 128;
        waitv_barx<3>();
        TS(27);
#pragma unroll
        for (int n = 0; n < PF; ++n) bf[n] = lds8(HB(n));
        SB();
        sfor<0, 125>([&](auto N_) __attribute__((always_inline)) {
            constexpr int n = decltype(N_)::v, p = n / 28, s = n % 28;
            if constexpr (n == 3) waitv_barx<3>();
            if constexpr (n == 11) waitv_barx<7>();
            if constexpr (n == 27) { waitv_barx<3>(); TS(13); }
            if constexpr (n == 64) { waitv_barx<10>(); if (!GWG || w == 0) set_flag(ws, fo4, tagv); TS(14); }
            if constexpr (n == 85) { if (GWG) waitv_barx<7>(); else waitv<7>(); if (!GWG || w == 0) set_flag(ws, fo4 + 2048, tagv); }
            if constexpr (n == 38) { if (__builtin_expect(!wave_all(pv == tag), 0)) poll_flag(ws, rdo, tag); FENCE(); }
            if constexpr (d_prio(n) != d_prio(n - 1)) __builtin_amdgcn_s_setprio(d_prio(n));
            if constexpr (n < 112) { if constexpr (s == 0) { f32x16 z = {}; acc[p & 1] = MFMA(wf[s], bf[n % (PF + 1)], z); } else acc[p & 1] = MFMA(wf[s], bf[n % (PF + 1)], acc[p & 1]); PIN(acc[p & 1]); }
            SB();
            if constexpr (n + PF < 112) bf[(n + PF) % (PF + 1)] = lds8(HB(n + PF));
            if constexpr (n % 3 == 2 && n < 30) H_PIECE(4 + n / 3);
            if constexpr (n == 4) glds4_dev(l16, hss, lds0 + 114688 + w * 5120);
            if constexpr (n == 16) { waitv<4>();
#pragma unroll
                for (int j = 0; j < 7; ++j) sp[j] = *(LAS f32x2*)(lds + 114688 + w * 5120 + (hf * 7 + j) * 256 + r * 8);
            }
            if constexpr (n == 20) { const f32x2 t = ((sp[0] + sp[1]) + (sp[2] + sp[3])) + ((sp[4] + sp[5]) + sp[6]); stp[w * 32] = (f32x2){swap_add(t[0]), swap_add(t[1])}; }
            if constexpr (n >= 39 && n < 48 && n % 2 == 1) glds_dev(l16, wds + WDO((n - 39) / 2), lds0 + 114688 + w * 5120 + (n - 39) / 2 * 1024);
            if constexpr (n >= 48 && n < 64 && n % 3 == 0) WDLD(12 + (n - 48) / 3);
            if constexpr (n >= 70 && n < 84 && n % 2 == 0) glds_dev(l16, wds + WDO(5 + (n - 70) / 2), lds0 + w * 7168 + (n - 70) / 2 * 1024);
            if constexpr (n >= 85 && n < 105 && n % 2 == 1) WDLD(18 + (n - 85) / 2);
            if constexpr (n == 107) gfl = peek_flag(ws, gfi);
            if constexpr (n == 32) pv = peeked(pk);
            if constexpr (n == 18) peek_lds(ws, rdo, pkd);
            if constexpr (p >= 1) {
                const f32x16& a = acc[(p - 1) & 1];
                if constexpr (n == 100) { sv0 = stp[3 * 32]; sv1 = stp[7 * 32]; }
                if constexpr (s == 0 && p < 4) { sv0 = stp[(p - 1) * 32]; sv1 = stp[(p + 3) * 32];
#pragma unroll
                    for (int g = 0; g < 4; ++g) c4[g] = *(LAS f32x4*)(csp + g * 16); }
                if constexpr ((s == 3 && p < 4) || n == 104) { const f32x2 sv = sv0 + sv1; mean = sv[0] * (1.f / D); r2 = __builtin_amdgcn_rcpf(__builtin_fmaxf(sv[1] * (1.f / D) - mean * mean, 0.f) + EPS); PIN2(mean, r2); }
                if constexpr (s >= 4 && s < 12) { constexpr int i = s - 4;
                    const float h0 = __builtin_fmaxf(a[i] - mean * c4[i >> 2][i & 3], 0.f) * r2 * (a[8 + i] - mean * c4[2 + (i >> 2)][i & 3]);
                    if constexpr (i & 1) { hv[i >> 1] = p >= 3 ? umin(cvtpk(hp, h0), ~1u) : cvtpk(hp, h0); PIN(hv); } else { hp = h0; PIN(hp); } }
                if constexpr (s == 12) { st16_dev(hv, rws, go + (p - 1) * 114688, 0); FENCE(); }
            }
            SB();
        });
        TS(15);
        if (__builtin_expect(!wave_all(gfl == tag), 0)) poll_flag(ws, gfi, tag);
        FENCE();
#pragma unroll
        for (int s = 0; s < GW; ++s) { gf[s] = BF8(ld16_dev(rws, l16, gxo + E_KS(s) * 1024)); SB(); }
        FENCE();
#pragma unroll
        for (int s = 0; s < 12; ++s) wdf[s] = lds8(s < 5 ? wl + s * 1024 : wl2 + (s - 5) * 1024);
    }

    {
        const int etok = tid >> 4, eblk = tid & 15;
        const unsigned hro = (unsigned)((((eblk >> 2) & 3) * 64 + (eblk & 1) * 32 + etok) * 16 + ((eblk >> 1) & 1) * 8) + WS_HF + (unsigned)(ta * 28 + m7 * 4) * 1024;
        u32x2 hres[2];
        GAS u64* op = (GAS u64*)(out + (size_t)(ta * 32 + etok) * 896 + m7 * 128 + eblk * 8);
        lds_ptr rp = opq(lds + w * 8448 + hf * 528 + r * 16), rq = opq(lds + eblk * 528 + etok * 16);
        lds_ptr rp2 = opq(lds + 67584 + w * 8448 + hf * 528 + r * 16), rq2 = opq(lds + 67584 + eblk * 528 + etok * 16);
#define G_LD(s) gf[(s) % GW] = BF8(ld16_dev(rws, l16, gxo + ((s) / 14) * 229376 + E_KS((s) % 14) * 1024))
#define G_PUT(RP) do { \
            _Pragma("unroll") for (int g = 0; g < 4; ++g) *(LAS f32x4*)(RP + g * 1056) = (f32x4){c0[4 * g], c0[4 * g + 1], c0[4 * g + 2], c0[4 * g + 3]}; \
            _Pragma("unroll") for (int g = 0; g < 4; ++g) *(LAS f32x4*)(RP + 4224 + g * 1056) = (f32x4){c1[4 * g], c1[4 * g + 1], c1[4 * g + 2], c1[4 * g + 3]}; } while (0)
#define G_OUT(hh, RQ) do { f32x4 pa[8]; \
            _Pragma("unroll") for (int ww = 0; ww < 8; ++ww) pa[ww] = *(LAS f32x4*)(RQ + ww * 8448); SB(); \
            const f32x4 a = ((pa[0] + pa[1]) + (pa[2] + pa[3])) + ((pa[4] + pa[5]) + (pa[6] + pa[7])); \
            const u32x2 ov = {cvtpk(a[0] + bflo(hres[hh][0]), a[1] + bfhi(hres[hh][0])), cvtpk(a[2] + bflo(hres[hh][1]), a[3] + bfhi(hres[hh][1]))}; \
            __hip_atomic_store(op + (hh) * (64 * 896 / 8), __builtin_bit_cast(u64, ov), __ATOMIC_RELAXED, __HIP_MEMORY_SCOPE_AGENT); } while (0)
        f32x16 c0 = {}, c1 = {};
        TS(16);
        unsigned seen = 0;
#pragma unroll
        for (int s = 0; s < 28; ++s) {
            if (s == 1) { hres[0] = ld8_dev(rws, hro, 0); hres[1] = ld8_dev(rws, hro, 57344); FENCE(); }
            if (s == 10) BAR();
            if (s == 14) { G_PUT(rp); c0 = (f32x16){}; c1 = (f32x16){}; TS(17); }
            if (s == 20) { waitv<8>(); PIN2(hres[0], hres[1]); BAR(); if (!GWG || w == 0) { set_flag(ws, fo4 + 4096, tagv); set_flag(ws, fo4 + 6144, tagv); } G_OUT(0, rq); FENCE(); TS(18); }
            SB();
            c0 = MFMA(wdf[2 * (s % 14)], gf[s % GW], c0); PIN(c0); c1 = MFMA(wdf[2 * (s % 14) + 1], gf[s % GW], c1); PIN(c1);
            if (s >= 14) { const u32x4 u = U4(gf[s % GW]); seen = umax3(umax3(seen, u[0], u[1]), u[2], u[3]); PIN(seen); }
            SB();
            if (s + GW < 28) G_LD(s + GW);
        }
        if (__builtin_expect(wave_any(seen == ~0u), 0)) {
            poll_flag(ws, gfi + 4096, tag);
            c0 = (f32x16){}; c1 = (f32x16){};
#pragma unroll
            for (int s = 14; s < 14 + GW; ++s) G_LD(s);
#pragma unroll
            for (int s = 14; s < 28; ++s) { c0 = MFMA(wdf[2 * (s % 14)], gf[s % GW], c0); c1 = MFMA(wdf[2 * (s % 14) + 1], gf[s % GW], c1); PIN2(c0, c1); SB(); if (s + GW < 28) G_LD(s + GW); SB(); }
        }
        G_PUT(rp2);
        TS(19);
        BAR();
        G_OUT(1, rq2);
    }
    TS(20);
}

#define WS_OFF 0
extern "C" void kernel_launch(void* const* d_in, const int* in_sizes, int n_in, void* d_out, int out_size, void* d_ws, size_t ws_size, hipStream_t stream) {
    (void)hipFuncSetAttribute((const void*)block_fwd, hipFuncAttributeMaxDynamicSharedMemorySize, LDS_BYTES);
    block_fwd<<<256, 512, LDS_BYTES, stream>>>((const char*)d_in[0], (const char*)d_in[1], (const char*)d_in[2], (const char*)d_in[4], (const char*)d_in[5], (const char*)d_in[6], (char*)d_out, (char*)d_ws + WS_OFF);
}
```

```cpp
#define WS_OFF ((size_t)192  __H0__ )
#include <hip/hip_runtime.h>
#include <hip/hip_bf16.h>
#include <stdint.h>

typedef __attribute__((ext_vector_type(8))) short bf16x8;
typedef __attribute__((ext_vector_type(4))) short s16x4;
typedef __attribute__((ext_vector_type(2))) __bf16 bf16x2;
typedef __attribute__((ext_vector_type(16))) float f32x16;
typedef __attribute__((ext_vector_type(4))) float f32x4;
typedef __attribute__((ext_vector_type(2))) float f32x2;
typedef __attribute__((ext_vector_type(4))) unsigned u32x4;
typedef __attribute__((ext_vector_type(2))) unsigned u32x2;
typedef short v4i16_t __attribute__((ext_vector_type(4)));
typedef unsigned long long u64;
#define LAS __attribute__((address_space(3)))
#define GAS __attribute__((address_space(1)))
typedef LAS char* lds_ptr;
#if defined(__HIP_DEVICE_COMPILE__)
#define NOPK __attribute__((target("no-packed-fp32-ops")))
#else
#define NOPK
#endif
#define DI __device__ __forceinline__
#define MFMA(a, b, c) __builtin_amdgcn_mfma_f32_32x32x16_bf16(a, b, c, 0, 0, 0)
#define MFMA16(a, b, c) __builtin_amdgcn_mfma_f32_16x16x32_bf16(a, b, c, 0, 0, 0)
#define BF8(x) __builtin_bit_cast(bf16x8, x)
#define U4(x) __builtin_bit_cast(u32x4, x)

constexpr int D = 448;
constexpr float EPS = 1e-5f;
constexpr unsigned WS_FLAG = 0;
constexpr unsigned FL_EPOCH = 0, FL_KV = 1, FL_RT = FL_KV + 256, FL_RD = FL_RT + 32, FL_CL = FL_RD + 32, FL_O = FL_CL + 32, FL_H = FL_O + 256, FL_G = FL_H + 256;
constexpr unsigned WS_KV = 1u << 20;
constexpr unsigned WS_QF = WS_KV + 4194304;
constexpr unsigned WS_OF = WS_QF + 2097152;
constexpr unsigned WS_HF = WS_OF + 2097152;
constexpr unsigned WS_GF = WS_HF + 64 * 28 * 1024;
constexpr unsigned WS_WGU = WS_GF + 64 * 112 * 1024;
constexpr unsigned WS_WD = WS_WGU + 112 * 28 * 1024;
constexpr unsigned WS_HS = WS_WD + 7 * 112 * 2 * 1024;
constexpr unsigned WS_END = WS_HS + 64 * 28 * 256 + 1024;
constexpr unsigned OOB = 0x40000000u;
constexpr int LDS_BYTES = 163840;

DI float u2f(unsigned u) { return __builtin_bit_cast(float, u); }
DI unsigned f2u(float f) { return __builtin_bit_cast(unsigned, f); }
DI unsigned umin(unsigned a, unsigned b) { return a < b ? a : b; }
DI unsigned umax3(unsigned a, unsigned b, unsigned c) { const unsigned m = a > b ? a : b; return m > c ? m : c; }
DI bool wave_any(bool p) { return __builtin_amdgcn_ballot_w64(p) != 0; }
DI bool wave_all(bool p) { return __builtin_amdgcn_ballot_w64(!p) == 0; }
DI unsigned cvtpk(float lo, float hi) { return __builtin_bit_cast(unsigned, __builtin_convertvector((f32x2){lo, hi}, bf16x2)); }
DI float bflo(unsigned u) { return u2f(u << 16); }
DI float bfhi(unsigned u) { return u2f(u & 0xffff0000u); }
DI void glds(unsigned voff, const void* sbase, unsigned dst) {
    asm volatile("s_mov_b32 m0, %2\n\ts_nop 0\n\tglobal_load_lds_dwordx4 %0, %1" :: "v"(voff), "s"(sbase), "s"(dst) : "memory");
}
DI void glds_dev(unsigned voff, const void* sbase, unsigned dst) {
    asm volatile("s_mov_b32 m0, %2\n\ts_nop 0\n\tglobal_load_lds_dwordx4 %0, %1 sc1" :: "v"(voff), "s"(sbase), "s"(dst) : "memory");
}
DI void glds2_dev(unsigned voff, const void* sbase, unsigned dst) {
    asm volatile("s_mov_b32 m0, %2\n\ts_nop 0\n\tglobal_load_lds_dwordx4 %0, %1 sc1\n\tglobal_load_lds_dwordx4 %0, %1 offset:1024 sc1" :: "v"(voff), "s"(sbase), "s"(dst) : "memory");
}
DI void glds4_dev(unsigned voff, const void* sbase, unsigned dst) {
    asm volatile("s_mov_b32 m0, %2\n\ts_nop 0\n\tglobal_load_lds_dwordx4 %0, %1 sc1\n\tglobal_load_lds_dwordx4 %0, %1 offset:1024 sc1\n\t"
                 "global_load_lds_dwordx4 %0, %1 offset:2048 sc1\n\tglobal_load_lds_dwordx4 %0, %1 offset:3072 sc1" :: "v"(voff), "s"(sbase), "s"(dst) : "memory");
}
DI s16x4 trd(lds_ptr p) { return __builtin_bit_cast(s16x4, __builtin_amdgcn_ds_read_tr16_b64_v4i16((LAS v4i16_t*)p)); }
DI bf16x8 cat(s16x4 a, s16x4 b) { return __builtin_shufflevector(a, b, 0, 1, 2, 3, 4, 5, 6, 7); }
DI bf16x8 lds8(lds_ptr p) { return *(LAS bf16x8*)p; }
DI float swap_add(float v) { auto r = __builtin_amdgcn_permlane32_swap(f2u(v), f2u(v), false, false); return u2f(r[0]) + u2f(r[1]); }
DI float swap_max(float v) { auto r = __builtin_amdgcn_permlane32_swap(f2u(v), f2u(v), false, false); return __builtin_fmaxf(u2f(r[0]), u2f(r[1])); }
DI u32x4 pack8(const f32x16& a, int s) { return (u32x4){cvtpk(a[8 * s], a[8 * s + 1]), cvtpk(a[8 * s + 2], a[8 * s + 3]), cvtpk(a[8 * s + 4], a[8 * s + 5]), cvtpk(a[8 * s + 6], a[8 * s + 7])}; }
DI float dot2(unsigned a, unsigned b, float c) { return __builtin_amdgcn_fdot2_f32_bf16(__builtin_bit_cast(bf16x2, a), __builtin_bit_cast(bf16x2, b), c, false); }
DI void acc_stats(u32x4 u, float& s1, float& s2) {
#pragma unroll
    for (int i = 0; i < 4; ++i) { const float a = bflo(u[i]), b = bfhi(u[i]); s1 += a; s2 = __builtin_fmaf(a, a, s2); s1 += b; s2 = __builtin_fmaf(b, b, s2); }
}
DI void acc_stats2(unsigned u0, unsigned u1, f32x4& s) {
    const float a = bflo(u0), b = bfhi(u0), c = bflo(u1), d = bfhi(u1);
    s[0] += a; s[1] += b; s[2] = __builtin_fmaf(a, a, s[2]); s[3] = __builtin_fmaf(b, b, s[3]); s[0] += c; s[1] += d; s[2] = __builtin_fmaf(c, c, s[2]); s[3] = __builtin_fmaf(d, d, s[3]);
}
DI float sum8(float v) {
    v += u2f(__builtin_amdgcn_update_dpp(0u, f2u(v), 0xB1, 0xf, 0xf, true));
    v += u2f(__builtin_amdgcn_update_dpp(0u, f2u(v), 0x4E, 0xf, 0xf, true));
    v += u2f(__builtin_amdgcn_update_dpp(0u, f2u(v), 0x141, 0xf, 0xf, true));
    return v;
}
DI u32x4 shift8(u32x4 x, float c) {
    u32x4 o; float t0, t1, t2, t3, t4, t5, t6, t7;
    asm("v_dot2_f32_bf16 %4, %12, %16, %18\n\tv_dot2_f32_bf16 %5, %12, %17, %18\n\tv_dot2_f32_bf16 %6, %13, %16, %18\n\tv_dot2_f32_bf16 %7, %13, %17, %18\n\t"
        "v_dot2_f32_bf16 %8, %14, %16, %18\n\tv_dot2_f32_bf16 %9, %14, %17, %18\n\tv_dot2_f32_bf16 %10, %15, %16, %18\n\tv_dot2_f32_bf16 %11, %15, %17, %18\n\t"
        "v_cvt_pk_bf16_f32 %0, %4, %5\n\tv_cvt_pk_bf16_f32 %1, %6, %7\n\tv_cvt_pk_bf16_f32 %2, %8, %9\n\tv_cvt_pk_bf16_f32 %3, %10, %11"
        : "=&v"(o[0]), "=&v"(o[1]), "=&v"(o[2]), "=&v"(o[3]), "=&v"(t0), "=&v"(t1), "=&v"(t2), "=&v"(t3), "=&v"(t4), "=&v"(t5), "=&v"(t6), "=&v"(t7)
        : "v"(x[0]), "v"(x[1]), "v"(x[2]), "v"(x[3]), "s"(0x00003f80u), "s"(0x3f800000u), "v"(c));
    return o;
}
template <int N> struct IC { static constexpr int v = N; };
template <int B, int E, class F> DI void sfor(F&& f) { if constexpr (B < E) { f(IC<B>{}); sfor<B + 1, E>(f); } }
template <int N> DI void waitv() { asm volatile("s_waitcnt vmcnt(%0)" :: "i"(N) : "memory"); }
template <int N> DI void waitv_bar() { asm volatile("s_waitcnt vmcnt(%0) lgkmcnt(0)\n\ts_barrier" :: "i"(N) : "memory"); }
template <int N> DI void waitv_barx() { asm volatile("s_waitcnt vmcnt(%0)\n\ts_barrier" :: "i"(N) : "memory"); }
template <int N> DI void waitv_bar2() { asm volatile("s_waitcnt vmcnt(%0) lgkmcnt(2)\n\ts_barrier" :: "i"(N) : "memory"); }
DI void touch(unsigned voff, const void* sbase, unsigned junk) {
    asm volatile("s_mov_b32 m0, %2\n\ts_nop 0\n\tglobal_load_lds_dword %0, %1 sc1" :: "v"(voff), "s"(sbase), "s"(junk) : "memory");
}
#define BAR() asm volatile("s_waitcnt lgkmcnt(0)\n\ts_barrier" ::: "memory")
#define FENCE() asm volatile("" ::: "memory")
typedef __amdgpu_buffer_rsrc_t rsrc_t;
DI u32x4 ld16(rsrc_t rs, unsigned voff, unsigned soff) { return U4(__builtin_amdgcn_raw_buffer_load_b128(rs, voff, soff, 0)); }
DI u32x4 ld16_dev(rsrc_t rs, unsigned voff, unsigned soff) { return U4(__builtin_amdgcn_raw_buffer_load_b128(rs, voff, soff, 16)); }
DI u32x2 ld8(rsrc_t rs, unsigned voff, unsigned soff) { return __builtin_bit_cast(u32x2, __builtin_amdgcn_raw_buffer_load_b64(rs, voff, soff, 0)); }
DI u32x2 ld8_dev(rsrc_t rs, unsigned voff, unsigned soff) { return __builtin_bit_cast(u32x2, __builtin_amdgcn_raw_buffer_load_b64(rs, voff, soff, 16)); }
DI void st16_dev(u32x4 v, rsrc_t rs, unsigned voff, unsigned soff) { __builtin_amdgcn_raw_buffer_store_b128(v, rs, voff + soff, 0, 16); }
DI void st8_dev(u32x2 v, rsrc_t rs, unsigned voff, unsigned soff) { __builtin_amdgcn_raw_buffer_store_b64(v, rs, voff, soff, 16); }
#define POLL_SLEEP 1
DI void set_flag(char* ws, unsigned fo, u64 tagv) {
    asm volatile("s_mov_b64 exec, 1\n\tglobal_store_dwordx2 %0, %1, %2 sc1\n\ts_mov_b64 exec, -1" :: "v"(fo), "v"(tagv), "s"(ws) : "memory");
}
DI u64 peek_flag(char* ws, unsigned fo) { return __hip_atomic_load((GAS u64*)(ws + fo), __ATOMIC_RELAXED, __HIP_MEMORY_SCOPE_AGENT); }
DI void peek_lds(char* ws, unsigned fo, unsigned dst) {
    asm volatile("s_mov_b32 m0, %2\n\ts_nop 0\n\tglobal_load_lds_dword %0, %1 sc1\n\ts_add_u32 m0, %2, 252\n\ts_nop 0\n\tglobal_load_lds_dword %0, %1 offset:4 sc1" :: "v"(fo), "s"(ws), "s"(dst) : "memory", "scc");
}
DI u64 peeked(lds_ptr p) { return (u64)*(LAS unsigned*)(p + 256) << 32 | *(LAS unsigned*)p; }
DI void poll_flag(char* ws, unsigned fo, u64 tag, u64 lanes = 0xffff) {
#pragma unroll 1
    for (int spin = 0; spin < (1 << 16); ++spin) {
        if (wave_all(peek_flag(ws, fo) == tag)) break;
        __builtin_amdgcn_s_sleep(POLL_SLEEP);
    }
    FENCE();
}
#define SB() __builtin_amdgcn_sched_barrier(0)
#define PINS(a) asm volatile("" : "+s"(a))
#define PIN(a) asm volatile("" : "+v"(a))
#define PIN2(a, b) asm volatile("" : "+v"(a), "+v"(b))
DI lds_ptr opq(lds_ptr p) { unsigned u = (unsigned)(uintptr_t)p; asm volatile("" : "+v"(u)); return (lds_ptr)(uintptr_t)u; }
#define TS(i) do {} while (0)

DI constexpr int wd_slot(int s) { return (s * 17 + 27) / 28 != ((s + 1) * 17 + 27) / 28 ? (s * 17 + 27) / 28 : -1; }
#define EROT 0
#define CLR_SLEEP 90
#define ROTMODE 0
#define EARLYG 0
#define GWG 1
#define GW 8
#define CLRMODE 0
#define DLY1 0
#define DLY2 0
#define DLY3 0
#define ENL 4
#define HOP3_POLLER 1
DI constexpr int d_prio(int n) { return n < 0 ? -1 : n < 27 ? 3 - n / 7 : n < 64 ? 3 - (n - 27) / 10 : n < 84 ? 3 - (n - 64) / 5 : n < 112 ? 3 - (n - 84) / 7 : 0; }
DI constexpr int phys(int t) { return ((t % 9) + 2) % 9 * 16384; }

__global__ NOPK __launch_bounds__(512, 2) void block_fwd(const char* x, const char* wqkv, const char* wo, const char* wg, const char* wu, const char* wd, char* out, char* ws) {
    extern __shared__ __attribute__((aligned(16))) char smem[];
    lds_ptr lds = (lds_ptr)smem;
    const int tid = __builtin_amdgcn_workitem_id_x(), lane = tid & 63, w = __builtin_amdgcn_readfirstlane(tid >> 6);
    const int bid = __builtin_amdgcn_workgroup_id_x(), xcd = bid & 7, loc = bid >> 3;
    const int r = lane & 31, hf = lane >> 5, q4 = (lane & 15) >> 2, g1 = (lane >> 4) & 1, p4 = lane & 3;
    const unsigned lds0 = (unsigned)(uintptr_t)smem;
    const unsigned l16 = lane * 16;
    TS(0);
    const int sp3 = (loc + 8) >> 5;
    const int atb = (xcd >> 1) * 8 + (loc & 7) + sp3 * (xcd & 1) * 4, acg = (loc >> 3) + (1 - sp3) * (xcd & 1) * 4;
    const int row8 = lane >> 3;
    const int rot8 = ((loc & 7) << sp3) + sp3 * (xcd & 1), rot = rot8 == 7 ? 0 : rot8;
    if (loc < 28) {
        const unsigned xo = (unsigned)((atb * 64 + (tid >> 3)) * 896 + (((tid ^ (tid >> 4)) & 7) * 16));
        const unsigned xd0 = lds0 + 1024 + (w >> 2) * 28672 + (w & 3) * 1024;
        unsigned xd = xd0 + (7 - rot) * 4096;
        const unsigned w0 = (unsigned)((rot * 64 + (tid >> 3)) * 2688 + acg * 128 + (((tid & 7) ^ ((tid >> 2) & 4)) * 16));
        asm volatile("s_cmp_eq_u32 %0, %3\n\ts_cselect_b32 %0, %2, %0\n\ts_mov_b32 m0, %0\n\ts_add_u32 %0, %0, 0x1000\n\tglobal_load_lds_dwordx4 %1, %4\n\t"
#define XP(o) "s_cmp_eq_u32 %0, %3\n\ts_cselect_b32 %0, %2, %0\n\ts_sub_u32 m0, %0, " #o "\n\ts_add_u32 %0, %0, 0x1000\n\tglobal_load_lds_dwordx4 %1, %4 offset:" #o "\n\t"
                     XP(128) XP(256) XP(384) XP(512) XP(640) XP(768)
                     "s_add_u32 m0, %6, 8192-896\n\ts_barrier\n\tglobal_load_lds_dwordx4 %5, %7 offset:896\n\t"
                     "s_add_u32 m0, %6, 16384-1792\n\ts_nop 0\n\tglobal_load_lds_dwordx4 %5, %7 offset:1792\n\t"
                     "s_mov_b32 m0, %6\n\ts_nop 0\n\tglobal_load_lds_dwordx4 %5, %7"
                     : "+s"(xd) : "v"(xo), "s"(xd0), "s"(xd0 + 7 * 4096), "s"(x), "v"(w0), "s"(lds0 + 58368 + w * 1024), "s"(wqkv) : "memory", "scc");
    }
    SB();
    const rsrc_t rws = __builtin_amdgcn_make_buffer_rsrc((void*)ws, 0, WS_END, 0x00020000);
    u64 epoch, tagv;
    unsigned fl1;
    const unsigned ones = 0x3f803f80u;
    const bool ffn = loc < 28;
    const int l7 = loc / 7, m7 = loc % 7;
    const int tb = xcd * 4 + l7;
    const int tt = w & 1, ct = (w >> 1) & 1, kh = w >> 2;
    const unsigned swz = (unsigned)(((lane & 7) ^ (((row8 >> 1) & 1) << 2)) * 16);

    if (__builtin_expect(ffn, 1)) {
        constexpr int A_W = 58368, A_WS = 24576;
#define A_KC(c) (rot + (c) >= 7 ? rot + (c) - 7 : rot + (c))
        const unsigned wvo = (unsigned)(row8 * 2688) + swz;
        const char* wsrc = wqkv + acg * 128 + (size_t)w * 8 * 2688;
        const unsigned wdst = lds0 + A_W + w * 1024;
#define A_PIECE(c, i) glds(wvo, wsrc + A_KC(c) * (64 * 2688) + (i) * 896, wdst + ((c) & 3) * A_WS + (i) * 8192)
#define A_GROUP(c) do { A_PIECE(c, 1); A_PIECE(c, 2); A_PIECE(c, 0); } while (0)
        epoch = peek_flag(ws, FL_EPOCH * 128); FENCE(); fl1 = (FL_KV + atb * 8 + acg) * 128;
        if (EARLYG) A_GROUP(1);
        lds_ptr xb[4], wb1, wb1h;
#pragma unroll
        for (int j = 0; j < 4; ++j) xb[j] = lds + 1024 + tt * 28672 + (r >> 3) * 1024 + (r & 7) * 128 + (((2 * j + hf) ^ ((r >> 1) & 7)) * 16);
        wb1 = opq(lds + A_W + (kh ? 0 : 8192) + (8 * hf + q4) * 128 + ((ct ^ (q4 >> 1)) * 64) + g1 * 32 + p4 * 8);
        wb1h = opq(wb1 + 2 * A_WS);
        unsigned go1[2], go2[2]; int gi[2];
#pragma unroll
        for (int p = 0; p < 2; ++p) {
            const int G = acg * 8 + ct * 4 + 2 * p + hf, hp = G / 7, i = G - hp * 7;
            const unsigned hs = kh ? 131072u : 262144u;
            const unsigned to = (kh ? WS_QF + (atb * 2 + tt) * 2048 : WS_KV + (atb * 2 + tt) * 4096) + r * 16;
            go1[p] = (unsigned)(2 * hp + (i > 3)) * hs + (unsigned)(i > 3 ? i - 3 : i) * 512 + to;
            go2[p] = i == 3 ? (unsigned)(2 * hp + 1) * hs + to : OOB;
            gi[p] = i;
        }
        const int vcol = acg * 64 + ct * 32 + r, vh = vcol / 28, vd = vcol - vh * 28;
        unsigned vo = WS_KV + (atb * 2 + tt) * 4096 + (unsigned)vh * 262144 + 2048 + hf * 512 + vd * 16;
        PIN2(go1[0], go1[1]); PIN2(go2[0], go2[1]); PIN(vo);
        lds_ptr xw = opq(lds + 1024 + kh * 28672 + (w & 3) * 1024 + l16);
        waitv<EARLYG ? 7 : 4>();
        TS(28);
        u32x4 xv[7];
#pragma unroll
        for (int c = 0; c < 7; ++c) xv[c] = *(LAS u32x4*)(xw + c * 4096);
        SB();
        f32x4 sm = {};
#pragma unroll
        for (int c = 0; c < 7; ++c)
#pragma unroll
            for (int j = 0; j < 4; ++j) sm[j] = dot2(xv[c][j], ones, sm[j]);
        float nm = sum8((sm[0] + sm[1]) + (sm[2] + sm[3])) * (-1.f / D);
        PIN(nm);
#pragma unroll
        for (int c = 0; c < 7; ++c) { xv[c] = shift8(xv[c], nm); *(LAS u32x4*)(xw + c * 4096) = xv[c]; }
        if (EARLYG) A_GROUP(2); else A_GROUP(1);
        SB();
        sm = (f32x4){};
#pragma unroll
        for (int c = 0; c < 7; ++c)
#pragma unroll
            for (int j = 0; j < 4; ++j) sm[j] = dot2(xv[c][j], xv[c][j], sm[j]);
        LAS float* rst = (LAS float*)lds;
        rst[w * 8 + row8] = __builtin_amdgcn_rsqf(sum8((sm[0] + sm[1]) + (sm[2] + sm[3])) * (1.f / D) + EPS);
        if (EARLYG) A_GROUP(3); else A_GROUP(2);
        TS(31);
        bf16x8 fx[4], f1[4], f2[4];
#define A_WAIT(c) do { if ((c) == 0) waitv_bar<EARLYG ? 9 : 6>(); else if ((c) == 1) waitv_barx<6>(); else if ((c) < 6) waitv_barx<3>(); else waitv_barx<0>(); \
                       if ((c) == 0) TS(1); if ((c) == 6) TS(2); } while (0)
#define A_WP(c, ks) ((((c) & 2) ? wb1h : wb1) + ((c) & 1) * A_WS + (ks) * 2048)
        if (w < 4) {
            f32x16 ak = {}, av = {};
            f32x4 rv[4];
            float rs = 0.f;
#pragma unroll
            for (int n = 0; n < 30; ++n) {
                const int c = n >> 2, ks = n & 3, km = (n - 2) & 3;
                if (n < 28 && ks == 0) A_WAIT(c);
                if (n >= 2) { ak = MFMA(f1[km], fx[km], ak); PIN(ak); } SB();
                if (n < 28) { fx[ks] = lds8(xb[ks] + c * 4096); f1[ks] = cat(trd(A_WP(c, ks)), trd(A_WP(c, ks) + 512)); } SB();
                if (n >= 2) { av = MFMA(fx[km], f2[km], av); PIN(av); } SB();
                if (n < 28) f2[ks] = cat(trd(A_WP(c, ks) + 8192), trd(A_WP(c, ks) + 8192 + 512));
                if (!EARLYG && c == 0 && ks < 3) A_PIECE(3, ks); if (c >= 2 && c < 5 && ks < 3) A_PIECE(c + 2, ks);
                if (n == 21) {
                    rs = rst[tt * 32 + r];
#pragma unroll
                    for (int g = 0; g < 4; ++g) rv[g] = *(LAS f32x4*)(rst + tt * 32 + 8 * g + 4 * hf);
                }
                if (n == 24) { PIN(epoch); tagv = epoch + 1; PIN(tagv); }
                if (n == 24) {
                    const unsigned pv = vd == 0 ? ones : 0u, po = vd < 4 ? vo + 448 : OOB;
                    st16_dev((u32x4){pv, pv, pv, pv}, rws, po, 0); st16_dev((u32x4){pv, pv, pv, pv}, rws, po + 1024, 0);
                }
                SB();
            }
            TS(3);
#pragma unroll
            for (int i = 0; i < 16; ++i) { ak[i] *= rs; av[i] *= rv[i >> 2][i & 3]; }
#pragma unroll
            for (int p = 0; p < 2; ++p) {
                const u32x4 f = pack8(ak, p);
                const auto s0 = __builtin_amdgcn_permlane32_swap(f[0], f[2], false, false), s1 = __builtin_amdgcn_permlane32_swap(f[1], f[3], false, false);
                const u32x4 c = {s0[0], s1[0], s0[1], s1[1]};
                st16_dev(c, rws, go1[p], 0); st16_dev(c, rws, go2[p], 0);
            }
            st16_dev(pack8(av, 0), rws, vo, 0); st16_dev(pack8(av, 1), rws, vo + 1024, 0);
        } else {
            f32x16 aq = {};
            __builtin_amdgcn_s_setprio(1);
#pragma unroll
            for (int n = 0; n < 30; ++n) {
                const int c = n >> 2, ks = n & 3, km = (n - 2) & 3;
                if (n < 28 && ks == 0) A_WAIT(c);
                if (n == 24) { PIN(epoch); tagv = epoch + 1; PIN(tagv); }
                if (n >= 2) { aq = MFMA(f1[km], fx[km], aq); PIN(aq); } SB();
                if (n < 28) { fx[ks] = lds8(xb[ks] + c * 4096); f1[ks] = cat(trd(A_WP(c, ks)), trd(A_WP(c, ks) + 512)); }
                if (!EARLYG && c == 0 && ks < 3) A_PIECE(3, ks); if (c >= 2 && c < 5 && ks < 3) A_PIECE(c + 2, ks);
                SB();
            }
            __builtin_amdgcn_s_setprio(0);
            const float QS = rst[tt * 32 + r] * (0.18898223650461362f * 1.4426950408889634f);
#pragma unroll
            for (int i = 0; i < 16; ++i) aq[i] *= QS;
#pragma unroll
            for (int p = 0; p < 2; ++p) {
                const u32x4 f = pack8(aq, p);
                const auto s0 = __builtin_amdgcn_permlane32_swap(f[0], f[2], false, false), s1 = __builtin_amdgcn_permlane32_swap(f[1], f[3], false, false);
                const bool sp = gi[p] == 3;
                st16_dev((u32x4){s0[0], s1[0], sp ? 0u : s0[1], sp ? 0u : s1[1]}, rws, go1[p], 0);
                st16_dev((u32x4){0u, 0u, s0[1], s1[1]}, rws, go2[p], 0);
            }
        }
    } else {
        epoch = peek_flag(ws, FL_EPOCH * 128); fl1 = (FL_RT + xcd * 4 + loc - 28) * 128;
        const int ri = xcd * 4 + loc - 28, nu = (w == 0 && ri < 16) ? 4 : 3;
        const unsigned vo = (unsigned)(row8 * 3584 + (lane & 7) * 16);
#pragma unroll 1
        for (int i = 0; i < nu; ++i) {
            const int u = i < 3 ? i * 256 + ri * 8 + w : 768 + ri;
            const size_t so = (size_t)(u / 28) * 16 * 3584 + (u % 28) * 128;
            const unsigned d_ = lds0 + w * 16384 + i * 4096;
            glds(vo, wg + so, d_); glds(vo, wg + so + 8 * 3584, d_ + 1024); glds(vo, wu + so, d_ + 2048); glds(vo, wu + so + 8 * 3584, d_ + 3072);
        }
#pragma unroll 1
        for (int i = 0; i < nu; ++i) {
            const int u = i < 3 ? i * 256 + ri * 8 + w : 768 + ri;
            waitv<8>();
            lds_ptr sg = lds + w * 16384 + i * 4096 + g1 * 2048 + (4 * hf + q4) * 128 + p4 * 8;
#pragma unroll
            for (int e = 0; e < 4; ++e) st16_dev(U4(cat(trd(sg + e * 32), trd(sg + e * 32 + 1024))), rws, l16, WS_WGU + (unsigned)(((u % 28) * 4 + e) * 28 + u / 28) * 1024);
            FENCE();
        }
        if (CLRMODE >= 2) {
            const unsigned c_ = WS_GF + (unsigned)(((ri >> 1) * 4 + 2 + (ri & 1)) * 112 + w * 14) * 1024 + l16;
#pragma unroll 1
            for (int i = 0; i < (CLRMODE == 2 ? 7 : 14); ++i) st16_dev((u32x4){~0u, ~0u, ~0u, ~0u}, rws, c_ + i * 1024, 0);
        }
        tagv = epoch + 1;
    }
    TS(24);
    const int head = xcd * 2 + (loc & 1), rb = loc >> 1, qt = w & 3;
    const char* kvs = ws + WS_KV + (size_t)head * 262144 + w * 2048;
    const char* qs = ws + WS_QF + (size_t)(head * 64 + rb * 4 + qt) * 2048 + kh * 1024;
    unsigned kvd = lds0 + w * 2048, qd = lds0 + 147456 + qt * 2048 + kh * 1024;
    unsigned pf1 = (FL_KV + (lane >> 1) * 8 + (head * 28 + (lane & 1) * 27) / 64) * 128;
    PINS(kvs); PINS(qs); PINS(kvd); PINS(qd); PIN2(pf1, fl1);
    waitv_bar<0>();
    if (w == 0) set_flag(ws, fl1, tagv);
    const u64 tag = ((u64)(unsigned)__builtin_amdgcn_readfirstlane((unsigned)(tagv >> 32)) << 32 | (u64)(unsigned)__builtin_amdgcn_readfirstlane((unsigned)tagv));
    TS(4);

    const int drb = (xcd >> 1) * 4 + l7, dfg = (xcd & 1) * 7 + m7;
    bf16x8 wf[28];
    u32x2 xa, xb;
    f32x16 cs = {}, acc = {};
    const u32x4 one4 = {ones, ones, ones, ones};
    const unsigned hi_ = (unsigned)((tb * 2 + tt) * 28 + m7 * 4 + ct * 2 + kh), ofo = (unsigned)(tt * 32768 + kh * 16384 + ct * 4096);
    unsigned ho = WS_HF + hi_ * 1024 + l16, so = hf ? OOB : WS_HS + hi_ * 256 + r * 8;
    const char* os = ws + WS_OF + (size_t)tb * 65536 + ofo;
    unsigned od = lds0 + 65536 + ofo;
    unsigned pf2 = (FL_O + (tb >> 1) * 16 + kh * 8 + ct * 2 + ((lane >> 1) & 1) + (lane & 4)) * 128 + ((lane & 1) * 4 + (tb & 1) * 2 + tt) * 8;
    pf2 &= ~127u;
    unsigned go = ffn ? WS_GF + (unsigned)(drb * 4 * 112 + dfg * 8 + w) * 1024 + l16 : OOB;
    unsigned fo3 = (FL_H + tb * 8 + m7) * 128;
    {
        lds_ptr ap = opq(lds + kh * 8192 + l16), apB = opq(lds + 65536 + kh * 8192 + l16), apC = opq(lds + 131072 + kh * 8192 + l16);
        const unsigned lv = kh * 8192 + (l16 ^ (kh * 256));
        lds_ptr vp = opq(lds + lv), vpB = opq(lds + 65536 + lv), vpC = opq(lds + 131072 + lv);
#define AP(off) ((off) < 65536 ? ap + (off) : (off) < 131072 ? apB + ((off) - 65536) : apC + ((off) - 131072))
#define VP(off) ((off) < 65536 ? vp + (off) : (off) < 131072 ? vpB + ((off) - 65536) : vpC + ((off) - 131072))
#define KV_ISSUE(t) glds2_dev(l16, kvs + (t) * 16384, kvd + phys(t))
        unsigned wgo = ffn ? WS_WGU + (unsigned)(dfg * 8 + w) * 28672 : OOB;
        unsigned clro = WS_GF + (unsigned)((((xcd * 4 + loc - 28) >> 1) * 4 + 2 + (loc & 1)) * 112 + w * 14) * 1024 + l16;
        const int ri = umin(xcd * 4 + loc - 28, 27);
        const char* wos = (ffn ? wo + m7 * 128 : wd + (ri % 7) * 128 + (size_t)(ri / 7) * 448 * 896) + (size_t)w * 56 * 896;
        unsigned wov = (unsigned)(row8 * 896) + swz;
        const rsrc_t rx = __builtin_amdgcn_make_buffer_rsrc((void*)x, 0, 2048 * 448 * 2, 0x00020000);
        unsigned xo_ = (unsigned)(((tb * 2 + tt) * 32 + r) * 896 + m7 * 128 + ct * 64 + kh * 32 + hf * 8);
        unsigned rto = (FL_RT + r) * 128;
        lds_ptr qp = opq(lds + 147456 + qt * 2048 + l16);
        PINS(wgo); PINS(wos); PIN2(wov, xo_); PIN(rto); PIN2(ho, so); PIN2(pf2, fo3); PIN(go);
        lds_ptr xw = opq(lds + 147456 + w * 2048 + l16), xr = opq(lds + 147456 + (w ^ 4) * 2048 + l16);
        lds_ptr lw = opq(lds + 58368 + w * 256 + lane * 4), lr = opq(lds + 58368 + qt * 256 + 128 + r * 4);
        lds_ptr bw = opq(lds + 60416 + w * 4), br = opq(lds + 60416 + (lane & 7) * 4);
        unsigned oo = WS_OF + (unsigned)((rb * 4 + qt) * 32 + head * 2 + kh) * 1024 + l16;
        unsigned fo2 = (FL_O + rb * 16 + head) * 128 + w * 8;
        PIN2(oo, fo2);
        if (w == HOP3_POLLER) { if (DLY1) __builtin_amdgcn_s_sleep(DLY1); poll_flag(ws, pf1, tag, ~0ull); }
        BAR();
        TS(5);
        glds_dev(l16, qs, qd);
        KV_ISSUE(0); KV_ISSUE(1);
        asm volatile("s_barrier" ::: "memory");
        KV_ISSUE(2); KV_ISSUE(3);
        waitv_bar<4>();
        TS(6);
        const bf16x8 q0 = lds8(qp), q1 = lds8(qp + 1024);
        f32x16 o = {}, sa = {}, sb;
        bf16x8 k0 = lds8(ap + phys(0)), k1 = lds8(ap + phys(0) + 1024), v0 = lds8(vp + phys(0) + 2048), v1 = {};
        u32x4 p0, p1 = {};
        sa = MFMA(k0, q0, sa); sa = MFMA(k1, q1, sa);
        k0 = lds8(ap + phys(0) + 4096); k1 = lds8(ap + phys(0) + 5120);
        PIN(sa); SB();
        u64 rtf = 0;
#define EXP4(S, o4) _Pragma("unroll") for (int i_ = o4; i_ < o4 + 4; ++i_) S[i_] = __builtin_amdgcn_exp2f(S[i_]);
#define WLD(s) wf[s] = BF8(ld16_dev(rws, l16 + ((s) & 3) * 1024, wgo + ((s) >> 2) * 4096))
#define SIDE(t, g) do { \
            if ((t) == 0 && (g) == 0) KV_ISSUE(4); if ((t) == 0 && (g) == 2) KV_ISSUE(5); if ((t) == 1 && (g) == 0) KV_ISSUE(6); if ((t) == 1 && (g) == 2) KV_ISSUE(7); \
            if ((t) == 2 && (g) == 0) KV_ISSUE(8); if ((t) == 1 && (g) == 3) rtf = peek_flag(ws, rto); \
            if ((t) == 3 && (g) < 3) KV_ISSUE(9 + (g)); \
            if (CLRMODE == 0 && (t) >= 4 && (t) <= 10 && ((g) & 1)) { if (__builtin_expect(!ffn, 0)) st16_dev((u32x4){~0u, ~0u, ~0u, ~0u}, rws, clro + (((t) - 4) * 2 + ((g) >> 1)) * 1024, 0); } \
            if (CLRMODE == 1 && (t) >= 11 && ((t) - 11) * 4 + (g) < 14) { if (__builtin_expect(!ffn, 0)) st16_dev((u32x4){~0u, ~0u, ~0u, ~0u}, rws, clro + (((t) - 11) * 4 + (g)) * 1024, 0); } \
            if (CLRMODE == 2 && (t) >= 11 && !((g) & 1) && ((t) - 11) * 2 + (g) / 2 < 7) { if (__builtin_expect(!ffn, 0)) st16_dev((u32x4){~0u, ~0u, ~0u, ~0u}, rws, clro + (7 + ((t) - 11) * 2 + (g) / 2) * 1024, 0); } \
            if ((t) == 7 && (g) == 0) KV_ISSUE(12); if ((t) == 7 && (g) == 2) KV_ISSUE(13); if ((t) == 8 && (g) == 0) KV_ISSUE(14); if ((t) == 8 && (g) == 2) KV_ISSUE(15); \
            if ((t) >= 4 && (t) <= 6) WLD(((t) - 4) * 4 + (g)); \
            if (((t) == 7 || (t) == 8) && ((g) & 1)) WLD(12 + ((t) - 7) * 2 + ((g) >> 1)); \
            if ((t) >= 9 && (t) <= 11) WLD(((t) - 5) * 4 + (g)); \
            if ((t) == 12 || ((t) == 13 && (g) < 3)) glds(wov, wos + (((t) - 12) * 4 + (g)) * 8 * 896, lds0 + (w * 7 + ((t) - 12) * 4 + (g)) * 1024); \
            if ((t) == 13 && (g) == 3) { xa = ld8(rx, xo_, 0); xb = ld8(rx, xo_, 16); } \
            if ((t) == 14 && (g) == 1) { if (w >= 6) touch(lane * (w == 6 ? (WS_END / 64 & ~127u) : 28672), w == 6 ? ws : out, lds0 + 61440); } \
            if ((t) == 14 && (g) == 0) { if (tid < 32) *(LAS u32x4*)(lds + 57344 + tid * 16) = (u32x4){0, 0, 0, 0}; } \
        } while (0)
#define TOFF(i) (phys((i) >> 1) + ((i) & 1) * 4096)
#define TILE(i, SC, SN) do { \
            __builtin_amdgcn_s_setprio(((i) & 1) ? 1 : 3); \
            if ((i) < 31) { f32x16 z_ = {}; SN = MFMA(k0, q0, z_); PIN(SN); } if ((i) < 30) k0 = lds8(AP(TOFF((i) + 2))); SB(); \
            EXP4(SC, 0); PIN(SC); SB(); \
            o = MFMA(v1, BF8(p1), o); PIN(o); v1 = lds8(VP(TOFF(i) + 3072)); SIDE((i) >> 1, ((i) & 1) * 2); SB(); \
            EXP4(SC, 4); p0 = pack8(SC, 0); PIN(p0); SB(); \
            __builtin_amdgcn_s_setprio(((i) & 1) ? 0 : 2); \
            if ((i) < 31) { SN = MFMA(k1, q1, SN); PIN(SN); } if ((i) < 30) k1 = lds8(AP(TOFF((i) + 2) + 1024)); SB(); \
            EXP4(SC, 8); PIN(SC); SB(); \
            o = MFMA(v0, BF8(p0), o); PIN(o); if ((i) < 31) v0 = lds8(VP(TOFF((i) + 1) + 2048)); SIDE((i) >> 1, ((i) & 1) * 2 + 1); SB(); \
            EXP4(SC, 12); p1 = pack8(SC, 1); PIN(p1); SB(); } while (0)
#define STEP(t) do { TILE(2 * (t), sa, sb); TILE(2 * (t) + 1, sb, sa); } while (0)
        STEP(0);
        waitv_barx<4>();
        STEP(1); STEP(2);
        waitv_bar2<3>();
        TS(21);
        if (__builtin_expect(!wave_all(rtf == tag), 0)) poll_flag(ws, rto, tag, 0xffffffffull);
        FENCE();
        STEP(3); STEP(4); STEP(5); STEP(6);
        waitv_bar2<12>();
        TS(22);
        STEP(7); STEP(8); STEP(9); STEP(10);
        waitv_bar2<9>();
        TS(23);
        STEP(11); STEP(12); STEP(13); STEP(14); STEP(15);
        o = MFMA(v1, BF8(p1), o);
        TS(7);
        u32x4 ov;
        {
            *(LAS f32x4*)xw = (f32x4){o[8], o[9], o[10], o[11]}; *(LAS f32x4*)(xw + 1024) = (f32x4){o[12], o[13], o[14], o[15]};
            const float l = kh ? o[4] : o[12];
            *(LAS float*)lw = l;
            float sa_ = 0.f;
#pragma unroll
            for (int i = 0; i < 16; ++i) sa_ += __builtin_fabsf(o[i]);
            const bool ok = sa_ < 0x1p100f && (hf == 0 || l > 0x1p-80f);
            *(LAS unsigned*)bw = wave_all(ok) ? 0u : 1u;
            BAR();
            const f32x4 a = *(LAS f32x4*)xr, c = *(LAS f32x4*)(xr + 1024);
            const float inv = __builtin_amdgcn_rcpf(*(LAS float*)lr + *(LAS float*)(lr + 1024));
            const unsigned bad = *(LAS unsigned*)br;
            const float iz = (kh && hf) ? 0.f : inv;
            ov = (u32x4){cvtpk((o[0] + a[0]) * inv, (o[1] + a[1]) * inv), cvtpk((o[2] + a[2]) * inv, (o[3] + a[3]) * inv),
                         cvtpk((o[4] + c[0]) * iz, (o[5] + c[1]) * iz), cvtpk((o[6] + c[2]) * iz, (o[7] + c[3]) * iz)};
            if (__builtin_expect(wave_any(bad != 0), 0)) {
                f32x16 of = {};
                float m = -0x1p126f;
#pragma unroll 1
                for (int t = 0; t < 16; ++t) {
                    BAR();
                    glds2_dev(l16, kvs + t * 16384, lds0 + 65536 + w * 2048);
                    waitv_bar<0>();
#pragma unroll 1
                    for (int u = 0; u < 2; ++u) {
                        f32x16 s = {};
                        s = MFMA(lds8(apB + u * 4096), q0, s); s = MFMA(lds8(apB + u * 4096 + 1024), q1, s);
                        float mx = s[0];
#pragma unroll
                        for (int i = 1; i < 16; ++i) mx = __builtin_fmaxf(mx, s[i]);
                        mx = __builtin_fmaxf(swap_max(mx), m);
                        const float alpha = __builtin_amdgcn_exp2f(m - mx);
                        m = mx;
#pragma unroll
                        for (int i = 0; i < 16; ++i) { s[i] = __builtin_amdgcn_exp2f(s[i] - mx); of[i] *= alpha; }
                        of = MFMA(lds8(vpB + u * 4096 + 2048), BF8(pack8(s, 0)), of);
                        of = MFMA(lds8(vpB + u * 4096 + 3072), BF8(pack8(s, 1)), of);
                    }
                }
                *(LAS f32x4*)xw = (f32x4){of[8], of[9], of[10], of[11]}; *(LAS f32x4*)(xw + 1024) = (f32x4){of[12], of[13], of[14], of[15]};
                *(LAS float*)lw = hf ? (kh ? of[4] : of[12]) : m;
                BAR();
                const f32x4 a2 = *(LAS f32x4*)xr, c2 = *(LAS f32x4*)(xr + 1024);
                const float m0 = *(LAS float*)(lr - 128), m1 = *(LAS float*)(lr + 1024 - 128), mm = __builtin_fmaxf(m0, m1);
                const float e0 = __builtin_amdgcn_exp2f(m0 - mm), e1 = __builtin_amdgcn_exp2f(m1 - mm);
                const float iv = __builtin_amdgcn_rcpf(*(LAS float*)lr * e0 + *(LAS float*)(lr + 1024) * e1);
                const float im = (kh ? e1 : e0) * iv, io = (kh ? e0 : e1) * iv;
                const bool z = kh && hf;
                ov = (u32x4){cvtpk(of[0] * im + a2[0] * io, of[1] * im + a2[1] * io), cvtpk(of[2] * im + a2[2] * io, of[3] * im + a2[3] * io),
                             z ? 0u : cvtpk(of[4] * im + c2[0] * io, of[5] * im + c2[1] * io), z ? 0u : cvtpk(of[6] * im + c2[2] * io, of[7] * im + c2[3] * io)};
            }
        }
        st16_dev(ov, rws, oo, 0);
        TS(25);
        FENCE();
        PIN2(xa, xb);
#pragma unroll
        for (int s = 27; s >= 0; --s) PIN(wf[s]);
#pragma unroll
        for (int s = 0; s < 8; ++s) cs = MFMA(wf[s], BF8(one4), cs);
        acc[0] = bflo(xa[0]); acc[1] = bfhi(xa[0]); acc[2] = bflo(xa[1]); acc[3] = bfhi(xa[1]); acc[4] = bflo(xb[0]); acc[5] = bfhi(xb[0]); acc[6] = bflo(xb[1]); acc[7] = bfhi(xb[1]);
        PIN(acc); PIN2(ho, so); PIN2(pf2, fo3); PINS(os); PINS(od); PIN(cs);
        waitv_bar<0>();
        if (w == 0) set_flag(ws, fo2, tagv);
        TS(8);
    }
    if (__builtin_expect(!ffn, 0)) {
        const int ri = xcd * 4 + loc - 28;
        if (ri < 28) {
#pragma unroll 1
            for (int u = w; u < 28; u += 8) {
                lds_ptr sd = lds + u * 2048 + (4 * hf + q4) * 128 + g1 * 32 + p4 * 8;
#pragma unroll
                for (int e = 0; e < 2; ++e) { lds_ptr p_ = sd + ((e ^ (q4 >> 1)) * 64);
                    st16_dev(U4(cat(trd(p_), trd(p_ + 1024))), rws, l16, WS_WD + (unsigned)(((ri % 7) * 112 + (ri / 7) * 28 + u) * 2 + e) * 1024); }
            }
        }
        waitv_bar<0>();
        if (w == 0) set_flag(ws, (FL_RD + ri) * 128, tagv);
        return;
    }

    const int erb = tb >> 1;
    const char* hs = ws + WS_HF + (size_t)drb * 114688 + w * 1024;
    const char* hss = ws + WS_HS + (size_t)((drb * 4 + (w & 3)) * 28 + kh * 14) * 256;
    unsigned pf3, fo4 = (FL_G + drb * 64 + dfg) * 128 + w * 8;
    {
        lds_ptr wb = lds + kh * (8 * 28 * 128) + (4 * hf + q4) * 128 + ((ct ^ (q4 >> 1)) * 64) + (g1 ^ kh) * 32 + p4 * 8;
        bf16x8 wr[16];
#pragma unroll
        for (int s = 0; s < 12; ++s) { const int k0 = ((s >> 1) * 28 + (s & 1) * 16) * 128; wr[s] = cat(trd(wb + k0), trd(wb + k0 + 1024)); }
        lds_ptr cw = opq(lds + 131072 + w * 2048 + l16), cr = opq(lds + 131072 + (w ^ 4) * 2048 + l16);
        if (DLY2) __builtin_amdgcn_s_sleep(DLY2);
        poll_flag(ws, pf2, tag, 0x55);
        TS(9);
        glds4_dev(l16, os, od);
        asm volatile("s_barrier" ::: "memory");
        glds4_dev(l16, os + 8192, od + 8192);
        lds_ptr ob = opq(lds + 65536 + tt * 32768 + kh * 16384 + l16);
        bf16x8 of[8];
        waitv_bar<4>();
        TS(26);
#pragma unroll
        for (int s = 0; s < 8; ++s) of[s] = lds8(ob + s * 1024);
        SB();
#pragma unroll
        for (int s = 0; s < 8; ++s) acc = MFMA(wr[s], of[s], acc);
        PIN(acc); SB();
#pragma unroll
        for (int s = 12; s < 16; ++s) { const int k0 = ((s >> 1) * 28 + (s & 1) * 16) * 128; wr[s] = cat(trd(wb + k0), trd(wb + k0 + 1024)); }
        waitv_bar<0>();
#pragma unroll
        for (int s = 0; s < 8; ++s) of[s] = lds8(ob + (8 + s) * 1024);
        SB();
#pragma unroll
        for (int s = 0; s < 8; ++s) acc = MFMA(wr[8 + s], of[s], acc);
        TS(10);
        *(LAS f32x4*)cw = (f32x4){acc[8], acc[9], acc[10], acc[11]}; *(LAS f32x4*)(cw + 1024) = (f32x4){acc[12], acc[13], acc[14], acc[15]};
        BAR();
        const f32x4 a = *(LAS f32x4*)cr + (f32x4){acc[0], acc[1], acc[2], acc[3]}, b = *(LAS f32x4*)(cr + 1024) + (f32x4){acc[4], acc[5], acc[6], acc[7]};
        const u32x4 hv = {cvtpk(a[0], a[1]), cvtpk(a[2], a[3]), cvtpk(b[0], b[1]), cvtpk(b[2], b[3])};
        st16_dev(hv, rws, ho, 0);
        float s1 = 0.f, s2 = 0.f;
        acc_stats(hv, s1, s2);
        s1 = swap_add(s1); s2 = swap_add(s2);
        st8_dev((u32x2){f2u(s1), f2u(s2)}, rws, so, 0);
        FENCE();
#pragma unroll
        for (int s = 8; s < 14; ++s) cs = MFMA(wf[s], BF8(one4), cs);
        pf3 = (FL_H + drb * 16 + (lane & 8) + umin(lane & 7, 6)) * 128;
        PIN(go); PIN2(pf3, fo4); PINS(hs); PINS(hss); PIN(cs);
        waitv_bar<0>();
        if (w == 0) set_flag(ws, fo3, tagv);
        if (bid == 0 && w == 0) set_flag(ws, FL_EPOCH * 128, tagv);
        TS(11);
    }

    const unsigned wdo = WS_WD + (unsigned)(m7 * 112 + w * 14) * 2048;
    bf16x8 wdf[28];
    bf16x8 gf[GW];
    const unsigned erot = EROT * m7;
#define E_KS(s) (erot + (s) >= 14 ? erot + (s) - 14 : erot + (s))
    const int ta = erb * 4 + (tb & 1);
    const unsigned gxo = WS_GF + (unsigned)(ta * 112 + w * 14) * 1024;
    u64 gfl = 0;
    const unsigned gks = w * 14 + umin(lane & 15, 13), gfi = (FL_G + (erb * 4 + (tb & 1)) * 16 + (gks >> 3)) * 128 + (GWG ? 0 : (gks & 7) * 8);
    {
#pragma unroll
        for (int s = 14; s < 28; ++s) cs = MFMA(wf[s], BF8(one4), cs);
        lds_ptr csp = opq(lds + 161792 + w * 128 + hf * 64);
        if (r == 0) {
#pragma unroll
            for (int g = 0; g < 4; ++g) *(LAS f32x4*)(csp + g * 16) = (f32x4){cs[4 * g], cs[4 * g + 1], cs[4 * g + 2], cs[4 * g + 3]};
        }
        const unsigned hd = lds0 + w * 1024;
        const char* wds = ws + wdo;
        lds_ptr hb = opq(lds + l16), hb2 = opq(lds + 57344 + l16);
        lds_ptr wl = opq(lds + 114688 + w * 5120 + l16), wl2 = opq(lds + w * 7168 + l16);
        LAS f32x2* stp = (LAS f32x2*)opq(lds + 159744 + r * 8);
        if (w == HOP3_POLLER) { if (DLY3) __builtin_amdgcn_s_sleep(DLY3); poll_flag(ws, pf3, tag, 0x7f7f); }
        BAR();
        TS(12);
#define H_PIECE(j) glds_dev(l16, hs + (j) * 8192, hd + (j) * 8192)
#define WDO(s) (E_KS((s) >> 1) * 2048 + ((s) & 1) * 1024)
#define WDLD(s) wdf[s] = BF8(ld16_dev(rws, l16, wdo + WDO(s)))
#define HB(n) ((n) < 56 ? hb + (n) * 1024 : hb2 + ((n) - 56) * 1024)
        H_PIECE(0);
        asm volatile("s_barrier" ::: "memory");
        H_PIECE(1); H_PIECE(2); H_PIECE(3);
        constexpr int PF = 5;
        bf16x8 bf[PF + 1];
        f32x16 acc[2];
        f32x2 sp[7], sv0, sv1;
        f32x4 c4[4];
        float mean = 0.f, r2 = 0.f, hp = 0.f;
        u32x4 hv;
        lds_ptr pk = opq(lds + 155648 + w * 512 + lane * 4);
        u64 pv = 0;
        const unsigned pkd = lds0 + 155648 + w * 512;
        const unsigned rdo = (FL_RD + ((lane & 2) ? drb * 2 + (lane & 1) : (lane & 1) ? tb : (w >> 1) * 7 + m7)) * 128;
        waitv_barx<3>();
        TS(27);
#pragma unroll
        for (int n = 0; n < PF; ++n) bf[n] = lds8(HB(n));
        SB();
        sfor<0, 125>([&](auto N_) __attribute__((always_inline)) {
            constexpr int n = decltype(N_)::v, p = n / 28, s = n % 28;
            if constexpr (n == 3) waitv_barx<3>();
            if constexpr (n == 11) waitv_barx<7>();
            if constexpr (n == 27) { waitv_barx<3>(); TS(13); }
            if constexpr (n == 64) { waitv_barx<10>(); if (!GWG || w == 0) set_flag(ws, fo4, tagv); TS(14); }
            if constexpr (n == 85) { if (GWG) waitv_barx<7>(); else waitv<7>(); if (!GWG || w == 0) set_flag(ws, fo4 + 2048, tagv); }
            if constexpr (n == 38) { if (__builtin_expect(!wave_all(pv == tag), 0)) poll_flag(ws, rdo, tag); FENCE(); }
            if constexpr (d_prio(n) != d_prio(n - 1)) __builtin_amdgcn_s_setprio(d_prio(n));
            if constexpr (n < 112) { if constexpr (s == 0) { f32x16 z = {}; acc[p & 1] = MFMA(wf[s], bf[n % (PF + 1)], z); } else acc[p & 1] = MFMA(wf[s], bf[n % (PF + 1)], acc[p & 1]); PIN(acc[p & 1]); }
            SB();
            if constexpr (n + PF < 112) bf[(n + PF) % (PF + 1)] = lds8(HB(n + PF));
            if constexpr (n % 3 == 2 && n < 30) H_PIECE(4 + n / 3);
            if constexpr (n == 4) glds4_dev(l16, hss, lds0 + 114688 + w * 5120);
            if constexpr (n == 16) { waitv<4>();
#pragma unroll
                for (int j = 0; j < 7; ++j) sp[j] = *(LAS f32x2*)(lds + 114688 + w * 5120 + (hf * 7 + j) * 256 + r * 8);
            }
            if constexpr (n == 20) { const f32x2 t = ((sp[0] + sp[1]) + (sp[2] + sp[3])) + ((sp[4] + sp[5]) + sp[6]); stp[w * 32] = (f32x2){swap_add(t[0]), swap_add(t[1])}; }
            if constexpr (n >= 39 && n < 48 && n % 2 == 1) glds_dev(l16, wds + WDO((n - 39) / 2), lds0 + 114688 + w * 5120 + (n - 39) / 2 * 1024);
            if constexpr (n >= 48 && n < 64 && n % 3 == 0) WDLD(12 + (n - 48) / 3);
            if constexpr (n >= 70 && n < 84 && n % 2 == 0) glds_dev(l16, wds + WDO(5 + (n - 70) / 2), lds0 + w * 7168 + (n - 70) / 2 * 1024);
            if constexpr (n >= 85 && n < 105 && n % 2 == 1) WDLD(18 + (n - 85) / 2);
            if constexpr (n == 107) gfl = peek_flag(ws, gfi);
            if constexpr (n == 32) pv = peeked(pk);
            if constexpr (n == 18) peek_lds(ws, rdo, pkd);
            if constexpr (p >= 1) {
                const f32x16& a = acc[(p - 1) & 1];
                if constexpr (n == 100) { sv0 = stp[3 * 32]; sv1 = stp[7 * 32]; }
                if constexpr (s == 0 && p < 4) { sv0 = stp[(p - 1) * 32]; sv1 = stp[(p + 3) * 32];
#pragma unroll
                    for (int g = 0; g < 4; ++g) c4[g] = *(LAS f32x4*)(csp + g * 16); }
                if constexpr ((s == 3 && p < 4) || n == 104) { const f32x2 sv = sv0 + sv1; mean = sv[0] * (1.f / D); r2 = __builtin_amdgcn_rcpf(__builtin_fmaxf(sv[1] * (1.f / D) - mean * mean, 0.f) + EPS); PIN2(mean, r2); }
                if constexpr (s >= 4 && s < 12) { constexpr int i = s - 4;
                    const float h0 = __builtin_fmaxf(a[i] - mean * c4[i >> 2][i & 3], 0.f) * r2 * (a[8 + i] - mean * c4[2 + (i >> 2)][i & 3]);
                    if constexpr (i & 1) { hv[i >> 1] = p >= 3 ? umin(cvtpk(hp, h0), ~1u) : cvtpk(hp, h0); PIN(hv); } else { hp = h0; PIN(hp); } }
                if constexpr (s == 12) { st16_dev(hv, rws, go + (p - 1) * 114688, 0); FENCE(); }
            }
            SB();
        });
        TS(15);
        if (__builtin_expect(!wave_all(gfl == tag), 0)) poll_flag(ws, gfi, tag);
        FENCE();
#pragma unroll
        for (int s = 0; s < GW; ++s) { gf[s] = BF8(ld16_dev(rws, l16, gxo + E_KS(s) * 1024)); SB(); }
        FENCE();
#pragma unroll
        for (int s = 0; s < 12; ++s) wdf[s] = lds8(s < 5 ? wl + s * 1024 : wl2 + (s - 5) * 1024);
    }

    {
        const int etok = tid >> 4, eblk = tid & 15;
        const unsigned hro = (unsigned)((((eblk >> 2) & 3) * 64 + (eblk & 1) * 32 + etok) * 16 + ((eblk >> 1) & 1) * 8) + WS_HF + (unsigned)(ta * 28 + m7 * 4) * 1024;
        u32x2 hres[2];
        GAS u64* op = (GAS u64*)(out + (size_t)(ta * 32 + etok) * 896 + m7 * 128 + eblk * 8);
        lds_ptr rp = opq(lds + w * 8448 + hf * 528 + r * 16), rq = opq(lds + eblk * 528 + etok * 16);
        lds_ptr rp2 = opq(lds + 67584 + w * 8448 + hf * 528 + r * 16), rq2 = opq(lds + 67584 + eblk * 528 + etok * 16);
#define G_LD(s) gf[(s) % GW] = BF8(ld16_dev(rws, l16, gxo + ((s) / 14) * 229376 + E_KS((s) % 14) * 1024))
#define G_PUT(RP) do { \
            _Pragma("unroll") for (int g = 0; g < 4; ++g) *(LAS f32x4*)(RP + g * 1056) = (f32x4){c0[4 * g], c0[4 * g + 1], c0[4 * g + 2], c0[4 * g + 3]}; \
            _Pragma("unroll") for (int g = 0; g < 4; ++g) *(LAS f32x4*)(RP + 4224 + g * 1056) = (f32x4){c1[4 * g], c1[4 * g + 1], c1[4 * g + 2], c1[4 * g + 3]}; } while (0)
#define G_OUT(hh, RQ) do { f32x4 pa[8]; \
            _Pragma("unroll") for (int ww = 0; ww < 8; ++ww) pa[ww] = *(LAS f32x4*)(RQ + ww * 8448); SB(); \
            const f32x4 a = ((pa[0] + pa[1]) + (pa[2] + pa[3])) + ((pa[4] + pa[5]) + (pa[6] + pa[7])); \
            const u32x2 ov = {cvtpk(a[0] + bflo(hres[hh][0]), a[1] + bfhi(hres[hh][0])), cvtpk(a[2] + bflo(hres[hh][1]), a[3] + bfhi(hres[hh][1]))}; \
            __hip_atomic_store(op + (hh) * (64 * 896 / 8), __builtin_bit_cast(u64, ov), __ATOMIC_RELAXED, __HIP_MEMORY_SCOPE_AGENT); } while (0)
        f32x16 c0 = {}, c1 = {};
        TS(16);
        unsigned seen = 0;
#pragma unroll
        for (int s = 0; s < 28; ++s) {
            if (s == 1) { hres[0] = ld8_dev(rws, hro, 0); hres[1] = ld8_dev(rws, hro, 57344); FENCE(); }
            if (s == 10) BAR();
            if (s == 14) { G_PUT(rp); c0 = (f32x16){}; c1 = (f32x16){}; TS(17); }
            if (s == 20) { waitv<8>(); PIN2(hres[0], hres[1]); BAR(); if (!GWG || w == 0) { set_flag(ws, fo4 + 4096, tagv); set_flag(ws, fo4 + 6144, tagv); } G_OUT(0, rq); FENCE(); TS(18); }
            SB();
            c0 = MFMA(wdf[2 * (s % 14)], gf[s % GW], c0); PIN(c0); c1 = MFMA(wdf[2 * (s % 14) + 1], gf[s % GW], c1); PIN(c1);
            if (s >= 14) { const u32x4 u = U4(gf[s % GW]); seen = umax3(umax3(seen, u[0], u[1]), u[2], u[3]); PIN(seen); }
            SB();
            if (s + GW < 28) G_LD(s + GW);
        }
        if (__builtin_expect(wave_any(seen == ~0u), 0)) {
            poll_flag(ws, gfi + 4096, tag);
            c0 = (f32x16){}; c1 = (f32x16){};
#pragma unroll
            for (int s = 14; s < 14 + GW; ++s) G_LD(s);
#pragma unroll
            for (int s = 14; s < 28; ++s) { c0 = MFMA(wdf[2 * (s % 14)], gf[s % GW], c0); c1 = MFMA(wdf[2 * (s % 14) + 1], gf[s % GW], c1); PIN2(c0, c1); SB(); if (s + GW < 28) G_LD(s + GW); SB(); }
        }
        G_PUT(rp2);
        TS(19);
        BAR();
        G_OUT(1, rq2);
    }
    TS(20);
}

extern "C" void kernel_launch(void* const* d_in, const int* in_sizes, int n_in, void* d_out, int out_size, void* d_ws, size_t ws_size, hipStream_t stream) {
    (void)hipFuncSetAttribute((const void*)block_fwd, hipFuncAttributeMaxDynamicSharedMemorySize, LDS_BYTES);
    block_fwd<<<256, 512, LDS_BYTES, stream>>>((const char*)d_in[0], (const char*)d_in[1], (const char*)d_in[2], (const char*)d_in[4], (const char*)d_in[5], (const char*)d_in[6], (char*)d_out, (char*)d_ws + WS_OFF);
}
```

```cpp
#define SPDLY 20
#include <hip/hip_runtime.h>
#include <hip/hip_bf16.h>
#include <stdint.h>

typedef __attribute__((ext_vector_type(8))) short bf16x8;
typedef __attribute__((ext_vector_type(4))) short s16x4;
typedef __attribute__((ext_vector_type(2))) __bf16 bf16x2;
typedef __attribute__((ext_vector_type(16))) float f32x16;
typedef __attribute__((ext_vector_type(4))) float f32x4;
typedef __attribute__((ext_vector_type(2))) float f32x2;
typedef __attribute__((ext_vector_type(4))) unsigned u32x4;
typedef __attribute__((ext_vector_type(2))) unsigned u32x2;
typedef short v4i16_t __attribute__((ext_vector_type(4)));
typedef unsigned long long u64;
#define LAS __attribute__((address_space(3)))
#define GAS __attribute__((address_space(1)))
typedef LAS char* lds_ptr;
#if defined(__HIP_DEVICE_COMPILE__)
#define NOPK __attribute__((target("no-packed-fp32-ops")))
#else
#define NOPK
#endif
#define DI __device__ __forceinline__
#define MFMA(a, b, c) __builtin_amdgcn_mfma_f32_32x32x16_bf16(a, b, c, 0, 0, 0)
#define MFMA16(a, b, c) __builtin_amdgcn_mfma_f32_16x16x32_bf16(a, b, c, 0, 0, 0)
#define BF8(x) __builtin_bit_cast(bf16x8, x)
#define U4(x) __builtin_bit_cast(u32x4, x)

constexpr int D = 448;
constexpr float EPS = 1e-5f;
constexpr unsigned WS_FLAG = 0;
constexpr unsigned FL_EPOCH = 0, FL_KV = 1, FL_RT = FL_KV + 256, FL_RD = FL_RT + 32, FL_CL = FL_RD + 32, FL_O = FL_CL + 32, FL_H = FL_O + 256, FL_G = FL_H + 256;
constexpr unsigned WS_KV = 1u << 20;
constexpr unsigned WS_QF = WS_KV + 4194304;
constexpr unsigned WS_OF = WS_QF + 2097152;
constexpr unsigned WS_HF = WS_OF + 2097152;
constexpr unsigned WS_GF = WS_HF + 64 * 28 * 1024;
constexpr unsigned WS_WGU = WS_GF + 64 * 112 * 1024;
constexpr unsigned WS_WD = WS_WGU + 112 * 28 * 1024;
constexpr unsigned WS_HS = WS_WD + 7 * 112 * 2 * 1024;
constexpr unsigned WS_END = WS_HS + 64 * 28 * 256 + 1024;
constexpr unsigned OOB = 0x40000000u;
constexpr int LDS_BYTES = 163840;

DI float u2f(unsigned u) { return __builtin_bit_cast(float, u); }
DI unsigned f2u(float f) { return __builtin_bit_cast(unsigned, f); }
DI unsigned umin(unsigned a, unsigned b) { return a < b ? a : b; }
DI unsigned umax3(unsigned a, unsigned b, unsigned c) { const unsigned m = a > b ? a : b; return m > c ? m : c; }
DI bool wave_any(bool p) { return __builtin_amdgcn_ballot_w64(p) != 0; }
DI bool wave_all(bool p) { return __builtin_amdgcn_ballot_w64(!p) == 0; }
DI unsigned cvtpk(float lo, float hi) { return __builtin_bit_cast(unsigned, __builtin_convertvector((f32x2){lo, hi}, bf16x2)); }
DI float bflo(unsigned u) { return u2f(u << 16); }
DI float bfhi(unsigned u) { return u2f(u & 0xffff0000u); }
DI void glds(unsigned voff, const void* sbase, unsigned dst) {
    asm volatile("s_mov_b32 m0, %2\n\ts_nop 0\n\tglobal_load_lds_dwordx4 %0, %1" :: "v"(voff), "s"(sbase), "s"(dst) : "memory");
}
DI void glds_dev(unsigned voff, const void* sbase, unsigned dst) {
    asm volatile("s_mov_b32 m0, %2\n\ts_nop 0\n\tglobal_load_lds_dwordx4 %0, %1 sc1" :: "v"(voff), "s"(sbase), "s"(dst) : "memory");
}
DI void glds2_dev(unsigned voff, const void* sbase, unsigned dst) {
    asm volatile("s_mov_b32 m0, %2\n\ts_nop 0\n\tglobal_load_lds_dwordx4 %0, %1 sc1\n\tglobal_load_lds_dwordx4 %0, %1 offset:1024 sc1" :: "v"(voff), "s"(sbase), "s"(dst) : "memory");
}
DI void glds4_dev(unsigned voff, const void* sbase, unsigned dst) {
    asm volatile("s_mov_b32 m0, %2\n\ts_nop 0\n\tglobal_load_lds_dwordx4 %0, %1 sc1\n\tglobal_load_lds_dwordx4 %0, %1 offset:1024 sc1\n\t"
                 "global_load_lds_dwordx4 %0, %1 offset:2048 sc1\n\tglobal_load_lds_dwordx4 %0, %1 offset:3072 sc1" :: "v"(voff), "s"(sbase), "s"(dst) : "memory");
}
DI s16x4 trd(lds_ptr p) { return __builtin_bit_cast(s16x4, __builtin_amdgcn_ds_read_tr16_b64_v4i16((LAS v4i16_t*)p)); }
DI bf16x8 cat(s16x4 a, s16x4 b) { return __builtin_shufflevector(a, b, 0, 1, 2, 3, 4, 5, 6, 7); }
DI bf16x8 lds8(lds_ptr p) { return *(LAS bf16x8*)p; }
DI float swap_add(float v) { auto r = __builtin_amdgcn_permlane32_swap(f2u(v), f2u(v), false, false); return u2f(r[0]) + u2f(r[1]); }
DI float swap_max(float v) { auto r = __builtin_amdgcn_permlane32_swap(f2u(v), f2u(v), false, false); return __builtin_fmaxf(u2f(r[0]), u2f(r[1])); }
DI u32x4 pack8(const f32x16& a, int s) { return (u32x4){cvtpk(a[8 * s], a[8 * s + 1]), cvtpk(a[8 * s + 2], a[8 * s + 3]), cvtpk(a[8 * s + 4], a[8 * s + 5]), cvtpk(a[8 * s + 6], a[8 * s + 7])}; }
DI float dot2(unsigned a, unsigned b, float c) { return __builtin_amdgcn_fdot2_f32_bf16(__builtin_bit_cast(bf16x2, a), __builtin_bit_cast(bf16x2, b), c, false); }
DI void acc_stats(u32x4 u, float& s1, float& s2) {
#pragma unroll
    for (int i = 0; i < 4; ++i) { const float a = bflo(u[i]), b = bfhi(u[i]); s1 += a; s2 = __builtin_fmaf(a, a, s2); s1 += b; s2 = __builtin_fmaf(b, b, s2); }
}
DI void acc_stats2(unsigned u0, unsigned u1, f32x4& s) {
    const float a = bflo(u0), b = bfhi(u0), c = bflo(u1), d = bfhi(u1);
    s[0] += a; s[1] += b; s[2] = __builtin_fmaf(a, a, s[2]); s[3] = __builtin_fmaf(b, b, s[3]); s[0] += c; s[1] += d; s[2] = __builtin_fmaf(c, c, s[2]); s[3] = __builtin_fmaf(d, d, s[3]);
}
DI float sum8(float v) {
    v += u2f(__builtin_amdgcn_update_dpp(0u, f2u(v), 0xB1, 0xf, 0xf, true));
    v += u2f(__builtin_amdgcn_update_dpp(0u, f2u(v), 0x4E, 0xf, 0xf, true));
    v += u2f(__builtin_amdgcn_update_dpp(0u, f2u(v), 0x141, 0xf, 0xf, true));
    return v;
}
DI u32x4 shift8(u32x4 x, float c) {
    u32x4 o; float t0, t1, t2, t3, t4, t5, t6, t7;
    asm("v_dot2_f32_bf16 %4, %12, %16, %18\n\tv_dot2_f32_bf16 %5, %12, %17, %18\n\tv_dot2_f32_bf16 %6, %13, %16, %18\n\tv_dot2_f32_bf16 %7, %13, %17, %18\n\t"
        "v_dot2_f32_bf16 %8, %14, %16, %18\n\tv_dot2_f32_bf16 %9, %14, %17, %18\n\tv_dot2_f32_bf16 %10, %15, %16, %18\n\tv_dot2_f32_bf16 %11, %15, %17, %18\n\t"
        "v_cvt_pk_bf16_f32 %0, %4, %5\n\tv_cvt_pk_bf16_f32 %1, %6, %7\n\tv_cvt_pk_bf16_f32 %2, %8, %9\n\tv_cvt_pk_bf16_f32 %3, %10, %11"
        : "=&v"(o[0]), "=&v"(o[1]), "=&v"(o[2]), "=&v"(o[3]), "=&v"(t0), "=&v"(t1), "=&v"(t2), "=&v"(t3), "=&v"(t4), "=&v"(t5), "=&v"(t6), "=&v"(t7)
        : "v"(x[0]), "v"(x[1]), "v"(x[2]), "v"(x[3]), "s"(0x00003f80u), "s"(0x3f800000u), "v"(c));
    return o;
}
template <int N> struct IC { static constexpr int v = N; };
template <int B, int E, class F> DI void sfor(F&& f) { if constexpr (B < E) { f(IC<B>{}); sfor<B + 1, E>(f); } }
template <int N> DI void waitv() { asm volatile("s_waitcnt vmcnt(%0)" :: "i"(N) : "memory"); }
template <int N> DI void waitv_bar() { if (N == 0) __builtin_amdgcn_s_waitcnt(0x0F70); asm volatile("s_waitcnt vmcnt(%0) lgkmcnt(0)\n\ts_barrier" :: "i"(N) : "memory"); }
template <int N> DI void waitv_barx() { asm volatile("s_waitcnt vmcnt(%0)\n\ts_barrier" :: "i"(N) : "memory"); }
template <int N> DI void waitv_bar2() { asm volatile("s_waitcnt vmcnt(%0) lgkmcnt(2)\n\ts_barrier" :: "i"(N) : "memory"); }
DI void touch(unsigned voff, const void* sbase, unsigned junk) {
    asm volatile("s_mov_b32 m0, %2\n\ts_nop 0\n\tglobal_load_lds_dword %0, %1 sc1" :: "v"(voff), "s"(sbase), "s"(junk) : "memory");
}
#define BAR() asm volatile("s_waitcnt lgkmcnt(0)\n\ts_barrier" ::: "memory")
#define FENCE() asm volatile("" ::: "memory")
typedef __amdgpu_buffer_rsrc_t rsrc_t;
DI u32x4 ld16(rsrc_t rs, unsigned voff, unsigned soff) { return U4(__builtin_amdgcn_raw_buffer_load_b128(rs, voff, soff, 0)); }
DI u32x4 ld16_dev(rsrc_t rs, unsigned voff, unsigned soff) { return U4(__builtin_amdgcn_raw_buffer_load_b128(rs, voff, soff, 16)); }
DI u32x2 ld8(rsrc_t rs, unsigned voff, unsigned soff) { return __builtin_bit_cast(u32x2, __builtin_amdgcn_raw_buffer_load_b64(rs, voff, soff, 0)); }
DI u32x2 ld8_dev(rsrc_t rs, unsigned voff, unsigned soff) { return __builtin_bit_cast(u32x2, __builtin_amdgcn_raw_buffer_load_b64(rs, voff, soff, 16)); }
DI void st16_dev(u32x4 v, rsrc_t rs, unsigned voff, unsigned soff) { __builtin_amdgcn_raw_buffer_store_b128(v, rs, voff + soff, 0, 16); }
DI void st8_dev(u32x2 v, rsrc_t rs, unsigned voff, unsigned soff) { __builtin_amdgcn_raw_buffer_store_b64(v, rs, voff, soff, 16); }
#define POLL_SLEEP 1
DI void set_flag(char* ws, unsigned fo, u64 tagv) {
    asm volatile("s_mov_b64 exec, 1\n\tglobal_store_dwordx2 %0, %1, %2 sc1\n\ts_mov_b64 exec, -1" :: "v"(fo), "v"(tagv), "s"(ws) : "memory");
}
DI u64 peek_flag(char* ws, unsigned fo) { return __hip_atomic_load((GAS u64*)(ws + fo), __ATOMIC_RELAXED, __HIP_MEMORY_SCOPE_AGENT); }
DI void peek_lds(char* ws, unsigned fo, unsigned dst) {
    asm volatile("s_mov_b32 m0, %2\n\ts_nop 0\n\tglobal_load_lds_dword %0, %1 sc1\n\ts_add_u32 m0, %2, 252\n\ts_nop 0\n\tglobal_load_lds_dword %0, %1 offset:4 sc1" :: "v"(fo), "s"(ws), "s"(dst) : "memory", "scc");
}
DI u64 peeked(lds_ptr p) { return (u64)*(LAS unsigned*)(p + 256) << 32 | *(LAS unsigned*)p; }
DI void poll_flag(char* ws, unsigned fo, u64 tag, u64 lanes = 0xffff) {
#pragma unroll 1
    for (int spin = 0; spin < (1 << 16); ++spin) {
        if (wave_all(peek_flag(ws, fo) == tag)) break;
        __builtin_amdgcn_s_sleep(POLL_SLEEP);
    }
    FENCE();
}
#define SB() __builtin_amdgcn_sched_barrier(0)
#define PINS(a) asm volatile("" : "+s"(a))
#define PIN(a) asm volatile("" : "+v"(a))
#define PIN2(a, b) asm volatile("" : "+v"(a), "+v"(b))
DI lds_ptr opq(lds_ptr p) { unsigned u = (unsigned)(uintptr_t)p; asm volatile("" : "+v"(u)); return (lds_ptr)(uintptr_t)u; }
#define TS(i) do {} while (0)

DI constexpr int wd_slot(int s) { return (s * 17 + 27) / 28 != ((s + 1) * 17 + 27) / 28 ? (s * 17 + 27) / 28 : -1; }
#define EROT 0
#define CLR_SLEEP 90
#define ROTMODE 0
#define EARLYG 0
#define GWG 1
#define GW 8
#define CLRMODE 0
#define DLY1 0
#define DLY2 0
#define DLY3 0
#define P3 1
#define VARLATE 0
#define CST 0
#define ENL 4
#define HOP3_POLLER 1
DI constexpr int d_prio(int n) { return n < 0 ? -1 : n < 27 ? 3 - n / 7 : n < 64 ? 3 - (n - 27) / 10 : n < 84 ? 3 - (n - 64) / 5 : n < 112 ? 3 - (n - 84) / 7 : 0; }
DI constexpr int phys(int t) { return ((t % 9) + 2) % 9 * 16384; }

__global__ NOPK __launch_bounds__(512, 2) void block_fwd(const char* x, const char* wqkv, const char* wo, const char* wg, const char* wu, const char* wd, char* out, char* ws) {
    extern __shared__ __attribute__((aligned(16))) char smem[];
    lds_ptr lds = (lds_ptr)smem;
    const int tid = __builtin_amdgcn_workitem_id_x(), lane = tid & 63, w = __builtin_amdgcn_readfirstlane(tid >> 6);
    const int bid = __builtin_amdgcn_workgroup_id_x(), xcd = bid & 7, loc = bid >> 3;
    const int r = lane & 31, hf = lane >> 5, q4 = (lane & 15) >> 2, g1 = (lane >> 4) & 1, p4 = lane & 3;
    const unsigned lds0 = (unsigned)(uintptr_t)smem;
    const unsigned l16 = lane * 16;
    TS(0);
    const int sp3 = (loc + 8) >> 5;
    const int atb = (xcd >> 1) * 8 + (loc & 7) + sp3 * (xcd & 1) * 4, acg = (loc >> 3) + (1 - sp3) * (xcd & 1) * 4;
    const int row8 = lane >> 3;
    const int rot8 = ((loc & 7) << sp3) + sp3 * (xcd & 1), rot = rot8 == 7 ? 0 : rot8;
    if (loc < 28) {
        const unsigned xo = (unsigned)((atb * 64 + (tid >> 3)) * 896 + (((tid ^ (tid >> 4)) & 7) * 16));
        const unsigned xd0 = lds0 + 1024 + (w >> 2) * 28672 + (w & 3) * 1024;
        unsigned xd = xd0 + (7 - rot) * 4096;
        const unsigned w0 = (unsigned)((rot * 64 + (tid >> 3)) * 2688 + acg * 128 + (((tid & 7) ^ ((tid >> 2) & 4)) * 16));
        asm volatile("s_cmp_eq_u32 %0, %3\n\ts_cselect_b32 %0, %2, %0\n\ts_mov_b32 m0, %0\n\ts_add_u32 %0, %0, 0x1000\n\tglobal_load_lds_dwordx4 %1, %4\n\t"
#define XP(o) "s_cmp_eq_u32 %0, %3\n\ts_cselect_b32 %0, %2, %0\n\ts_sub_u32 m0, %0, " #o "\n\ts_add_u32 %0, %0, 0x1000\n\tglobal_load_lds_dwordx4 %1, %4 offset:" #o "\n\t"
                     XP(128) XP(256) XP(384) XP(512) XP(640) XP(768)
                     "s_add_u32 m0, %6, 8192-896\n\ts_barrier\n\tglobal_load_lds_dwordx4 %5, %7 offset:896\n\t"
                     "s_add_u32 m0, %6, 16384-1792\n\ts_nop 0\n\tglobal_load_lds_dwordx4 %5, %7 offset:1792\n\t"
                     "s_mov_b32 m0, %6\n\ts_nop 0\n\tglobal_load_lds_dwordx4 %5, %7"
                     : "+s"(xd) : "v"(xo), "s"(xd0), "s"(xd0 + 7 * 4096), "s"(x), "v"(w0), "s"(lds0 + 58368 + w * 1024), "s"(wqkv) : "memory", "scc");
    }
    SB();
    const rsrc_t rws = __builtin_amdgcn_make_buffer_rsrc((void*)ws, 0, WS_END, 0x00020000);
    u64 epoch, tagv;
    unsigned fl1;
    const unsigned ones = 0x3f803f80u;
    const bool ffn = loc < 28;
    const int l7 = loc / 7, m7 = loc % 7;
    const int tb = xcd * 4 + l7;
    const int tt = w & 1, ct = (w >> 1) & 1, kh = w >> 2;
    const unsigned swz = (unsigned)(((lane & 7) ^ (((row8 >> 1) & 1) << 2)) * 16);

    if (__builtin_expect(ffn, 1)) {
        constexpr int A_W = 58368, A_WS = 24576;
#define A_KC(c) (rot + (c) >= 7 ? rot + (c) - 7 : rot + (c))
        const unsigned wvo = (unsigned)(row8 * 2688) + swz;
        const char* wsrc = wqkv + acg * 128 + (size_t)w * 8 * 2688;
        const unsigned wdst = lds0 + A_W + w * 1024;
#define A_PIECE(c, i) glds(wvo, wsrc + A_KC(c) * (64 * 2688) + (i) * 896, wdst + ((c) & 3) * A_WS + (i) * 8192)
#define A_GROUP(c) do { A_PIECE(c, 1); A_PIECE(c, 2); A_PIECE(c, 0); } while (0)
        epoch = peek_flag(ws, FL_EPOCH * 128); FENCE(); fl1 = (FL_KV + atb * 8 + acg) * 128;
        if (EARLYG) A_GROUP(1);
        lds_ptr xb[4], wb1, wb1h;
#pragma unroll
        for (int j = 0; j < 4; ++j) xb[j] = lds + 1024 + tt * 28672 + (r >> 3) * 1024 + (r & 7) * 128 + (((2 * j + hf) ^ ((r >> 1) & 7)) * 16);
        wb1 = opq(lds + A_W + (kh ? 0 : 8192) + (8 * hf + q4) * 128 + ((ct ^ (q4 >> 1)) * 64) + g1 * 32 + p4 * 8);
        wb1h = opq(wb1 + 2 * A_WS);
        unsigned go1[2], go2[2]; int gi[2];
#pragma unroll
        for (int p = 0; p < 2; ++p) {
            const int G = acg * 8 + ct * 4 + 2 * p + hf, hp = G / 7, i = G - hp * 7;
            const unsigned hs = kh ? 131072u : 262144u;
            const unsigned to = (kh ? WS_QF + (atb * 2 + tt) * 2048 : WS_KV + (atb * 2 + tt) * 4096) + r * 16;
            go1[p] = (unsigned)(2 * hp + (i > 3)) * hs + (unsigned)(i > 3 ? i - 3 : i) * 512 + to;
            go2[p] = i == 3 ? (unsigned)(2 * hp + 1) * hs + to : OOB;
            gi[p] = i;
        }
        const int vcol = acg * 64 + ct * 32 + r, vh = vcol / 28, vd = vcol - vh * 28;
        unsigned vo = WS_KV + (atb * 2 + tt) * 4096 + (unsigned)vh * 262144 + 2048 + hf * 512 + vd * 16;
        PIN2(go1[0], go1[1]); PIN2(go2[0], go2[1]); PIN(vo);
        lds_ptr xw = opq(lds + 1024 + kh * 28672 + (w & 3) * 1024 + l16);
        waitv<EARLYG ? 7 : 4>();
        TS(28);
        u32x4 xv[7];
#pragma unroll
        for (int c = 0; c < 7; ++c) xv[c] = *(LAS u32x4*)(xw + c * 4096);
        SB();
        f32x4 sm = {};
#pragma unroll
        for (int c = 0; c < 7; ++c)
#pragma unroll
            for (int j = 0; j < 4; ++j) sm[j] = dot2(xv[c][j], ones, sm[j]);
        float nm = sum8((sm[0] + sm[1]) + (sm[2] + sm[3])) * (-1.f / D);
        PIN(nm);
#pragma unroll
        for (int c = 0; c < 7; ++c) { xv[c] = shift8(xv[c], nm); *(LAS u32x4*)(xw + c * 4096) = xv[c]; }
        if (EARLYG) A_GROUP(2); else A_GROUP(1);
        SB();
        LAS float* rst = (LAS float*)lds;
#define A_VAR() do { f32x4 sq = {}; _Pragma("unroll") for (int c_ = 0; c_ < 7; ++c_) _Pragma("unroll") for (int j = 0; j < 4; ++j) sq[j] = dot2(xv[c_][j], xv[c_][j], sq[j]); \
            rst[w * 8 + row8] = __builtin_amdgcn_rsqf(sum8((sq[0] + sq[1]) + (sq[2] + sq[3])) * (1.f / D) + EPS); } while (0)
        if (!VARLATE) A_VAR();
        if (EARLYG) A_GROUP(3); else A_GROUP(2);
        TS(31);
        bf16x8 fx[4], f1[4], f2[4];
#define A_WAIT(c) do { if ((c) == 0) waitv_bar<EARLYG ? 9 : 6>(); else if ((c) == 1) waitv_barx<6>(); else if ((c) < 6) waitv_barx<3>(); else waitv_barx<0>(); \
                       if ((c) == 0) TS(1); if ((c) == 6) TS(2); } while (0)
#define A_WP(c, ks) ((((c) & 2) ? wb1h : wb1) + ((c) & 1) * A_WS + (ks) * 2048)
        if (w < 4) {
            f32x16 ak = {}, av = {};
            f32x4 rv[4];
            float rs = 0.f;
#pragma unroll
            for (int n = 0; n < 30; ++n) {
                const int c = n >> 2, ks = n & 3, km = (n - 2) & 3;
                if (n < 28 && ks == 0) A_WAIT(c);
                if (n >= 2) { ak = MFMA(f1[km], fx[km], ak); PIN(ak); } SB();
                if (n < 28) { fx[ks] = lds8(xb[ks] + c * 4096); f1[ks] = cat(trd(A_WP(c, ks)), trd(A_WP(c, ks) + 512)); } SB();
                if (n >= 2) { av = MFMA(fx[km], f2[km], av); PIN(av); } SB();
                if (n < 28) f2[ks] = cat(trd(A_WP(c, ks) + 8192), trd(A_WP(c, ks) + 8192 + 512));
                if (!EARLYG && c == 0 && ks < 3) A_PIECE(3, ks); if (c >= 2 && c < 5 && ks < 3) A_PIECE(c + 2, ks);
                if (VARLATE && n == 1) { SB(); A_VAR(); }
                if (n == 21) {
                    rs = rst[tt * 32 + r];
#pragma unroll
                    for (int g = 0; g < 4; ++g) rv[g] = *(LAS f32x4*)(rst + tt * 32 + 8 * g + 4 * hf);
                }
                if (n == 24) { PIN(epoch); tagv = epoch + 1; PIN(tagv); }
                if (n == 24) {
                    const unsigned pv = vd == 0 ? ones : 0u, po = vd < 4 ? vo + 448 : OOB;
                    st16_dev((u32x4){pv, pv, pv, pv}, rws, po, 0); st16_dev((u32x4){pv, pv, pv, pv}, rws, po + 1024, 0);
                }
                SB();
            }
            TS(3);
#pragma unroll
            for (int i = 0; i < 16; ++i) { ak[i] *= rs; av[i] *= rv[i >> 2][i & 3]; }
#pragma unroll
            for (int p = 0; p < 2; ++p) {
                const u32x4 f = pack8(ak, p);
                const auto s0 = __builtin_amdgcn_permlane32_swap(f[0], f[2], false, false), s1 = __builtin_amdgcn_permlane32_swap(f[1], f[3], false, false);
                const u32x4 c = {s0[0], s1[0], s0[1], s1[1]};
                st16_dev(c, rws, go1[p], 0); st16_dev(c, rws, go2[p], 0);
            }
            st16_dev(pack8(av, 0), rws, vo, 0); st16_dev(pack8(av, 1), rws, vo + 1024, 0);
        } else {
            f32x16 aq = {};
            __builtin_amdgcn_s_setprio(1);
#pragma unroll
            for (int n = 0; n < 30; ++n) {
                const int c = n >> 2, ks = n & 3, km = (n - 2) & 3;
                if (n < 28 && ks == 0) A_WAIT(c);
                if (n == 24) { PIN(epoch); tagv = epoch + 1; PIN(tagv); }
                if (n >= 2) { aq = MFMA(f1[km], fx[km], aq); PIN(aq); } SB();
                if (n < 28) { fx[ks] = lds8(xb[ks] + c * 4096); f1[ks] = cat(trd(A_WP(c, ks)), trd(A_WP(c, ks) + 512)); }
                if (!EARLYG && c == 0 && ks < 3) A_PIECE(3, ks); if (c >= 2 && c < 5 && ks < 3) A_PIECE(c + 2, ks);
                if (VARLATE && n == 1) { SB(); A_VAR(); }
                SB();
            }
            __builtin_amdgcn_s_setprio(0);
            const float QS = rst[tt * 32 + r] * (0.18898223650461362f * 1.4426950408889634f);
#pragma unroll
            for (int i = 0; i < 16; ++i) aq[i] *= QS;
#pragma unroll
            for (int p = 0; p < 2; ++p) {
                const u32x4 f = pack8(aq, p);
                const auto s0 = __builtin_amdgcn_permlane32_swap(f[0], f[2], false, false), s1 = __builtin_amdgcn_permlane32_swap(f[1], f[3], false, false);
                const bool sp = gi[p] == 3;
                st16_dev((u32x4){s0[0], s1[0], sp ? 0u : s0[1], sp ? 0u : s1[1]}, rws, go1[p], 0);
                st16_dev((u32x4){0u, 0u, s0[1], s1[1]}, rws, go2[p], 0);
            }
        }
    } else {
        if (SPDLY) __builtin_amdgcn_s_sleep(SPDLY);
        epoch = peek_flag(ws, FL_EPOCH * 128); fl1 = (FL_RT + xcd * 4 + loc - 28) * 128;
        const int ri = xcd * 4 + loc - 28, nu = (w == 0 && ri < 16) ? 4 : 3;
        const unsigned vo = (unsigned)(row8 * 3584 + (lane & 7) * 16);
#pragma unroll 1
        for (int i = 0; i < nu; ++i) {
            const int u = i < 3 ? i * 256 + ri * 8 + w : 768 + ri;
            const size_t so = (size_t)(u / 28) * 16 * 3584 + (u % 28) * 128;
            const unsigned d_ = lds0 + w * 16384 + i * 4096;
            glds(vo, wg + so, d_); glds(vo, wg + so + 8 * 3584, d_ + 1024); glds(vo, wu + so, d_ + 2048); glds(vo, wu + so + 8 * 3584, d_ + 3072);
        }
#pragma unroll 1
        for (int i = 0; i < nu; ++i) {
            const int u = i < 3 ? i * 256 + ri * 8 + w : 768 + ri;
            waitv<8>();
            lds_ptr sg = lds + w * 16384 + i * 4096 + g1 * 2048 + (4 * hf + q4) * 128 + p4 * 8;
#pragma unroll
            for (int e = 0; e < 4; ++e) st16_dev(U4(cat(trd(sg + e * 32), trd(sg + e * 32 + 1024))), rws, l16, WS_WGU + (unsigned)(((u % 28) * 4 + e) * 28 + u / 28) * 1024);
            FENCE();
        }
        if (CLRMODE >= 2) {
            const unsigned c_ = WS_GF + (unsigned)(((ri >> 1) * 4 + 2 + (ri & 1)) * 112 + w * 14) * 1024 + l16;
#pragma unroll 1
            for (int i = 0; i < (CLRMODE == 2 ? 7 : 14); ++i) st16_dev((u32x4){~0u, ~0u, ~0u, ~0u}, rws, c_ + i * 1024, 0);
        }
        tagv = epoch + 1;
    }
    TS(24);
    const int head = xcd * 2 + (loc & 1), rb = loc >> 1, qt = w & 3;
    const char* kvs = ws + WS_KV + (size_t)head * 262144 + w * 2048;
    const char* qs = ws + WS_QF + (size_t)(head * 64 + rb * 4 + qt) * 2048 + kh * 1024;
    unsigned kvd = lds0 + w * 2048, qd = lds0 + 147456 + qt * 2048 + kh * 1024;
    unsigned pf1 = (FL_KV + (lane >> 1) * 8 + (head * 28 + (lane & 1) * 27) / 64) * 128;
    PINS(kvs); PINS(qs); PINS(kvd); PINS(qd); PIN2(pf1, fl1);
    waitv_bar<0>();
    if (w == 0) set_flag(ws, fl1, tagv);
    const u64 tag = ((u64)(unsigned)__builtin_amdgcn_readfirstlane((unsigned)(tagv >> 32)) << 32 | (u64)(unsigned)__builtin_amdgcn_readfirstlane((unsigned)tagv));
    TS(4);

    const int drb = (xcd >> 1) * 4 + l7, dfg = (xcd & 1) * 7 + m7;
    bf16x8 wf[28];
    u32x2 xa, xb;
    f32x16 cs = {}, acc = {};
    const u32x4 one4 = {ones, ones, ones, ones};
    const unsigned hi_ = (unsigned)((tb * 2 + tt) * 28 + m7 * 4 + ct * 2 + kh), ofo = (unsigned)(tt * 32768 + kh * 16384 + ct * 4096);
    unsigned ho = WS_HF + hi_ * 1024 + l16, so = hf ? OOB : WS_HS + hi_ * 256 + r * 8;
    const char* os = ws + WS_OF + (size_t)tb * 65536 + ofo;
    unsigned od = lds0 + 65536 + ofo;
    unsigned pf2 = (FL_O + (tb >> 1) * 16 + kh * 8 + ct * 2 + ((lane >> 1) & 1) + (lane & 4)) * 128 + ((lane & 1) * 4 + (tb & 1) * 2 + tt) * 8;
    pf2 &= ~127u;
    unsigned go = ffn ? WS_GF + (unsigned)(drb * 4 * 112 + dfg * 8 + w) * 1024 + l16 : OOB;
    unsigned fo3 = (FL_H + tb * 8 + m7) * 128;
    {
        lds_ptr ap = opq(lds + kh * 8192 + l16), apB = opq(lds + 65536 + kh * 8192 + l16), apC = opq(lds + 131072 + kh * 8192 + l16);
        const unsigned lv = kh * 8192 + (l16 ^ (kh * 256));
        lds_ptr vp = opq(lds + lv), vpB = opq(lds + 65536 + lv), vpC = opq(lds + 131072 + lv);
#define AP(off) ((off) < 65536 ? ap + (off) : (off) < 131072 ? apB + ((off) - 65536) : apC + ((off) - 131072))
#define VP(off) ((off) < 65536 ? vp + (off) : (off) < 131072 ? vpB + ((off) - 65536) : vpC + ((off) - 131072))
#define KV_ISSUE(t) glds2_dev(l16, kvs + (t) * 16384, kvd + phys(t))
        unsigned wgo = ffn ? WS_WGU + (unsigned)(dfg * 8 + w) * 28672 : OOB;
        unsigned clro = WS_GF + (unsigned)((((xcd * 4 + loc - 28) >> 1) * 4 + 2 + (loc & 1)) * 112 + w * 14) * 1024 + l16;
        u32x4 clv = {~0u, ~0u, ~0u, ~0u};
        if (CLRMODE == 1) { if (ffn) clro = OOB; PIN(clv); PIN(clro); }
        const int ri = umin(xcd * 4 + loc - 28, 27);
        const char* wos = (ffn ? wo + m7 * 128 : wd + (ri % 7) * 128 + (size_t)(ri / 7) * 448 * 896) + (size_t)w * 56 * 896;
        unsigned wov = (unsigned)(row8 * 896) + swz;
        const rsrc_t rx = __builtin_amdgcn_make_buffer_rsrc((void*)x, 0, 2048 * 448 * 2, 0x00020000);
        unsigned xo_ = (unsigned)(((tb * 2 + tt) * 32 + r) * 896 + m7 * 128 + ct * 64 + kh * 32 + hf * 8);
        unsigned rto = (FL_RT + (P3 ? w * 4 + (lane & 3) : r)) * 128;
        lds_ptr qp = opq(lds + 147456 + qt * 2048 + l16);
        PINS(wgo); PINS(wos); PIN2(wov, xo_); PIN(rto); PIN2(ho, so); PIN2(pf2, fo3); PIN(go);
        lds_ptr xw = opq(lds + 147456 + w * 2048 + l16), xr = opq(lds + 147456 + (w ^ 4) * 2048 + l16);
        lds_ptr lw = opq(lds + 58368 + w * 256 + lane * 4), lr = opq(lds + 58368 + qt * 256 + 128 + r * 4);
        lds_ptr bw = opq(lds + 60416 + w * 4), br = opq(lds + 60416 + (lane & 7) * 4);
        unsigned oo = WS_OF + (unsigned)((rb * 4 + qt) * 32 + head * 2 + kh) * 1024 + l16;
        unsigned fo2 = (FL_O + rb * 16 + head) * 128 + w * 8;
        PIN2(oo, fo2);
        if (w == HOP3_POLLER) { if (DLY1) __builtin_amdgcn_s_sleep(DLY1); poll_flag(ws, pf1, tag, ~0ull); }
        BAR();
        TS(5);
        glds_dev(l16, qs, qd);
        KV_ISSUE(0); KV_ISSUE(1);
        asm volatile("s_barrier" ::: "memory");
        KV_ISSUE(2); KV_ISSUE(3);
        waitv_bar<4>();
        TS(6);
        const bf16x8 q0 = lds8(qp), q1 = lds8(qp + 1024);
        f32x16 o = {}, sa = {}, sb;
        bf16x8 k0 = lds8(ap + phys(0)), k1 = lds8(ap + phys(0) + 1024), v0 = lds8(vp + phys(0) + 2048), v1 = {};
        u32x4 p0, p1 = {};
        sa = MFMA(k0, q0, sa); sa = MFMA(k1, q1, sa);
        k0 = lds8(ap + phys(0) + 4096); k1 = lds8(ap + phys(0) + 5120);
        PIN(sa); SB();
        u64 rtf = 0;
#define EXP4(S, o4) _Pragma("unroll") for (int i_ = o4; i_ < o4 + 4; ++i_) S[i_] = __builtin_amdgcn_exp2f(S[i_]);
#define WLD(s) wf[s] = BF8(ld16_dev(rws, l16 + ((s) & 3) * 1024, wgo + ((s) >> 2) * 4096))
#define SIDE(t, g) do { \
            if ((t) == 0 && (g) == 0) KV_ISSUE(4); if ((t) == 0 && (g) == 2) KV_ISSUE(5); if ((t) == 1 && (g) == 0) KV_ISSUE(6); if ((t) == 1 && (g) == 2) KV_ISSUE(7); \
            if ((t) == 2 && (g) == 0) KV_ISSUE(8); if (!P3 && (t) == 1 && (g) == 3) rtf = peek_flag(ws, rto); if (P3 && (t) == 1 && (g) == 1) peek_lds(ws, rto, lds0 + 155648 + w * 512); if (P3 && (t) == 2 && (g) == 2) { waitv<4>(); rtf = peeked(lds + 155648 + w * 512 + lane * 4); } \
            if ((t) == 3 && (g) < 3) KV_ISSUE(9 + (g)); \
            if (CLRMODE == 0 && (t) >= 4 && (t) <= 10 && ((g) & 1)) { if (__builtin_expect(!ffn, 0)) st16_dev((u32x4){~0u, ~0u, ~0u, ~0u}, rws, clro + (((t) - 4) * 2 + ((g) >> 1)) * 1024, 0); } \
            if (CLRMODE == 1 && (t) >= 4 && (t) <= 10 && ((g) & 1)) __builtin_amdgcn_raw_buffer_store_b128(clv, rws, clro, (((t) - 4) * 2 + ((g) >> 1)) * 1024, 16); \
            if (CLRMODE == 2 && (t) >= 11 && !((g) & 1) && ((t) - 11) * 2 + (g) / 2 < 7) { if (__builtin_expect(!ffn, 0)) st16_dev((u32x4){~0u, ~0u, ~0u, ~0u}, rws, clro + (7 + ((t) - 11) * 2 + (g) / 2) * 1024, 0); } \
            if ((t) == 7 && (g) == 0) KV_ISSUE(12); if ((t) == 7 && (g) == 2) KV_ISSUE(13); if ((t) == 8 && (g) == 0) KV_ISSUE(14); if ((t) == 8 && (g) == 2) KV_ISSUE(15); \
            if ((t) >= 4 && (t) <= 6) WLD(((t) - 4) * 4 + (g)); \
            if (((t) == 7 || (t) == 8) && ((g) & 1)) WLD(12 + ((t) - 7) * 2 + ((g) >> 1)); \
            if ((t) >= 9 && (t) <= 11) WLD(((t) - 5) * 4 + (g)); \
            if ((t) == 12 || ((t) == 13 && (g) < 3)) glds(wov, wos + (((t) - 12) * 4 + (g)) * 8 * 896, lds0 + (w * 7 + ((t) - 12) * 4 + (g)) * 1024); \
            if ((t) == 13 && (g) == 3) { xa = ld8(rx, xo_, 0); xb = ld8(rx, xo_, 16); } \
            if ((t) == 14 && (g) == 1) { if (w >= 6) touch(lane * (w == 6 ? (WS_END / 64 & ~127u) : 28672), w == 6 ? ws : out, lds0 + 61440); } \
            if ((t) == 14 && (g) == 0) { if (tid < 32) *(LAS u32x4*)(lds + 57344 + tid * 16) = (u32x4){0, 0, 0, 0}; } \
        } while (0)
#define TOFF(i) (phys((i) >> 1) + ((i) & 1) * 4096)
#define TILE(i, SC, SN) do { \
            __builtin_amdgcn_s_setprio(((i) & 1) ? 1 : 3); \
            if ((i) < 31) { f32x16 z_ = {}; SN = MFMA(k0, q0, z_); PIN(SN); } if ((i) < 30) k0 = lds8(AP(TOFF((i) + 2))); SB(); \
            EXP4(SC, 0); PIN(SC); SB(); \
            o = MFMA(v1, BF8(p1), o); PIN(o); v1 = lds8(VP(TOFF(i) + 3072)); SIDE((i) >> 1, ((i) & 1) * 2); SB(); \
            EXP4(SC, 4); p0 = pack8(SC, 0); PIN(p0); SB(); \
            __builtin_amdgcn_s_setprio(((i) & 1) ? 0 : 2); \
            if ((i) < 31) { SN = MFMA(k1, q1, SN); PIN(SN); } if ((i) < 30) k1 = lds8(AP(TOFF((i) + 2) + 1024)); SB(); \
            EXP4(SC, 8); PIN(SC); SB(); \
            o = MFMA(v0, BF8(p0), o); PIN(o); if ((i) < 31) v0 = lds8(VP(TOFF((i) + 1) + 2048)); SIDE((i) >> 1, ((i) & 1) * 2 + 1); SB(); \
            EXP4(SC, 12); p1 = pack8(SC, 1); PIN(p1); SB(); } while (0)
#define STEP(t) do { TILE(2 * (t), sa, sb); TILE(2 * (t) + 1, sb, sa); } while (0)
        STEP(0);
        waitv_barx<4>();
        STEP(1); STEP(2);
        if (P3 && __builtin_expect(!wave_all(rtf == tag), 0)) poll_flag(ws, rto, tag, 0xffffffffull);
        waitv_bar2<P3 ? 2 : 3>();
        TS(21);
        if (!P3 && __builtin_expect(!wave_all(rtf == tag), 0)) poll_flag(ws, rto, tag, 0xffffffffull);
        FENCE();
        STEP(3); STEP(4); STEP(5); STEP(6);
        waitv_bar2<CLRMODE == 1 ? 18 : 12>();
        TS(22);
        STEP(7); STEP(8); STEP(9); STEP(10);
        waitv_bar2<CLRMODE == 1 ? 14 : 9>();
        if (CLRMODE == 1) PIN(clv);
        TS(23);
        STEP(11); STEP(12); STEP(13); STEP(14); STEP(15);
        o = MFMA(v1, BF8(p1), o);
        TS(7);
        u32x4 ov;
        {
            *(LAS f32x4*)xw = (f32x4){o[8], o[9], o[10], o[11]}; *(LAS f32x4*)(xw + 1024) = (f32x4){o[12], o[13], o[14], o[15]};
            const float l = kh ? o[4] : o[12];
            *(LAS float*)lw = l;
#define AMX(a, b, c) __builtin_fmaxf(__builtin_fmaxf(__builtin_fabsf(a), __builtin_fabsf(b)), __builtin_fabsf(c))
            const float mx_ = AMX(AMX(AMX(o[0], o[1], o[2]), AMX(o[3], o[4], o[5]), AMX(o[6], o[7], o[8])), AMX(AMX(o[9], o[10], o[11]), AMX(o[12], o[13], o[14]), o[15]), 0.f);
            const bool ok = (mx_ < 0x1p100f) & ((hf ? l : 1.f) > 0x1p-80f);
            *(LAS unsigned*)bw = wave_all(ok) ? 0u : 1u;
            BAR();
            const f32x4 a = *(LAS f32x4*)xr, c = *(LAS f32x4*)(xr + 1024);
            const float inv = __builtin_amdgcn_rcpf(*(LAS float*)lr + *(LAS float*)(lr + 1024));
            const unsigned bad = *(LAS unsigned*)br;
            const float iz = (kh && hf) ? 0.f : inv;
            ov = (u32x4){cvtpk((o[0] + a[0]) * inv, (o[1] + a[1]) * inv), cvtpk((o[2] + a[2]) * inv, (o[3] + a[3]) * inv),
                         cvtpk((o[4] + c[0]) * iz, (o[5] + c[1]) * iz), cvtpk((o[6] + c[2]) * iz, (o[7] + c[3]) * iz)};
            if (__builtin_expect(wave_any(bad != 0), 0)) {
                f32x16 of = {};
                float m = -0x1p126f;
#pragma unroll 1
                for (int t = 0; t < 16; ++t) {
                    BAR();
                    glds2_dev(l16, kvs + t * 16384, lds0 + 65536 + w * 2048);
                    waitv_bar<0>();
#pragma unroll 1
                    for (int u = 0; u < 2; ++u) {
                        f32x16 s = {};
                        s = MFMA(lds8(apB + u * 4096), q0, s); s = MFMA(lds8(apB + u * 4096 + 1024), q1, s);
                        float mx = s[0];
#pragma unroll
                        for (int i = 1; i < 16; ++i) mx = __builtin_fmaxf(mx, s[i]);
                        mx = __builtin_fmaxf(swap_max(mx), m);
                        const float alpha = __builtin_amdgcn_exp2f(m - mx);
                        m = mx;
#pragma unroll
                        for (int i = 0; i < 16; ++i) { s[i] = __builtin_amdgcn_exp2f(s[i] - mx); of[i] *= alpha; }
                        of = MFMA(lds8(vpB + u * 4096 + 2048), BF8(pack8(s, 0)), of);
                        of = MFMA(lds8(vpB + u * 4096 + 3072), BF8(pack8(s, 1)), of);
                    }
                }
                *(LAS f32x4*)xw = (f32x4){of[8], of[9], of[10], of[11]}; *(LAS f32x4*)(xw + 1024) = (f32x4){of[12], of[13], of[14], of[15]};
                *(LAS float*)lw = hf ? (kh ? of[4] : of[12]) : m;
                BAR();
                const f32x4 a2 = *(LAS f32x4*)xr, c2 = *(LAS f32x4*)(xr + 1024);
                const float m0 = *(LAS float*)(lr - 128), m1 = *(LAS float*)(lr + 1024 - 128), mm = __builtin_fmaxf(m0, m1);
                const float e0 = __builtin_amdgcn_exp2f(m0 - mm), e1 = __builtin_amdgcn_exp2f(m1 - mm);
                const float iv = __builtin_amdgcn_rcpf(*(LAS float*)lr * e0 + *(LAS float*)(lr + 1024) * e1);
                const float im = (kh ? e1 : e0) * iv, io = (kh ? e0 : e1) * iv;
                const bool z = kh && hf;
                ov = (u32x4){cvtpk(of[0] * im + a2[0] * io, of[1] * im + a2[1] * io), cvtpk(of[2] * im + a2[2] * io, of[3] * im + a2[3] * io),
                             z ? 0u : cvtpk(of[4] * im + c2[0] * io, of[5] * im + c2[1] * io), z ? 0u : cvtpk(of[6] * im + c2[2] * io, of[7] * im + c2[3] * io)};
            }
        }
        st16_dev(ov, rws, oo, 0);
        TS(25);
        FENCE();
        PIN2(xa, xb);
#pragma unroll
        for (int s = 27; s >= 0; --s) PIN(wf[s]);
#pragma unroll
        for (int s = 0; s < 8; ++s) cs = MFMA(wf[s], BF8(one4), cs);
        acc[0] = bflo(xa[0]); acc[1] = bfhi(xa[0]); acc[2] = bflo(xa[1]); acc[3] = bfhi(xa[1]); acc[4] = bflo(xb[0]); acc[5] = bfhi(xb[0]); acc[6] = bflo(xb[1]); acc[7] = bfhi(xb[1]);
        PIN(acc); PIN2(ho, so); PIN2(pf2, fo3); PINS(os); PINS(od); PIN(cs);
        waitv_bar<0>();
        if (w == 0) set_flag(ws, fo2, tagv);
        TS(8);
    }
    if (__builtin_expect(!ffn, 0)) {
        const int ri = xcd * 4 + loc - 28;
        if (ri < 28) {
#pragma unroll 1
            for (int u = w; u < 28; u += 8) {
                lds_ptr sd = lds + u * 2048 + (4 * hf + q4) * 128 + g1 * 32 + p4 * 8;
#pragma unroll
                for (int e = 0; e < 2; ++e) { lds_ptr p_ = sd + ((e ^ (q4 >> 1)) * 64);
                    st16_dev(U4(cat(trd(p_), trd(p_ + 1024))), rws, l16, WS_WD + (unsigned)(((ri % 7) * 112 + (ri / 7) * 28 + u) * 2 + e) * 1024); }
            }
        }
        waitv_bar<0>();
        if (w == 0) set_flag(ws, (FL_RD + ri) * 128, tagv);
        return;
    }

    const int erb = tb >> 1;
    const char* hs = ws + WS_HF + (size_t)drb * 114688 + w * 1024;
    const char* hss = ws + WS_HS + (size_t)((drb * 4 + (w & 3)) * 28 + kh * 14) * 256;
    unsigned pf3, fo4 = (FL_G + drb * 64 + dfg) * 128 + w * 8;
    {
        lds_ptr wb = lds + kh * (8 * 28 * 128) + (4 * hf + q4) * 128 + ((ct ^ (q4 >> 1)) * 64) + (g1 ^ kh) * 32 + p4 * 8;
        bf16x8 wr[16];
#pragma unroll
        for (int s = 0; s < 12; ++s) { const int k0 = ((s >> 1) * 28 + (s & 1) * 16) * 128; wr[s] = cat(trd(wb + k0), trd(wb + k0 + 1024)); }
        lds_ptr cw = opq(lds + 131072 + w * 2048 + l16), cr = opq(lds + 131072 + (w ^ 4) * 2048 + l16);
        lds_ptr ob = opq(lds + 65536 + tt * 32768 + kh * 16384 + l16);
        pf3 = (FL_H + drb * 16 + (lane & 8) + umin(lane & 7, 6)) * 128;
        PIN2(pf3, fo4); PINS(hs); PINS(hss);
        if (DLY2) __builtin_amdgcn_s_sleep(DLY2);
        poll_flag(ws, pf2, tag, 0x55);
        TS(9);
        glds4_dev(l16, os, od);
        if (!CST) asm volatile("s_barrier" ::: "memory");
        glds4_dev(l16, os + 8192, od + 8192);
        bf16x8 of[8];
        waitv_bar<CST ? 0 : 4>();
        TS(26);
#pragma unroll
        for (int s = 0; s < 8; ++s) of[s] = lds8(ob + s * 1024);
        SB();
#pragma unroll
        for (int s = 0; s < 8; ++s) {
            acc = MFMA(wr[s], of[s], acc); PIN(acc); SB();
            if (s >= 2 && s < 6) { const int k0 = (((s + 10) >> 1) * 28 + (s & 1) * 16) * 128; wr[s + 10] = cat(trd(wb + k0), trd(wb + k0 + 1024)); SB(); }
            if (CST) { of[s] = lds8(ob + (8 + s) * 1024); SB(); }
        }
        if (!CST) {
            waitv_barx<0>();
#pragma unroll
            for (int s = 0; s < 8; ++s) of[s] = lds8(ob + (8 + s) * 1024);
        }
        SB();
#pragma unroll
        for (int s = 0; s < 8; ++s) acc = MFMA(wr[8 + s], of[s], acc);
        TS(10);
        *(LAS f32x4*)cw = (f32x4){acc[8], acc[9], acc[10], acc[11]}; *(LAS f32x4*)(cw + 1024) = (f32x4){acc[12], acc[13], acc[14], acc[15]};
        BAR();
        const f32x4 a = *(LAS f32x4*)cr + (f32x4){acc[0], acc[1], acc[2], acc[3]}, b = *(LAS f32x4*)(cr + 1024) + (f32x4){acc[4], acc[5], acc[6], acc[7]};
        const u32x4 hv = {cvtpk(a[0], a[1]), cvtpk(a[2], a[3]), cvtpk(b[0], b[1]), cvtpk(b[2], b[3])};
        st16_dev(hv, rws, ho, 0);
        FENCE(); SB();
        float s1 = 0.f, s2 = 0.f;
        { f32x4 q4_ = {}; acc_stats2(hv[0], hv[1], q4_); acc_stats2(hv[2], hv[3], q4_); s1 = swap_add(q4_[0] + q4_[1]); s2 = swap_add(q4_[2] + q4_[3]); }
        st8_dev((u32x2){f2u(s1), f2u(s2)}, rws, so, 0);
        FENCE(); PIN(cs); SB();
#pragma unroll
        for (int s = 8; s < 14; ++s) cs = MFMA(wf[s], BF8(one4), cs);
        PIN(cs);
        waitv_bar<0>();
        if (w == 0) set_flag(ws, fo3, tagv);
        if (bid == 0 && w == 0) set_flag(ws, FL_EPOCH * 128, tagv);
        TS(11);
    }

    const unsigned wdo = WS_WD + (unsigned)(m7 * 112 + w * 14) * 2048;
    bf16x8 wdf[28];
    bf16x8 gf[GW];
    const unsigned erot = EROT * m7;
#define E_KS(s) (erot + (s) >= 14 ? erot + (s) - 14 : erot + (s))
    const int ta = erb * 4 + (tb & 1);
    const unsigned gxo = WS_GF + (unsigned)(ta * 112 + w * 14) * 1024;
    u64 gfl = 0;
    const unsigned gks = w * 14 + umin(lane & 15, 13), gfi = (FL_G + (erb * 4 + (tb & 1)) * 16 + (gks >> 3)) * 128 + (GWG ? 0 : (gks & 7) * 8);
    {
#pragma unroll
        for (int s = 14; s < 28; ++s) cs = MFMA(wf[s], BF8(one4), cs);
        lds_ptr csp = opq(lds + 161792 + w * 128 + hf * 64);
        if (r == 0) {
#pragma unroll
            for (int g = 0; g < 4; ++g) *(LAS f32x4*)(csp + g * 16) = (f32x4){cs[4 * g], cs[4 * g + 1], cs[4 * g + 2], cs[4 * g + 3]};
        }
        const unsigned hd = lds0 + w * 1024;
        const char* wds = ws + wdo;
        lds_ptr hb = opq(lds + l16), hb2 = opq(lds + 57344 + l16);
        lds_ptr wl = opq(lds + 114688 + w * 5120 + l16), wl2 = opq(lds + w * 7168 + l16);
        LAS f32x2* stp = (LAS f32x2*)opq(lds + 159744 + r * 8);
        if (w == HOP3_POLLER) { if (DLY3) __builtin_amdgcn_s_sleep(DLY3); poll_flag(ws, pf3, tag, 0x7f7f); }
        BAR();
        TS(12);
#define H_PIECE(j) glds_dev(l16, hs + (j) * 8192, hd + (j) * 8192)
#define WDO(s) (E_KS((s) >> 1) * 2048 + ((s) & 1) * 1024)
#define WDLD(s) wdf[s] = BF8(ld16_dev(rws, l16, wdo + WDO(s)))
#define HB(n) ((n) < 56 ? hb + (n) * 1024 : hb2 + ((n) - 56) * 1024)
        H_PIECE(0);
        asm volatile("s_barrier" ::: "memory");
        H_PIECE(1); H_PIECE(2); H_PIECE(3);
        constexpr int PF = 5;
        bf16x8 bf[PF + 1];
        f32x16 acc[2];
        f32x2 sp[7], sv0, sv1;
        f32x4 c4[4];
        float mean = 0.f, r2 = 0.f, hp = 0.f;
        u32x4 hv;
        lds_ptr pk = opq(lds + 155648 + w * 512 + lane * 4);
        u64 pv = 0;
        const unsigned pkd = lds0 + 155648 + w * 512;
        const unsigned rdo = (FL_RD + ((lane & 2) ? drb * 2 + (lane & 1) : (lane & 1) ? tb : (w >> 1) * 7 + m7)) * 128;
        waitv_barx<3>();
        TS(27);
#pragma unroll
        for (int n = 0; n < PF; ++n) bf[n] = lds8(HB(n));
        SB();
        sfor<0, 125>([&](auto N_) __attribute__((always_inline)) {
            constexpr int n = decltype(N_)::v, p = n / 28, s = n % 28;
            if constexpr (n == 3) waitv_barx<3>();
            if constexpr (n == 11) waitv_barx<7>();
            if constexpr (n == 27) { waitv_barx<3>(); TS(13); }
            if constexpr (n == 64) { waitv_barx<10>(); if (!GWG || w == 0) set_flag(ws, fo4, tagv); TS(14); }
            if constexpr (n == 85) { if (GWG) waitv_barx<7>(); else waitv<7>(); if (!GWG || w == 0) set_flag(ws, fo4 + 2048, tagv); }
            if constexpr (n == 38) { if (__builtin_expect(!wave_all(pv == tag), 0)) poll_flag(ws, rdo, tag); FENCE(); }
            if constexpr (d_prio(n) != d_prio(n - 1)) __builtin_amdgcn_s_setprio(d_prio(n));
            if constexpr (n < 112) { if constexpr (s == 0) { f32x16 z = {}; acc[p & 1] = MFMA(wf[s], bf[n % (PF + 1)], z); } else acc[p & 1] = MFMA(wf[s], bf[n % (PF + 1)], acc[p & 1]); PIN(acc[p & 1]); }
            SB();
            if constexpr (n + PF < 112) bf[(n + PF) % (PF + 1)] = lds8(HB(n + PF));
            if constexpr (n % 3 == 2 && n < 30) H_PIECE(4 + n / 3);
            if constexpr (n == 4) glds4_dev(l16, hss, lds0 + 114688 + w * 5120);
            if constexpr (n == 16) { waitv<4>();
#pragma unroll
                for (int j = 0; j < 7; ++j) sp[j] = *(LAS f32x2*)(lds + 114688 + w * 5120 + (hf * 7 + j) * 256 + r * 8);
            }
            if constexpr (n == 20) { const f32x2 t = ((sp[0] + sp[1]) + (sp[2] + sp[3])) + ((sp[4] + sp[5]) + sp[6]); stp[w * 32] = (f32x2){swap_add(t[0]), swap_add(t[1])}; }
            if constexpr (n >= 39 && n < 48 && n % 2 == 1) glds_dev(l16, wds + WDO((n - 39) / 2), lds0 + 114688 + w * 5120 + (n - 39) / 2 * 1024);
            if constexpr (n >= 48 && n < 64 && n % 3 == 0) WDLD(12 + (n - 48) / 3);
            if constexpr (n >= 70 && n < 84 && n % 2 == 0) glds_dev(l16, wds + WDO(5 + (n - 70) / 2), lds0 + w * 7168 + (n - 70) / 2 * 1024);
            if constexpr (n >= 85 && n < 105 && n % 2 == 1) WDLD(18 + (n - 85) / 2);
            if constexpr (n == 107) gfl = peek_flag(ws, gfi);
            if constexpr (n == 32) pv = peeked(pk);
            if constexpr (n == 18) peek_lds(ws, rdo, pkd);
            if constexpr (p >= 1) {
                const f32x16& a = acc[(p - 1) & 1];
                if constexpr (n == 100) { sv0 = stp[3 * 32]; sv1 = stp[7 * 32]; }
                if constexpr (s == 0 && p < 4) { sv0 = stp[(p - 1) * 32]; sv1 = stp[(p + 3) * 32];
#pragma unroll
                    for (int g = 0; g < 4; ++g) c4[g] = *(LAS f32x4*)(csp + g * 16); }
                if constexpr ((s == 3 && p < 4) || n == 104) { const f32x2 sv = sv0 + sv1; mean = sv[0] * (1.f / D); r2 = __builtin_amdgcn_rcpf(__builtin_fmaxf(sv[1] * (1.f / D) - mean * mean, 0.f) + EPS); PIN2(mean, r2); }
                if constexpr (s >= 4 && s < 12) { constexpr int i = s - 4;
                    const float h0 = __builtin_fmaxf(a[i] - mean * c4[i >> 2][i & 3], 0.f) * r2 * (a[8 + i] - mean * c4[2 + (i >> 2)][i & 3]);
                    if constexpr (i & 1) { hv[i >> 1] = p >= 3 ? umin(cvtpk(hp, h0), ~1u) : cvtpk(hp, h0); PIN(hv); } else { hp = h0; PIN(hp); } }
                if constexpr (s == 12) { st16_dev(hv, rws, go + (p - 1) * 114688, 0); FENCE(); }
            }
            SB();
        });
        TS(15);
        if (__builtin_expect(!wave_all(gfl == tag), 0)) poll_flag(ws, gfi, tag);
        FENCE();
#pragma unroll
        for (int s = 0; s < GW; ++s) { gf[s] = BF8(ld16_dev(rws, l16, gxo + E_KS(s) * 1024)); SB(); }
        FENCE();
#pragma unroll
        for (int s = 0; s < 12; ++s) wdf[s] = lds8(s < 5 ? wl + s * 1024 : wl2 + (s - 5) * 1024);
    }

    {
        const int etok = tid >> 4, eblk = tid & 15;
        const unsigned hro = (unsigned)((((eblk >> 2) & 3) * 64 + (eblk & 1) * 32 + etok) * 16 + ((eblk >> 1) & 1) * 8) + WS_HF + (unsigned)(ta * 28 + m7 * 4) * 1024;
        u32x2 hres[2];
        GAS u64* op = (GAS u64*)(out + (size_t)(ta * 32 + etok) * 896 + m7 * 128 + eblk * 8);
        lds_ptr rp = opq(lds + w * 8448 + hf * 528 + r * 16), rq = opq(lds + eblk * 528 + etok * 16);
        lds_ptr rp2 = opq(lds + 67584 + w * 8448 + hf * 528 + r * 16), rq2 = opq(lds + 67584 + eblk * 528 + etok * 16);
#define G_LD(s) gf[(s) % GW] = BF8(ld16_dev(rws, l16, gxo + ((s) / 14) * 229376 + E_KS((s) % 14) * 1024))
#define G_PUT(RP) do { \
            _Pragma("unroll") for (int g = 0; g < 4; ++g) *(LAS f32x4*)(RP + g * 1056) = (f32x4){c0[4 * g], c0[4 * g + 1], c0[4 * g + 2], c0[4 * g + 3]}; \
            _Pragma("unroll") for (int g = 0; g < 4; ++g) *(LAS f32x4*)(RP + 4224 + g * 1056) = (f32x4){c1[4 * g], c1[4 * g + 1], c1[4 * g + 2], c1[4 * g + 3]}; } while (0)
#define G_OUT(hh, RQ) do { f32x4 pa[8]; \
            _Pragma("unroll") for (int ww = 0; ww < 8; ++ww) pa[ww] = *(LAS f32x4*)(RQ + ww * 8448); SB(); \
            const f32x4 a = ((pa[0] + pa[1]) + (pa[2] + pa[3])) + ((pa[4] + pa[5]) + (pa[6] + pa[7])); \
            const u32x2 ov = {cvtpk(a[0] + bflo(hres[hh][0]), a[1] + bfhi(hres[hh][0])), cvtpk(a[2] + bflo(hres[hh][1]), a[3] + bfhi(hres[hh][1]))}; \
            __hip_atomic_store(op + (hh) * (64 * 896 / 8), __builtin_bit_cast(u64, ov), __ATOMIC_RELAXED, __HIP_MEMORY_SCOPE_AGENT); } while (0)
        f32x16 c0 = {}, c1 = {};
        TS(16);
        unsigned seen = 0;
#pragma unroll
        for (int s = 0; s < 28; ++s) {
            if (s == 1) { hres[0] = ld8_dev(rws, hro, 0); hres[1] = ld8_dev(rws, hro, 57344); FENCE(); }
            if (s == 10) BAR();
            if (s == 14) { G_PUT(rp); c0 = (f32x16){}; c1 = (f32x16){}; TS(17); }
            if (s == 20) { waitv<8>(); PIN2(hres[0], hres[1]); BAR(); G_OUT(0, rq); FENCE(); TS(18); }
            SB();
            c0 = MFMA(wdf[2 * (s % 14)], gf[s % GW], c0); PIN(c0); c1 = MFMA(wdf[2 * (s % 14) + 1], gf[s % GW], c1); PIN(c1);
            if (s >= 14) { const u32x4 u = U4(gf[s % GW]); seen = umax3(umax3(seen, u[0], u[1]), u[2], u[3]); PIN(seen); }
            SB();
            if (s + GW < 28) G_LD(s + GW);
        }
        if (w == 0) { set_flag(ws, fo4 + 4096, tagv); set_flag(ws, fo4 + 6144, tagv); }
        if (__builtin_expect(wave_any(seen == ~0u), 0)) {
            poll_flag(ws, gfi + 4096, tag);
            c0 = (f32x16){}; c1 = (f32x16){};
#pragma unroll
            for (int s = 14; s < 14 + GW; ++s) G_LD(s);
#pragma unroll
            for (int s = 14; s < 28; ++s) { c0 = MFMA(wdf[2 * (s % 14)], gf[s % GW], c0); c1 = MFMA(wdf[2 * (s % 14) + 1], gf[s % GW], c1); PIN2(c0, c1); SB(); if (s + GW < 28) G_LD(s + GW); SB(); }
        }
        G_PUT(rp2);
        TS(19);
        BAR();
        G_OUT(1, rq2);
    }
    TS(20);
}

#define WS_OFF 0
extern "C" void kernel_launch(void* const* d_in, const int* in_sizes, int n_in, void* d_out, int out_size, void* d_ws, size_t ws_size, hipStream_t stream) {
    (void)hipFuncSetAttribute((const void*)block_fwd, hipFuncAttributeMaxDynamicSharedMemorySize, LDS_BYTES);
    block_fwd<<<256, 512, LDS_BYTES, stream>>>((const char*)d_in[0], (const char*)d_in[1], (const char*)d_in[2], (const char*)d_in[4], (const char*)d_in[5], (const char*)d_in[6], (char*)d_out, (char*)d_ws + WS_OFF);
}
```

```cpp
#include <hip/hip_runtime.h>
#include <hip/hip_bf16.h>
#include <stdint.h>

typedef __attribute__((ext_vector_type(8))) short bf16x8;
typedef __attribute__((ext_vector_type(4))) short s16x4;
typedef __attribute__((ext_vector_type(2))) __bf16 bf16x2;
typedef __attribute__((ext_vector_type(16))) float f32x16;
typedef __attribute__((ext_vector_type(4))) float f32x4;
typedef __attribute__((ext_vector_type(2))) float f32x2;
typedef __attribute__((ext_vector_type(4))) unsigned u32x4;
typedef __attribute__((ext_vector_type(2))) unsigned u32x2;
typedef short v4i16_t __attribute__((ext_vector_type(4)));
typedef unsigned long long u64;
#define LAS __attribute__((address_space(3)))
#define GAS __attribute__((address_space(1)))
typedef LAS char* lds_ptr;
#if defined(__HIP_DEVICE_COMPILE__)
#define NOPK __attribute__((target("no-packed-fp32-ops")))
#else
#define NOPK
#endif
#define DI __device__ __forceinline__
#define MFMA(a, b, c) __builtin_amdgcn_mfma_f32_32x32x16_bf16(a, b, c, 0, 0, 0)
#define MFMA16(a, b, c) __builtin_amdgcn_mfma_f32_16x16x32_bf16(a, b, c, 0, 0, 0)
#define BF8(x) __builtin_bit_cast(bf16x8, x)
#define U4(x) __builtin_bit_cast(u32x4, x)

constexpr int D = 448;
constexpr float EPS = 1e-5f;
constexpr unsigned WS_FLAG = 0;
constexpr unsigned FL_EPOCH = 0, FL_KV = 1, FL_RT = FL_KV + 256, FL_RD = FL_RT + 32, FL_CL = FL_RD + 32, FL_O = FL_CL + 32, FL_H = FL_O + 256, FL_G = FL_H + 256;
constexpr unsigned WS_KV = 1u << 20;
constexpr unsigned WS_QF = WS_KV + 4194304;
constexpr unsigned WS_OF = WS_QF + 2097152;
constexpr unsigned WS_HF = WS_OF + 2097152;
constexpr unsigned WS_GF = WS_HF + 64 * 28 * 1024;
constexpr unsigned WS_WGU = WS_GF + 64 * 112 * 1024;
constexpr unsigned WS_WD = WS_WGU + 112 * 28 * 1024;
constexpr unsigned WS_HS = WS_WD + 7 * 112 * 2 * 1024;
constexpr unsigned WS_END = WS_HS + 64 * 28 * 256 + 1024;
constexpr unsigned OOB = 0x40000000u;
constexpr int LDS_BYTES = 163840;

DI float u2f(unsigned u) { return __builtin_bit_cast(float, u); }
DI unsigned f2u(float f) { return __builtin_bit_cast(unsigned, f); }
DI unsigned umin(unsigned a, unsigned b) { return a < b ? a : b; }
DI unsigned umax3(unsigned a, unsigned b, unsigned c) { const unsigned m = a > b ? a : b; return m > c ? m : c; }
DI bool wave_any(bool p) { return __builtin_amdgcn_ballot_w64(p) != 0; }
DI bool wave_all(bool p) { return __builtin_amdgcn_ballot_w64(!p) == 0; }
DI unsigned cvtpk(float lo, float hi) { return __builtin_bit_cast(unsigned, __builtin_convertvector((f32x2){lo, hi}, bf16x2)); }
DI float bflo(unsigned u) { return u2f(u << 16); }
DI float bfhi(unsigned u) { return u2f(u & 0xffff0000u); }
DI void glds(unsigned voff, const void* sbase, unsigned dst) {
    asm volatile("s_mov_b32 m0, %2\n\ts_nop 0\n\tglobal_load_lds_dwordx4 %0, %1" :: "v"(voff), "s"(sbase), "s"(dst) : "memory");
}
DI void glds_dev(unsigned voff, const void* sbase, unsigned dst) {
    asm volatile("s_mov_b32 m0, %2\n\ts_nop 0\n\tglobal_load_lds_dwordx4 %0, %1 sc1" :: "v"(voff), "s"(sbase), "s"(dst) : "memory");
}
DI void glds2_dev(unsigned voff, const void* sbase, unsigned dst) {
    asm volatile("s_mov_b32 m0, %2\n\ts_nop 0\n\tglobal_load_lds_dwordx4 %0, %1 sc1\n\tglobal_load_lds_dwordx4 %0, %1 offset:1024 sc1" :: "v"(voff), "s"(sbase), "s"(dst) : "memory");
}
DI void glds4_dev(unsigned voff, const void* sbase, unsigned dst) {
    asm volatile("s_mov_b32 m0, %2\n\ts_nop 0\n\tglobal_load_lds_dwordx4 %0, %1 sc1\n\tglobal_load_lds_dwordx4 %0, %1 offset:1024 sc1\n\t"
                 "global_load_lds_dwordx4 %0, %1 offset:2048 sc1\n\tglobal_load_lds_dwordx4 %0, %1 offset:3072 sc1" :: "v"(voff), "s"(sbase), "s"(dst) : "memory");
}
DI s16x4 trd(lds_ptr p) { return __builtin_bit_cast(s16x4, __builtin_amdgcn_ds_read_tr16_b64_v4i16((LAS v4i16_t*)p)); }
DI bf16x8 cat(s16x4 a, s16x4 b) { return __builtin_shufflevector(a, b, 0, 1, 2, 3, 4, 5, 6, 7); }
DI bf16x8 lds8(lds_ptr p) { return *(LAS bf16x8*)p; }
DI float swap_add(float v) { auto r = __builtin_amdgcn_permlane32_swap(f2u(v), f2u(v), false, false); return u2f(r[0]) + u2f(r[1]); }
DI float swap_max(float v) { auto r = __builtin_amdgcn_permlane32_swap(f2u(v), f2u(v), false, false); return __builtin_fmaxf(u2f(r[0]), u2f(r[1])); }
DI u32x4 pack8(const f32x16& a, int s) { return (u32x4){cvtpk(a[8 * s], a[8 * s + 1]), cvtpk(a[8 * s + 2], a[8 * s + 3]), cvtpk(a[8 * s + 4], a[8 * s + 5]), cvtpk(a[8 * s + 6], a[8 * s + 7])}; }
DI float dot2(unsigned a, unsigned b, float c) { return __builtin_amdgcn_fdot2_f32_bf16(__builtin_bit_cast(bf16x2, a), __builtin_bit_cast(bf16x2, b), c, false); }
DI void acc_stats(u32x4 u, float& s1, float& s2) {
#pragma unroll
    for (int i = 0; i < 4; ++i) { const float a = bflo(u[i]), b = bfhi(u[i]); s1 += a; s2 = __builtin_fmaf(a, a, s2); s1 += b; s2 = __builtin_fmaf(b, b, s2); }
}
DI void acc_stats2(unsigned u0, unsigned u1, f32x4& s) {
    const float a = bflo(u0), b = bfhi(u0), c = bflo(u1), d = bfhi(u1);
    s[0] += a; s[1] += b; s[2] = __builtin_fmaf(a, a, s[2]); s[3] = __builtin_fmaf(b, b, s[3]); s[0] += c; s[1] += d; s[2] = __builtin_fmaf(c, c, s[2]); s[3] = __builtin_fmaf(d, d, s[3]);
}
DI float sum8(float v) {
    v += u2f(__builtin_amdgcn_update_dpp(0u, f2u(v), 0xB1, 0xf, 0xf, true));
    v += u2f(__builtin_amdgcn_update_dpp(0u, f2u(v), 0x4E, 0xf, 0xf, true));
    v += u2f(__builtin_amdgcn_update_dpp(0u, f2u(v), 0x141, 0xf, 0xf, true));
    return v;
}
DI u32x4 shift8(u32x4 x, float c) {
    u32x4 o; float t0, t1, t2, t3, t4, t5, t6, t7;
    asm("v_dot2_f32_bf16 %4, %12, %16, %18\n\tv_dot2_f32_bf16 %5, %12, %17, %18\n\tv_dot2_f32_bf16 %6, %13, %16, %18\n\tv_dot2_f32_bf16 %7, %13, %17, %18\n\t"
        "v_dot2_f32_bf16 %8, %14, %16, %18\n\tv_dot2_f32_bf16 %9, %14, %17, %18\n\tv_dot2_f32_bf16 %10, %15, %16, %18\n\tv_dot2_f32_bf16 %11, %15, %17, %18\n\t"
        "v_cvt_pk_bf16_f32 %0, %4, %5\n\tv_cvt_pk_bf16_f32 %1, %6, %7\n\tv_cvt_pk_bf16_f32 %2, %8, %9\n\tv_cvt_pk_bf16_f32 %3, %10, %11"
        : "=&v"(o[0]), "=&v"(o[1]), "=&v"(o[2]), "=&v"(o[3]), "=&v"(t0), "=&v"(t1), "=&v"(t2), "=&v"(t3), "=&v"(t4), "=&v"(t5), "=&v"(t6), "=&v"(t7)
        : "v"(x[0]), "v"(x[1]), "v"(x[2]), "v"(x[3]), "s"(0x00003f80u), "s"(0x3f800000u), "v"(c));
    return o;
}
template <int N> struct IC { static constexpr int v = N; };
template <int B, int E, class F> DI void sfor(F&& f) { if constexpr (B < E) { f(IC<B>{}); sfor<B + 1, E>(f); } }
template <int N> DI void waitv() { asm volatile("s_waitcnt vmcnt(%0)" :: "i"(N) : "memory"); }
template <int N> DI void waitv_bar() { if (N == 0) __builtin_amdgcn_s_waitcnt(0x0F70); asm volatile("s_waitcnt vmcnt(%0) lgkmcnt(0)\n\ts_barrier" :: "i"(N) : "memory"); }
template <int N> DI void waitv_barx() { asm volatile("s_waitcnt vmcnt(%0)\n\ts_barrier" :: "i"(N) : "memory"); }
template <int N> DI void waitv_bar2() { asm volatile("s_waitcnt vmcnt(%0) lgkmcnt(2)\n\ts_barrier" :: "i"(N) : "memory"); }
DI void touch(unsigned voff, const void* sbase, unsigned junk) {
    asm volatile("s_mov_b32 m0, %2\n\ts_nop 0\n\tglobal_load_lds_dword %0, %1 sc1" :: "v"(voff), "s"(sbase), "s"(junk) : "memory");
}
#define BAR() asm volatile("s_waitcnt lgkmcnt(0)\n\ts_barrier" ::: "memory")
#define FENCE() asm volatile("" ::: "memory")
typedef __amdgpu_buffer_rsrc_t rsrc_t;
DI u32x4 ld16(rsrc_t rs, unsigned voff, unsigned soff) { return U4(__builtin_amdgcn_raw_buffer_load_b128(rs, voff, soff, 0)); }
DI u32x4 ld16_dev(rsrc_t rs, unsigned voff, unsigned soff) { return U4(__builtin_amdgcn_raw_buffer_load_b128(rs, voff, soff, 16)); }
DI u32x2 ld8(rsrc_t rs, unsigned voff, unsigned soff) { return __builtin_bit_cast(u32x2, __builtin_amdgcn_raw_buffer_load_b64(rs, voff, soff, 0)); }
DI u32x2 ld8_dev(rsrc_t rs, unsigned voff, unsigned soff) { return __builtin_bit_cast(u32x2, __builtin_amdgcn_raw_buffer_load_b64(rs, voff, soff, 16)); }
DI void st16_dev(u32x4 v, rsrc_t rs, unsigned voff, unsigned soff) { __builtin_amdgcn_raw_buffer_store_b128(v, rs, voff + soff, 0, 16); }
DI void st8_dev(u32x2 v, rsrc_t rs, unsigned voff, unsigned soff) { __builtin_amdgcn_raw_buffer_store_b64(v, rs, voff, soff, 16); }
#define POLL_SLEEP 1
DI void set_flag(char* ws, unsigned fo, u64 tagv) {
    asm volatile("s_mov_b64 exec, 1\n\tglobal_store_dwordx2 %0, %1, %2 sc1\n\ts_mov_b64 exec, -1" :: "v"(fo), "v"(tagv), "s"(ws) : "memory");
}
DI u64 peek_flag(char* ws, unsigned fo) { return __hip_atomic_load((GAS u64*)(ws + fo), __ATOMIC_RELAXED, __HIP_MEMORY_SCOPE_AGENT); }
DI void peek_lds(char* ws, unsigned fo, unsigned dst) {
    asm volatile("s_mov_b32 m0, %2\n\ts_nop 0\n\tglobal_load_lds_dword %0, %1 sc1\n\ts_add_u32 m0, %2, 252\n\ts_nop 0\n\tglobal_load_lds_dword %0, %1 offset:4 sc1" :: "v"(fo), "s"(ws), "s"(dst) : "memory", "scc");
}
DI u64 peeked(lds_ptr p) { return (u64)*(LAS unsigned*)(p + 256) << 32 | *(LAS unsigned*)p; }
DI void poll_flag(char* ws, unsigned fo, u64 tag, u64 lanes = 0xffff) {
#pragma unroll 1
    for (int spin = 0; spin < (1 << 16); ++spin) {
        if (wave_all(peek_flag(ws, fo) == tag)) break;
        __builtin_amdgcn_s_sleep(POLL_SLEEP);
    }
    FENCE();
}
#define SB() __builtin_amdgcn_sched_barrier(0)
#define PINS(a) asm volatile("" : "+s"(a))
#define PIN(a) asm volatile("" : "+v"(a))
#define PIN2(a, b) asm volatile("" : "+v"(a), "+v"(b))
DI lds_ptr opq(lds_ptr p) { unsigned u = (unsigned)(uintptr_t)p; asm volatile("" : "+v"(u)); return (lds_ptr)(uintptr_t)u; }
#define TS(i) do {} while (0)

DI constexpr int wd_slot(int s) { return (s * 17 + 27) / 28 != ((s + 1) * 17 + 27) / 28 ? (s * 17 + 27) / 28 : -1; }
#define EROT 0
#define CLR_SLEEP 90
#define ROTMODE 0
#define EARLYG 0
#define GWG 1
#define GW 8
#define CLRMODE 0
#define DLY1 0
#define DLY2 0
#define DLY3 0
#define P3 1
#define VARLATE 0
#define CST 0
#define SPDLY 20
#define ENL 4
#define HOP3_POLLER 1
DI constexpr int d_prio(int n) { return n < 0 ? -1 : n < 27 ? 3 - n / 7 : n < 64 ? 3 - (n - 27) / 10 : n < 84 ? 3 - (n - 64) / 5 : n < 112 ? 3 - (n - 84) / 7 : 0; }
DI constexpr int phys(int t) { return ((t % 9) + 2) % 9 * 16384; }

__global__ NOPK __launch_bounds__(512, 2) void block_fwd(const char* x, const char* wqkv, const char* wo, const char* wg, const char* wu, const char* wd, char* out, char* ws) {
    extern __shared__ __attribute__((aligned(16))) char smem[];
    lds_ptr lds = (lds_ptr)smem;
    const int tid = __builtin_amdgcn_workitem_id_x(), lane = tid & 63, w = __builtin_amdgcn_readfirstlane(tid >> 6);
    const int bid = __builtin_amdgcn_workgroup_id_x(), xcd = bid & 7, loc = bid >> 3;
    const int r = lane & 31, hf = lane >> 5, q4 = (lane & 15) >> 2, g1 = (lane >> 4) & 1, p4 = lane & 3;
    const unsigned lds0 = (unsigned)(uintptr_t)smem;
    const unsigned l16 = lane * 16;
    TS(0);
    const int sp3 = (loc + 8) >> 5;
    const int atb = (xcd >> 1) * 8 + (loc & 7) + sp3 * (xcd & 1) * 4, acg = (loc >> 3) + (1 - sp3) * (xcd & 1) * 4;
    const int row8 = lane >> 3;
    const int rot8 = ((loc & 7) << sp3) + sp3 * (xcd & 1), rot = rot8 == 7 ? 0 : rot8;
    if (loc < 28) {
        const unsigned xo = (unsigned)((atb * 64 + (tid >> 3)) * 896 + (((tid ^ (tid >> 4)) & 7) * 16));
        const unsigned xd0 = lds0 + 1024 + (w >> 2) * 28672 + (w & 3) * 1024;
        unsigned xd = xd0 + (7 - rot) * 4096;
        const unsigned w0 = (unsigned)((rot * 64 + (tid >> 3)) * 2688 + acg * 128 + (((tid & 7) ^ ((tid >> 2) & 4)) * 16));
        asm volatile("s_cmp_eq_u32 %0, %3\n\ts_cselect_b32 %0, %2, %0\n\ts_mov_b32 m0, %0\n\ts_add_u32 %0, %0, 0x1000\n\tglobal_load_lds_dwordx4 %1, %4\n\t"
#define XP(o) "s_cmp_eq_u32 %0, %3\n\ts_cselect_b32 %0, %2, %0\n\ts_sub_u32 m0, %0, " #o "\n\ts_add_u32 %0, %0, 0x1000\n\tglobal_load_lds_dwordx4 %1, %4 offset:" #o "\n\t"
                     XP(128) XP(256) XP(384) XP(512) XP(640) XP(768)
                     "s_add_u32 m0, %6, 8192-896\n\ts_barrier\n\tglobal_load_lds_dwordx4 %5, %7 offset:896\n\t"
                     "s_add_u32 m0, %6, 16384-1792\n\ts_nop 0\n\tglobal_load_lds_dwordx4 %5, %7 offset:1792\n\t"
                     "s_mov_b32 m0, %6\n\ts_nop 0\n\tglobal_load_lds_dwordx4 %5, %7"
                     : "+s"(xd) : "v"(xo), "s"(xd0), "s"(xd0 + 7 * 4096), "s"(x), "v"(w0), "s"(lds0 + 58368 + w * 1024), "s"(wqkv) : "memory", "scc");
    }
    SB();
    const rsrc_t rws = __builtin_amdgcn_make_buffer_rsrc((void*)ws, 0, WS_END, 0x00020000);
    u64 epoch, tagv;
    unsigned fl1;
    const unsigned ones = 0x3f803f80u;
    const bool ffn = loc < 28;
    const int l7 = loc / 7, m7 = loc % 7;
    const int tb = xcd * 4 + l7;
    const int tt = w & 1, ct = (w >> 1) & 1, kh = w >> 2;
    const unsigned swz = (unsigned)(((lane & 7) ^ (((row8 >> 1) & 1) << 2)) * 16);

    if (__builtin_expect(ffn, 1)) {
        constexpr int A_W = 58368, A_WS = 24576;
#define A_KC(c) (rot + (c) >= 7 ? rot + (c) - 7 : rot + (c))
        const unsigned wvo = (unsigned)(row8 * 2688) + swz;
        const char* wsrc = wqkv + acg * 128 + (size_t)w * 8 * 2688;
        const unsigned wdst = lds0 + A_W + w * 1024;
#define A_PIECE(c, i) glds(wvo, wsrc + A_KC(c) * (64 * 2688) + (i) * 896, wdst + ((c) & 3) * A_WS + (i) * 8192)
#define A_GROUP(c) do { A_PIECE(c, 1); A_PIECE(c, 2); A_PIECE(c, 0); } while (0)
        epoch = peek_flag(ws, FL_EPOCH * 128); FENCE(); fl1 = (FL_KV + atb * 8 + acg) * 128;
        if (EARLYG) A_GROUP(1);
        lds_ptr xb[4], wb1, wb1h;
#pragma unroll
        for (int j = 0; j < 4; ++j) xb[j] = lds + 1024 + tt * 28672 + (r >> 3) * 1024 + (r & 7) * 128 + (((2 * j + hf) ^ ((r >> 1) & 7)) * 16);
        wb1 = opq(lds + A_W + (kh ? 0 : 8192) + (8 * hf + q4) * 128 + ((ct ^ (q4 >> 1)) * 64) + g1 * 32 + p4 * 8);
        wb1h = opq(wb1 + 2 * A_WS);
        unsigned go1[2], go2[2]; int gi[2];
#pragma unroll
        for (int p = 0; p < 2; ++p) {
            const int G = acg * 8 + ct * 4 + 2 * p + hf, hp = G / 7, i = G - hp * 7;
            const unsigned hs = kh ? 131072u : 262144u;
            const unsigned to = (kh ? WS_QF + (atb * 2 + tt) * 2048 : WS_KV + (atb * 2 + tt) * 4096) + r * 16;
            go1[p] = (unsigned)(2 * hp + (i > 3)) * hs + (unsigned)(i > 3 ? i - 3 : i) * 512 + to;
            go2[p] = i == 3 ? (unsigned)(2 * hp + 1) * hs + to : OOB;
            gi[p] = i;
        }
        const int vcol = acg * 64 + ct * 32 + r, vh = vcol / 28, vd = vcol - vh * 28;
        unsigned vo = WS_KV + (atb * 2 + tt) * 4096 + (unsigned)vh * 262144 + 2048 + hf * 512 + vd * 16;
        PIN2(go1[0], go1[1]); PIN2(go2[0], go2[1]); PIN(vo);
        lds_ptr xw = opq(lds + 1024 + kh * 28672 + (w & 3) * 1024 + l16);
        waitv<EARLYG ? 7 : 4>();
        TS(28);
        u32x4 xv[7];
#pragma unroll
        for (int c = 0; c < 7; ++c) xv[c] = *(LAS u32x4*)(xw + c * 4096);
        SB();
        f32x4 sm = {};
#pragma unroll
        for (int c = 0; c < 7; ++c)
#pragma unroll
            for (int j = 0; j < 4; ++j) sm[j] = dot2(xv[c][j], ones, sm[j]);
        float nm = sum8((sm[0] + sm[1]) + (sm[2] + sm[3])) * (-1.f / D);
        PIN(nm);
#pragma unroll
        for (int c = 0; c < 7; ++c) { xv[c] = shift8(xv[c], nm); *(LAS u32x4*)(xw + c * 4096) = xv[c]; }
        if (EARLYG) A_GROUP(2); else A_GROUP(1);
        SB();
        LAS float* rst = (LAS float*)lds;
#define A_VAR() do { f32x4 sq = {}; _Pragma("unroll") for (int c_ = 0; c_ < 7; ++c_) _Pragma("unroll") for (int j = 0; j < 4; ++j) sq[j] = dot2(xv[c_][j], xv[c_][j], sq[j]); \
            rst[w * 8 + row8] = __builtin_amdgcn_rsqf(sum8((sq[0] + sq[1]) + (sq[2] + sq[3])) * (1.f / D) + EPS); } while (0)
        if (!VARLATE) A_VAR();
        if (EARLYG) A_GROUP(3); else A_GROUP(2);
        TS(31);
        bf16x8 fx[4], f1[4], f2[4];
#define A_WAIT(c) do { if ((c) == 0) waitv_bar<EARLYG ? 9 : 6>(); else if ((c) == 1) waitv_barx<6>(); else if ((c) < 6) waitv_barx<3>(); else waitv_barx<0>(); \
                       if ((c) == 0) TS(1); if ((c) == 6) TS(2); } while (0)
#define A_WP(c, ks) ((((c) & 2) ? wb1h : wb1) + ((c) & 1) * A_WS + (ks) * 2048)
        if (w < 4) {
            f32x16 ak = {}, av = {};
            f32x4 rv[4];
            float rs = 0.f;
#pragma unroll
            for (int n = 0; n < 30; ++n) {
                const int c = n >> 2, ks = n & 3, km = (n - 2) & 3;
                if (n < 28 && ks == 0) A_WAIT(c);
                if (n >= 2) { ak = MFMA(f1[km], fx[km], ak); PIN(ak); } SB();
                if (n < 28) { fx[ks] = lds8(xb[ks] + c * 4096); f1[ks] = cat(trd(A_WP(c, ks)), trd(A_WP(c, ks) + 512)); } SB();
                if (n >= 2) { av = MFMA(fx[km], f2[km], av); PIN(av); } SB();
                if (n < 28) f2[ks] = cat(trd(A_WP(c, ks) + 8192), trd(A_WP(c, ks) + 8192 + 512));
                if (!EARLYG && c == 0 && ks < 3) A_PIECE(3, ks); if (c >= 2 && c < 5 && ks < 3) A_PIECE(c + 2, ks);
                if (VARLATE && n == 1) { SB(); A_VAR(); }
                if (n == 21) {
                    rs = rst[tt * 32 + r];
#pragma unroll
                    for (int g = 0; g < 4; ++g) rv[g] = *(LAS f32x4*)(rst + tt * 32 + 8 * g + 4 * hf);
                }
                if (n == 24) { PIN(epoch); tagv = epoch + 1; PIN(tagv); }
                if (n == 24) {
                    const unsigned pv = vd == 0 ? ones : 0u, po = vd < 4 ? vo + 448 : OOB;
                    st16_dev((u32x4){pv, pv, pv, pv}, rws, po, 0); st16_dev((u32x4){pv, pv, pv, pv}, rws, po + 1024, 0);
                }
                SB();
            }
            TS(3);
#pragma unroll
            for (int i = 0; i < 16; ++i) { ak[i] *= rs; av[i] *= rv[i >> 2][i & 3]; }
#pragma unroll
            for (int p = 0; p < 2; ++p) {
                const u32x4 f = pack8(ak, p);
                const auto s0 = __builtin_amdgcn_permlane32_swap(f[0], f[2], false, false), s1 = __builtin_amdgcn_permlane32_swap(f[1], f[3], false, false);
                const u32x4 c = {s0[0], s1[0], s0[1], s1[1]};
                st16_dev(c, rws, go1[p], 0); st16_dev(c, rws, go2[p], 0);
            }
            st16_dev(pack8(av, 0), rws, vo, 0); st16_dev(pack8(av, 1), rws, vo + 1024, 0);
        } else {
            f32x16 aq = {};
            __builtin_amdgcn_s_setprio(1);
#pragma unroll
            for (int n = 0; n < 30; ++n) {
                const int c = n >> 2, ks = n & 3, km = (n - 2) & 3;
                if (n < 28 && ks == 0) A_WAIT(c);
                if (n == 24) { PIN(epoch); tagv = epoch + 1; PIN(tagv); }
                if (n >= 2) { aq = MFMA(f1[km], fx[km], aq); PIN(aq); } SB();
                if (n < 28) { fx[ks] = lds8(xb[ks] + c * 4096); f1[ks] = cat(trd(A_WP(c, ks)), trd(A_WP(c, ks) + 512)); }
                if (!EARLYG && c == 0 && ks < 3) A_PIECE(3, ks); if (c >= 2 && c < 5 && ks < 3) A_PIECE(c + 2, ks);
                if (VARLATE && n == 1) { SB(); A_VAR(); }
                SB();
            }
            __builtin_amdgcn_s_setprio(0);
            const float QS = rst[tt * 32 + r] * (0.18898223650461362f * 1.4426950408889634f);
#pragma unroll
            for (int i = 0; i < 16; ++i) aq[i] *= QS;
#pragma unroll
            for (int p = 0; p < 2; ++p) {
                const u32x4 f = pack8(aq, p);
                const auto s0 = __builtin_amdgcn_permlane32_swap(f[0], f[2], false, false), s1 = __builtin_amdgcn_permlane32_swap(f[1], f[3], false, false);
                const bool sp = gi[p] == 3;
                st16_dev((u32x4){s0[0], s1[0], sp ? 0u : s0[1], sp ? 0u : s1[1]}, rws, go1[p], 0);
                st16_dev((u32x4){0u, 0u, s0[1], s1[1]}, rws, go2[p], 0);
            }
        }
    } else {
        if (SPDLY) __builtin_amdgcn_s_sleep(SPDLY);
        epoch = peek_flag(ws, FL_EPOCH * 128); fl1 = (FL_RT + xcd * 4 + loc - 28) * 128;
        const int ri = xcd * 4 + loc - 28, nu = (w == 0 && ri < 16) ? 4 : 3;
        const unsigned vo = (unsigned)(row8 * 3584 + (lane & 7) * 16);
#pragma unroll 1
        for (int i = 0; i < nu; ++i) {
            const int u = i < 3 ? i * 256 + ri * 8 + w : 768 + ri;
            const size_t so = (size_t)(u / 28) * 16 * 3584 + (u % 28) * 128;
            const unsigned d_ = lds0 + w * 16384 + i * 4096;
            glds(vo, wg + so, d_); glds(vo, wg + so + 8 * 3584, d_ + 1024); glds(vo, wu + so, d_ + 2048); glds(vo, wu + so + 8 * 3584, d_ + 3072);
        }
#pragma unroll 1
        for (int i = 0; i < nu; ++i) {
            const int u = i < 3 ? i * 256 + ri * 8 + w : 768 + ri;
            waitv<8>();
            lds_ptr sg = lds + w * 16384 + i * 4096 + g1 * 2048 + (4 * hf + q4) * 128 + p4 * 8;
#pragma unroll
            for (int e = 0; e < 4; ++e) st16_dev(U4(cat(trd(sg + e * 32), trd(sg + e * 32 + 1024))), rws, l16, WS_WGU + (unsigned)(((u % 28) * 4 + e) * 28 + u / 28) * 1024);
            FENCE();
        }
        if (CLRMODE >= 2) {
            const unsigned c_ = WS_GF + (unsigned)(((ri >> 1) * 4 + 2 + (ri & 1)) * 112 + w * 14) * 1024 + l16;
#pragma unroll 1
            for (int i = 0; i < (CLRMODE == 2 ? 7 : 14); ++i) st16_dev((u32x4){~0u, ~0u, ~0u, ~0u}, rws, c_ + i * 1024, 0);
        }
        tagv = epoch + 1;
    }
    TS(24);
    const int head = xcd * 2 + (loc & 1), rb = loc >> 1, qt = w & 3;
    const char* kvs = ws + WS_KV + (size_t)head * 262144 + w * 2048;
    const char* qs = ws + WS_QF + (size_t)(head * 64 + rb * 4 + qt) * 2048 + kh * 1024;
    unsigned kvd = lds0 + w * 2048, qd = lds0 + 147456 + qt * 2048 + kh * 1024;
    unsigned pf1 = (FL_KV + (lane >> 1) * 8 + (head * 28 + (lane & 1) * 27) / 64) * 128;
    PINS(kvs); PINS(qs); PINS(kvd); PINS(qd); PIN2(pf1, fl1);
    waitv_bar<0>();
    if (w == 0) set_flag(ws, fl1, tagv);
    const u64 tag = ((u64)(unsigned)__builtin_amdgcn_readfirstlane((unsigned)(tagv >> 32)) << 32 | (u64)(unsigned)__builtin_amdgcn_readfirstlane((unsigned)tagv));
    TS(4);

    const int drb = (xcd >> 1) * 4 + l7, dfg = (xcd & 1) * 7 + m7;
    bf16x8 wf[28];
    u32x2 xa, xb;
    f32x4 cs = {};
    f32x16 acc = {};
    const unsigned sel = ((lane & 15) == g1) ? ones : 0u;
    const u32x4 one4 = {sel, sel, sel, sel};
    const unsigned hi_ = (unsigned)((tb * 2 + tt) * 28 + m7 * 4 + ct * 2 + kh), ofo = (unsigned)(tt * 32768 + kh * 16384 + ct * 4096);
    unsigned ho = WS_HF + hi_ * 1024 + l16, so = hf ? OOB : WS_HS + hi_ * 256 + r * 8;
    const char* os = ws + WS_OF + (size_t)tb * 65536 + ofo;
    unsigned od = lds0 + 65536 + ofo;
    unsigned pf2 = (FL_O + (tb >> 1) * 16 + kh * 8 + ct * 2 + ((lane >> 1) & 1) + (lane & 4)) * 128 + ((lane & 1) * 4 + (tb & 1) * 2 + tt) * 8;
    pf2 &= ~127u;
    unsigned go = ffn ? WS_GF + (unsigned)(drb * 4 * 112 + dfg * 8 + w) * 1024 + l16 : OOB;
    unsigned fo3 = (FL_H + tb * 8 + m7) * 128;
    {
        lds_ptr ap = opq(lds + kh * 8192 + l16), apB = opq(lds + 65536 + kh * 8192 + l16), apC = opq(lds + 131072 + kh * 8192 + l16);
        const unsigned lv = kh * 8192 + (l16 ^ (kh * 256));
        lds_ptr vp = opq(lds + lv), vpB = opq(lds + 65536 + lv), vpC = opq(lds + 131072 + lv);
#define AP(off) ((off) < 65536 ? ap + (off) : (off) < 131072 ? apB + ((off) - 65536) : apC + ((off) - 131072))
#define VP(off) ((off) < 65536 ? vp + (off) : (off) < 131072 ? vpB + ((off) - 65536) : vpC + ((off) - 131072))
#define KV_ISSUE(t) glds2_dev(l16, kvs + (t) * 16384, kvd + phys(t))
        unsigned wgo = ffn ? WS_WGU + (unsigned)(dfg * 8 + w) * 28672 : OOB;
        unsigned clro = WS_GF + (unsigned)((((xcd * 4 + loc - 28) >> 1) * 4 + 2 + (loc & 1)) * 112 + w * 14) * 1024 + l16;
        u32x4 clv = {~0u, ~0u, ~0u, ~0u};
        if (CLRMODE == 1) { if (ffn) clro = OOB; PIN(clv); PIN(clro); }
        const int ri = umin(xcd * 4 + loc - 28, 27);
        const char* wos = (ffn ? wo + m7 * 128 : wd + (ri % 7) * 128 + (size_t)(ri / 7) * 448 * 896) + (size_t)w * 56 * 896;
        unsigned wov = (unsigned)(row8 * 896) + swz;
        const rsrc_t rx = __builtin_amdgcn_make_buffer_rsrc((void*)x, 0, 2048 * 448 * 2, 0x00020000);
        unsigned xo_ = (unsigned)(((tb * 2 + tt) * 32 + r) * 896 + m7 * 128 + ct * 64 + kh * 32 + hf * 8);
        unsigned rto = (FL_RT + (P3 ? w * 4 + (lane & 3) : r)) * 128;
        lds_ptr qp = opq(lds + 147456 + qt * 2048 + l16);
        PINS(wgo); PINS(wos); PIN2(wov, xo_); PIN(rto); PIN2(ho, so); PIN2(pf2, fo3); PIN(go);
        lds_ptr xw = opq(lds + 147456 + w * 2048 + l16), xr = opq(lds + 147456 + (w ^ 4) * 2048 + l16);
        lds_ptr lw = opq(lds + 58368 + w * 256 + lane * 4), lr = opq(lds + 58368 + qt * 256 + 128 + r * 4);
        lds_ptr bw = opq(lds + 60416 + w * 4), br = opq(lds + 60416 + (lane & 7) * 4);
        unsigned oo = WS_OF + (unsigned)((rb * 4 + qt) * 32 + head * 2 + kh) * 1024 + l16;
        unsigned fo2 = (FL_O + rb * 16 + head) * 128 + w * 8;
        PIN2(oo, fo2);
        if (w == HOP3_POLLER) { if (DLY1) __builtin_amdgcn_s_sleep(DLY1); poll_flag(ws, pf1, tag, ~0ull); }
        BAR();
        TS(5);
        glds_dev(l16, qs, qd);
        KV_ISSUE(0); KV_ISSUE(1);
        asm volatile("s_barrier" ::: "memory");
        KV_ISSUE(2); KV_ISSUE(3);
        waitv_bar<4>();
        TS(6);
        const bf16x8 q0 = lds8(qp), q1 = lds8(qp + 1024);
        f32x16 o = {}, sa = {}, sb;
        bf16x8 k0 = lds8(ap + phys(0)), k1 = lds8(ap + phys(0) + 1024), v0 = lds8(vp + phys(0) + 2048), v1 = {};
        u32x4 p0, p1 = {};
        sa = MFMA(k0, q0, sa); sa = MFMA(k1, q1, sa);
        k0 = lds8(ap + phys(0) + 4096); k1 = lds8(ap + phys(0) + 5120);
        PIN(sa); SB();
        u64 rtf = 0;
#define EXP4(S, o4) _Pragma("unroll") for (int i_ = o4; i_ < o4 + 4; ++i_) S[i_] = __builtin_amdgcn_exp2f(S[i_]);
#define WLD(s) wf[s] = BF8(ld16_dev(rws, l16 + ((s) & 3) * 1024, wgo + ((s) >> 2) * 4096))
#define SIDE(t, g) do { \
            if ((t) == 0 && (g) == 0) KV_ISSUE(4); if ((t) == 0 && (g) == 2) KV_ISSUE(5); if ((t) == 1 && (g) == 0) KV_ISSUE(6); if ((t) == 1 && (g) == 2) KV_ISSUE(7); \
            if ((t) == 2 && (g) == 0) KV_ISSUE(8); if (!P3 && (t) == 1 && (g) == 3) rtf = peek_flag(ws, rto); if (P3 && (t) == 1 && (g) == 1) peek_lds(ws, rto, lds0 + 155648 + w * 512); if (P3 && (t) == 2 && (g) == 2) { waitv<4>(); rtf = peeked(lds + 155648 + w * 512 + lane * 4); } \
            if ((t) == 3 && (g) < 3) KV_ISSUE(9 + (g)); \
            if (CLRMODE == 0 && (t) >= 4 && (t) <= 10 && ((g) & 1)) { if (__builtin_expect(!ffn, 0)) st16_dev((u32x4){~0u, ~0u, ~0u, ~0u}, rws, clro + (((t) - 4) * 2 + ((g) >> 1)) * 1024, 0); } \
            if (CLRMODE == 1 && (t) >= 4 && (t) <= 10 && ((g) & 1)) __builtin_amdgcn_raw_buffer_store_b128(clv, rws, clro, (((t) - 4) * 2 + ((g) >> 1)) * 1024, 16); \
            if (CLRMODE == 2 && (t) >= 11 && !((g) & 1) && ((t) - 11) * 2 + (g) / 2 < 7) { if (__builtin_expect(!ffn, 0)) st16_dev((u32x4){~0u, ~0u, ~0u, ~0u}, rws, clro + (7 + ((t) - 11) * 2 + (g) / 2) * 1024, 0); } \
            if ((t) == 7 && (g) == 0) KV_ISSUE(12); if ((t) == 7 && (g) == 2) KV_ISSUE(13); if ((t) == 8 && (g) == 0) KV_ISSUE(14); if ((t) == 8 && (g) == 2) KV_ISSUE(15); \
            if ((t) >= 4 && (t) <= 6) WLD(((t) - 4) * 4 + (g)); \
            if (((t) == 7 || (t) == 8) && ((g) & 1)) WLD(12 + ((t) - 7) * 2 + ((g) >> 1)); \
            if ((t) >= 9 && (t) <= 11) WLD(((t) - 5) * 4 + (g)); \
            if ((t) == 12 || ((t) == 13 && (g) < 3)) glds(wov, wos + (((t) - 12) * 4 + (g)) * 8 * 896, lds0 + (w * 7 + ((t) - 12) * 4 + (g)) * 1024); \
            if ((t) == 13 && (g) == 3) { xa = ld8(rx, xo_, 0); xb = ld8(rx, xo_, 16); } \
            if ((t) == 14 && (g) == 1) { if (w >= 6) touch(lane * (w == 6 ? (WS_END / 64 & ~127u) : 28672), w == 6 ? ws : out, lds0 + 61440); } \
            if ((t) == 14 && (g) == 0) { if (tid < 32) *(LAS u32x4*)(lds + 57344 + tid * 16) = (u32x4){0, 0, 0, 0}; } \
        } while (0)
#define TOFF(i) (phys((i) >> 1) + ((i) & 1) * 4096)
#define TILE(i, SC, SN) do { \
            __builtin_amdgcn_s_setprio(((i) & 1) ? 1 : 3); \
            if ((i) < 31) { f32x16 z_ = {}; SN = MFMA(k0, q0, z_); PIN(SN); } if ((i) < 30) k0 = lds8(AP(TOFF((i) + 2))); SB(); \
            EXP4(SC, 0); PIN(SC); SB(); \
            o = MFMA(v1, BF8(p1), o); PIN(o); v1 = lds8(VP(TOFF(i) + 3072)); SIDE((i) >> 1, ((i) & 1) * 2); SB(); \
            EXP4(SC, 4); p0 = pack8(SC, 0); PIN(p0); SB(); \
            __builtin_amdgcn_s_setprio(((i) & 1) ? 0 : 2); \
            if ((i) < 31) { SN = MFMA(k1, q1, SN); PIN(SN); } if ((i) < 30) k1 = lds8(AP(TOFF((i) + 2) + 1024)); SB(); \
            EXP4(SC, 8); PIN(SC); SB(); \
            o = MFMA(v0, BF8(p0), o); PIN(o); if ((i) < 31) v0 = lds8(VP(TOFF((i) + 1) + 2048)); SIDE((i) >> 1, ((i) & 1) * 2 + 1); SB(); \
            EXP4(SC, 12); p1 = pack8(SC, 1); PIN(p1); SB(); } while (0)
#define STEP(t) do { TILE(2 * (t), sa, sb); TILE(2 * (t) + 1, sb, sa); } while (0)
        STEP(0);
        waitv_barx<4>();
        STEP(1); STEP(2);
        if (P3 && __builtin_expect(!wave_all(rtf == tag), 0)) poll_flag(ws, rto, tag, 0xffffffffull);
        waitv_bar2<P3 ? 2 : 3>();
        TS(21);
        if (!P3 && __builtin_expect(!wave_all(rtf == tag), 0)) poll_flag(ws, rto, tag, 0xffffffffull);
        FENCE();
        STEP(3); STEP(4); STEP(5); STEP(6);
        waitv_bar2<CLRMODE == 1 ? 18 : 12>();
        TS(22);
        STEP(7); STEP(8); STEP(9); STEP(10);
        waitv_bar2<CLRMODE == 1 ? 14 : 9>();
        if (CLRMODE == 1) PIN(clv);
        TS(23);
        STEP(11); STEP(12); STEP(13); STEP(14); STEP(15);
        o = MFMA(v1, BF8(p1), o);
        TS(7);
        u32x4 ov;
        {
            *(LAS f32x4*)xw = (f32x4){o[8], o[9], o[10], o[11]}; *(LAS f32x4*)(xw + 1024) = (f32x4){o[12], o[13], o[14], o[15]};
            const float l = kh ? o[4] : o[12];
            *(LAS float*)lw = l;
#define AMX(a, b, c) __builtin_fmaxf(__builtin_fmaxf(__builtin_fabsf(a), __builtin_fabsf(b)), __builtin_fabsf(c))
            const float mx_ = AMX(AMX(AMX(o[0], o[1], o[2]), AMX(o[3], o[4], o[5]), AMX(o[6], o[7], o[8])), AMX(AMX(o[9], o[10], o[11]), AMX(o[12], o[13], o[14]), o[15]), 0.f);
            const bool ok = (mx_ < 0x1p100f) & ((hf ? l : 1.f) > 0x1p-80f);
            *(LAS unsigned*)bw = wave_all(ok) ? 0u : 1u;
            BAR();
            const f32x4 a = *(LAS f32x4*)xr, c = *(LAS f32x4*)(xr + 1024);
            const float inv = __builtin_amdgcn_rcpf(*(LAS float*)lr + *(LAS float*)(lr + 1024));
            const unsigned bad = *(LAS unsigned*)br;
            const float iz = (kh && hf) ? 0.f : inv;
            ov = (u32x4){cvtpk((o[0] + a[0]) * inv, (o[1] + a[1]) * inv), cvtpk((o[2] + a[2]) * inv, (o[3] + a[3]) * inv),
                         cvtpk((o[4] + c[0]) * iz, (o[5] + c[1]) * iz), cvtpk((o[6] + c[2]) * iz, (o[7] + c[3]) * iz)};
            if (__builtin_expect(wave_any(bad != 0), 0)) {
                f32x16 of = {};
                float m = -0x1p126f;
#pragma unroll 1
                for (int t = 0; t < 16; ++t) {
                    BAR();
                    glds2_dev(l16, kvs + t * 16384, lds0 + 65536 + w * 2048);
                    waitv_bar<0>();
#pragma unroll 1
                    for (int u = 0; u < 2; ++u) {
                        f32x16 s = {};
                        s = MFMA(lds8(apB + u * 4096), q0, s); s = MFMA(lds8(apB + u * 4096 + 1024), q1, s);
                        float mx = s[0];
#pragma unroll
                        for (int i = 1; i < 16; ++i) mx = __builtin_fmaxf(mx, s[i]);
                        mx = __builtin_fmaxf(swap_max(mx), m);
                        const float alpha = __builtin_amdgcn_exp2f(m - mx);
                        m = mx;
#pragma unroll
                        for (int i = 0; i < 16; ++i) { s[i] = __builtin_amdgcn_exp2f(s[i] - mx); of[i] *= alpha; }
                        of = MFMA(lds8(vpB + u * 4096 + 2048), BF8(pack8(s, 0)), of);
                        of = MFMA(lds8(vpB + u * 4096 + 3072), BF8(pack8(s, 1)), of);
                    }
                }
                *(LAS f32x4*)xw = (f32x4){of[8], of[9], of[10], of[11]}; *(LAS f32x4*)(xw + 1024) = (f32x4){of[12], of[13], of[14], of[15]};
                *(LAS float*)lw = hf ? (kh ? of[4] : of[12]) : m;
                BAR();
                const f32x4 a2 = *(LAS f32x4*)xr, c2 = *(LAS f32x4*)(xr + 1024);
                const float m0 = *(LAS float*)(lr - 128), m1 = *(LAS float*)(lr + 1024 - 128), mm = __builtin_fmaxf(m0, m1);
                const float e0 = __builtin_amdgcn_exp2f(m0 - mm), e1 = __builtin_amdgcn_exp2f(m1 - mm);
                const float iv = __builtin_amdgcn_rcpf(*(LAS float*)lr * e0 + *(LAS float*)(lr + 1024) * e1);
                const float im = (kh ? e1 : e0) * iv, io = (kh ? e0 : e1) * iv;
                const bool z = kh && hf;
                ov = (u32x4){cvtpk(of[0] * im + a2[0] * io, of[1] * im + a2[1] * io), cvtpk(of[2] * im + a2[2] * io, of[3] * im + a2[3] * io),
                             z ? 0u : cvtpk(of[4] * im + c2[0] * io, of[5] * im + c2[1] * io), z ? 0u : cvtpk(of[6] * im + c2[2] * io, of[7] * im + c2[3] * io)};
            }
        }
        st16_dev(ov, rws, oo, 0);
        TS(25);
        FENCE();
        PIN2(xa, xb);
#pragma unroll
        for (int s = 27; s >= 0; --s) PIN(wf[s]);
#pragma unroll
        for (int s = 0; s < 16; ++s) cs = MFMA16(BF8(one4), wf[s], cs);
        acc[0] = bflo(xa[0]); acc[1] = bfhi(xa[0]); acc[2] = bflo(xa[1]); acc[3] = bfhi(xa[1]); acc[4] = bflo(xb[0]); acc[5] = bfhi(xb[0]); acc[6] = bflo(xb[1]); acc[7] = bfhi(xb[1]);
        PIN(acc); PIN2(ho, so); PIN2(pf2, fo3); PINS(os); PINS(od); PIN(cs);
        waitv_bar<0>();
        if (w == 0) set_flag(ws, fo2, tagv);
        TS(8);
    }
    if (__builtin_expect(!ffn, 0)) {
        const int ri = xcd * 4 + loc - 28;
        if (ri < 28) {
#pragma unroll 1
            for (int u = w; u < 28; u += 8) {
                lds_ptr sd = lds + u * 2048 + (4 * hf + q4) * 128 + g1 * 32 + p4 * 8;
#pragma unroll
                for (int e = 0; e < 2; ++e) { lds_ptr p_ = sd + ((e ^ (q4 >> 1)) * 64);
                    st16_dev(U4(cat(trd(p_), trd(p_ + 1024))), rws, l16, WS_WD + (unsigned)(((ri % 7) * 112 + (ri / 7) * 28 + u) * 2 + e) * 1024); }
            }
        }
        waitv_bar<0>();
        if (w == 0) set_flag(ws, (FL_RD + ri) * 128, tagv);
        return;
    }

    const int erb = tb >> 1;
    const char* hs = ws + WS_HF + (size_t)drb * 114688 + w * 1024;
    const char* hss = ws + WS_HS + (size_t)((drb * 4 + (w & 3)) * 28 + kh * 14) * 256;
    unsigned pf3, fo4 = (FL_G + drb * 64 + dfg) * 128 + w * 8;
    {
        lds_ptr wb = lds + kh * (8 * 28 * 128) + (4 * hf + q4) * 128 + ((ct ^ (q4 >> 1)) * 64) + (g1 ^ kh) * 32 + p4 * 8;
        bf16x8 wr[16];
#pragma unroll
        for (int s = 0; s < 12; ++s) { const int k0 = ((s >> 1) * 28 + (s & 1) * 16) * 128; wr[s] = cat(trd(wb + k0), trd(wb + k0 + 1024)); }
        lds_ptr cw = opq(lds + 131072 + w * 2048 + l16), cr = opq(lds + 131072 + (w ^ 4) * 2048 + l16);
        lds_ptr ob = opq(lds + 65536 + tt * 32768 + kh * 16384 + l16);
        pf3 = (FL_H + drb * 16 + (lane & 8) + umin(lane & 7, 6)) * 128;
        PIN2(pf3, fo4); PINS(hs); PINS(hss);
        if (DLY2) __builtin_amdgcn_s_sleep(DLY2);
        poll_flag(ws, pf2, tag, 0x55);
        TS(9);
        glds4_dev(l16, os, od);
        if (!CST) asm volatile("s_barrier" ::: "memory");
        glds4_dev(l16, os + 8192, od + 8192);
        bf16x8 of[8];
        waitv_bar<CST ? 0 : 4>();
        TS(26);
#pragma unroll
        for (int s = 0; s < 8; ++s) of[s] = lds8(ob + s * 1024);
        SB();
#pragma unroll
        for (int s = 0; s < 8; ++s) {
            acc = MFMA(wr[s], of[s], acc); PIN(acc); SB();
            if (s >= 2 && s < 6) { const int k0 = (((s + 10) >> 1) * 28 + (s & 1) * 16) * 128; wr[s + 10] = cat(trd(wb + k0), trd(wb + k0 + 1024)); SB(); }
            if (CST) { of[s] = lds8(ob + (8 + s) * 1024); SB(); }
        }
        if (!CST) {
            waitv_barx<0>();
#pragma unroll
            for (int s = 0; s < 8; ++s) of[s] = lds8(ob + (8 + s) * 1024);
        }
        SB();
#pragma unroll
        for (int s = 0; s < 8; ++s) acc = MFMA(wr[8 + s], of[s], acc);
        TS(10);
        *(LAS f32x4*)cw = (f32x4){acc[8], acc[9], acc[10], acc[11]}; *(LAS f32x4*)(cw + 1024) = (f32x4){acc[12], acc[13], acc[14], acc[15]};
        BAR();
        const f32x4 a = *(LAS f32x4*)cr + (f32x4){acc[0], acc[1], acc[2], acc[3]}, b = *(LAS f32x4*)(cr + 1024) + (f32x4){acc[4], acc[5], acc[6], acc[7]};
        const u32x4 hv = {cvtpk(a[0], a[1]), cvtpk(a[2], a[3]), cvtpk(b[0], b[1]), cvtpk(b[2], b[3])};
        st16_dev(hv, rws, ho, 0);
        FENCE(); SB();
        float s1 = 0.f, s2 = 0.f;
        { f32x4 q4_ = {}; acc_stats2(hv[0], hv[1], q4_); acc_stats2(hv[2], hv[3], q4_); s1 = swap_add(q4_[0] + q4_[1]); s2 = swap_add(q4_[2] + q4_[3]); }
        st8_dev((u32x2){f2u(s1), f2u(s2)}, rws, so, 0);
        FENCE(); PIN(cs); SB();
#pragma unroll
        for (int s = 16; s < 28; ++s) cs = MFMA16(BF8(one4), wf[s], cs);
        PIN(cs);
        waitv_bar<0>();
        if (w == 0) set_flag(ws, fo3, tagv);
        if (bid == 0 && w == 0) set_flag(ws, FL_EPOCH * 128, tagv);
        TS(11);
    }

    const unsigned wdo = WS_WD + (unsigned)(m7 * 112 + w * 14) * 2048;
    bf16x8 wdf[28];
    bf16x8 gf[GW];
    const unsigned erot = EROT * m7;
#define E_KS(s) (erot + (s) >= 14 ? erot + (s) - 14 : erot + (s))
    const int ta = erb * 4 + (tb & 1);
    const unsigned gxo = WS_GF + (unsigned)(ta * 112 + w * 14) * 1024;
    u64 gfl = 0;
    const unsigned gks = w * 14 + umin(lane & 15, 13), gfi = (FL_G + (erb * 4 + (tb & 1)) * 16 + (gks >> 3)) * 128 + (GWG ? 0 : (gks & 7) * 8);
    {
        if (lane < 16) { *(LAS float*)(lds + 161792 + w * 128 + lane * 4) = cs[0]; *(LAS float*)(lds + 161792 + w * 128 + 64 + lane * 4) = cs[1]; }
        lds_ptr csp = opq(lds + 161792 + w * 128 + hf * 16);
        const unsigned hd = lds0 + w * 1024;
        const char* wds = ws + wdo;
        lds_ptr hb = opq(lds + l16), hb2 = opq(lds + 57344 + l16);
        lds_ptr wl = opq(lds + 114688 + w * 5120 + l16), wl2 = opq(lds + w * 7168 + l16);
        LAS f32x2* stp = (LAS f32x2*)opq(lds + 159744 + r * 8);
        if (w == HOP3_POLLER) { if (DLY3) __builtin_amdgcn_s_sleep(DLY3); poll_flag(ws, pf3, tag, 0x7f7f); }
        BAR();
        TS(12);
#define H_PIECE(j) glds_dev(l16, hs + (j) * 8192, hd + (j) * 8192)
#define WDO(s) (E_KS((s) >> 1) * 2048 + ((s) & 1) * 1024)
#define WDLD(s) wdf[s] = BF8(ld16_dev(rws, l16, wdo + WDO(s)))
#define HB(n) ((n) < 56 ? hb + (n) * 1024 : hb2 + ((n) - 56) * 1024)
        H_PIECE(0);
        asm volatile("s_barrier" ::: "memory");
        H_PIECE(1); H_PIECE(2); H_PIECE(3);
        constexpr int PF = 5;
        bf16x8 bf[PF + 1];
        f32x16 acc[2];
        f32x2 sp[7], sv0, sv1;
        f32x4 c4[4];
        float mean = 0.f, r2 = 0.f, hp = 0.f;
        u32x4 hv;
        lds_ptr pk = opq(lds + 155648 + w * 512 + lane * 4);
        u64 pv = 0;
        const unsigned pkd = lds0 + 155648 + w * 512;
        const unsigned rdo = (FL_RD + ((lane & 2) ? drb * 2 + (lane & 1) : (lane & 1) ? tb : (w >> 1) * 7 + m7)) * 128;
        waitv_barx<3>();
        TS(27);
#pragma unroll
        for (int n = 0; n < PF; ++n) bf[n] = lds8(HB(n));
        SB();
        sfor<0, 125>([&](auto N_) __attribute__((always_inline)) {
            constexpr int n = decltype(N_)::v, p = n / 28, s = n % 28;
            if constexpr (n == 3) waitv_barx<3>();
            if constexpr (n == 11) waitv_barx<7>();
            if constexpr (n == 27) { waitv_barx<3>(); TS(13); }
            if constexpr (n == 64) { waitv_barx<10>(); if (!GWG || w == 0) set_flag(ws, fo4, tagv); TS(14); }
            if constexpr (n == 85) { if (GWG) waitv_barx<7>(); else waitv<7>(); if (!GWG || w == 0) set_flag(ws, fo4 + 2048, tagv); }
            if constexpr (n == 38) { if (__builtin_expect(!wave_all(pv == tag), 0)) poll_flag(ws, rdo, tag); FENCE(); }
            if constexpr (d_prio(n) != d_prio(n - 1)) __builtin_amdgcn_s_setprio(d_prio(n));
            if constexpr (n < 112) { if constexpr (s == 0) { f32x16 z = {}; acc[p & 1] = MFMA(wf[s], bf[n % (PF + 1)], z); } else acc[p & 1] = MFMA(wf[s], bf[n % (PF + 1)], acc[p & 1]); PIN(acc[p & 1]); }
            SB();
            if constexpr (n + PF < 112) bf[(n + PF) % (PF + 1)] = lds8(HB(n + PF));
            if constexpr (n % 3 == 2 && n < 30) H_PIECE(4 + n / 3);
            if constexpr (n == 4) glds4_dev(l16, hss, lds0 + 114688 + w * 5120);
            if constexpr (n == 16) { waitv<4>();
#pragma unroll
                for (int j = 0; j < 7; ++j) sp[j] = *(LAS f32x2*)(lds + 114688 + w * 5120 + (hf * 7 + j) * 256 + r * 8);
            }
            if constexpr (n == 20) { const f32x2 t = ((sp[0] + sp[1]) + (sp[2] + sp[3])) + ((sp[4] + sp[5]) + sp[6]); stp[w * 32] = (f32x2){swap_add(t[0]), swap_add(t[1])}; }
            if constexpr (n >= 39 && n < 48 && n % 2 == 1) glds_dev(l16, wds + WDO((n - 39) / 2), lds0 + 114688 + w * 5120 + (n - 39) / 2 * 1024);
            if constexpr (n >= 48 && n < 64 && n % 3 == 0) WDLD(12 + (n - 48) / 3);
            if constexpr (n >= 70 && n < 84 && n % 2 == 0) glds_dev(l16, wds + WDO(5 + (n - 70) / 2), lds0 + w * 7168 + (n - 70) / 2 * 1024);
            if constexpr (n >= 85 && n < 105 && n % 2 == 1) WDLD(18 + (n - 85) / 2);
            if constexpr (n == 107) gfl = peek_flag(ws, gfi);
            if constexpr (n == 32) pv = peeked(pk);
            if constexpr (n == 18) peek_lds(ws, rdo, pkd);
            if constexpr (p >= 1) {
                const f32x16& a = acc[(p - 1) & 1];
                if constexpr (n == 100) { sv0 = stp[3 * 32]; sv1 = stp[7 * 32]; }
                if constexpr (s == 0 && p < 4) { sv0 = stp[(p - 1) * 32]; sv1 = stp[(p + 3) * 32];
#pragma unroll
                    for (int g = 0; g < 4; ++g) c4[g] = *(LAS f32x4*)(csp + g * 32); }
                if constexpr ((s == 3 && p < 4) || n == 104) { const f32x2 sv = sv0 + sv1; mean = sv[0] * (1.f / D); r2 = __builtin_amdgcn_rcpf(__builtin_fmaxf(sv[1] * (1.f / D) - mean * mean, 0.f) + EPS); PIN2(mean, r2); }
                if constexpr (s >= 4 && s < 12) { constexpr int i = s - 4;
                    const float h0 = __builtin_fmaxf(a[i] - mean * c4[i >> 2][i & 3], 0.f) * r2 * (a[8 + i] - mean * c4[2 + (i >> 2)][i & 3]);
                    if constexpr (i & 1) { hv[i >> 1] = p >= 3 ? umin(cvtpk(hp, h0), ~1u) : cvtpk(hp, h0); PIN(hv); } else { hp = h0; PIN(hp); } }
                if constexpr (s == 12) { st16_dev(hv, rws, go + (p - 1) * 114688, 0); FENCE(); }
            }
            SB();
        });
        TS(15);
        if (__builtin_expect(!wave_all(gfl == tag), 0)) poll_flag(ws, gfi, tag);
        FENCE();
#pragma unroll
        for (int s = 0; s < GW; ++s) { gf[s] = BF8(ld16_dev(rws, l16, gxo + E_KS(s) * 1024)); SB(); }
        FENCE();
#pragma unroll
        for (int s = 0; s < 12; ++s) wdf[s] = lds8(s < 5 ? wl + s * 1024 : wl2 + (s - 5) * 1024);
    }

    {
        const int etok = tid >> 4, eblk = tid & 15;
        const unsigned hro = (unsigned)((((eblk >> 2) & 3) * 64 + (eblk & 1) * 32 + etok) * 16 + ((eblk >> 1) & 1) * 8) + WS_HF + (unsigned)(ta * 28 + m7 * 4) * 1024;
        u32x2 hres[2];
        GAS u64* op = (GAS u64*)(out + (size_t)(ta * 32 + etok) * 896 + m7 * 128 + eblk * 8);
        lds_ptr rp = opq(lds + w * 8448 + hf * 528 + r * 16), rq = opq(lds + eblk * 528 + etok * 16);
        lds_ptr rp2 = opq(lds + 67584 + w * 8448 + hf * 528 + r * 16), rq2 = opq(lds + 67584 + eblk * 528 + etok * 16);
#define G_LD(s) gf[(s) % GW] = BF8(ld16_dev(rws, l16, gxo + ((s) / 14) * 229376 + E_KS((s) % 14) * 1024))
#define G_PUT(RP) do { \
            _Pragma("unroll") for (int g = 0; g < 4; ++g) *(LAS f32x4*)(RP + g * 1056) = (f32x4){c0[4 * g], c0[4 * g + 1], c0[4 * g + 2], c0[4 * g + 3]}; \
            _Pragma("unroll") for (int g = 0; g < 4; ++g) *(LAS f32x4*)(RP + 4224 + g * 1056) = (f32x4){c1[4 * g], c1[4 * g + 1], c1[4 * g + 2], c1[4 * g + 3]}; } while (0)
#define G_OUT(hh, RQ) do { f32x4 pa[8]; \
            _Pragma("unroll") for (int ww = 0; ww < 8; ++ww) pa[ww] = *(LAS f32x4*)(RQ + ww * 8448); SB(); \
            const f32x4 a = ((pa[0] + pa[1]) + (pa[2] + pa[3])) + ((pa[4] + pa[5]) + (pa[6] + pa[7])); \
            const u32x2 ov = {cvtpk(a[0] + bflo(hres[hh][0]), a[1] + bfhi(hres[hh][0])), cvtpk(a[2] + bflo(hres[hh][1]), a[3] + bfhi(hres[hh][1]))}; \
            __hip_atomic_store(op + (hh) * (64 * 896 / 8), __builtin_bit_cast(u64, ov), __ATOMIC_RELAXED, __HIP_MEMORY_SCOPE_AGENT); } while (0)
        f32x16 c0 = {}, c1 = {};
        TS(16);
        unsigned seen = 0;
#pragma unroll
        for (int s = 0; s < 28; ++s) {
            if (s == 1) { hres[0] = ld8_dev(rws, hro, 0); hres[1] = ld8_dev(rws, hro, 57344); FENCE(); }
            if (s == 10) BAR();
            if (s == 14) { G_PUT(rp); c0 = (f32x16){}; c1 = (f32x16){}; TS(17); }
            if (s == 20) { waitv<8>(); PIN2(hres[0], hres[1]); BAR(); G_OUT(0, rq); FENCE(); TS(18); }
            SB();
            c0 = MFMA(wdf[2 * (s % 14)], gf[s % GW], c0); PIN(c0); c1 = MFMA(wdf[2 * (s % 14) + 1], gf[s % GW], c1); PIN(c1);
            if (s >= 14) { const u32x4 u = U4(gf[s % GW]); seen = umax3(umax3(seen, u[0], u[1]), u[2], u[3]); PIN(seen); }
            SB();
            if (s + GW < 28) G_LD(s + GW);
        }
        if (w == 0) { set_flag(ws, fo4 + 4096, tagv); set_flag(ws, fo4 + 6144, tagv); }
        if (__builtin_expect(wave_any(seen == ~0u), 0)) {
            poll_flag(ws, gfi + 4096, tag);
            c0 = (f32x16){}; c1 = (f32x16){};
#pragma unroll
            for (int s = 14; s < 14 + GW; ++s) G_LD(s);
#pragma unroll
            for (int s = 14; s < 28; ++s) { c0 = MFMA(wdf[2 * (s % 14)], gf[s % GW], c0); c1 = MFMA(wdf[2 * (s % 14) + 1], gf[s % GW], c1); PIN2(c0, c1); SB(); if (s + GW < 28) G_LD(s + GW); SB(); }
        }
        G_PUT(rp2);
        TS(19);
        BAR();
        G_OUT(1, rq2);
    }
    TS(20);
}

#define WS_OFF 0
extern "C" void kernel_launch(void* const* d_in, const int* in_sizes, int n_in, void* d_out, int out_size, void* d_ws, size_t ws_size, hipStream_t stream) {
    (void)hipFuncSetAttribute((const void*)block_fwd, hipFuncAttributeMaxDynamicSharedMemorySize, LDS_BYTES);
    block_fwd<<<256, 512, LDS_BYTES, stream>>>((const char*)d_in[0], (const char*)d_in[1], (const char*)d_in[2], (const char*)d_in[4], (const char*)d_in[5], (const char*)d_in[6], (char*)d_out, (char*)d_ws + WS_OFF);
}
```

```cpp
#include <hip/hip_runtime.h>
#include <hip/hip_bf16.h>
#include <stdint.h>

typedef __attribute__((ext_vector_type(8))) short bf16x8;
typedef __attribute__((ext_vector_type(4))) short s16x4;
typedef __attribute__((ext_vector_type(2))) __bf16 bf16x2;
typedef __attribute__((ext_vector_type(16))) float f32x16;
typedef __attribute__((ext_vector_type(4))) float f32x4;
typedef __attribute__((ext_vector_type(2))) float f32x2;
typedef __attribute__((ext_vector_type(4))) unsigned u32x4;
typedef __attribute__((ext_vector_type(2))) unsigned u32x2;
typedef short v4i16_t __attribute__((ext_vector_type(4)));
typedef unsigned long long u64;
#define LAS __attribute__((address_space(3)))
#define GAS __attribute__((address_space(1)))
typedef LAS char* lds_ptr;
#if defined(__HIP_DEVICE_COMPILE__)
#define NOPK __attribute__((target("no-packed-fp32-ops")))
#else
#define NOPK
#endif
#define DI __device__ __forceinline__
#define MFMA(a, b, c) __builtin_amdgcn_mfma_f32_32x32x16_bf16(a, b, c, 0, 0, 0)
#define MFMA16(a, b, c) __builtin_amdgcn_mfma_f32_16x16x32_bf16(a, b, c, 0, 0, 0)
#define BF8(x) __builtin_bit_cast(bf16x8, x)
#define U4(x) __builtin_bit_cast(u32x4, x)

constexpr int D = 448;
constexpr float EPS = 1e-5f;
constexpr unsigned WS_FLAG = 0;
constexpr unsigned FL_EPOCH = 0, FL_KV = 1, FL_RT = FL_KV + 256, FL_RD = FL_RT + 32, FL_CL = FL_RD + 32, FL_O = FL_CL + 32, FL_H = FL_O + 256, FL_G = FL_H + 256;
constexpr unsigned WS_KV = 1u << 20;
constexpr unsigned WS_QF = WS_KV + 4194304;
constexpr unsigned WS_OF = WS_QF + 2097152;
constexpr unsigned WS_HF = WS_OF + 2097152;
constexpr unsigned WS_GF = WS_HF + 64 * 28 * 1024;
constexpr unsigned WS_WGU = WS_GF + 64 * 112 * 1024;
constexpr unsigned WS_WD = WS_WGU + 112 * 28 * 1024;
constexpr unsigned WS_HS = WS_WD + 7 * 112 * 2 * 1024;
constexpr unsigned WS_END = WS_HS + 64 * 28 * 256 + 1024;
constexpr unsigned OOB = 0x40000000u;
constexpr int LDS_BYTES = 163840;

DI float u2f(unsigned u) { return __builtin_bit_cast(float, u); }
DI unsigned f2u(float f) { return __builtin_bit_cast(unsigned, f); }
DI unsigned umin(unsigned a, unsigned b) { return a < b ? a : b; }
DI unsigned umax3(unsigned a, unsigned b, unsigned c) { const unsigned m = a > b ? a : b; return m > c ? m : c; }
DI bool wave_any(bool p) { return __builtin_amdgcn_ballot_w64(p) != 0; }
DI bool wave_all(bool p) { return __builtin_amdgcn_ballot_w64(!p) == 0; }
DI unsigned cvtpk(float lo, float hi) { return __builtin_bit_cast(unsigned, __builtin_convertvector((f32x2){lo, hi}, bf16x2)); }
DI float bflo(unsigned u) { return u2f(u << 16); }
DI float bfhi(unsigned u) { return u2f(u & 0xffff0000u); }
DI void glds(unsigned voff, const void* sbase, unsigned dst) {
    asm volatile("s_mov_b32 m0, %2\n\ts_nop 0\n\tglobal_load_lds_dwordx4 %0, %1" :: "v"(voff), "s"(sbase), "s"(dst) : "memory");
}
DI void glds_dev(unsigned voff, const void* sbase, unsigned dst) {
    asm volatile("s_mov_b32 m0, %2\n\ts_nop 0\n\tglobal_load_lds_dwordx4 %0, %1 sc1" :: "v"(voff), "s"(sbase), "s"(dst) : "memory");
}
DI void glds2_dev(unsigned voff, const void* sbase, unsigned dst) {
    asm volatile("s_mov_b32 m0, %2\n\ts_nop 0\n\tglobal_load_lds_dwordx4 %0, %1 sc1\n\tglobal_load_lds_dwordx4 %0, %1 offset:1024 sc1" :: "v"(voff), "s"(sbase), "s"(dst) : "memory");
}
DI void glds4_dev(unsigned voff, const void* sbase, unsigned dst) {
    asm volatile("s_mov_b32 m0, %2\n\ts_nop 0\n\tglobal_load_lds_dwordx4 %0, %1 sc1\n\tglobal_load_lds_dwordx4 %0, %1 offset:1024 sc1\n\t"
                 "global_load_lds_dwordx4 %0, %1 offset:2048 sc1\n\tglobal_load_lds_dwordx4 %0, %1 offset:3072 sc1" :: "v"(voff), "s"(sbase), "s"(dst) : "memory");
}
DI s16x4 trd(lds_ptr p) { return __builtin_bit_cast(s16x4, __builtin_amdgcn_ds_read_tr16_b64_v4i16((LAS v4i16_t*)p)); }
DI bf16x8 cat(s16x4 a, s16x4 b) { return __builtin_shufflevector(a, b, 0, 1, 2, 3, 4, 5, 6, 7); }
DI bf16x8 lds8(lds_ptr p) { return *(LAS bf16x8*)p; }
DI float swap_add(float v) { auto r = __builtin_amdgcn_permlane32_swap(f2u(v), f2u(v), false, false); return u2f(r[0]) + u2f(r[1]); }
DI float swap_max(float v) { auto r = __builtin_amdgcn_permlane32_swap(f2u(v), f2u(v), false, false); return __builtin_fmaxf(u2f(r[0]), u2f(r[1])); }
DI u32x4 pack8(const f32x16& a, int s) { return (u32x4){cvtpk(a[8 * s], a[8 * s + 1]), cvtpk(a[8 * s + 2], a[8 * s + 3]), cvtpk(a[8 * s + 4], a[8 * s + 5]), cvtpk(a[8 * s + 6], a[8 * s + 7])}; }
DI float dot2(unsigned a, unsigned b, float c) { return __builtin_amdgcn_fdot2_f32_bf16(__builtin_bit_cast(bf16x2, a), __builtin_bit_cast(bf16x2, b), c, false); }
DI void acc_stats(u32x4 u, float& s1, float& s2) {
#pragma unroll
    for (int i = 0; i < 4; ++i) { const float a = bflo(u[i]), b = bfhi(u[i]); s1 += a; s2 = __builtin_fmaf(a, a, s2); s1 += b; s2 = __builtin_fmaf(b, b, s2); }
}
DI void acc_stats2(unsigned u0, unsigned u1, f32x4& s) {
    const float a = bflo(u0), b = bfhi(u0), c = bflo(u1), d = bfhi(u1);
    s[0] += a; s[1] += b; s[2] = __builtin_fmaf(a, a, s[2]); s[3] = __builtin_fmaf(b, b, s[3]); s[0] += c; s[1] += d; s[2] = __builtin_fmaf(c, c, s[2]); s[3] = __builtin_fmaf(d, d, s[3]);
}
DI float sum8(float v) {
    v += u2f(__builtin_amdgcn_update_dpp(0u, f2u(v), 0xB1, 0xf, 0xf, true));
    v += u2f(__builtin_amdgcn_update_dpp(0u, f2u(v), 0x4E, 0xf, 0xf, true));
    v += u2f(__builtin_amdgcn_update_dpp(0u, f2u(v), 0x141, 0xf, 0xf, true));
    return v;
}
DI u32x4 shift8(u32x4 x, float c) {
    u32x4 o; float t0, t1, t2, t3, t4, t5, t6, t7;
    asm("v_dot2_f32_bf16 %4, %12, %16, %18\n\tv_dot2_f32_bf16 %5, %12, %17, %18\n\tv_dot2_f32_bf16 %6, %13, %16, %18\n\tv_dot2_f32_bf16 %7, %13, %17, %18\n\t"
        "v_dot2_f32_bf16 %8, %14, %16, %18\n\tv_dot2_f32_bf16 %9, %14, %17, %18\n\tv_dot2_f32_bf16 %10, %15, %16, %18\n\tv_dot2_f32_bf16 %11, %15, %17, %18\n\t"
        "v_cvt_pk_bf16_f32 %0, %4, %5\n\tv_cvt_pk_bf16_f32 %1, %6, %7\n\tv_cvt_pk_bf16_f32 %2, %8, %9\n\tv_cvt_pk_bf16_f32 %3, %10, %11"
        : "=&v"(o[0]), "=&v"(o[1]), "=&v"(o[2]), "=&v"(o[3]), "=&v"(t0), "=&v"(t1), "=&v"(t2), "=&v"(t3), "=&v"(t4), "=&v"(t5), "=&v"(t6), "=&v"(t7)
        : "v"(x[0]), "v"(x[1]), "v"(x[2]), "v"(x[3]), "s"(0x00003f80u), "s"(0x3f800000u), "v"(c));
    return o;
}
template <int N> struct IC { static constexpr int v = N; };
template <int B, int E, class F> DI void sfor(F&& f) { if constexpr (B < E) { f(IC<B>{}); sfor<B + 1, E>(f); } }
template <int N> DI void waitv() { asm volatile("s_waitcnt vmcnt(%0)" :: "i"(N) : "memory"); }
template <int N> DI void waitv_bar() { if (N == 0) __builtin_amdgcn_s_waitcnt(0x0F70); asm volatile("s_waitcnt vmcnt(%0) lgkmcnt(0)\n\ts_barrier" :: "i"(N) : "memory"); }
template <int N> DI void waitv_barx() { asm volatile("s_waitcnt vmcnt(%0)\n\ts_barrier" :: "i"(N) : "memory"); }
template <int N> DI void waitv_bar2() { asm volatile("s_waitcnt vmcnt(%0) lgkmcnt(2)\n\ts_barrier" :: "i"(N) : "memory"); }
DI void touch(unsigned voff, const void* sbase, unsigned junk) {
    asm volatile("s_mov_b32 m0, %2\n\ts_nop 0\n\tglobal_load_lds_dword %0, %1 sc1" :: "v"(voff), "s"(sbase), "s"(junk) : "memory");
}
#define BAR() asm volatile("s_waitcnt lgkmcnt(0)\n\ts_barrier" ::: "memory")
#define FENCE() asm volatile("" ::: "memory")
typedef __amdgpu_buffer_rsrc_t rsrc_t;
DI u32x4 ld16(rsrc_t rs, unsigned voff, unsigned soff) { return U4(__builtin_amdgcn_raw_buffer_load_b128(rs, voff, soff, 0)); }
DI u32x4 ld16_dev(rsrc_t rs, unsigned voff, unsigned soff) { return U4(__builtin_amdgcn_raw_buffer_load_b128(rs, voff, soff, 16)); }
DI u32x2 ld8(rsrc_t rs, unsigned voff, unsigned soff) { return __builtin_bit_cast(u32x2, __builtin_amdgcn_raw_buffer_load_b64(rs, voff, soff, 0)); }
DI u32x2 ld8_dev(rsrc_t rs, unsigned voff, unsigned soff) { return __builtin_bit_cast(u32x2, __builtin_amdgcn_raw_buffer_load_b64(rs, voff, soff, 16)); }
DI void st16_dev(u32x4 v, rsrc_t rs, unsigned voff, unsigned soff) { __builtin_amdgcn_raw_buffer_store_b128(v, rs, voff + soff, 0, 16); }
DI void st8_dev(u32x2 v, rsrc_t rs, unsigned voff, unsigned soff) { __builtin_amdgcn_raw_buffer_store_b64(v, rs, voff, soff, 16); }
#define POLL_SLEEP 1
DI void set_flag(char* ws, unsigned fo, u64 tagv) {
    asm volatile("s_mov_b64 exec, 1\n\tglobal_store_dwordx2 %0, %1, %2 sc1\n\ts_mov_b64 exec, -1" :: "v"(fo), "v"(tagv), "s"(ws) : "memory");
}
DI u64 peek_flag(char* ws, unsigned fo) { return __hip_atomic_load((GAS u64*)(ws + fo), __ATOMIC_RELAXED, __HIP_MEMORY_SCOPE_AGENT); }
DI void peek_lds(char* ws, unsigned fo, unsigned dst) {
    asm volatile("s_mov_b32 m0, %2\n\ts_nop 0\n\tglobal_load_lds_dword %0, %1 sc1\n\ts_add_u32 m0, %2, 252\n\ts_nop 0\n\tglobal_load_lds_dword %0, %1 offset:4 sc1" :: "v"(fo), "s"(ws), "s"(dst) : "memory", "scc");
}
DI u64 peeked(lds_ptr p) { return (u64)*(LAS unsigned*)(p + 256) << 32 | *(LAS unsigned*)p; }
DI void poll_flag(char* ws, unsigned fo, u64 tag, u64 lanes = 0xffff) {
#pragma unroll 1
    for (int spin = 0; spin < (1 << 16); ++spin) {
        if (wave_all(peek_flag(ws, fo) == tag)) break;
        __builtin_amdgcn_s_sleep(POLL_SLEEP);
    }
    FENCE();
}
#define SB() __builtin_amdgcn_sched_barrier(0)
#define PINS(a) asm volatile("" : "+s"(a))
#define PIN(a) asm volatile("" : "+v"(a))
#define PIN2(a, b) asm volatile("" : "+v"(a), "+v"(b))
DI lds_ptr opq(lds_ptr p) { unsigned u = (unsigned)(uintptr_t)p; asm volatile("" : "+v"(u)); return (lds_ptr)(uintptr_t)u; }
#define TS(i) do {} while (0)

DI constexpr int wd_slot(int s) { return (s * 17 + 27) / 28 != ((s + 1) * 17 + 27) / 28 ? (s * 17 + 27) / 28 : -1; }
#define EROT 0
#define CLR_SLEEP 90
#define ROTMODE 0
#define EARLYG 0
#define GWG 1
#define GW 7
#define CLRMODE 0
#define DLY1 0
#define DLY2 0
#define DLY3 0
#define P3 1
#define VARLATE 0
#define CST 0
#define SPDLY 20
#define XN 4
#define ENL 4
#define HOP3_POLLER 1
DI constexpr int d_prio(int n) { return n < 0 ? -1 : n < 27 ? 3 - n / 7 : n < 64 ? 3 - (n - 27) / 10 : n < 84 ? 3 - (n - 64) / 5 : n < 112 ? 3 - (n - 84) / 7 : 0; }
DI constexpr int phys(int t) { return ((t % 9) + 2) % 9 * 16384; }

__global__ NOPK __launch_bounds__(512, 2) void block_fwd(const char* x, const char* wqkv, const char* wo, const char* wg, const char* wu, const char* wd, char* out, char* ws) {
    extern __shared__ __attribute__((aligned(16))) char smem[];
    lds_ptr lds = (lds_ptr)smem;
    const int tid = __builtin_amdgcn_workitem_id_x(), lane = tid & 63, w = __builtin_amdgcn_readfirstlane(tid >> 6);
    const int bid = __builtin_amdgcn_workgroup_id_x(), xcd = bid & 7, loc = bid >> 3;
    const int r = lane & 31, hf = lane >> 5, q4 = (lane & 15) >> 2, g1 = (lane >> 4) & 1, p4 = lane & 3;
    const unsigned lds0 = (unsigned)(uintptr_t)smem;
    const unsigned l16 = lane * 16;
    TS(0);
    const int sp3 = (loc + 8) >> 5;
    const int atb = (xcd >> 1) * 8 + (loc & 7) + sp3 * (xcd & 1) * 4, acg = (loc >> 3) + (1 - sp3) * (xcd & 1) * 4;
    const int row8 = lane >> 3;
    const int rot8 = ((loc & 7) << sp3) + sp3 * (xcd & 1), rot = rot8 == 7 ? 0 : rot8;
    if (loc < 28) {
        const unsigned xo = (unsigned)((atb * 64 + (tid >> 3)) * 896 + (((tid ^ (tid >> 4)) & 7) * 16));
        const unsigned xd0 = lds0 + 1024 + (w >> 2) * 28672 + (w & 3) * 1024;
        unsigned xd = xd0 + (7 - rot) * 4096;
        const unsigned w0 = (unsigned)((rot * 64 + (tid >> 3)) * 2688 + acg * 128 + (((tid & 7) ^ ((tid >> 2) & 4)) * 16));
        asm volatile("s_cmp_eq_u32 %0, %3\n\ts_cselect_b32 %0, %2, %0\n\ts_mov_b32 m0, %0\n\ts_add_u32 %0, %0, 0x1000\n\tglobal_load_lds_dwordx4 %1, %4\n\t"
#define XP(o) "s_cmp_eq_u32 %0, %3\n\ts_cselect_b32 %0, %2, %0\n\ts_sub_u32 m0, %0, " #o "\n\ts_add_u32 %0, %0, 0x1000\n\tglobal_load_lds_dwordx4 %1, %4 offset:" #o "\n\t"
                     XP(128) XP(256) XP(384) XP(512) XP(640) XP(768)
                     "s_add_u32 m0, %6, 8192-896\n\ts_barrier\n\tglobal_load_lds_dwordx4 %5, %7 offset:896\n\t"
                     "s_add_u32 m0, %6, 16384-1792\n\ts_nop 0\n\tglobal_load_lds_dwordx4 %5, %7 offset:1792\n\t"
                     "s_mov_b32 m0, %6\n\ts_nop 0\n\tglobal_load_lds_dwordx4 %5, %7"
                     : "+s"(xd) : "v"(xo), "s"(xd0), "s"(xd0 + 7 * 4096), "s"(x), "v"(w0), "s"(lds0 + 58368 + w * 1024), "s"(wqkv) : "memory", "scc");
    }
    SB();
    const rsrc_t rws = __builtin_amdgcn_make_buffer_rsrc((void*)ws, 0, WS_END, 0x00020000);
    u64 epoch, tagv;
    unsigned fl1;
    const unsigned ones = 0x3f803f80u;
    const bool ffn = loc < 28;
    const int l7 = loc / 7, m7 = loc % 7;
    const int tb = xcd * 4 + l7;
    const int tt = w & 1, ct = (w >> 1) & 1, kh = w >> 2;
    const unsigned swz = (unsigned)(((lane & 7) ^ (((row8 >> 1) & 1) << 2)) * 16);

    if (__builtin_expect(ffn, 1)) {
        constexpr int A_W = 58368, A_WS = 24576;
#define A_KC(c) (rot + (c) >= 7 ? rot + (c) - 7 : rot + (c))
        const unsigned wvo = (unsigned)(row8 * 2688) + swz;
        const char* wsrc = wqkv + acg * 128 + (size_t)w * 8 * 2688;
        const unsigned wdst = lds0 + A_W + w * 1024;
#define A_PIECE(c, i) glds(wvo, wsrc + A_KC(c) * (64 * 2688) + (i) * 896, wdst + ((c) & 3) * A_WS + (i) * 8192)
#define A_GROUP(c) do { A_PIECE(c, 1); A_PIECE(c, 2); A_PIECE(c, 0); } while (0)
        epoch = peek_flag(ws, FL_EPOCH * 128); FENCE(); fl1 = (FL_KV + atb * 8 + acg) * 128;
        if (EARLYG) A_GROUP(1);
        lds_ptr xb[4], wb1, wb1h;
#pragma unroll
        for (int j = 0; j < 4; ++j) xb[j] = lds + 1024 + tt * 28672 + (r >> 3) * 1024 + (r & 7) * 128 + (((2 * j + hf) ^ ((r >> 1) & 7)) * 16);
        wb1 = opq(lds + A_W + (kh ? 0 : 8192) + (8 * hf + q4) * 128 + ((ct ^ (q4 >> 1)) * 64) + g1 * 32 + p4 * 8);
        wb1h = opq(wb1 + 2 * A_WS);
        unsigned go1[2], go2[2]; int gi[2];
#pragma unroll
        for (int p = 0; p < 2; ++p) {
            const int G = acg * 8 + ct * 4 + 2 * p + hf, hp = G / 7, i = G - hp * 7;
            const unsigned hs = kh ? 131072u : 262144u;
            const unsigned to = (kh ? WS_QF + (atb * 2 + tt) * 2048 : WS_KV + (atb * 2 + tt) * 4096) + r * 16;
            go1[p] = (unsigned)(2 * hp + (i > 3)) * hs + (unsigned)(i > 3 ? i - 3 : i) * 512 + to;
            go2[p] = i == 3 ? (unsigned)(2 * hp + 1) * hs + to : OOB;
            gi[p] = i;
        }
        const int vcol = acg * 64 + ct * 32 + r, vh = vcol / 28, vd = vcol - vh * 28;
        unsigned vo = WS_KV + (atb * 2 + tt) * 4096 + (unsigned)vh * 262144 + 2048 + hf * 512 + vd * 16;
        PIN2(go1[0], go1[1]); PIN2(go2[0], go2[1]); PIN(vo);
        lds_ptr xw = opq(lds + 1024 + kh * 28672 + (w & 3) * 1024 + l16);
        waitv<EARLYG ? 7 : 4>();
        TS(28);
        u32x4 xv[7];
#pragma unroll
        for (int c = 0; c < 7; ++c) xv[c] = *(LAS u32x4*)(xw + c * 4096);
        SB();
        f32x4 sm = {};
#pragma unroll
        for (int c = 0; c < 7; ++c)
#pragma unroll
            for (int j = 0; j < 4; ++j) sm[j] = dot2(xv[c][j], ones, sm[j]);
        float nm = sum8((sm[0] + sm[1]) + (sm[2] + sm[3])) * (-1.f / D);
        PIN(nm);
#pragma unroll
        for (int c = 0; c < 7; ++c) { xv[c] = shift8(xv[c], nm); *(LAS u32x4*)(xw + c * 4096) = xv[c]; }
        if (EARLYG) A_GROUP(2); else A_GROUP(1);
        SB();
        LAS float* rst = (LAS float*)lds;
#define A_VAR() do { f32x4 sq = {}; _Pragma("unroll") for (int c_ = 0; c_ < 7; ++c_) _Pragma("unroll") for (int j = 0; j < 4; ++j) sq[j] = dot2(xv[c_][j], xv[c_][j], sq[j]); \
            rst[w * 8 + row8] = __builtin_amdgcn_rsqf(sum8((sq[0] + sq[1]) + (sq[2] + sq[3])) * (1.f / D) + EPS); } while (0)
        if (!VARLATE) A_VAR();
        if (EARLYG) A_GROUP(3); else A_GROUP(2);
        TS(31);
        bf16x8 fx[4], f1[4], f2[4];
#define A_WAIT(c) do { if ((c) == 0) waitv_bar<EARLYG ? 9 : 6>(); else if ((c) == 1) waitv_barx<6>(); else if ((c) < 6) waitv_barx<3>(); else waitv_barx<0>(); \
                       if ((c) == 0) TS(1); if ((c) == 6) TS(2); } while (0)
#define A_WP(c, ks) ((((c) & 2) ? wb1h : wb1) + ((c) & 1) * A_WS + (ks) * 2048)
        if (w < 4) {
            f32x16 ak = {}, av = {};
            f32x4 rv[4];
            float rs = 0.f;
#pragma unroll
            for (int n = 0; n < 30; ++n) {
                const int c = n >> 2, ks = n & 3, km = (n - 2) & 3;
                if (n < 28 && ks == 0) A_WAIT(c);
                if (n >= 2) { ak = MFMA(f1[km], fx[km], ak); PIN(ak); } SB();
                if (n < 28) { fx[ks] = lds8(xb[ks] + c * 4096); f1[ks] = cat(trd(A_WP(c, ks)), trd(A_WP(c, ks) + 512)); } SB();
                if (n >= 2) { av = MFMA(fx[km], f2[km], av); PIN(av); } SB();
                if (n < 28) f2[ks] = cat(trd(A_WP(c, ks) + 8192), trd(A_WP(c, ks) + 8192 + 512));
                if (!EARLYG && c == 0 && ks < 3) A_PIECE(3, ks); if (c >= 2 && c < 5 && ks < 3) A_PIECE(c + 2, ks);
                if (VARLATE && n == 1) { SB(); A_VAR(); }
                if (n == 21) {
                    rs = rst[tt * 32 + r];
#pragma unroll
                    for (int g = 0; g < 4; ++g) rv[g] = *(LAS f32x4*)(rst + tt * 32 + 8 * g + 4 * hf);
                }
                if (n == 24) { PIN(epoch); tagv = epoch + 1; PIN(tagv); }
                if (n == 24) {
                    const unsigned pv = vd == 0 ? ones : 0u, po = vd < 4 ? vo + 448 : OOB;
                    st16_dev((u32x4){pv, pv, pv, pv}, rws, po, 0); st16_dev((u32x4){pv, pv, pv, pv}, rws, po + 1024, 0);
                }
                SB();
            }
            TS(3);
#pragma unroll
            for (int i = 0; i < 16; ++i) { ak[i] *= rs; av[i] *= rv[i >> 2][i & 3]; }
#pragma unroll
            for (int p = 0; p < 2; ++p) {
                const u32x4 f = pack8(ak, p);
                const auto s0 = __builtin_amdgcn_permlane32_swap(f[0], f[2], false, false), s1 = __builtin_amdgcn_permlane32_swap(f[1], f[3], false, false);
                const u32x4 c = {s0[0], s1[0], s0[1], s1[1]};
                st16_dev(c, rws, go1[p], 0); st16_dev(c, rws, go2[p], 0);
            }
            st16_dev(pack8(av, 0), rws, vo, 0); st16_dev(pack8(av, 1), rws, vo + 1024, 0);
        } else {
            f32x16 aq = {};
            __builtin_amdgcn_s_setprio(1);
#pragma unroll
            for (int n = 0; n < 30; ++n) {
                const int c = n >> 2, ks = n & 3, km = (n - 2) & 3;
                if (n < 28 && ks == 0) A_WAIT(c);
                if (n == 24) { PIN(epoch); tagv = epoch + 1; PIN(tagv); }
                if (n >= 2) { aq = MFMA(f1[km], fx[km], aq); PIN(aq); } SB();
                if (n < 28) { fx[ks] = lds8(xb[ks] + c * 4096); f1[ks] = cat(trd(A_WP(c, ks)), trd(A_WP(c, ks) + 512)); }
                if (!EARLYG && c == 0 && ks < 3) A_PIECE(3, ks); if (c >= 2 && c < 5 && ks < 3) A_PIECE(c + 2, ks);
                if (VARLATE && n == 1) { SB(); A_VAR(); }
                SB();
            }
            __builtin_amdgcn_s_setprio(0);
            const float QS = rst[tt * 32 + r] * (0.18898223650461362f * 1.4426950408889634f);
#pragma unroll
            for (int i = 0; i < 16; ++i) aq[i] *= QS;
#pragma unroll
            for (int p = 0; p < 2; ++p) {
                const u32x4 f = pack8(aq, p);
                const auto s0 = __builtin_amdgcn_permlane32_swap(f[0], f[2], false, false), s1 = __builtin_amdgcn_permlane32_swap(f[1], f[3], false, false);
                const bool sp = gi[p] == 3;
                st16_dev((u32x4){s0[0], s1[0], sp ? 0u : s0[1], sp ? 0u : s1[1]}, rws, go1[p], 0);
                st16_dev((u32x4){0u, 0u, s0[1], s1[1]}, rws, go2[p], 0);
            }
        }
    } else {
        if (SPDLY) __builtin_amdgcn_s_sleep(SPDLY);
        epoch = peek_flag(ws, FL_EPOCH * 128); fl1 = (FL_RT + xcd * 4 + loc - 28) * 128;
        const int ri = xcd * 4 + loc - 28, nu = (w == 0 && ri < 16) ? 4 : 3;
        const unsigned vo = (unsigned)(row8 * 3584 + (lane & 7) * 16);
#pragma unroll 1
        for (int i = 0; i < nu; ++i) {
            const int u = i < 3 ? i * 256 + ri * 8 + w : 768 + ri;
            const size_t so = (size_t)(u / 28) * 16 * 3584 + (u % 28) * 128;
            const unsigned d_ = lds0 + w * 16384 + i * 4096;
            glds(vo, wg + so, d_); glds(vo, wg + so + 8 * 3584, d_ + 1024); glds(vo, wu + so, d_ + 2048); glds(vo, wu + so + 8 * 3584, d_ + 3072);
        }
#pragma unroll 1
        for (int i = 0; i < nu; ++i) {
            const int u = i < 3 ? i * 256 + ri * 8 + w : 768 + ri;
            waitv<8>();
            lds_ptr sg = lds + w * 16384 + i * 4096 + g1 * 2048 + (4 * hf + q4) * 128 + p4 * 8;
#pragma unroll
            for (int e = 0; e < 4; ++e) st16_dev(U4(cat(trd(sg + e * 32), trd(sg + e * 32 + 1024))), rws, l16, WS_WGU + (unsigned)(((u % 28) * 4 + e) * 28 + u / 28) * 1024);
            FENCE();
        }
        if (CLRMODE >= 2) {
            const unsigned c_ = WS_GF + (unsigned)(((ri >> 1) * 4 + 2 + (ri & 1)) * 112 + w * 14) * 1024 + l16;
#pragma unroll 1
            for (int i = 0; i < (CLRMODE == 2 ? 7 : 14); ++i) st16_dev((u32x4){~0u, ~0u, ~0u, ~0u}, rws, c_ + i * 1024, 0);
        }
        tagv = epoch + 1;
    }
    TS(24);
    const int head = xcd * 2 + (loc & 1), rb = loc >> 1, qt = w & 3;
    const char* kvs = ws + WS_KV + (size_t)head * 262144 + w * 2048;
    const char* qs = ws + WS_QF + (size_t)(head * 64 + rb * 4 + qt) * 2048 + kh * 1024;
    unsigned kvd = lds0 + w * 2048, qd = lds0 + 147456 + qt * 2048 + kh * 1024;
    unsigned pf1 = (FL_KV + (lane >> 1) * 8 + (head * 28 + (lane & 1) * 27) / 64) * 128;
    PINS(kvs); PINS(qs); PINS(kvd); PINS(qd); PIN2(pf1, fl1);
    waitv_bar<0>();
    if (w == 0) set_flag(ws, fl1, tagv);
    const u64 tag = ((u64)(unsigned)__builtin_amdgcn_readfirstlane((unsigned)(tagv >> 32)) << 32 | (u64)(unsigned)__builtin_amdgcn_readfirstlane((unsigned)tagv));
    TS(4);

    const int drb = (xcd >> 1) * 4 + l7, dfg = (xcd & 1) * 7 + m7;
    bf16x8 wf[28];
    const unsigned p16 = hf * 1024 + g1 * 512 + (lane & 15) * 16;
    u32x2 xa, xb;
    f32x4 csg = {}, csu = {};
    f32x16 acc = {};
    const u32x4 one4 = {ones, ones, ones, ones};
    const unsigned hi_ = (unsigned)((tb * 2 + tt) * 28 + m7 * 4 + ct * 2 + kh), ofo = (unsigned)(tt * 32768 + kh * 16384 + ct * 4096);
    unsigned ho = WS_HF + hi_ * 1024 + l16, so = hf ? OOB : WS_HS + hi_ * 256 + r * 8;
    const char* os = ws + WS_OF + (size_t)tb * 65536 + ofo;
    unsigned od = lds0 + 65536 + ofo;
    unsigned pf2 = (FL_O + (tb >> 1) * 16 + kh * 8 + ct * 2 + ((lane >> 1) & 1) + (lane & 4)) * 128 + ((lane & 1) * 4 + (tb & 1) * 2 + tt) * 8;
    pf2 &= ~127u;
    unsigned go = ffn ? WS_GF + (unsigned)(drb * 4 * 112 + dfg * 8 + w) * 1024 + g1 * 512 + (hf * 16 + (lane & 15)) * 16 : OOB;
    unsigned fo3 = (FL_H + tb * 8 + m7) * 128;
    {
        lds_ptr ap = opq(lds + kh * 8192 + l16), apB = opq(lds + 65536 + kh * 8192 + l16), apC = opq(lds + 131072 + kh * 8192 + l16);
        const unsigned lv = kh * 8192 + (l16 ^ (kh * 256));
        lds_ptr vp = opq(lds + lv), vpB = opq(lds + 65536 + lv), vpC = opq(lds + 131072 + lv);
#define AP(off) ((off) < 65536 ? ap + (off) : (off) < 131072 ? apB + ((off) - 65536) : apC + ((off) - 131072))
#define VP(off) ((off) < 65536 ? vp + (off) : (off) < 131072 ? vpB + ((off) - 65536) : vpC + ((off) - 131072))
#define KV_ISSUE(t) glds2_dev(l16, kvs + (t) * 16384, kvd + phys(t))
        unsigned wgo = ffn ? WS_WGU + (unsigned)(dfg * 8 + w) * 28672 : OOB;
        unsigned clro = WS_GF + (unsigned)((((xcd * 4 + loc - 28) >> 1) * 4 + 2 + (loc & 1)) * 112 + w * 14) * 1024 + l16;
        u32x4 clv = {~0u, ~0u, ~0u, ~0u};
        if (CLRMODE == 1) { if (ffn) clro = OOB; PIN(clv); PIN(clro); }
        const int ri = umin(xcd * 4 + loc - 28, 27);
        const char* wos = (ffn ? wo + m7 * 128 : wd + (ri % 7) * 128 + (size_t)(ri / 7) * 448 * 896) + (size_t)w * 56 * 896;
        unsigned wov = (unsigned)(row8 * 896) + swz;
        const rsrc_t rx = __builtin_amdgcn_make_buffer_rsrc((void*)x, 0, 2048 * 448 * 2, 0x00020000);
        unsigned xo_ = (unsigned)(((tb * 2 + tt) * 32 + r) * 896 + m7 * 128 + ct * 64 + kh * 32 + hf * 8);
        unsigned rto = (FL_RT + (P3 ? w * 4 + (lane & 3) : r)) * 128;
        lds_ptr qp = opq(lds + 147456 + qt * 2048 + l16);
        PINS(wgo); PINS(wos); PIN2(wov, xo_); PIN(rto); PIN2(ho, so); PIN2(pf2, fo3); PIN(go);
        lds_ptr xw = opq(lds + 147456 + w * 2048 + l16), xr = opq(lds + 147456 + (w ^ 4) * 2048 + l16);
        lds_ptr lw = opq(lds + 58368 + w * 256 + lane * 4), lr = opq(lds + 58368 + qt * 256 + 128 + r * 4);
        lds_ptr bw = opq(lds + 60416 + w * 4), br = opq(lds + 60416 + (lane & 7) * 4);
        unsigned oo = WS_OF + (unsigned)((rb * 4 + qt) * 32 + head * 2 + kh) * 1024 + l16;
        unsigned fo2 = (FL_O + rb * 16 + head) * 128 + w * 8;
        PIN2(oo, fo2);
        if (w == HOP3_POLLER) { if (DLY1) __builtin_amdgcn_s_sleep(DLY1); poll_flag(ws, pf1, tag, ~0ull); }
        BAR();
        TS(5);
        glds_dev(l16, qs, qd);
        KV_ISSUE(0); KV_ISSUE(1);
        asm volatile("s_barrier" ::: "memory");
        KV_ISSUE(2); KV_ISSUE(3);
        waitv_bar<4>();
        TS(6);
        const bf16x8 q0 = lds8(qp), q1 = lds8(qp + 1024);
        f32x16 o = {}, sa = {}, sb;
        bf16x8 k0 = lds8(ap + phys(0)), k1 = lds8(ap + phys(0) + 1024), v0 = lds8(vp + phys(0) + 2048), v1 = {};
        u32x4 p0, p1 = {};
        sa = MFMA(k0, q0, sa); sa = MFMA(k1, q1, sa);
        k0 = lds8(ap + phys(0) + 4096); k1 = lds8(ap + phys(0) + 5120);
        PIN(sa); SB();
        u64 rtf = 0;
#define EXP4(S, o4) _Pragma("unroll") for (int i_ = o4; i_ < o4 + 4; ++i_) S[i_] = __builtin_amdgcn_exp2f(S[i_]);
#define WLD(s) wf[s] = BF8(ld16_dev(rws, p16 + ((s) & 1) * 256 + ((s) & 2) * 1024, wgo + ((s) >> 2) * 4096))
#define SIDE(t, g) do { \
            if ((t) == 0 && (g) == 0) KV_ISSUE(4); if ((t) == 0 && (g) == 2) KV_ISSUE(5); if ((t) == 1 && (g) == 0) KV_ISSUE(6); if ((t) == 1 && (g) == 2) KV_ISSUE(7); \
            if ((t) == 2 && (g) == 0) KV_ISSUE(8); if (!P3 && (t) == 1 && (g) == 3) rtf = peek_flag(ws, rto); if (P3 && (t) == 1 && (g) == 1) peek_lds(ws, rto, lds0 + 155648 + w * 512); if (P3 && (t) == 2 && (g) == 2) { waitv<4>(); rtf = peeked(lds + 155648 + w * 512 + lane * 4); } \
            if ((t) == 3 && (g) < 3) KV_ISSUE(9 + (g)); \
            if (CLRMODE == 0 && (t) >= 4 && (t) <= 10 && ((g) & 1)) { if (__builtin_expect(!ffn, 0)) st16_dev((u32x4){~0u, ~0u, ~0u, ~0u}, rws, clro + (((t) - 4) * 2 + ((g) >> 1)) * 1024, 0); } \
            if (CLRMODE == 1 && (t) >= 4 && (t) <= 10 && ((g) & 1)) __builtin_amdgcn_raw_buffer_store_b128(clv, rws, clro, (((t) - 4) * 2 + ((g) >> 1)) * 1024, 16); \
            if (CLRMODE == 2 && (t) >= 11 && !((g) & 1) && ((t) - 11) * 2 + (g) / 2 < 7) { if (__builtin_expect(!ffn, 0)) st16_dev((u32x4){~0u, ~0u, ~0u, ~0u}, rws, clro + (7 + ((t) - 11) * 2 + (g) / 2) * 1024, 0); } \
            if ((t) == 7 && (g) == 0) KV_ISSUE(12); if ((t) == 7 && (g) == 2) KV_ISSUE(13); if ((t) == 8 && (g) == 0) KV_ISSUE(14); if ((t) == 8 && (g) == 2) KV_ISSUE(15); \
            if ((t) >= 4 && (t) <= 6) WLD(((t) - 4) * 4 + (g)); \
            if (((t) == 7 || (t) == 8) && ((g) & 1)) WLD(12 + ((t) - 7) * 2 + ((g) >> 1)); \
            if ((t) >= 9 && (t) <= 11) WLD(((t) - 5) * 4 + (g)); \
            if ((t) == 12 || ((t) == 13 && (g) < 3)) glds(wov, wos + (((t) - 12) * 4 + (g)) * 8 * 896, lds0 + (w * 7 + ((t) - 12) * 4 + (g)) * 1024); \
            if ((t) == 13 && (g) == 3) { xa = ld8(rx, xo_, 0); xb = ld8(rx, xo_, 16); } \
            if ((t) == 14 && (g) == 1) { if (w >= 6) touch(lane * (w == 6 ? (WS_END / 64 & ~127u) : 28672), w == 6 ? ws : out, lds0 + 61440); } \
            if ((t) == 14 && (g) == 0) { if (tid < 32) *(LAS u32x4*)(lds + 57344 + tid * 16) = (u32x4){0, 0, 0, 0}; } \
        } while (0)
#define TOFF(i) (phys((i) >> 1) + ((i) & 1) * 4096)
#define TILE(i, SC, SN) do { \
            __builtin_amdgcn_s_setprio(((i) & 1) ? 1 : 3); \
            if ((i) < 31) { f32x16 z_ = {}; SN = MFMA(k0, q0, z_); PIN(SN); } if ((i) < 30) k0 = lds8(AP(TOFF((i) + 2))); SB(); \
            EXP4(SC, 0); PIN(SC); SB(); \
            o = MFMA(v1, BF8(p1), o); PIN(o); v1 = lds8(VP(TOFF(i) + 3072)); SIDE((i) >> 1, ((i) & 1) * 2); SB(); \
            EXP4(SC, 4); p0 = pack8(SC, 0); PIN(p0); SB(); \
            __builtin_amdgcn_s_setprio(((i) & 1) ? 0 : 2); \
            if ((i) < 31) { SN = MFMA(k1, q1, SN); PIN(SN); } if ((i) < 30) k1 = lds8(AP(TOFF((i) + 2) + 1024)); SB(); \
            EXP4(SC, 8); PIN(SC); SB(); \
            o = MFMA(v0, BF8(p0), o); PIN(o); if ((i) < 31) v0 = lds8(VP(TOFF((i) + 1) + 2048)); SIDE((i) >> 1, ((i) & 1) * 2 + 1); SB(); \
            EXP4(SC, 12); p1 = pack8(SC, 1); PIN(p1); SB(); } while (0)
#define STEP(t) do { TILE(2 * (t), sa, sb); TILE(2 * (t) + 1, sb, sa); } while (0)
        STEP(0);
        waitv_barx<4>();
        STEP(1); STEP(2);
        if (P3 && __builtin_expect(!wave_all(rtf == tag), 0)) poll_flag(ws, rto, tag, 0xffffffffull);
        waitv_bar2<P3 ? 2 : 3>();
        TS(21);
        if (!P3 && __builtin_expect(!wave_all(rtf == tag), 0)) poll_flag(ws, rto, tag, 0xffffffffull);
        FENCE();
        STEP(3); STEP(4); STEP(5); STEP(6);
        waitv_bar2<CLRMODE == 1 ? 18 : 12>();
        TS(22);
        STEP(7); STEP(8); STEP(9); STEP(10);
        waitv_bar2<CLRMODE == 1 ? 14 : 9>();
        if (CLRMODE == 1) PIN(clv);
        TS(23);
        STEP(11); STEP(12); STEP(13); STEP(14); STEP(15);
        o = MFMA(v1, BF8(p1), o);
        TS(7);
        u32x4 ov;
        {
            *(LAS f32x4*)xw = (f32x4){o[8], o[9], o[10], o[11]}; *(LAS f32x4*)(xw + 1024) = (f32x4){o[12], o[13], o[14], o[15]};
            const float l = kh ? o[4] : o[12];
            *(LAS float*)lw = l;
#define AMX(a, b, c) __builtin_fmaxf(__builtin_fmaxf(__builtin_fabsf(a), __builtin_fabsf(b)), __builtin_fabsf(c))
            const float mx_ = AMX(AMX(AMX(o[0], o[1], o[2]), AMX(o[3], o[4], o[5]), AMX(o[6], o[7], o[8])), AMX(AMX(o[9], o[10], o[11]), AMX(o[12], o[13], o[14]), o[15]), 0.f);
            const bool ok = (mx_ < 0x1p100f) & ((hf ? l : 1.f) > 0x1p-80f);
            *(LAS unsigned*)bw = wave_all(ok) ? 0u : 1u;
            BAR();
            const f32x4 a = *(LAS f32x4*)xr, c = *(LAS f32x4*)(xr + 1024);
            const float inv = __builtin_amdgcn_rcpf(*(LAS float*)lr + *(LAS float*)(lr + 1024));
            const unsigned bad = *(LAS unsigned*)br;
            const float iz = (kh && hf) ? 0.f : inv;
            ov = (u32x4){cvtpk((o[0] + a[0]) * inv, (o[1] + a[1]) * inv), cvtpk((o[2] + a[2]) * inv, (o[3] + a[3]) * inv),
                         cvtpk((o[4] + c[0]) * iz, (o[5] + c[1]) * iz), cvtpk((o[6] + c[2]) * iz, (o[7] + c[3]) * iz)};
            if (__builtin_expect(wave_any(bad != 0), 0)) {
                f32x16 of = {};
                float m = -0x1p126f;
#pragma unroll 1
                for (int t = 0; t < 16; ++t) {
                    BAR();
                    glds2_dev(l16, kvs + t * 16384, lds0 + 65536 + w * 2048);
                    waitv_bar<0>();
#pragma unroll 1
                    for (int u = 0; u < 2; ++u) {
                        f32x16 s = {};
                        s = MFMA(lds8(apB + u * 4096), q0, s); s = MFMA(lds8(apB + u * 4096 + 1024), q1, s);
                        float mx = s[0];
#pragma unroll
                        for (int i = 1; i < 16; ++i) mx = __builtin_fmaxf(mx, s[i]);
                        mx = __builtin_fmaxf(swap_max(mx), m);
                        const float alpha = __builtin_amdgcn_exp2f(m - mx);
                        m = mx;
#pragma unroll
                        for (int i = 0; i < 16; ++i) { s[i] = __builtin_amdgcn_exp2f(s[i] - mx); of[i] *= alpha; }
                        of = MFMA(lds8(vpB + u * 4096 + 2048), BF8(pack8(s, 0)), of);
                        of = MFMA(lds8(vpB + u * 4096 + 3072), BF8(pack8(s, 1)), of);
                    }
                }
                *(LAS f32x4*)xw = (f32x4){of[8], of[9], of[10], of[11]}; *(LAS f32x4*)(xw + 1024) = (f32x4){of[12], of[13], of[14], of[15]};
                *(LAS float*)lw = hf ? (kh ? of[4] : of[12]) : m;
                BAR();
                const f32x4 a2 = *(LAS f32x4*)xr, c2 = *(LAS f32x4*)(xr + 1024);
                const float m0 = *(LAS float*)(lr - 128), m1 = *(LAS float*)(lr + 1024 - 128), mm = __builtin_fmaxf(m0, m1);
                const float e0 = __builtin_amdgcn_exp2f(m0 - mm), e1 = __builtin_amdgcn_exp2f(m1 - mm);
                const float iv = __builtin_amdgcn_rcpf(*(LAS float*)lr * e0 + *(LAS float*)(lr + 1024) * e1);
                const float im = (kh ? e1 : e0) * iv, io = (kh ? e0 : e1) * iv;
                const bool z = kh && hf;
                ov = (u32x4){cvtpk(of[0] * im + a2[0] * io, of[1] * im + a2[1] * io), cvtpk(of[2] * im + a2[2] * io, of[3] * im + a2[3] * io),
                             z ? 0u : cvtpk(of[4] * im + c2[0] * io, of[5] * im + c2[1] * io), z ? 0u : cvtpk(of[6] * im + c2[2] * io, of[7] * im + c2[3] * io)};
            }
        }
        st16_dev(ov, rws, oo, 0);
        TS(25);
        FENCE();
        PIN2(xa, xb);
#pragma unroll
        for (int s = 27; s >= 0; --s) PIN(wf[s]);
#pragma unroll
        for (int s = 0; s < 16; s += 2) { csg = MFMA16(wf[s], BF8(one4), csg); csu = MFMA16(wf[s + 1], BF8(one4), csu); }
        acc[0] = bflo(xa[0]); acc[1] = bfhi(xa[0]); acc[2] = bflo(xa[1]); acc[3] = bfhi(xa[1]); acc[4] = bflo(xb[0]); acc[5] = bfhi(xb[0]); acc[6] = bflo(xb[1]); acc[7] = bfhi(xb[1]);
        PIN(acc); PIN2(ho, so); PIN2(pf2, fo3); PINS(os); PINS(od); PIN2(csg, csu);
        waitv_bar<0>();
        if (w == 0) set_flag(ws, fo2, tagv);
        TS(8);
    }
    if (__builtin_expect(!ffn, 0)) {
        const int ri = xcd * 4 + loc - 28;
        if (ri < 28) {
#pragma unroll 1
            for (int u = w; u < 28; u += 8) {
                lds_ptr sd = lds + u * 2048 + (4 * hf + q4) * 128 + g1 * 32 + p4 * 8;
#pragma unroll
                for (int e = 0; e < 2; ++e) { lds_ptr p_ = sd + ((e ^ (q4 >> 1)) * 64);
                    st16_dev(U4(cat(trd(p_), trd(p_ + 1024))), rws, l16, WS_WD + (unsigned)(((ri % 7) * 112 + (ri / 7) * 28 + u) * 2 + e) * 1024); }
            }
        }
        waitv_bar<0>();
        if (w == 0) set_flag(ws, (FL_RD + ri) * 128, tagv);
        return;
    }

    const int erb = tb >> 1;
    const char* hs = ws + WS_HF + (size_t)drb * 114688 + w * 1024;
    const char* hss = ws + WS_HS + (size_t)((drb * 4 + (w & 3)) * 28 + kh * 14) * 256;
    unsigned pf3, fo4 = (FL_G + drb * 64 + dfg) * 128 + w * 8;
    {
        lds_ptr wb = lds + kh * (8 * 28 * 128) + (4 * hf + q4) * 128 + ((ct ^ (q4 >> 1)) * 64) + (g1 ^ kh) * 32 + p4 * 8;
        bf16x8 wr[16];
#pragma unroll
        for (int s = 0; s < 12; ++s) { const int k0 = ((s >> 1) * 28 + (s & 1) * 16) * 128; wr[s] = cat(trd(wb + k0), trd(wb + k0 + 1024)); }
        lds_ptr cw = opq(lds + 131072 + w * 2048 + l16), cr = opq(lds + 131072 + (w ^ 4) * 2048 + l16);
        lds_ptr ob = opq(lds + 65536 + tt * 32768 + kh * 16384 + l16);
        pf3 = (FL_H + drb * 16 + (lane & 8) + umin(lane & 7, 6)) * 128;
        PIN2(pf3, fo4); PINS(hs); PINS(hss);
        if (DLY2) __builtin_amdgcn_s_sleep(DLY2);
        poll_flag(ws, pf2, tag, 0x55);
        TS(9);
        glds4_dev(l16, os, od);
        if (!CST) asm volatile("s_barrier" ::: "memory");
        glds4_dev(l16, os + 8192, od + 8192);
        bf16x8 of[8];
        waitv_bar<CST ? 0 : 4>();
        TS(26);
#pragma unroll
        for (int s = 0; s < 8; ++s) of[s] = lds8(ob + s * 1024);
        SB();
#pragma unroll
        for (int s = 0; s < 8; ++s) {
            acc = MFMA(wr[s], of[s], acc); PIN(acc); SB();
            if (s >= 2 && s < 6) { const int k0 = (((s + 10) >> 1) * 28 + (s & 1) * 16) * 128; wr[s + 10] = cat(trd(wb + k0), trd(wb + k0 + 1024)); SB(); }
            if (CST) { of[s] = lds8(ob + (8 + s) * 1024); SB(); }
        }
        if (!CST) {
            waitv_barx<0>();
#pragma unroll
            for (int s = 0; s < 8; ++s) of[s] = lds8(ob + (8 + s) * 1024);
        }
        SB();
#pragma unroll
        for (int s = 0; s < 8; ++s) acc = MFMA(wr[8 + s], of[s], acc);
        TS(10);
        *(LAS f32x4*)cw = (f32x4){acc[8], acc[9], acc[10], acc[11]}; *(LAS f32x4*)(cw + 1024) = (f32x4){acc[12], acc[13], acc[14], acc[15]};
        BAR();
        const f32x4 a = *(LAS f32x4*)cr + (f32x4){acc[0], acc[1], acc[2], acc[3]}, b = *(LAS f32x4*)(cr + 1024) + (f32x4){acc[4], acc[5], acc[6], acc[7]};
        const u32x4 hv = {cvtpk(a[0], a[1]), cvtpk(a[2], a[3]), cvtpk(b[0], b[1]), cvtpk(b[2], b[3])};
        st16_dev(hv, rws, ho, 0);
        FENCE(); SB();
        float s1 = 0.f, s2 = 0.f;
        { f32x4 q4_ = {}; acc_stats2(hv[0], hv[1], q4_); acc_stats2(hv[2], hv[3], q4_); s1 = swap_add(q4_[0] + q4_[1]); s2 = swap_add(q4_[2] + q4_[3]); }
        st8_dev((u32x2){f2u(s1), f2u(s2)}, rws, so, 0);
        FENCE(); PIN2(csg, csu); SB();
#pragma unroll
        for (int s = 16; s < 28; s += 2) { csg = MFMA16(wf[s], BF8(one4), csg); csu = MFMA16(wf[s + 1], BF8(one4), csu); }
        PIN2(csg, csu);
        waitv_bar<0>();
        if (w == 0) set_flag(ws, fo3, tagv);
        if (bid == 0 && w == 0) set_flag(ws, FL_EPOCH * 128, tagv);
        TS(11);
    }

    const unsigned wdo = WS_WD + (unsigned)(m7 * 112 + w * 14) * 2048;
    bf16x8 wdf[28];
    bf16x8 gf[GW];
    const unsigned gxo = WS_GF + (unsigned)(erb * 4 * 112 + w * 14) * 1024 + (tb & 1) * 256;
    const unsigned pwd = hf * 2048 + g1 * 512 + (lane & 15) * 16;
#define G_LD(s) gf[(s) % GW] = BF8(ld16_dev(rws, p16, gxo + ((s) / 7) * 114688 + ((s) % 7) * 2048))
    u64 gfl = 0;
    const unsigned gfi = (FL_G + (erb * 4 + g1) * 16 + ((w * 14 + umin(lane & 15, 13)) >> 3)) * 128;
    {
        const unsigned hd = lds0 + w * 1024;
        const char* wds = ws + wdo;
        lds_ptr hb = opq(lds + p16), hb2 = opq(lds + 57344 + p16);
        lds_ptr wl = opq(lds + 114688 + w * 5120 + l16), wl2 = opq(lds + w * 7168 + l16);
        LAS f32x2* stp = (LAS f32x2*)opq(lds + 159744 + (lane & 15) * 8);
        if (w == HOP3_POLLER) { if (DLY3) __builtin_amdgcn_s_sleep(DLY3); poll_flag(ws, pf3, tag, 0x7f7f); }
        BAR();
        TS(12);
#define H_PIECE(j) glds_dev(l16, hs + (j) * 8192, hd + (j) * 8192)
#define WDO(s) (((s) >> 2) * 4096 + (((s) >> 1) & 1) * 1024 + ((s) & 1) * 256)
#define WDLD(s) wdf[s] = BF8(ld16_dev(rws, pwd, wdo + WDO(s)))
#define HB(n) (((n) < 56 ? hb + ((n) & ~1) * 1024 : hb2 + (((n) & ~1) - 56) * 1024) + ((n) & 1) * 256)
        H_PIECE(0);
        asm volatile("s_barrier" ::: "memory");
        H_PIECE(1); H_PIECE(2); H_PIECE(3);
        constexpr int PF = 5;
        bf16x8 bf[PF + 1];
        f32x4 ag[2][2], au[2][2];
        f32x2 sp[7], sv[4];
        float mean[2] = {0.f, 0.f}, r2[2] = {0.f, 0.f}, hp = 0.f;
        u32x4 hv;
        lds_ptr pk = opq(lds + 155648 + w * 512 + lane * 4);
        u64 pv = 0;
        const unsigned pkd = lds0 + 155648 + w * 512;
        const unsigned rdo = (FL_RD + ((lane & 4) ? (w >> 1) * 7 + m7 : ((lane & 2) ? drb : erb) * 2 + (lane & 1))) * 128;
        waitv_barx<3>();
        TS(27);
#pragma unroll
        for (int n = 0; n < PF; ++n) bf[n] = lds8(HB(n));
        SB();
        sfor<0, 125>([&](auto N_) __attribute__((always_inline)) {
            constexpr int n = decltype(N_)::v, p = n / 28, s = n % 28;
            if constexpr (n == 3) waitv_barx<3>();
            if constexpr (n == 11) waitv_barx<7>();
            if constexpr (n == 27) { waitv_barx<3>(); TS(13); }
            if constexpr (n == 64) { waitv_barx<12>(); if (!GWG || w == 0) set_flag(ws, fo4, tagv); TS(14); }
            if constexpr (n == 85) { waitv_barx<XN ? 9 : 7>(); if (!GWG || w == 0) set_flag(ws, fo4 + 2048, tagv); }
            if constexpr (n == 38) { if (__builtin_expect(!wave_all(pv == tag), 0)) poll_flag(ws, rdo, tag); FENCE(); }
            if constexpr (d_prio(n) != d_prio(n - 1)) __builtin_amdgcn_s_setprio(d_prio(n));
            if constexpr (n < 112) {
                if constexpr (s < 2) { ag[p & 1][s] = (f32x4){}; au[p & 1][s] = (f32x4){}; }
                ag[p & 1][s & 1] = MFMA16(wf[s & ~1], bf[n % (PF + 1)], ag[p & 1][s & 1]); au[p & 1][s & 1] = MFMA16(wf[s | 1], bf[n % (PF + 1)], au[p & 1][s & 1]);
                PIN2(ag[p & 1][s & 1], au[p & 1][s & 1]);
            }
            SB();
            if constexpr (n + PF < 112) bf[(n + PF) % (PF + 1)] = lds8(HB(n + PF));
            if constexpr (n % 3 == 2 && n < 30) H_PIECE(4 + n / 3);
            if constexpr (n == 4) glds4_dev(l16, hss, lds0 + 114688 + w * 5120);
            if constexpr (n == 16) { waitv<4>();
#pragma unroll
                for (int j = 0; j < 7; ++j) sp[j] = *(LAS f32x2*)(lds + 114688 + w * 5120 + (hf * 7 + j) * 256 + r * 8);
            }
            if constexpr (n == 20) { const f32x2 t = ((sp[0] + sp[1]) + (sp[2] + sp[3])) + ((sp[4] + sp[5]) + sp[6]); stp[w * 32 + g1 * 16] = (f32x2){swap_add(t[0]), swap_add(t[1])}; }
            if constexpr (n >= 39 && n < 48 && n % 2 == 1) glds_dev(pwd, wds + WDO((n - 39) / 2), lds0 + 114688 + w * 5120 + (n - 39) / 2 * 1024);
            if constexpr (n >= 48 && n < 64 && n % 2 == 0) WDLD(12 + (n - 48) / 2);
            if constexpr (n >= 70 && n < 84 && n % 2 == 0) glds_dev(pwd, wds + WDO(5 + (n - 70) / 2), lds0 + w * 7168 + (n - 70) / 2 * 1024);
            if constexpr (n >= 86 && n < 102 && n % 2 == 0) WDLD(20 + (n - 86) / 2);
            if constexpr (XN > 0 && n == 84) peek_lds(ws, gfi - g1 * 2048, pkd);
            if constexpr (XN > 0 && n == 95) { waitv<5>(); pv = peeked(pk); }
            if constexpr (XN > 0 && n == 99) { if (__builtin_expect(!wave_all(pv == tag), 0)) poll_flag(ws, gfi - g1 * 2048, tag); FENCE(); }
            if constexpr (n >= 102 && n < 102 + 2 * XN && n % 2 == 0) G_LD((n - 102) / 2);
            if constexpr (n == 111) gfl = peek_flag(ws, gfi);
            if constexpr (n == 32) pv = peeked(pk);
            if constexpr (n == 18) peek_lds(ws, rdo, pkd);
            if constexpr (p >= 1) {
                constexpr int e = (p - 1) & 1, tp = n == 100 ? 3 : p - 1;
                if constexpr ((s == 0 && p < 4) || n == 100) { sv[0] = stp[tp * 32]; sv[1] = stp[(tp + 4) * 32]; sv[2] = stp[tp * 32 + 16]; sv[3] = stp[(tp + 4) * 32 + 16]; }
                if constexpr (((s == 2 || s == 3) && p < 4) || n == 104 || n == 105) { constexpr int k = n & 1; const f32x2 t = sv[2 * k] + sv[2 * k + 1];
                    mean[k] = t[0] * (1.f / D); r2[k] = __builtin_amdgcn_rcpf(__builtin_fmaxf(t[1] * (1.f / D) - mean[k] * mean[k], 0.f) + EPS); PIN2(mean[k], r2[k]); }
                if constexpr (s >= 4 && s < 12) { constexpr int i = s - 4, k = i >> 2;
                    const float h0 = __builtin_fmaxf(ag[e][k][i & 3] - mean[k] * csg[i & 3], 0.f) * r2[k] * (au[e][k][i & 3] - mean[k] * csu[i & 3]);
                    if constexpr (i & 1) { hv[i >> 1] = p >= 3 ? umin(cvtpk(hp, h0), ~1u) : cvtpk(hp, h0); PIN(hv); } else { hp = h0; PIN(hp); } }
                if constexpr (s == 12) {
                    const auto s0 = __builtin_amdgcn_permlane32_swap(hv[0], hv[2], false, false), s1 = __builtin_amdgcn_permlane32_swap(hv[1], hv[3], false, false);
                    st16_dev((u32x4){s0[0], s1[0], s0[1], s1[1]}, rws, go + (p - 1) * 114688, 0); FENCE();
                }
            }
            SB();
        });
        TS(15);
        if (XN == 0) { if (__builtin_expect(!wave_all(gfl == tag), 0)) poll_flag(ws, gfi, tag); FENCE(); }
#pragma unroll
        for (int s = XN; s < 7; ++s) { G_LD(s); SB(); }
        FENCE();
#pragma unroll
        for (int s = 0; s < 12; ++s) wdf[s] = lds8(s < 5 ? wl + s * 1024 : wl2 + (s - 5) * 1024);
    }

    {
        const int etok = tid >> 4, eblk = tid & 15;
        const int et = erb * 4 + (etok >> 4), er = (tb & 1) * 16 + (etok & 15);
        const unsigned hro = (unsigned)((((eblk >> 2) & 3) * 64 + (eblk & 1) * 32 + er) * 16 + ((eblk >> 1) & 1) * 8) + WS_HF + (unsigned)(et * 28 + m7 * 4) * 1024;
        u32x2 hres[2];
        GAS u64* op = (GAS u64*)(out + (size_t)(et * 32 + er) * 896 + m7 * 128 + eblk * 8);
        lds_ptr rp = opq(lds + w * 8448 + (lane >> 4) * 528 + (lane & 15) * 16), rq = opq(lds + eblk * 528 + etok * 16);
        lds_ptr rp2 = opq(rp + 67584), rq2 = opq(rq + 67584);
        f32x4 c[4];
#define G_STEP(s) do { _Pragma("unroll") for (int j = 0; j < 4; ++j) { if ((s) % 7 == 0) c[j] = (f32x4){}; c[j] = MFMA16(wdf[4 * ((s) % 7) + j], gf[(s) % GW], c[j]); } PIN2(c[0], c[1]); PIN2(c[2], c[3]); } while (0)
#define G_PUT(q) do { _Pragma("unroll") for (int j = 0; j < 4; ++j) *(LAS f32x4*)(((q) < 2 ? rp : rp2) + j * 2112 + ((q) & 1) * 256) = c[j]; } while (0)
#define G_OUT(hh, RQ) do { f32x4 pa[8]; \
            _Pragma("unroll") for (int ww = 0; ww < 8; ++ww) pa[ww] = *(LAS f32x4*)(RQ + ww * 8448); SB(); \
            const f32x4 a = ((pa[0] + pa[1]) + (pa[2] + pa[3])) + ((pa[4] + pa[5]) + (pa[6] + pa[7])); \
            const u32x2 ov = {cvtpk(a[0] + bflo(hres[hh][0]), a[1] + bfhi(hres[hh][0])), cvtpk(a[2] + bflo(hres[hh][1]), a[3] + bfhi(hres[hh][1]))}; \
            __hip_atomic_store(op + (hh) * (64 * 896 / 8), __builtin_bit_cast(u64, ov), __ATOMIC_RELAXED, __HIP_MEMORY_SCOPE_AGENT); } while (0)
        TS(16);
        unsigned seen = 0;
#pragma unroll
        for (int s = 0; s < 28; ++s) {
            if (s == 6) BAR();
            if (s == 7 || s == 14 || s == 21) G_PUT(s / 7 - 1);
            if (s == 14) TS(17);
            if (s == 21) { waitv<7>(); PIN2(hres[0], hres[1]); BAR(); G_OUT(0, rq); FENCE(); TS(18); }
            SB();
            G_STEP(s);
            if (s >= 14) { const u32x4 u = U4(gf[s % GW]); seen = umax3(umax3(seen, u[0], u[1]), u[2], u[3]); PIN(seen); }
            SB();
            if (s == 0) { if (XN > 0) { if (__builtin_expect(!wave_all(gfl == tag), 0)) poll_flag(ws, gfi, tag); FENCE(); } }
            if (s + GW < 28) G_LD(s + GW);
            if (s == 1) { hres[0] = ld8_dev(rws, hro, 0); hres[1] = ld8_dev(rws, hro, 57344); FENCE(); }
        }
        if (w == 0) { set_flag(ws, fo4 + 4096, tagv); set_flag(ws, fo4 + 6144, tagv); }
        if (__builtin_expect(wave_any(seen == ~0u), 0)) {
            poll_flag(ws, gfi + 4096, tag);
#pragma unroll
            for (int s = 14; s < 14 + GW; ++s) G_LD(s);
#pragma unroll
            for (int s = 14; s < 28; ++s) { if (s == 21) G_PUT(2); G_STEP(s); SB(); if (s + GW < 28) G_LD(s + GW); SB(); }
        }
        G_PUT(3);
        TS(19);
        BAR();
        G_OUT(1, rq2);
    }
    TS(20);
}

#define WS_OFF 0
extern "C" void kernel_launch(void* const* d_in, const int* in_sizes, int n_in, void* d_out, int out_size, void* d_ws, size_t ws_size, hipStream_t stream) {
    (void)hipFuncSetAttribute((const void*)block_fwd, hipFuncAttributeMaxDynamicSharedMemorySize, LDS_BYTES);
    block_fwd<<<256, 512, LDS_BYTES, stream>>>((const char*)d_in[0], (const char*)d_in[1], (const char*)d_in[2], (const char*)d_in[4], (const char*)d_in[5], (const char*)d_in[6], (char*)d_out, (char*)d_ws + WS_OFF);
}
```

```cpp
#define SPDLY 10
#include <hip/hip_runtime.h>
#include <hip/hip_bf16.h>
#include <stdint.h>

typedef __attribute__((ext_vector_type(8))) short bf16x8;
typedef __attribute__((ext_vector_type(4))) short s16x4;
typedef __attribute__((ext_vector_type(2))) __bf16 bf16x2;
typedef __attribute__((ext_vector_type(16))) float f32x16;
typedef __attribute__((ext_vector_type(4))) float f32x4;
typedef __attribute__((ext_vector_type(2))) float f32x2;
typedef __attribute__((ext_vector_type(4))) unsigned u32x4;
typedef __attribute__((ext_vector_type(2))) unsigned u32x2;
typedef short v4i16_t __attribute__((ext_vector_type(4)));
typedef unsigned long long u64;
#define LAS __attribute__((address_space(3)))
#define GAS __attribute__((address_space(1)))
typedef LAS char* lds_ptr;
#if defined(__HIP_DEVICE_COMPILE__)
#define NOPK __attribute__((target("no-packed-fp32-ops")))
#else
#define NOPK
#endif
#define DI __device__ __forceinline__
#define MFMA(a, b, c) __builtin_amdgcn_mfma_f32_32x32x16_bf16(a, b, c, 0, 0, 0)
#define MFMA16(a, b, c) __builtin_amdgcn_mfma_f32_16x16x32_bf16(a, b, c, 0, 0, 0)
#define BF8(x) __builtin_bit_cast(bf16x8, x)
#define U4(x) __builtin_bit_cast(u32x4, x)

constexpr int D = 448;
constexpr float EPS = 1e-5f;
constexpr unsigned WS_FLAG = 0;
constexpr unsigned FL_EPOCH = 0, FL_KV = 1, FL_RT = FL_KV + 256, FL_RD = FL_RT + 32, FL_CL = FL_RD + 32, FL_O = FL_CL + 32, FL_H = FL_O + 256, FL_G = FL_H + 256;
constexpr unsigned WS_KV = 1u << 20;
constexpr unsigned WS_QF = WS_KV + 4194304;
constexpr unsigned WS_OF = WS_QF + 2097152;
constexpr unsigned WS_HF = WS_OF + 2097152;
constexpr unsigned WS_GF = WS_HF + 64 * 28 * 1024;
constexpr unsigned WS_WGU = WS_GF + 64 * 112 * 1024;
constexpr unsigned WS_WD = WS_WGU + 112 * 28 * 1024;
constexpr unsigned WS_HS = WS_WD + 7 * 112 * 2 * 1024;
constexpr unsigned WS_END = WS_HS + 64 * 28 * 256 + 1024;
constexpr unsigned OOB = 0x40000000u;
constexpr int LDS_BYTES = 163840;

DI float u2f(unsigned u) { return __builtin_bit_cast(float, u); }
DI unsigned f2u(float f) { return __builtin_bit_cast(unsigned, f); }
DI unsigned umin(unsigned a, unsigned b) { return a < b ? a : b; }
DI unsigned umax3(unsigned a, unsigned b, unsigned c) { const unsigned m = a > b ? a : b; return m > c ? m : c; }
DI bool wave_any(bool p) { return __builtin_amdgcn_ballot_w64(p) != 0; }
DI bool wave_all(bool p) { return __builtin_amdgcn_ballot_w64(!p) == 0; }
DI unsigned cvtpk(float lo, float hi) { return __builtin_bit_cast(unsigned, __builtin_convertvector((f32x2){lo, hi}, bf16x2)); }
DI float bflo(unsigned u) { return u2f(u << 16); }
DI float bfhi(unsigned u) { return u2f(u & 0xffff0000u); }
DI void glds(unsigned voff, const void* sbase, unsigned dst) {
    asm volatile("s_mov_b32 m0, %2\n\ts_nop 0\n\tglobal_load_lds_dwordx4 %0, %1" :: "v"(voff), "s"(sbase), "s"(dst) : "memory");
}
DI void glds_dev(unsigned voff, const void* sbase, unsigned dst) {
    asm volatile("s_mov_b32 m0, %2\n\ts_nop 0\n\tglobal_load_lds_dwordx4 %0, %1 sc1" :: "v"(voff), "s"(sbase), "s"(dst) : "memory");
}
DI void glds2_dev(unsigned voff, const void* sbase, unsigned dst) {
    asm volatile("s_mov_b32 m0, %2\n\ts_nop 0\n\tglobal_load_lds_dwordx4 %0, %1 sc1\n\tglobal_load_lds_dwordx4 %0, %1 offset:1024 sc1" :: "v"(voff), "s"(sbase), "s"(dst) : "memory");
}
DI void glds4_dev(unsigned voff, const void* sbase, unsigned dst) {
    asm volatile("s_mov_b32 m0, %2\n\ts_nop 0\n\tglobal_load_lds_dwordx4 %0, %1 sc1\n\tglobal_load_lds_dwordx4 %0, %1 offset:1024 sc1\n\t"
                 "global_load_lds_dwordx4 %0, %1 offset:2048 sc1\n\tglobal_load_lds_dwordx4 %0, %1 offset:3072 sc1" :: "v"(voff), "s"(sbase), "s"(dst) : "memory");
}
DI s16x4 trd(lds_ptr p) { return __builtin_bit_cast(s16x4, __builtin_amdgcn_ds_read_tr16_b64_v4i16((LAS v4i16_t*)p)); }
DI bf16x8 cat(s16x4 a, s16x4 b) { return __builtin_shufflevector(a, b, 0, 1, 2, 3, 4, 5, 6, 7); }
DI bf16x8 lds8(lds_ptr p) { return *(LAS bf16x8*)p; }
DI float swap_add(float v) { auto r = __builtin_amdgcn_permlane32_swap(f2u(v), f2u(v), false, false); return u2f(r[0]) + u2f(r[1]); }
DI float swap_max(float v) { auto r = __builtin_amdgcn_permlane32_swap(f2u(v), f2u(v), false, false); return __builtin_fmaxf(u2f(r[0]), u2f(r[1])); }
DI u32x4 pack8(const f32x16& a, int s) { return (u32x4){cvtpk(a[8 * s], a[8 * s + 1]), cvtpk(a[8 * s + 2], a[8 * s + 3]), cvtpk(a[8 * s + 4], a[8 * s + 5]), cvtpk(a[8 * s + 6], a[8 * s + 7])}; }
DI float dot2(unsigned a, unsigned b, float c) { return __builtin_amdgcn_fdot2_f32_bf16(__builtin_bit_cast(bf16x2, a), __builtin_bit_cast(bf16x2, b), c, false); }
DI void acc_stats(u32x4 u, float& s1, float& s2) {
#pragma unroll
    for (int i = 0; i < 4; ++i) { const float a = bflo(u[i]), b = bfhi(u[i]); s1 += a; s2 = __builtin_fmaf(a, a, s2); s1 += b; s2 = __builtin_fmaf(b, b, s2); }
}
DI void acc_stats2(unsigned u0, unsigned u1, f32x4& s) {
    const float a = bflo(u0), b = bfhi(u0), c = bflo(u1), d = bfhi(u1);
    s[0] += a; s[1] += b; s[2] = __builtin_fmaf(a, a, s[2]); s[3] = __builtin_fmaf(b, b, s[3]); s[0] += c; s[1] += d; s[2] = __builtin_fmaf(c, c, s[2]); s[3] = __builtin_fmaf(d, d, s[3]);
}
DI float sum8(float v) {
    v += u2f(__builtin_amdgcn_update_dpp(0u, f2u(v), 0xB1, 0xf, 0xf, true));
    v += u2f(__builtin_amdgcn_update_dpp(0u, f2u(v), 0x4E, 0xf, 0xf, true));
    v += u2f(__builtin_amdgcn_update_dpp(0u, f2u(v), 0x141, 0xf, 0xf, true));
    return v;
}
DI u32x4 shift8(u32x4 x, float c) {
    u32x4 o; float t0, t1, t2, t3, t4, t5, t6, t7;
    asm("v_dot2_f32_bf16 %4, %12, %16, %18\n\tv_dot2_f32_bf16 %5, %12, %17, %18\n\tv_dot2_f32_bf16 %6, %13, %16, %18\n\tv_dot2_f32_bf16 %7, %13, %17, %18\n\t"
        "v_dot2_f32_bf16 %8, %14, %16, %18\n\tv_dot2_f32_bf16 %9, %14, %17, %18\n\tv_dot2_f32_bf16 %10, %15, %16, %18\n\tv_dot2_f32_bf16 %11, %15, %17, %18\n\t"
        "v_cvt_pk_bf16_f32 %0, %4, %5\n\tv_cvt_pk_bf16_f32 %1, %6, %7\n\tv_cvt_pk_bf16_f32 %2, %8, %9\n\tv_cvt_pk_bf16_f32 %3, %10, %11"
        : "=&v"(o[0]), "=&v"(o[1]), "=&v"(o[2]), "=&v"(o[3]), "=&v"(t0), "=&v"(t1), "=&v"(t2), "=&v"(t3), "=&v"(t4), "=&v"(t5), "=&v"(t6), "=&v"(t7)
        : "v"(x[0]), "v"(x[1]), "v"(x[2]), "v"(x[3]), "s"(0x00003f80u), "s"(0x3f800000u), "v"(c));
    return o;
}
template <int N> struct IC { static constexpr int v = N; };
template <int B, int E, class F> DI void sfor(F&& f) { if constexpr (B < E) { f(IC<B>{}); sfor<B + 1, E>(f); } }
template <int N> DI void waitv() { asm volatile("s_waitcnt vmcnt(%0)" :: "i"(N) : "memory"); }
template <int N> DI void waitv_bar() { if (N == 0) __builtin_amdgcn_s_waitcnt(0x0F70); asm volatile("s_waitcnt vmcnt(%0) lgkmcnt(0)\n\ts_barrier" :: "i"(N) : "memory"); }
template <int N> DI void waitv_barx() { asm volatile("s_waitcnt vmcnt(%0)\n\ts_barrier" :: "i"(N) : "memory"); }
template <int N> DI void waitv_bar2() { asm volatile("s_waitcnt vmcnt(%0) lgkmcnt(2)\n\ts_barrier" :: "i"(N) : "memory"); }
DI void touch(unsigned voff, const void* sbase, unsigned junk) {
    asm volatile("s_mov_b32 m0, %2\n\ts_nop 0\n\tglobal_load_lds_dword %0, %1 sc1" :: "v"(voff), "s"(sbase), "s"(junk) : "memory");
}
#define BAR() asm volatile("s_waitcnt lgkmcnt(0)\n\ts_barrier" ::: "memory")
#define FENCE() asm volatile("" ::: "memory")
typedef __amdgpu_buffer_rsrc_t rsrc_t;
DI u32x4 ld16(rsrc_t rs, unsigned voff, unsigned soff) { return U4(__builtin_amdgcn_raw_buffer_load_b128(rs, voff, soff, 0)); }
DI u32x4 ld16_dev(rsrc_t rs, unsigned voff, unsigned soff) { return U4(__builtin_amdgcn_raw_buffer_load_b128(rs, voff, soff, 16)); }
DI u32x2 ld8(rsrc_t rs, unsigned voff, unsigned soff) { return __builtin_bit_cast(u32x2, __builtin_amdgcn_raw_buffer_load_b64(rs, voff, soff, 0)); }
DI u32x2 ld8_dev(rsrc_t rs, unsigned voff, unsigned soff) { return __builtin_bit_cast(u32x2, __builtin_amdgcn_raw_buffer_load_b64(rs, voff, soff, 16)); }
DI void st16_dev(u32x4 v, rsrc_t rs, unsigned voff, unsigned soff) { __builtin_amdgcn_raw_buffer_store_b128(v, rs, voff + soff, 0, 16); }
DI void st8_dev(u32x2 v, rsrc_t rs, unsigned voff, unsigned soff) { __builtin_amdgcn_raw_buffer_store_b64(v, rs, voff, soff, 16); }
#define POLL_SLEEP 1
DI void set_flag(char* ws, unsigned fo, u64 tagv) {
    asm volatile("s_mov_b64 exec, 1\n\tglobal_store_dwordx2 %0, %1, %2 sc1\n\ts_mov_b64 exec, -1" :: "v"(fo), "v"(tagv), "s"(ws) : "memory");
}
DI u64 peek_flag(char* ws, unsigned fo) { return __hip_atomic_load((GAS u64*)(ws + fo), __ATOMIC_RELAXED, __HIP_MEMORY_SCOPE_AGENT); }
DI void peek_lds(char* ws, unsigned fo, unsigned dst) {
    asm volatile("s_mov_b32 m0, %2\n\ts_nop 0\n\tglobal_load_lds_dword %0, %1 sc1\n\ts_add_u32 m0, %2, 252\n\ts_nop 0\n\tglobal_load_lds_dword %0, %1 offset:4 sc1" :: "v"(fo), "s"(ws), "s"(dst) : "memory", "scc");
}
DI u64 peeked(lds_ptr p) { return (u64)*(LAS unsigned*)(p + 256) << 32 | *(LAS unsigned*)p; }
DI void poll_flag(char* ws, unsigned fo, u64 tag, u64 lanes = 0xffff) {
#pragma unroll 1
    for (int spin = 0; spin < (1 << 16); ++spin) {
        if (wave_all(peek_flag(ws, fo) == tag)) break;
        __builtin_amdgcn_s_sleep(POLL_SLEEP);
    }
    FENCE();
}
#define SB() __builtin_amdgcn_sched_barrier(0)
#define PINS(a) asm volatile("" : "+s"(a))
#define PIN(a) asm volatile("" : "+v"(a))
#define PIN2(a, b) asm volatile("" : "+v"(a), "+v"(b))
DI lds_ptr opq(lds_ptr p) { unsigned u = (unsigned)(uintptr_t)p; asm volatile("" : "+v"(u)); return (lds_ptr)(uintptr_t)u; }
#define TS(i) do {} while (0)

DI constexpr int wd_slot(int s) { return (s * 17 + 27) / 28 != ((s + 1) * 17 + 27) / 28 ? (s * 17 + 27) / 28 : -1; }
#define EROT 0
#define CLR_SLEEP 90
#define ROTMODE 0
#define EARLYG 0
#define GWG 1
#define GW 7
#define CLRMODE 0
#define DLY1 0
#define DLY2 0
#define DLY3 0
#define P3 1
#define VARLATE 0
#define CST 0
#define XN 4
#define PADQ 24
#define NOTOUCH 1
#define SPEC 1
#define FB 4
#define C2 9
#define C3 17
#define P3S 10
#define ENL 4
#define HOP3_POLLER 1
DI constexpr int d_prio(int n) { return n < 0 ? -1 : n < 27 ? 3 - n / 7 : n < 64 ? 3 - (n - 27) / 10 : n < 84 ? 3 - (n - 64) / 5 : n < 112 ? 3 - (n - 84) / 7 : 0; }
DI constexpr int phys(int t) { return ((t % 9) + 2) % 9 * 16384; }

__global__ NOPK __launch_bounds__(512, 2) void block_fwd(const char* x, const char* wqkv, const char* wo, const char* wg, const char* wu, const char* wd, char* out, char* ws) {
    extern __shared__ __attribute__((aligned(16))) char smem[];
    lds_ptr lds = (lds_ptr)smem;
    const int tid = __builtin_amdgcn_workitem_id_x(), lane = tid & 63, w = __builtin_amdgcn_readfirstlane(tid >> 6);
    const int bid = __builtin_amdgcn_workgroup_id_x(), xcd = bid & 7, loc = bid >> 3;
    const int r = lane & 31, hf = lane >> 5, q4 = (lane & 15) >> 2, g1 = (lane >> 4) & 1, p4 = lane & 3;
    const unsigned lds0 = (unsigned)(uintptr_t)smem;
    const unsigned l16 = lane * 16;
    TS(0);
    const int sp3 = (loc + 8) >> 5;
    const int atb = (xcd >> 1) * 8 + (loc & 7) + sp3 * (xcd & 1) * 4, acg = (loc >> 3) + (1 - sp3) * (xcd & 1) * 4;
    const int row8 = lane >> 3;
    const int rot8 = ((loc & 7) << sp3) + sp3 * (xcd & 1), rot = rot8 == 7 ? 0 : rot8;
    if (loc < 28) {
        const unsigned xo = (unsigned)((atb * 64 + (tid >> 3)) * 896 + (((tid ^ (tid >> 4)) & 7) * 16));
        const unsigned xd0 = lds0 + 1024 + (w >> 2) * 28672 + (w & 3) * 1024;
        unsigned xd = xd0 + (7 - rot) * 4096;
        const unsigned w0 = (unsigned)((rot * 64 + (tid >> 3)) * 2688 + acg * 128 + (((tid & 7) ^ ((tid >> 2) & 4)) * 16));
        asm volatile("s_cmp_eq_u32 %0, %3\n\ts_cselect_b32 %0, %2, %0\n\ts_mov_b32 m0, %0\n\ts_add_u32 %0, %0, 0x1000\n\tglobal_load_lds_dwordx4 %1, %4\n\t"
#define XP(o) "s_cmp_eq_u32 %0, %3\n\ts_cselect_b32 %0, %2, %0\n\ts_sub_u32 m0, %0, " #o "\n\ts_add_u32 %0, %0, 0x1000\n\tglobal_load_lds_dwordx4 %1, %4 offset:" #o "\n\t"
                     XP(128) XP(256) XP(384) XP(512) XP(640) XP(768)
                     "s_add_u32 m0, %6, 8192-896\n\ts_barrier\n\tglobal_load_lds_dwordx4 %5, %7 offset:896\n\t"
                     "s_add_u32 m0, %6, 16384-1792\n\ts_nop 0\n\tglobal_load_lds_dwordx4 %5, %7 offset:1792\n\t"
                     "s_mov_b32 m0, %6\n\ts_nop 0\n\tglobal_load_lds_dwordx4 %5, %7"
                     : "+s"(xd) : "v"(xo), "s"(xd0), "s"(xd0 + 7 * 4096), "s"(x), "v"(w0), "s"(lds0 + 58368 + w * 1024), "s"(wqkv) : "memory", "scc");
    }
    SB();
    const rsrc_t rws = __builtin_amdgcn_make_buffer_rsrc((void*)ws, 0, WS_END, 0x00020000);
    u64 epoch, tagv;
    unsigned fl1;
    const unsigned ones = 0x3f803f80u;
    const bool ffn = loc < 28;
    const int l7 = loc / 7, m7 = loc % 7;
    const int tb = xcd * 4 + l7;
    const int tt = w & 1, ct = (w >> 1) & 1, kh = w >> 2;
    const unsigned swz = (unsigned)(((lane & 7) ^ (((row8 >> 1) & 1) << 2)) * 16);

    if (__builtin_expect(ffn, 1)) {
        constexpr int A_W = 58368, A_WS = 24576;
#define A_KC(c) (rot + (c) >= 7 ? rot + (c) - 7 : rot + (c))
        const unsigned wvo = (unsigned)(row8 * 2688) + swz;
        const char* wsrc = wqkv + acg * 128 + (size_t)w * 8 * 2688;
        const unsigned wdst = lds0 + A_W + w * 1024;
#define A_PIECE(c, i) glds(wvo, wsrc + A_KC(c) * (64 * 2688) + (i) * 896, wdst + ((c) & 3) * A_WS + (i) * 8192)
#define A_GROUP(c) do { A_PIECE(c, 1); A_PIECE(c, 2); A_PIECE(c, 0); } while (0)
        epoch = peek_flag(ws, FL_EPOCH * 128); FENCE(); fl1 = (FL_KV + atb * 8 + acg) * 128;
        if (EARLYG) A_GROUP(1);
        lds_ptr xb[4], wb1, wb1h;
#pragma unroll
        for (int j = 0; j < 4; ++j) xb[j] = lds + 1024 + tt * 28672 + (r >> 3) * 1024 + (r & 7) * 128 + (((2 * j + hf) ^ ((r >> 1) & 7)) * 16);
        wb1 = opq(lds + A_W + (kh ? 0 : 8192) + (8 * hf + q4) * 128 + ((ct ^ (q4 >> 1)) * 64) + g1 * 32 + p4 * 8);
        wb1h = opq(wb1 + 2 * A_WS);
        unsigned go1[2], go2[2]; int gi[2];
#pragma unroll
        for (int p = 0; p < 2; ++p) {
            const int G = acg * 8 + ct * 4 + 2 * p + hf, hp = G / 7, i = G - hp * 7;
            const unsigned hs = kh ? 131072u : 262144u;
            const unsigned to = (kh ? WS_QF + (atb * 2 + tt) * 2048 : WS_KV + (atb * 2 + tt) * 4096) + r * 16;
            go1[p] = (unsigned)(2 * hp + (i > 3)) * hs + (unsigned)(i > 3 ? i - 3 : i) * 512 + to;
            go2[p] = i == 3 ? (unsigned)(2 * hp + 1) * hs + to : OOB;
            gi[p] = i;
        }
        const int vcol = acg * 64 + ct * 32 + r, vh = vcol / 28, vd = vcol - vh * 28;
        unsigned vo = WS_KV + (atb * 2 + tt) * 4096 + (unsigned)vh * 262144 + 2048 + hf * 512 + vd * 16;
        PIN2(go1[0], go1[1]); PIN2(go2[0], go2[1]); PIN(vo);
        lds_ptr xw = opq(lds + 1024 + kh * 28672 + (w & 3) * 1024 + l16);
        waitv<EARLYG ? 7 : 4>();
        TS(28);
        u32x4 xv[7];
#pragma unroll
        for (int c = 0; c < 7; ++c) xv[c] = *(LAS u32x4*)(xw + c * 4096);
        SB();
        f32x4 sm = {};
#pragma unroll
        for (int c = 0; c < 7; ++c)
#pragma unroll
            for (int j = 0; j < 4; ++j) sm[j] = dot2(xv[c][j], ones, sm[j]);
        float nm = sum8((sm[0] + sm[1]) + (sm[2] + sm[3])) * (-1.f / D);
        PIN(nm);
#pragma unroll
        for (int c = 0; c < 7; ++c) { xv[c] = shift8(xv[c], nm); *(LAS u32x4*)(xw + c * 4096) = xv[c]; }
        if (EARLYG) A_GROUP(2); else A_GROUP(1);
        SB();
        LAS float* rst = (LAS float*)lds;
#define A_VAR() do { f32x4 sq = {}; _Pragma("unroll") for (int c_ = 0; c_ < 7; ++c_) _Pragma("unroll") for (int j = 0; j < 4; ++j) sq[j] = dot2(xv[c_][j], xv[c_][j], sq[j]); \
            rst[w * 8 + row8] = __builtin_amdgcn_rsqf(sum8((sq[0] + sq[1]) + (sq[2] + sq[3])) * (1.f / D) + EPS); } while (0)
        if (!VARLATE) A_VAR();
        if (EARLYG) A_GROUP(3); else A_GROUP(2);
        TS(31);
        bf16x8 fx[4], f1[4], f2[4];
#define A_WAIT(c) do { if ((c) == 0) waitv_bar<EARLYG ? 9 : 6>(); else if ((c) == 1) waitv_barx<6>(); else if ((c) < 6) waitv_barx<3>(); else waitv_barx<0>(); \
                       if ((c) == 0) TS(1); if ((c) == 6) TS(2); } while (0)
#define A_WP(c, ks) ((((c) & 2) ? wb1h : wb1) + ((c) & 1) * A_WS + (ks) * 2048)
        if (w < 4) {
            f32x16 ak = {}, av = {};
            f32x4 rv[4];
            float rs = 0.f;
#pragma unroll
            for (int n = 0; n < 30; ++n) {
                const int c = n >> 2, ks = n & 3, km = (n - 2) & 3;
                if (n < 28 && ks == 0) A_WAIT(c);
                if (n >= 2) { ak = MFMA(f1[km], fx[km], ak); PIN(ak); } SB();
                if (n < 28) { fx[ks] = lds8(xb[ks] + c * 4096); f1[ks] = cat(trd(A_WP(c, ks)), trd(A_WP(c, ks) + 512)); } SB();
                if (n >= 2) { av = MFMA(fx[km], f2[km], av); PIN(av); } SB();
                if (n < 28) f2[ks] = cat(trd(A_WP(c, ks) + 8192), trd(A_WP(c, ks) + 8192 + 512));
                if (!EARLYG && c == 0 && ks < 3) A_PIECE(3, ks); if (c >= 2 && c < 5 && ks < 3) A_PIECE(c + 2, ks);
                if (VARLATE && n == 1) { SB(); A_VAR(); }
                if (n == 21) {
                    rs = rst[tt * 32 + r];
#pragma unroll
                    for (int g = 0; g < 4; ++g) rv[g] = *(LAS f32x4*)(rst + tt * 32 + 8 * g + 4 * hf);
                }
                if (n == 24) { PIN(epoch); tagv = epoch + 1; PIN(tagv); }
                if (PADQ == 0 && n == 24) {
                    const unsigned pv = vd == 0 ? ones : 0u, po = vd < 4 ? vo + 448 : OOB;
                    st16_dev((u32x4){pv, pv, pv, pv}, rws, po, 0); st16_dev((u32x4){pv, pv, pv, pv}, rws, po + 1024, 0);
                }
                SB();
            }
            TS(3);
#pragma unroll
            for (int i = 0; i < 16; ++i) { ak[i] *= rs; av[i] *= rv[i >> 2][i & 3]; }
#pragma unroll
            for (int p = 0; p < 2; ++p) {
                const u32x4 f = pack8(ak, p);
                const auto s0 = __builtin_amdgcn_permlane32_swap(f[0], f[2], false, false), s1 = __builtin_amdgcn_permlane32_swap(f[1], f[3], false, false);
                const u32x4 c = {s0[0], s1[0], s0[1], s1[1]};
                st16_dev(c, rws, go1[p], 0); st16_dev(c, rws, go2[p], 0);
            }
            st16_dev(pack8(av, 0), rws, vo, 0); st16_dev(pack8(av, 1), rws, vo + 1024, 0);
        } else {
            f32x16 aq = {};
            __builtin_amdgcn_s_setprio(1);
#pragma unroll
            for (int n = 0; n < 30; ++n) {
                const int c = n >> 2, ks = n & 3, km = (n - 2) & 3;
                if (n < 28 && ks == 0) A_WAIT(c);
                if (n == 24) { PIN(epoch); tagv = epoch + 1; PIN(tagv); }
                if (n >= 2) { aq = MFMA(f1[km], fx[km], aq); PIN(aq); } SB();
                if (n < 28) { fx[ks] = lds8(xb[ks] + c * 4096); f1[ks] = cat(trd(A_WP(c, ks)), trd(A_WP(c, ks) + 512)); }
                if (!EARLYG && c == 0 && ks < 3) A_PIECE(3, ks); if (c >= 2 && c < 5 && ks < 3) A_PIECE(c + 2, ks);
                if (VARLATE && n == 1) { SB(); A_VAR(); }
                if (PADQ && n == PADQ) {
                    const unsigned pv = vd == 0 ? ones : 0u, po = vd < 4 ? vo + 448 : OOB;
                    st16_dev((u32x4){pv, pv, pv, pv}, rws, po, 0); st16_dev((u32x4){pv, pv, pv, pv}, rws, po + 1024, 0);
                }
                SB();
            }
            __builtin_amdgcn_s_setprio(0);
            const float QS = rst[tt * 32 + r] * (0.18898223650461362f * 1.4426950408889634f);
#pragma unroll
            for (int i = 0; i < 16; ++i) aq[i] *= QS;
#pragma unroll
            for (int p = 0; p < 2; ++p) {
                const u32x4 f = pack8(aq, p);
                const auto s0 = __builtin_amdgcn_permlane32_swap(f[0], f[2], false, false), s1 = __builtin_amdgcn_permlane32_swap(f[1], f[3], false, false);
                const bool sp = gi[p] == 3;
                st16_dev((u32x4){s0[0], s1[0], sp ? 0u : s0[1], sp ? 0u : s1[1]}, rws, go1[p], 0);
                st16_dev((u32x4){0u, 0u, s0[1], s1[1]}, rws, go2[p], 0);
            }
        }
    } else {
        if (SPDLY) __builtin_amdgcn_s_sleep(SPDLY);
        epoch = peek_flag(ws, FL_EPOCH * 128); fl1 = (FL_RT + xcd * 4 + loc - 28) * 128;
        const int ri = xcd * 4 + loc - 28, nu = (w == 0 && ri < 16) ? 4 : 3;
        const unsigned vo = (unsigned)(row8 * 3584 + (lane & 7) * 16);
#pragma unroll 1
        for (int i = 0; i < nu; ++i) {
            const int u = i < 3 ? i * 256 + ri * 8 + w : 768 + ri;
            const size_t so = (size_t)(u / 28) * 16 * 3584 + (u % 28) * 128;
            const unsigned d_ = lds0 + w * 16384 + i * 4096;
            glds(vo, wg + so, d_); glds(vo, wg + so + 8 * 3584, d_ + 1024); glds(vo, wu + so, d_ + 2048); glds(vo, wu + so + 8 * 3584, d_ + 3072);
        }
#pragma unroll 1
        for (int i = 0; i < nu; ++i) {
            const int u = i < 3 ? i * 256 + ri * 8 + w : 768 + ri;
            waitv<8>();
            lds_ptr sg = lds + w * 16384 + i * 4096 + g1 * 2048 + (4 * hf + q4) * 128 + p4 * 8;
#pragma unroll
            for (int e = 0; e < 4; ++e) st16_dev(U4(cat(trd(sg + e * 32), trd(sg + e * 32 + 1024))), rws, l16, WS_WGU + (unsigned)(((u % 28) * 4 + e) * 28 + u / 28) * 1024);
            FENCE();
        }
        if (CLRMODE == 2 || CLRMODE == 3) {
            const unsigned c_ = WS_GF + (unsigned)(((ri >> 1) * 4 + 2 + (ri & 1)) * 112 + w * 14) * 1024 + l16;
#pragma unroll 1
            for (int i = 0; i < (CLRMODE == 2 ? 7 : 14); ++i) st16_dev((u32x4){~0u, ~0u, ~0u, ~0u}, rws, c_ + i * 1024, 0);
        }
        tagv = epoch + 1;
    }
    TS(24);
    const int head = xcd * 2 + (loc & 1), rb = loc >> 1, qt = w & 3;
    const char* kvs = ws + WS_KV + (size_t)head * 262144 + w * 2048;
    const char* qs = ws + WS_QF + (size_t)(head * 64 + rb * 4 + qt) * 2048 + kh * 1024;
    unsigned kvd = lds0 + w * 2048, qd = lds0 + 147456 + qt * 2048 + kh * 1024;
    unsigned pf1 = (FL_KV + (lane >> 1) * 8 + (head * 28 + (lane & 1) * 27) / 64) * 128;
    PINS(kvs); PINS(qs); PINS(kvd); PINS(qd); PIN2(pf1, fl1);
    waitv_bar<0>();
    if (w == 0) set_flag(ws, fl1, tagv);
    const u64 tag = ((u64)(unsigned)__builtin_amdgcn_readfirstlane((unsigned)(tagv >> 32)) << 32 | (u64)(unsigned)__builtin_amdgcn_readfirstlane((unsigned)tagv));
    TS(4);

    const int drb = (xcd >> 1) * 4 + l7, dfg = (xcd & 1) * 7 + m7;
    bf16x8 wf[28];
    const unsigned p16 = hf * 1024 + g1 * 512 + (lane & 15) * 16;
    u32x2 xa, xb;
    f32x4 csg = {}, csu = {};
    f32x16 acc = {};
    const u32x4 one4 = {ones, ones, ones, ones};
    const unsigned hi_ = (unsigned)((tb * 2 + tt) * 28 + m7 * 4 + ct * 2 + kh), ofo = (unsigned)(tt * 32768 + kh * 16384 + ct * 4096);
    unsigned ho = WS_HF + hi_ * 1024 + l16, so = hf ? OOB : WS_HS + hi_ * 256 + r * 8;
    const char* os = ws + WS_OF + (size_t)tb * 65536 + ofo;
    unsigned od = lds0 + 65536 + ofo;
    unsigned pf2 = (FL_O + (tb >> 1) * 16 + kh * 8 + ct * 2 + ((lane >> 1) & 1) + (lane & 4)) * 128 + ((lane & 1) * 4 + (tb & 1) * 2 + tt) * 8;
    pf2 &= ~127u;
    unsigned go = ffn ? WS_GF + (unsigned)(drb * 4 * 112 + dfg * 8 + w) * 1024 + g1 * 512 + (hf * 16 + (lane & 15)) * 16 : OOB;
    unsigned fo3 = (FL_H + tb * 8 + m7) * 128;
    {
        lds_ptr ap = opq(lds + kh * 8192 + l16), apB = opq(lds + 65536 + kh * 8192 + l16), apC = opq(lds + 131072 + kh * 8192 + l16);
        const unsigned lv = kh * 8192 + (l16 ^ (kh * 256));
        lds_ptr vp = opq(lds + lv), vpB = opq(lds + 65536 + lv), vpC = opq(lds + 131072 + lv);
#define AP(off) ((off) < 65536 ? ap + (off) : (off) < 131072 ? apB + ((off) - 65536) : apC + ((off) - 131072))
#define VP(off) ((off) < 65536 ? vp + (off) : (off) < 131072 ? vpB + ((off) - 65536) : vpC + ((off) - 131072))
#define KV_ISSUE(t) glds2_dev(l16, kvs + (t) * 16384, kvd + phys(t))
        unsigned wgo = ffn ? WS_WGU + (unsigned)(dfg * 8 + w) * 28672 : OOB;
        unsigned clro = WS_GF + (unsigned)((((xcd * 4 + loc - 28) >> 1) * 4 + 2 + (loc & 1)) * 112 + w * 14) * 1024 + l16;
        u32x4 clv = {~0u, ~0u, ~0u, ~0u};
        if (CLRMODE == 1) { if (ffn) clro = OOB; PIN(clv); PIN(clro); }
        const int ri = umin(xcd * 4 + loc - 28, 27);
        const char* wos = (ffn ? wo + m7 * 128 : wd + (ri % 7) * 128 + (size_t)(ri / 7) * 448 * 896) + (size_t)w * 56 * 896;
        unsigned wov = (unsigned)(row8 * 896) + swz;
        const rsrc_t rx = __builtin_amdgcn_make_buffer_rsrc((void*)x, 0, 2048 * 448 * 2, 0x00020000);
        unsigned xo_ = (unsigned)(((tb * 2 + tt) * 32 + r) * 896 + m7 * 128 + ct * 64 + kh * 32 + hf * 8);
        unsigned rto = (FL_RT + (P3 ? w * 4 + (lane & 3) : r)) * 128;
        lds_ptr qp = opq(lds + 147456 + qt * 2048 + l16);
        PINS(wgo); PINS(wos); PIN2(wov, xo_); PIN(rto); PIN2(ho, so); PIN2(pf2, fo3); PIN(go);
        lds_ptr xw = opq(lds + 147456 + w * 2048 + l16), xr = opq(lds + 147456 + (w ^ 4) * 2048 + l16);
        lds_ptr lw = opq(lds + 58368 + w * 256 + lane * 4), lr = opq(lds + 58368 + qt * 256 + 128 + r * 4);
        lds_ptr bw = opq(lds + 60416 + w * 4), br = opq(lds + 60416 + (lane & 7) * 4);
        unsigned oo = WS_OF + (unsigned)((rb * 4 + qt) * 32 + head * 2 + kh) * 1024 + l16;
        unsigned fo2 = (FL_O + rb * 16 + head) * 128 + w * 8;
        PIN2(oo, fo2);
        if (w == HOP3_POLLER) { if (DLY1) __builtin_amdgcn_s_sleep(DLY1); poll_flag(ws, pf1, tag, ~0ull); }
        BAR();
        TS(5);
        glds_dev(l16, qs, qd);
        KV_ISSUE(0); KV_ISSUE(1);
        asm volatile("s_barrier" ::: "memory");
        KV_ISSUE(2); KV_ISSUE(3);
        waitv_bar<4>();
        TS(6);
        const bf16x8 q0 = lds8(qp), q1 = lds8(qp + 1024);
        f32x16 o = {}, sa = {}, sb;
        bf16x8 k0 = lds8(ap + phys(0)), k1 = lds8(ap + phys(0) + 1024), v0 = lds8(vp + phys(0) + 2048), v1 = {};
        u32x4 p0, p1 = {};
        sa = MFMA(k0, q0, sa); sa = MFMA(k1, q1, sa);
        k0 = lds8(ap + phys(0) + 4096); k1 = lds8(ap + phys(0) + 5120);
        PIN(sa); SB();
        u64 rtf = 0;
#define EXP4(S, o4) _Pragma("unroll") for (int i_ = o4; i_ < o4 + 4; ++i_) S[i_] = __builtin_amdgcn_exp2f(S[i_]);
#define WLD(s) wf[s] = BF8(ld16_dev(rws, p16 + ((s) & 1) * 256 + ((s) & 2) * 1024, wgo + ((s) >> 2) * 4096))
#define SIDE(t, g) do { \
            if ((t) == 0 && (g) == 0) KV_ISSUE(4); if ((t) == 0 && (g) == 2) KV_ISSUE(5); if ((t) == 1 && (g) == 0) KV_ISSUE(6); if ((t) == 1 && (g) == 2) KV_ISSUE(7); \
            if ((t) == 2 && (g) == 0) KV_ISSUE(8); if (!P3 && (t) == 1 && (g) == 3) rtf = peek_flag(ws, rto); if (P3 && (t) == 1 && (g) == 1) peek_lds(ws, rto, lds0 + 155648 + w * 512); if (P3 && (t) == 2 && (g) == 2) { waitv<4>(); rtf = peeked(lds + 155648 + w * 512 + lane * 4); } \
            if ((t) == 3 && (g) < 3) KV_ISSUE(9 + (g)); \
            if (SPEC && CLRMODE == 0 && (t) >= 4 && (t) <= 10 && ((g) & 1)) { if (__builtin_expect(!ffn, 0)) st16_dev((u32x4){~0u, ~0u, ~0u, ~0u}, rws, clro + (((t) - 4) * 2 + ((g) >> 1)) * 1024, 0); } \
            if (CLRMODE == 1 && (t) >= 4 && (t) <= 10 && ((g) & 1)) __builtin_amdgcn_raw_buffer_store_b128(clv, rws, clro, (((t) - 4) * 2 + ((g) >> 1)) * 1024, 16); \
            if (CLRMODE == 2 && (t) >= 11 && !((g) & 1) && ((t) - 11) * 2 + (g) / 2 < 7) { if (__builtin_expect(!ffn, 0)) st16_dev((u32x4){~0u, ~0u, ~0u, ~0u}, rws, clro + (7 + ((t) - 11) * 2 + (g) / 2) * 1024, 0); } \
            if (CLRMODE == 4 && (t) >= 1 && (t) <= 14 && (g) == 3) { if (__builtin_expect(!ffn, 0)) st16_dev((u32x4){~0u, ~0u, ~0u, ~0u}, rws, clro + ((t) - 1) * 1024, 0); } \
            if (CLRMODE == 5 && ((t) == 4 || (t) == 5 || (t) == 8 || (t) == 9 || (t) == 11 || (t) == 12 || (t) == 13) && (g) == 3) { if (__builtin_expect(!ffn, 0)) { constexpr int k_ = (t) < 6 ? (t) - 4 : (t) < 10 ? (t) - 6 : (t) - 7; \
                st16_dev(clv, rws, clro + k_ * 2048, 0); st16_dev(clv, rws, clro + k_ * 2048 + 1024, 0); } } \
            if ((t) == 7 && (g) == 0) KV_ISSUE(12); if ((t) == 7 && (g) == 2) KV_ISSUE(13); if ((t) == 8 && (g) == 0) KV_ISSUE(14); if ((t) == 8 && (g) == 2) KV_ISSUE(15); \
            if ((t) >= 4 && (t) <= 6) WLD(((t) - 4) * 4 + (g)); \
            if (((t) == 7 || (t) == 8) && ((g) & 1)) WLD(12 + ((t) - 7) * 2 + ((g) >> 1)); \
            if ((t) >= 9 && (t) <= 11) WLD(((t) - 5) * 4 + (g)); \
            if ((t) == 12 || ((t) == 13 && (g) < 3)) glds(wov, wos + (((t) - 12) * 4 + (g)) * 8 * 896, lds0 + (w * 7 + ((t) - 12) * 4 + (g)) * 1024); \
            if ((t) == 13 && (g) == 3) { xa = ld8(rx, xo_, 0); xb = ld8(rx, xo_, 16); } \
            if (!NOTOUCH && (t) == 14 && (g) == 1) { if (w >= 6) touch(lane * (w == 6 ? (WS_END / 64 & ~127u) : 28672), w == 6 ? ws : out, lds0 + 61440); } \
            if ((t) == 14 && (g) == 0) { if (tid < 32) *(LAS u32x4*)(lds + 57344 + tid * 16) = (u32x4){0, 0, 0, 0}; } \
        } while (0)
#define TOFF(i) (phys((i) >> 1) + ((i) & 1) * 4096)
#define TILE(i, SC, SN) do { \
            __builtin_amdgcn_s_setprio(((i) & 1) ? 1 : 3); \
            if ((i) < 31) { f32x16 z_ = {}; SN = MFMA(k0, q0, z_); PIN(SN); } if ((i) < 30) k0 = lds8(AP(TOFF((i) + 2))); SB(); \
            EXP4(SC, 0); PIN(SC); SB(); \
            o = MFMA(v1, BF8(p1), o); PIN(o); v1 = lds8(VP(TOFF(i) + 3072)); SIDE((i) >> 1, ((i) & 1) * 2); SB(); \
            EXP4(SC, 4); p0 = pack8(SC, 0); PIN(p0); SB(); \
            __builtin_amdgcn_s_setprio(((i) & 1) ? 0 : 2); \
            if ((i) < 31) { SN = MFMA(k1, q1, SN); PIN(SN); } if ((i) < 30) k1 = lds8(AP(TOFF((i) + 2) + 1024)); SB(); \
            EXP4(SC, 8); PIN(SC); SB(); \
            o = MFMA(v0, BF8(p0), o); PIN(o); if ((i) < 31) v0 = lds8(VP(TOFF((i) + 1) + 2048)); SIDE((i) >> 1, ((i) & 1) * 2 + 1); SB(); \
            EXP4(SC, 12); p1 = pack8(SC, 1); PIN(p1); SB(); } while (0)
#define STEP(t) do { TILE(2 * (t), sa, sb); TILE(2 * (t) + 1, sb, sa); } while (0)
        STEP(0);
        waitv_barx<4>();
        STEP(1); STEP(2);
        if (P3 && __builtin_expect(!wave_all(rtf == tag), 0)) poll_flag(ws, rto, tag, 0xffffffffull);
        waitv_bar2<P3 ? 2 : 3>();
        TS(21);
        if (!P3 && __builtin_expect(!wave_all(rtf == tag), 0)) poll_flag(ws, rto, tag, 0xffffffffull);
        FENCE();
        STEP(3); STEP(4); STEP(5); STEP(6);
        waitv_bar2<CLRMODE == 1 ? 18 : 12>();
        TS(22);
        STEP(7); STEP(8); STEP(9); STEP(10);
        waitv_bar2<CLRMODE == 1 ? 14 : 9>();
        if (CLRMODE == 1) PIN(clv);
        TS(23);
        STEP(11); STEP(12); STEP(13); STEP(14); STEP(15);
        o = MFMA(v1, BF8(p1), o);
        TS(7);
        u32x4 ov;
        {
            *(LAS f32x4*)xw = (f32x4){o[8], o[9], o[10], o[11]}; *(LAS f32x4*)(xw + 1024) = (f32x4){o[12], o[13], o[14], o[15]};
            const float l = kh ? o[4] : o[12];
            *(LAS float*)lw = l;
#define AMX(a, b, c) __builtin_fmaxf(__builtin_fmaxf(__builtin_fabsf(a), __builtin_fabsf(b)), __builtin_fabsf(c))
            const float mx_ = AMX(AMX(AMX(o[0], o[1], o[2]), AMX(o[3], o[4], o[5]), AMX(o[6], o[7], o[8])), AMX(AMX(o[9], o[10], o[11]), AMX(o[12], o[13], o[14]), o[15]), 0.f);
            const bool ok = (mx_ < 0x1p100f) & ((hf ? l : 1.f) > 0x1p-80f);
            *(LAS unsigned*)bw = wave_all(ok) ? 0u : 1u;
            BAR();
            const f32x4 a = *(LAS f32x4*)xr, c = *(LAS f32x4*)(xr + 1024);
            const float inv = __builtin_amdgcn_rcpf(*(LAS float*)lr + *(LAS float*)(lr + 1024));
            const unsigned bad = *(LAS unsigned*)br;
            const float iz = (kh && hf) ? 0.f : inv;
            ov = (u32x4){cvtpk((o[0] + a[0]) * inv, (o[1] + a[1]) * inv), cvtpk((o[2] + a[2]) * inv, (o[3] + a[3]) * inv),
                         cvtpk((o[4] + c[0]) * iz, (o[5] + c[1]) * iz), cvtpk((o[6] + c[2]) * iz, (o[7] + c[3]) * iz)};
            if (__builtin_expect(wave_any(bad != 0), 0)) {
                f32x16 of = {};
                float m = -0x1p126f;
#pragma unroll 1
                for (int t = 0; t < 16; ++t) {
                    BAR();
                    glds2_dev(l16, kvs + t * 16384, lds0 + 65536 + w * 2048);
                    waitv_bar<0>();
#pragma unroll 1
                    for (int u = 0; u < 2; ++u) {
                        f32x16 s = {};
                        s = MFMA(lds8(apB + u * 4096), q0, s); s = MFMA(lds8(apB + u * 4096 + 1024), q1, s);
                        float mx = s[0];
#pragma unroll
                        for (int i = 1; i < 16; ++i) mx = __builtin_fmaxf(mx, s[i]);
                        mx = __builtin_fmaxf(swap_max(mx), m);
                        const float alpha = __builtin_amdgcn_exp2f(m - mx);
                        m = mx;
#pragma unroll
                        for (int i = 0; i < 16; ++i) { s[i] = __builtin_amdgcn_exp2f(s[i] - mx); of[i] *= alpha; }
                        of = MFMA(lds8(vpB + u * 4096 + 2048), BF8(pack8(s, 0)), of);
                        of = MFMA(lds8(vpB + u * 4096 + 3072), BF8(pack8(s, 1)), of);
                    }
                }
                *(LAS f32x4*)xw = (f32x4){of[8], of[9], of[10], of[11]}; *(LAS f32x4*)(xw + 1024) = (f32x4){of[12], of[13], of[14], of[15]};
                *(LAS float*)lw = hf ? (kh ? of[4] : of[12]) : m;
                BAR();
                const f32x4 a2 = *(LAS f32x4*)xr, c2 = *(LAS f32x4*)(xr + 1024);
                const float m0 = *(LAS float*)(lr - 128), m1 = *(LAS float*)(lr + 1024 - 128), mm = __builtin_fmaxf(m0, m1);
                const float e0 = __builtin_amdgcn_exp2f(m0 - mm), e1 = __builtin_amdgcn_exp2f(m1 - mm);
                const float iv = __builtin_amdgcn_rcpf(*(LAS float*)lr * e0 + *(LAS float*)(lr + 1024) * e1);
                const float im = (kh ? e1 : e0) * iv, io = (kh ? e0 : e1) * iv;
                const bool z = kh && hf;
                ov = (u32x4){cvtpk(of[0] * im + a2[0] * io, of[1] * im + a2[1] * io), cvtpk(of[2] * im + a2[2] * io, of[3] * im + a2[3] * io),
                             z ? 0u : cvtpk(of[4] * im + c2[0] * io, of[5] * im + c2[1] * io), z ? 0u : cvtpk(of[6] * im + c2[2] * io, of[7] * im + c2[3] * io)};
            }
        }
        st16_dev(ov, rws, oo, 0);
        TS(25);
        FENCE();
        PIN2(xa, xb);
#pragma unroll
        for (int s = 27; s >= 0; --s) PIN(wf[s]);
#pragma unroll
        for (int s = 0; s < 16; s += 2) { csg = MFMA16(wf[s], BF8(one4), csg); csu = MFMA16(wf[s + 1], BF8(one4), csu); }
        acc[0] = bflo(xa[0]); acc[1] = bfhi(xa[0]); acc[2] = bflo(xa[1]); acc[3] = bfhi(xa[1]); acc[4] = bflo(xb[0]); acc[5] = bfhi(xb[0]); acc[6] = bflo(xb[1]); acc[7] = bfhi(xb[1]);
        PIN(acc); PIN2(ho, so); PIN2(pf2, fo3); PINS(os); PINS(od); PIN2(csg, csu);
        waitv_bar<0>();
        if (w == 0) set_flag(ws, fo2, tagv);
        TS(8);
    }
    if (__builtin_expect(!ffn, 0)) {
        const int ri = xcd * 4 + loc - 28;
        if (ri < 28) {
#pragma unroll 1
            for (int u = w; u < 28; u += 8) {
                lds_ptr sd = lds + u * 2048 + (4 * hf + q4) * 128 + g1 * 32 + p4 * 8;
#pragma unroll
                for (int e = 0; e < 2; ++e) { lds_ptr p_ = sd + ((e ^ (q4 >> 1)) * 64);
                    st16_dev(U4(cat(trd(p_), trd(p_ + 1024))), rws, l16, WS_WD + (unsigned)(((ri % 7) * 112 + (ri / 7) * 28 + u) * 2 + e) * 1024); }
            }
        }
        waitv_bar<0>();
        if (w == 0) set_flag(ws, (FL_RD + ri) * 128, tagv);
        return;
    }

    const int erb = tb >> 1;
    const char* hs = ws + WS_HF + (size_t)drb * 114688 + w * 1024;
    const char* hss = ws + WS_HS + (size_t)((drb * 4 + (w & 3)) * 28 + kh * 14) * 256;
    unsigned pf3, fo4 = (FL_G + drb * 64 + dfg) * 128 + w * 8;
    {
        lds_ptr wb = lds + kh * (8 * 28 * 128) + (4 * hf + q4) * 128 + ((ct ^ (q4 >> 1)) * 64) + (g1 ^ kh) * 32 + p4 * 8;
        bf16x8 wr[16];
#pragma unroll
        for (int s = 0; s < 12; ++s) { const int k0 = ((s >> 1) * 28 + (s & 1) * 16) * 128; wr[s] = cat(trd(wb + k0), trd(wb + k0 + 1024)); }
        lds_ptr cw = opq(lds + 131072 + w * 2048 + l16), cr = opq(lds + 131072 + (w ^ 4) * 2048 + l16);
        lds_ptr ob = opq(lds + 65536 + tt * 32768 + kh * 16384 + l16);
        pf3 = (FL_H + drb * 16 + (lane & 8) + umin(lane & 7, 6)) * 128;
        PIN2(pf3, fo4); PINS(hs); PINS(hss);
        if (DLY2) __builtin_amdgcn_s_sleep(DLY2);
        poll_flag(ws, pf2, tag, 0x55);
        TS(9);
        glds4_dev(l16, os, od);
        if (!CST) asm volatile("s_barrier" ::: "memory");
        glds4_dev(l16, os + 8192, od + 8192);
        bf16x8 of[8];
        waitv_bar<CST ? 0 : 4>();
        TS(26);
#pragma unroll
        for (int s = 0; s < 8; ++s) of[s] = lds8(ob + s * 1024);
        SB();
#pragma unroll
        for (int s = 0; s < 8; ++s) {
            acc = MFMA(wr[s], of[s], acc); PIN(acc); SB();
            if (s >= 2 && s < 6) { const int k0 = (((s + 10) >> 1) * 28 + (s & 1) * 16) * 128; wr[s + 10] = cat(trd(wb + k0), trd(wb + k0 + 1024)); SB(); }
            if (CST) { of[s] = lds8(ob + (8 + s) * 1024); SB(); }
        }
        if (!CST) {
            waitv_barx<0>();
#pragma unroll
            for (int s = 0; s < 8; ++s) of[s] = lds8(ob + (8 + s) * 1024);
        }
        SB();
#pragma unroll
        for (int s = 0; s < 8; ++s) acc = MFMA(wr[8 + s], of[s], acc);
        TS(10);
        *(LAS f32x4*)cw = (f32x4){acc[8], acc[9], acc[10], acc[11]}; *(LAS f32x4*)(cw + 1024) = (f32x4){acc[12], acc[13], acc[14], acc[15]};
        BAR();
        const f32x4 a = *(LAS f32x4*)cr + (f32x4){acc[0], acc[1], acc[2], acc[3]}, b = *(LAS f32x4*)(cr + 1024) + (f32x4){acc[4], acc[5], acc[6], acc[7]};
        const u32x4 hv = {cvtpk(a[0], a[1]), cvtpk(a[2], a[3]), cvtpk(b[0], b[1]), cvtpk(b[2], b[3])};
        st16_dev(hv, rws, ho, 0);
        FENCE(); SB();
        float s1 = 0.f, s2 = 0.f;
        { f32x4 q4_ = {}; acc_stats2(hv[0], hv[1], q4_); acc_stats2(hv[2], hv[3], q4_); s1 = swap_add(q4_[0] + q4_[1]); s2 = swap_add(q4_[2] + q4_[3]); }
        st8_dev((u32x2){f2u(s1), f2u(s2)}, rws, so, 0);
        FENCE(); PIN2(csg, csu); SB();
#pragma unroll
        for (int s = 16; s < 28; s += 2) { csg = MFMA16(wf[s], BF8(one4), csg); csu = MFMA16(wf[s + 1], BF8(one4), csu); }
        PIN2(csg, csu);
        waitv_bar<0>();
        if (w == 0) set_flag(ws, fo3, tagv);
        if (bid == 0 && w == 0) set_flag(ws, FL_EPOCH * 128, tagv);
        TS(11);
    }

    const unsigned wdo = WS_WD + (unsigned)(m7 * 112 + w * 14) * 2048;
    bf16x8 wdf[28];
    bf16x8 gf[GW];
    const unsigned gxo = WS_GF + (unsigned)(erb * 4 * 112 + w * 14) * 1024 + (tb & 1) * 256;
    const unsigned pwd = hf * 2048 + g1 * 512 + (lane & 15) * 16;
#define G_LD(s) gf[(s) % GW] = BF8(ld16_dev(rws, p16, gxo + ((s) / 7) * 114688 + ((s) % 7) * 2048))
    u64 gfl = 0;
    const unsigned gfi = (FL_G + (erb * 4 + g1) * 16 + ((w * 14 + umin(lane & 15, 13)) >> 3)) * 128;
    {
        const unsigned hd = lds0 + w * 1024;
        const char* wds = ws + wdo;
        lds_ptr hb = opq(lds + p16), hb2 = opq(lds + 57344 + p16);
        lds_ptr wl = opq(lds + 114688 + w * 5120 + l16), wl2 = opq(lds + w * 7168 + l16);
        LAS f32x2* stp = (LAS f32x2*)opq(lds + 159744 + (lane & 15) * 8);
        if (w == HOP3_POLLER) { if (DLY3) __builtin_amdgcn_s_sleep(DLY3); poll_flag(ws, pf3, tag, 0x7f7f); }
        BAR();
        TS(12);
#define H_PIECE(j) glds_dev(l16, hs + (j) * 8192, hd + (j) * 8192)
#define WDO(s) (((s) >> 2) * 4096 + (((s) >> 1) & 1) * 1024 + ((s) & 1) * 256)
#define WDLD(s) wdf[s] = BF8(ld16_dev(rws, pwd, wdo + WDO(s)))
#define HB(n) (((n) < 56 ? hb + ((n) & ~1) * 1024 : hb2 + (((n) & ~1) - 56) * 1024) + ((n) & 1) * 256)
        H_PIECE(0);
        asm volatile("s_barrier" ::: "memory");
        H_PIECE(1); H_PIECE(2); H_PIECE(3);
        constexpr int PF = 5;
        bf16x8 bf[PF + 1];
        f32x4 ag[2][2], au[2][2];
        f32x2 sp[7], sv[4];
        float mean[2] = {0.f, 0.f}, r2[2] = {0.f, 0.f}, hp = 0.f;
        u32x4 hv;
        lds_ptr pk = opq(lds + 155648 + w * 512 + lane * 4);
        u64 pv = 0;
        const unsigned pkd = lds0 + 155648 + w * 512;
        const unsigned rdo = (FL_RD + ((lane & 4) ? (w >> 1) * 7 + m7 : ((lane & 2) ? drb : erb) * 2 + (lane & 1))) * 128;
        waitv_barx<3>();
        TS(27);
#pragma unroll
        for (int n = 0; n < PF; ++n) bf[n] = lds8(HB(n));
        SB();
        sfor<0, 125>([&](auto N_) __attribute__((always_inline)) {
            constexpr int n = decltype(N_)::v, p = n / 28, s = n % 28;
            if constexpr (n == 3) waitv_barx<3>();
            if constexpr (n == 11) waitv_barx<7>();
            if constexpr (n == 27) { waitv_barx<3>(); TS(13); }
            if constexpr (n == 64) { waitv_barx<12>(); if (!GWG || w == 0) set_flag(ws, fo4, tagv); TS(14); }
            if constexpr (n == 85) { waitv_barx<XN ? 9 : 7>(); if (!GWG || w == 0) set_flag(ws, fo4 + 2048, tagv); }
            if constexpr (n == 38) { if (__builtin_expect(!wave_all(pv == tag), 0)) poll_flag(ws, rdo, tag); FENCE(); }
            if constexpr (d_prio(n) != d_prio(n - 1)) __builtin_amdgcn_s_setprio(d_prio(n));
            if constexpr (n < 112) {
                if constexpr (s < 2) { ag[p & 1][s] = (f32x4){}; au[p & 1][s] = (f32x4){}; }
                ag[p & 1][s & 1] = MFMA16(wf[s & ~1], bf[n % (PF + 1)], ag[p & 1][s & 1]); au[p & 1][s & 1] = MFMA16(wf[s | 1], bf[n % (PF + 1)], au[p & 1][s & 1]);
                PIN2(ag[p & 1][s & 1], au[p & 1][s & 1]);
            }
            SB();
            if constexpr (n + PF < 112) bf[(n + PF) % (PF + 1)] = lds8(HB(n + PF));
            if constexpr (n % 3 == 2 && n < 30) H_PIECE(4 + n / 3);
            if constexpr (n == 4) glds4_dev(l16, hss, lds0 + 114688 + w * 5120);
            if constexpr (n == 16) { waitv<4>();
#pragma unroll
                for (int j = 0; j < 7; ++j) sp[j] = *(LAS f32x2*)(lds + 114688 + w * 5120 + (hf * 7 + j) * 256 + r * 8);
            }
            if constexpr (n == 20) { const f32x2 t = ((sp[0] + sp[1]) + (sp[2] + sp[3])) + ((sp[4] + sp[5]) + sp[6]); stp[w * 32 + g1 * 16] = (f32x2){swap_add(t[0]), swap_add(t[1])}; }
            if constexpr (n >= 39 && n < 48 && n % 2 == 1) glds_dev(pwd, wds + WDO((n - 39) / 2), lds0 + 114688 + w * 5120 + (n - 39) / 2 * 1024);
            if constexpr (n >= 48 && n < 64 && n % 2 == 0) WDLD(12 + (n - 48) / 2);
            if constexpr (n >= 70 && n < 84 && n % 2 == 0) glds_dev(pwd, wds + WDO(5 + (n - 70) / 2), lds0 + w * 7168 + (n - 70) / 2 * 1024);
            if constexpr (n >= 86 && n < 102 && n % 2 == 0) WDLD(20 + (n - 86) / 2);
            if constexpr (XN > 0 && n == 84) peek_lds(ws, gfi - g1 * 2048, pkd);
            if constexpr (XN > 0 && n == 95) { waitv<5>(); pv = peeked(pk); }
            if constexpr (XN > 0 && n == 99) { if (__builtin_expect(!wave_all(pv == tag), 0)) poll_flag(ws, gfi - g1 * 2048, tag); FENCE(); }
            if constexpr (n >= 102 && n < 102 + 2 * XN && n % 2 == 0) G_LD((n - 102) / 2);
            if constexpr (!SPEC && n == 110) { waitv_barx<2 + XN>(); if (w == 0) set_flag(ws, fo4 + 4096, tagv); }
            if constexpr (n == 111) gfl = peek_flag(ws, gfi);
            if constexpr (n == 32) pv = peeked(pk);
            if constexpr (n == 18) peek_lds(ws, rdo, pkd);
            if constexpr (p >= 1) {
                constexpr int e = (p - 1) & 1, tp = n == 100 ? 3 : p - 1;
                if constexpr ((s == 0 && p < 4) || n == 100) { sv[0] = stp[tp * 32]; sv[1] = stp[(tp + 4) * 32]; sv[2] = stp[tp * 32 + 16]; sv[3] = stp[(tp + 4) * 32 + 16]; }
                if constexpr (((s == 2 || s == 3) && p < 4) || n == 104 || n == 105) { constexpr int k = n & 1; const f32x2 t = sv[2 * k] + sv[2 * k + 1];
                    mean[k] = t[0] * (1.f / D); r2[k] = __builtin_amdgcn_rcpf(__builtin_fmaxf(t[1] * (1.f / D) - mean[k] * mean[k], 0.f) + EPS); PIN2(mean[k], r2[k]); }
                if constexpr (s >= 4 && s < 12) { constexpr int i = s - 4, k = i >> 2;
                    const float h0 = __builtin_fmaxf(ag[e][k][i & 3] - mean[k] * csg[i & 3], 0.f) * r2[k] * (au[e][k][i & 3] - mean[k] * csu[i & 3]);
                    if constexpr (i & 1) { hv[i >> 1] = p >= 3 ? umin(cvtpk(hp, h0), ~1u) : cvtpk(hp, h0); PIN(hv); } else { hp = h0; PIN(hp); } }
                if constexpr (s == 12) {
                    const auto s0 = __builtin_amdgcn_permlane32_swap(hv[0], hv[2], false, false), s1 = __builtin_amdgcn_permlane32_swap(hv[1], hv[3], false, false);
                    st16_dev((u32x4){s0[0], s1[0], s0[1], s1[1]}, rws, go + (p - 1) * 114688, 0); FENCE();
                }
            }
            SB();
        });
        TS(15);
        if (XN == 0) { if (__builtin_expect(!wave_all(gfl == tag), 0)) poll_flag(ws, gfi, tag); FENCE(); }
#pragma unroll
        for (int s = XN; s < 7; ++s) { G_LD(s); SB(); }
        FENCE();
#pragma unroll
        for (int s = 0; s < 12; ++s) wdf[s] = lds8(s < 5 ? wl + s * 1024 : wl2 + (s - 5) * 1024);
    }

    {
        const int etok = tid >> 4, eblk = tid & 15;
        const int et = erb * 4 + (etok >> 4), er = (tb & 1) * 16 + (etok & 15);
        const unsigned hro = (unsigned)((((eblk >> 2) & 3) * 64 + (eblk & 1) * 32 + er) * 16 + ((eblk >> 1) & 1) * 8) + WS_HF + (unsigned)(et * 28 + m7 * 4) * 1024;
        u32x2 hres[2];
        GAS u64* op = (GAS u64*)(out + (size_t)(et * 32 + er) * 896 + m7 * 128 + eblk * 8);
        lds_ptr rp = opq(lds + w * 8448 + (lane >> 4) * 528 + (lane & 15) * 16), rq = opq(lds + eblk * 528 + etok * 16);
        lds_ptr rp2 = opq(rp + 67584), rq2 = opq(rq + 67584);
        f32x4 c[4];
#define G_STEP(s) do { _Pragma("unroll") for (int j = 0; j < 4; ++j) { if ((s) % 7 == 0) c[j] = (f32x4){}; c[j] = MFMA16(wdf[4 * ((s) % 7) + j], gf[(s) % GW], c[j]); } PIN2(c[0], c[1]); PIN2(c[2], c[3]); } while (0)
#define G_PUT(q) do { _Pragma("unroll") for (int j = 0; j < 4; ++j) *(LAS f32x4*)(((q) < 2 ? rp : rp2) + j * 2112 + ((q) & 1) * 256) = c[j]; } while (0)
#define G_OUT(hh, RQ) do { f32x4 pa[8]; \
            _Pragma("unroll") for (int ww = 0; ww < 8; ++ww) pa[ww] = *(LAS f32x4*)(RQ + ww * 8448); SB(); \
            const f32x4 a = ((pa[0] + pa[1]) + (pa[2] + pa[3])) + ((pa[4] + pa[5]) + (pa[6] + pa[7])); \
            const u32x2 ov = {cvtpk(a[0] + bflo(hres[hh][0]), a[1] + bfhi(hres[hh][0])), cvtpk(a[2] + bflo(hres[hh][1]), a[3] + bfhi(hres[hh][1]))}; \
            __hip_atomic_store(op + (hh) * (64 * 896 / 8), __builtin_bit_cast(u64, ov), __ATOMIC_RELAXED, __HIP_MEMORY_SCOPE_AGENT); } while (0)
        TS(16);
        unsigned seen = 0;
#pragma unroll
        for (int s = 0; s < 28; ++s) {
            if (s == 6) BAR();
            if (s == 7 || s == 14 || s == 21) G_PUT(s / 7 - 1);
            if (s == 14) TS(17);
            if (s == 21) { waitv<7>(); PIN2(hres[0], hres[1]); BAR(); G_OUT(0, rq); FENCE(); TS(18); }
            SB();
            G_STEP(s);
            if (s >= 14) { const u32x4 u = U4(gf[s % GW]); seen = umax3(umax3(seen, u[0], u[1]), u[2], u[3]); PIN(seen); }
            SB();
            if (s == 0) { if (XN > 0) { if (__builtin_expect(!wave_all(gfl == tag), 0)) poll_flag(ws, gfi, tag); FENCE(); } }
            if (s + GW < 28) G_LD(s + GW);
            if (s == 1) { hres[0] = ld8_dev(rws, hro, 0); hres[1] = ld8_dev(rws, hro, 57344); FENCE(); }
        }
        if (w == 0) { set_flag(ws, fo4 + 4096, tagv); set_flag(ws, fo4 + 6144, tagv); }
        if (__builtin_expect(wave_any(seen == ~0u), 0)) {
            poll_flag(ws, gfi + 4096, tag);
#pragma unroll
            for (int s = 14; s < 14 + GW; ++s) G_LD(s);
#pragma unroll
            for (int s = 14; s < 28; ++s) { if (s == 21) G_PUT(2); G_STEP(s); SB(); if (s + GW < 28) G_LD(s + GW); SB(); }
        }
        G_PUT(3);
        TS(19);
        BAR();
        G_OUT(1, rq2);
    }
    TS(20);
}

#define WS_OFF 0
extern "C" void kernel_launch(void* const* d_in, const int* in_sizes, int n_in, void* d_out, int out_size, void* d_ws, size_t ws_size, hipStream_t stream) {
    (void)hipFuncSetAttribute((const void*)block_fwd, hipFuncAttributeMaxDynamicSharedMemorySize, LDS_BYTES);
    block_fwd<<<256, 512, LDS_BYTES, stream>>>((const char*)d_in[0], (const char*)d_in[1], (const char*)d_in[2], (const char*)d_in[4], (const char*)d_in[5], (const char*)d_in[6], (char*)d_out, (char*)d_ws + WS_OFF);
}
```

```cpp
#include <hip/hip_runtime.h>
#include <hip/hip_bf16.h>
#include <stdint.h>

typedef __attribute__((ext_vector_type(8))) short bf16x8;
typedef __attribute__((ext_vector_type(4))) short s16x4;
typedef __attribute__((ext_vector_type(2))) __bf16 bf16x2;
typedef __attribute__((ext_vector_type(16))) float f32x16;
typedef __attribute__((ext_vector_type(4))) float f32x4;
typedef __attribute__((ext_vector_type(2))) float f32x2;
typedef __attribute__((ext_vector_type(4))) unsigned u32x4;
typedef __attribute__((ext_vector_type(2))) unsigned u32x2;
typedef short v4i16_t __attribute__((ext_vector_type(4)));
typedef unsigned long long u64;
#define LAS __attribute__((address_space(3)))
#define GAS __attribute__((address_space(1)))
typedef LAS char* lds_ptr;
#if defined(__HIP_DEVICE_COMPILE__)
#define NOPK __attribute__((target("no-packed-fp32-ops")))
#else
#define NOPK
#endif
#define DI __device__ __forceinline__
#define MFMA(a, b, c) __builtin_amdgcn_mfma_f32_32x32x16_bf16(a, b, c, 0, 0, 0)
#define MFMA16(a, b, c) __builtin_amdgcn_mfma_f32_16x16x32_bf16(a, b, c, 0, 0, 0)
#define BF8(x) __builtin_bit_cast(bf16x8, x)
#define U4(x) __builtin_bit_cast(u32x4, x)

constexpr int D = 448;
constexpr float EPS = 1e-5f;
constexpr unsigned FL_EPOCH = 0, FL_KV = 1, FL_RT = FL_KV + 256, FL_RD = FL_RT + 32, FL_O = FL_RD + 32, FL_H = FL_O + 256, FL_G = FL_H + 256;
constexpr unsigned WS_KV = 1u << 20;
constexpr unsigned WS_QF = WS_KV + 4194304;
constexpr unsigned WS_OF = WS_QF + 2097152;
constexpr unsigned WS_HF = WS_OF + 2097152;
constexpr unsigned WS_GF = WS_HF + 64 * 28 * 1024;
constexpr unsigned WS_WGU = WS_GF + 64 * 112 * 1024;
constexpr unsigned WS_WD = WS_WGU + 112 * 28 * 1024;
constexpr unsigned WS_HS = WS_WD + 7 * 112 * 2 * 1024;
constexpr unsigned WS_END = WS_HS + 64 * 28 * 256 + 1024;
constexpr unsigned OOB = 0x40000000u;
constexpr int LDS_BYTES = 163840;

DI float u2f(unsigned u) { return __builtin_bit_cast(float, u); }
DI unsigned f2u(float f) { return __builtin_bit_cast(unsigned, f); }
DI unsigned umin(unsigned a, unsigned b) { return a < b ? a : b; }
DI unsigned umax3(unsigned a, unsigned b, unsigned c) { const unsigned m = a > b ? a : b; return m > c ? m : c; }
DI bool wave_any(bool p) { return __builtin_amdgcn_ballot_w64(p) != 0; }
DI bool wave_all(bool p) { return __builtin_amdgcn_ballot_w64(!p) == 0; }
DI unsigned cvtpk(float lo, float hi) { return __builtin_bit_cast(unsigned, __builtin_convertvector((f32x2){lo, hi}, bf16x2)); }
DI float bflo(unsigned u) { return u2f(u << 16); }
DI float bfhi(unsigned u) { return u2f(u & 0xffff0000u); }
DI void glds(unsigned voff, const void* sbase, unsigned dst) {
    asm volatile("s_mov_b32 m0, %2\n\ts_nop 0\n\tglobal_load_lds_dwordx4 %0, %1" :: "v"(voff), "s"(sbase), "s"(dst) : "memory");
}
DI void glds_dev(unsigned voff, const void* sbase, unsigned dst) {
    asm volatile("s_mov_b32 m0, %2\n\ts_nop 0\n\tglobal_load_lds_dwordx4 %0, %1 sc1" :: "v"(voff), "s"(sbase), "s"(dst) : "memory");
}
DI void glds2_dev(unsigned voff, const void* sbase, unsigned dst) {
    asm volatile("s_mov_b32 m0, %2\n\ts_nop 0\n\tglobal_load_lds_dwordx4 %0, %1 sc1\n\tglobal_load_lds_dwordx4 %0, %1 offset:1024 sc1" :: "v"(voff), "s"(sbase), "s"(dst) : "memory");
}
DI void glds4_dev(unsigned voff, const void* sbase, unsigned dst) {
    asm volatile("s_mov_b32 m0, %2\n\ts_nop 0\n\tglobal_load_lds_dwordx4 %0, %1 sc1\n\tglobal_load_lds_dwordx4 %0, %1 offset:1024 sc1\n\t"
                 "global_load_lds_dwordx4 %0, %1 offset:2048 sc1\n\tglobal_load_lds_dwordx4 %0, %1 offset:3072 sc1" :: "v"(voff), "s"(sbase), "s"(dst) : "memory");
}
DI s16x4 trd(lds_ptr p) { return __builtin_bit_cast(s16x4, __builtin_amdgcn_ds_read_tr16_b64_v4i16((LAS v4i16_t*)p)); }
DI bf16x8 cat(s16x4 a, s16x4 b) { return __builtin_shufflevector(a, b, 0, 1, 2, 3, 4, 5, 6, 7); }
DI bf16x8 lds8(lds_ptr p) { return *(LAS bf16x8*)p; }
DI float swap_add(float v) { auto r = __builtin_amdgcn_permlane32_swap(f2u(v), f2u(v), false, false); return u2f(r[0]) + u2f(r[1]); }
DI float swap_max(float v) { auto r = __builtin_amdgcn_permlane32_swap(f2u(v), f2u(v), false, false); return __builtin_fmaxf(u2f(r[0]), u2f(r[1])); }
DI u32x4 pack8(const f32x16& a, int s) { return (u32x4){cvtpk(a[8 * s], a[8 * s + 1]), cvtpk(a[8 * s + 2], a[8 * s + 3]), cvtpk(a[8 * s + 4], a[8 * s + 5]), cvtpk(a[8 * s + 6], a[8 * s + 7])}; }
DI float dot2(unsigned a, unsigned b, float c) { return __builtin_amdgcn_fdot2_f32_bf16(__builtin_bit_cast(bf16x2, a), __builtin_bit_cast(bf16x2, b), c, false); }
DI void acc_stats2(unsigned u0, unsigned u1, f32x4& s) {
    const float a = bflo(u0), b = bfhi(u0), c = bflo(u1), d = bfhi(u1);
    s[0] += a; s[1] += b; s[2] = __builtin_fmaf(a, a, s[2]); s[3] = __builtin_fmaf(b, b, s[3]); s[0] += c; s[1] += d; s[2] = __builtin_fmaf(c, c, s[2]); s[3] = __builtin_fmaf(d, d, s[3]);
}
DI float sum8(float v) {
    v += u2f(__builtin_amdgcn_update_dpp(0u, f2u(v), 0xB1, 0xf, 0xf, true));
    v += u2f(__builtin_amdgcn_update_dpp(0u, f2u(v), 0x4E, 0xf, 0xf, true));
    v += u2f(__builtin_amdgcn_update_dpp(0u, f2u(v), 0x141, 0xf, 0xf, true));
    return v;
}
DI u32x4 shift8(u32x4 x, float c) {
    u32x4 o; float t0, t1, t2, t3, t4, t5, t6, t7;
    asm("v_dot2_f32_bf16 %4, %12, %16, %18\n\tv_dot2_f32_bf16 %5, %12, %17, %18\n\tv_dot2_f32_bf16 %6, %13, %16, %18\n\tv_dot2_f32_bf16 %7, %13, %17, %18\n\t"
        "v_dot2_f32_bf16 %8, %14, %16, %18\n\tv_dot2_f32_bf16 %9, %14, %17, %18\n\tv_dot2_f32_bf16 %10, %15, %16, %18\n\tv_dot2_f32_bf16 %11, %15, %17, %18\n\t"
        "v_cvt_pk_bf16_f32 %0, %4, %5\n\tv_cvt_pk_bf16_f32 %1, %6, %7\n\tv_cvt_pk_bf16_f32 %2, %8, %9\n\tv_cvt_pk_bf16_f32 %3, %10, %11"
        : "=&v"(o[0]), "=&v"(o[1]), "=&v"(o[2]), "=&v"(o[3]), "=&v"(t0), "=&v"(t1), "=&v"(t2), "=&v"(t3), "=&v"(t4), "=&v"(t5), "=&v"(t6), "=&v"(t7)
        : "v"(x[0]), "v"(x[1]), "v"(x[2]), "v"(x[3]), "s"(0x00003f80u), "s"(0x3f800000u), "v"(c));
    return o;
}
template <int N> struct IC { static constexpr int v = N; };
template <int B, int E, class F> DI void sfor(F&& f) { if constexpr (B < E) { f(IC<B>{}); sfor<B + 1, E>(f); } }
template <int N> DI void waitv() { asm volatile("s_waitcnt vmcnt(%0)" :: "i"(N) : "memory"); }
template <int N> DI void waitv_bar() { if (N == 0) __builtin_amdgcn_s_waitcnt(0x0F70); asm volatile("s_waitcnt vmcnt(%0) lgkmcnt(0)\n\ts_barrier" :: "i"(N) : "memory"); }
template <int N> DI void waitv_barx() { asm volatile("s_waitcnt vmcnt(%0)\n\ts_barrier" :: "i"(N) : "memory"); }
template <int N> DI void waitv_bar2() { asm volatile("s_waitcnt vmcnt(%0) lgkmcnt(2)\n\ts_barrier" :: "i"(N) : "memory"); }
#define BAR() asm volatile("s_waitcnt lgkmcnt(0)\n\ts_barrier" ::: "memory")
#define FENCE() asm volatile("" ::: "memory")
typedef __amdgpu_buffer_rsrc_t rsrc_t;
DI u32x4 ld16_dev(rsrc_t rs, unsigned voff, unsigned soff) { return U4(__builtin_amdgcn_raw_buffer_load_b128(rs, voff, soff, 16)); }
DI u32x2 ld8(rsrc_t rs, unsigned voff, unsigned soff) { return __builtin_bit_cast(u32x2, __builtin_amdgcn_raw_buffer_load_b64(rs, voff, soff, 0)); }
DI u32x2 ld8_dev(rsrc_t rs, unsigned voff, unsigned soff) { return __builtin_bit_cast(u32x2, __builtin_amdgcn_raw_buffer_load_b64(rs, voff, soff, 16)); }
DI void st16_dev(u32x4 v, rsrc_t rs, unsigned voff, unsigned soff) { __builtin_amdgcn_raw_buffer_store_b128(v, rs, voff + soff, 0, 16); }
DI void st8_dev(u32x2 v, rsrc_t rs, unsigned voff, unsigned soff) { __builtin_amdgcn_raw_buffer_store_b64(v, rs, voff, soff, 16); }
DI void set_flag(char* ws, unsigned fo, u64 tagv) {
    asm volatile("s_mov_b64 exec, 1\n\tglobal_store_dwordx2 %0, %1, %2 sc1\n\ts_mov_b64 exec, -1" :: "v"(fo), "v"(tagv), "s"(ws) : "memory");
}
DI u64 peek_flag(char* ws, unsigned fo) { return __hip_atomic_load((GAS u64*)(ws + fo), __ATOMIC_RELAXED, __HIP_MEMORY_SCOPE_AGENT); }
DI void peek_lds(char* ws, unsigned fo, unsigned dst) {
    asm volatile("s_mov_b32 m0, %2\n\ts_nop 0\n\tglobal_load_lds_dword %0, %1 sc1\n\ts_add_u32 m0, %2, 252\n\ts_nop 0\n\tglobal_load_lds_dword %0, %1 offset:4 sc1" :: "v"(fo), "s"(ws), "s"(dst) : "memory", "scc");
}
DI u64 peeked(lds_ptr p) { return (u64)*(LAS unsigned*)(p + 256) << 32 | *(LAS unsigned*)p; }
DI void poll_flag(char* ws, unsigned fo, u64 tag) {
#pragma unroll 1
    for (int spin = 0; spin < (1 << 21); ++spin) {
        if (wave_all(peek_flag(ws, fo) == tag)) break;
        __builtin_amdgcn_s_sleep(1);
    }
    FENCE();
}
#define SB() __builtin_amdgcn_sched_barrier(0)
#define PINS(a) asm volatile("" : "+s"(a))
#define PIN(a) asm volatile("" : "+v"(a))
#define PIN2(a, b) asm volatile("" : "+v"(a), "+v"(b))
DI lds_ptr opq(lds_ptr p) { unsigned u = (unsigned)(uintptr_t)p; asm volatile("" : "+v"(u)); return (lds_ptr)(uintptr_t)u; }

constexpr int GW = 7, XN = 4;
DI constexpr int d_prio(int n) { return n < 0 ? -1 : n < 27 ? 3 - n / 7 : n < 64 ? 3 - (n - 27) / 10 : n < 84 ? 3 - (n - 64) / 5 : n < 112 ? 3 - (n - 84) / 7 : 0; }
DI constexpr int phys(int t) { return ((t % 9) + 2) % 9 * 16384; }

__global__ NOPK __launch_bounds__(512, 2) void block_fwd(const char* x, const char* wqkv, const char* wo, const char* wg, const char* wu, const char* wd, char* out, char* ws) {
    extern __shared__ __attribute__((aligned(16))) char smem[];
    lds_ptr lds = (lds_ptr)smem;
    const int tid = __builtin_amdgcn_workitem_id_x(), lane = tid & 63, w = __builtin_amdgcn_readfirstlane(tid >> 6);
    const int bid = __builtin_amdgcn_workgroup_id_x(), xcd = bid & 7, loc = bid >> 3;
    const int r = lane & 31, hf = lane >> 5, q4 = (lane & 15) >> 2, g1 = (lane >> 4) & 1, p4 = lane & 3;
    const unsigned lds0 = (unsigned)(uintptr_t)smem;
    const unsigned l16 = lane * 16;
    const int sp3 = (loc + 8) >> 5;
    const int atb = (xcd >> 1) * 8 + (loc & 7) + sp3 * (xcd & 1) * 4, acg = (loc >> 3) + (1 - sp3) * (xcd & 1) * 4;
    const int row8 = lane >> 3;
    const int rot8 = ((loc & 7) << sp3) + sp3 * (xcd & 1), rot = rot8 == 7 ? 0 : rot8;
    if (loc < 28) {
        const unsigned xo = (unsigned)((atb * 64 + (tid >> 3)) * 896 + (((tid ^ (tid >> 4)) & 7) * 16));
        const unsigned xd0 = lds0 + 1024 + (w >> 2) * 28672 + (w & 3) * 1024;
        unsigned xd = xd0 + (7 - rot) * 4096;
        const unsigned w0 = (unsigned)((rot * 64 + (tid >> 3)) * 2688 + acg * 128 + (((tid & 7) ^ ((tid >> 2) & 4)) * 16));
        asm volatile("s_cmp_eq_u32 %0, %3\n\ts_cselect_b32 %0, %2, %0\n\ts_mov_b32 m0, %0\n\ts_add_u32 %0, %0, 0x1000\n\tglobal_load_lds_dwordx4 %1, %4\n\t"
#define XP(o) "s_cmp_eq_u32 %0, %3\n\ts_cselect_b32 %0, %2, %0\n\ts_sub_u32 m0, %0, " #o "\n\ts_add_u32 %0, %0, 0x1000\n\tglobal_load_lds_dwordx4 %1, %4 offset:" #o "\n\t"
                     XP(128) XP(256) XP(384) XP(512) XP(640) XP(768)
                     "s_add_u32 m0, %6, 8192-896\n\ts_barrier\n\tglobal_load_lds_dwordx4 %5, %7 offset:896\n\t"
                     "s_add_u32 m0, %6, 16384-1792\n\ts_nop 0\n\tglobal_load_lds_dwordx4 %5, %7 offset:1792\n\t"
                     "s_mov_b32 m0, %6\n\ts_nop 0\n\tglobal_load_lds_dwordx4 %5, %7"
                     : "+&s"(xd) : "v"(xo), "s"(xd0), "s"(xd0 + 7 * 4096), "s"(x), "v"(w0), "s"(lds0 + 58368 + w * 1024), "s"(wqkv) : "memory", "scc");
    }
    SB();
    const rsrc_t rws = __builtin_amdgcn_make_buffer_rsrc((void*)ws, 0, WS_END, 0x00020000);
    u64 epoch, tagv;
    unsigned fl1;
    const unsigned ones = 0x3f803f80u;
    const bool ffn = loc < 28;
    const int l7 = loc / 7, m7 = loc % 7;
    const int tb = xcd * 4 + l7;
    const int tt = w & 1, ct = (w >> 1) & 1, kh = w >> 2;
    const unsigned swz = (unsigned)(((lane & 7) ^ (((row8 >> 1) & 1) << 2)) * 16);

    if (__builtin_expect(ffn, 1)) {
        constexpr int A_W = 58368, A_WS = 24576;
#define A_KC(c) (rot + (c) >= 7 ? rot + (c) - 7 : rot + (c))
        const unsigned wvo = (unsigned)(row8 * 2688) + swz;
        const char* wsrc = wqkv + acg * 128 + (size_t)w * 8 * 2688;
        const unsigned wdst = lds0 + A_W + w * 1024;
#define A_PIECE(c, i) glds(wvo, wsrc + A_KC(c) * (64 * 2688) + (i) * 896, wdst + ((c) & 3) * A_WS + (i) * 8192)
#define A_GROUP(c) do { A_PIECE(c, 1); A_PIECE(c, 2); A_PIECE(c, 0); } while (0)
        epoch = peek_flag(ws, FL_EPOCH * 128); FENCE(); fl1 = (FL_KV + atb * 8 + acg) * 128;
        lds_ptr xb[4], wb1, wb1h;
#pragma unroll
        for (int j = 0; j < 4; ++j) xb[j] = lds + 1024 + tt * 28672 + (r >> 3) * 1024 + (r & 7) * 128 + (((2 * j + hf) ^ ((r >> 1) & 7)) * 16);
        wb1 = opq(lds + A_W + (kh ? 0 : 8192) + (8 * hf + q4) * 128 + ((ct ^ (q4 >> 1)) * 64) + g1 * 32 + p4 * 8);
        wb1h = opq(wb1 + 2 * A_WS);
        unsigned go1[2], go2[2]; int gi[2];
#pragma unroll
        for (int p = 0; p < 2; ++p) {
            const int G = acg * 8 + ct * 4 + 2 * p + hf, hp = G / 7, i = G - hp * 7;
            const unsigned hs = kh ? 131072u : 262144u;
            const unsigned to = (kh ? WS_QF + (atb * 2 + tt) * 2048 : WS_KV + (atb * 2 + tt) * 4096) + r * 16;
            go1[p] = (unsigned)(2 * hp + (i > 3)) * hs + (unsigned)(i > 3 ? i - 3 : i) * 512 + to;
            go2[p] = i == 3 ? (unsigned)(2 * hp + 1) * hs + to : OOB;
            gi[p] = i;
        }
        const int vcol = acg * 64 + ct * 32 + r, vh = vcol / 28, vd = vcol - vh * 28;
        unsigned vo = WS_KV + (atb * 2 + tt) * 4096 + (unsigned)vh * 262144 + 2048 + hf * 512 + vd * 16;
        PIN2(go1[0], go1[1]); PIN2(go2[0], go2[1]); PIN(vo);
        lds_ptr xw = opq(lds + 1024 + kh * 28672 + (w & 3) * 1024 + l16);
        waitv<4>();
        u32x4 xv[7];
#pragma unroll
        for (int c = 0; c < 7; ++c) xv[c] = *(LAS u32x4*)(xw + c * 4096);
        SB();
        f32x4 sm = {};
#pragma unroll
        for (int c = 0; c < 7; ++c)
#pragma unroll
            for (int j = 0; j < 4; ++j) sm[j] = dot2(xv[c][j], ones, sm[j]);
        float nm = sum8((sm[0] + sm[1]) + (sm[2] + sm[3])) * (-1.f / D);
        PIN(nm);
#pragma unroll
        for (int c = 0; c < 7; ++c) { xv[c] = shift8(xv[c], nm); *(LAS u32x4*)(xw + c * 4096) = xv[c]; }
        A_GROUP(1);
        SB();
        LAS float* rst = (LAS float*)lds;
        sm = (f32x4){};
#pragma unroll
        for (int c = 0; c < 7; ++c)
#pragma unroll
            for (int j = 0; j < 4; ++j) sm[j] = dot2(xv[c][j], xv[c][j], sm[j]);
        rst[w * 8 + row8] = __builtin_amdgcn_rsqf(sum8((sm[0] + sm[1]) + (sm[2] + sm[3])) * (1.f / D) + EPS);
        A_GROUP(2);
        bf16x8 fx[4], f1[4], f2[4];
#define A_WAIT(c) do { if ((c) == 0) waitv_bar<6>(); else if ((c) == 1) waitv_barx<6>(); else if ((c) < 6) waitv_barx<3>(); else waitv_barx<0>(); } while (0)
#define A_WP(c, ks) ((((c) & 2) ? wb1h : wb1) + ((c) & 1) * A_WS + (ks) * 2048)
        if (w < 4) {
            f32x16 ak = {}, av = {};
            f32x4 rv[4];
            float rs = 0.f;
#pragma unroll
            for (int n = 0; n < 30; ++n) {
                const int c = n >> 2, ks = n & 3, km = (n - 2) & 3;
                if (n < 28 && ks == 0) A_WAIT(c);
                if (n >= 2) { ak = MFMA(f1[km], fx[km], ak); PIN(ak); } SB();
                if (n < 28) { fx[ks] = lds8(xb[ks] + c * 4096); f1[ks] = cat(trd(A_WP(c, ks)), trd(A_WP(c, ks) + 512)); } SB();
                if (n >= 2) { av = MFMA(fx[km], f2[km], av); PIN(av); } SB();
                if (n < 28) f2[ks] = cat(trd(A_WP(c, ks) + 8192), trd(A_WP(c, ks) + 8192 + 512));
                if (c == 0 && ks < 3) A_PIECE(3, ks);
                if (c >= 2 && c < 5 && ks < 3) A_PIECE(c + 2, ks);
                if (n == 21) {
                    rs = rst[tt * 32 + r];
#pragma unroll
                    for (int g = 0; g < 4; ++g) rv[g] = *(LAS f32x4*)(rst + tt * 32 + 8 * g + 4 * hf);
                }
                if (n == 24) { PIN(epoch); tagv = epoch + 1; PIN(tagv); }
                SB();
            }
#pragma unroll
            for (int i = 0; i < 16; ++i) { ak[i] *= rs; av[i] *= rv[i >> 2][i & 3]; }
#pragma unroll
            for (int p = 0; p < 2; ++p) {
                const u32x4 f = pack8(ak, p);
                const auto s0 = __builtin_amdgcn_permlane32_swap(f[0], f[2], false, false), s1 = __builtin_amdgcn_permlane32_swap(f[1], f[3], false, false);
                const u32x4 c = {s0[0], s1[0], s0[1], s1[1]};
                st16_dev(c, rws, go1[p], 0); st16_dev(c, rws, go2[p], 0);
            }
            st16_dev(pack8(av, 0), rws, vo, 0); st16_dev(pack8(av, 1), rws, vo + 1024, 0);
        } else {
            f32x16 aq = {};
            __builtin_amdgcn_s_setprio(1);
#pragma unroll
            for (int n = 0; n < 30; ++n) {
                const int c = n >> 2, ks = n & 3, km = (n - 2) & 3;
                if (n < 28 && ks == 0) A_WAIT(c);
                if (n == 24) { PIN(epoch); tagv = epoch + 1; PIN(tagv); }
                if (n >= 2) { aq = MFMA(f1[km], fx[km], aq); PIN(aq); } SB();
                if (n < 28) { fx[ks] = lds8(xb[ks] + c * 4096); f1[ks] = cat(trd(A_WP(c, ks)), trd(A_WP(c, ks) + 512)); }
                if (c == 0 && ks < 3) A_PIECE(3, ks);
                if (c >= 2 && c < 5 && ks < 3) A_PIECE(c + 2, ks);
                if (n == 24) {
                    const unsigned pv = vd == 0 ? ones : 0u, po = vd < 4 ? vo + 448 : OOB;
                    st16_dev((u32x4){pv, pv, pv, pv}, rws, po, 0); st16_dev((u32x4){pv, pv, pv, pv}, rws, po + 1024, 0);
                }
                SB();
            }
            __builtin_amdgcn_s_setprio(0);
            const float QS = rst[tt * 32 + r] * (0.18898223650461362f * 1.4426950408889634f);
#pragma unroll
            for (int i = 0; i < 16; ++i) aq[i] *= QS;
#pragma unroll
            for (int p = 0; p < 2; ++p) {
                const u32x4 f = pack8(aq, p);
                const auto s0 = __builtin_amdgcn_permlane32_swap(f[0], f[2], false, false), s1 = __builtin_amdgcn_permlane32_swap(f[1], f[3], false, false);
                const bool sp = gi[p] == 3;
                st16_dev((u32x4){s0[0], s1[0], sp ? 0u : s0[1], sp ? 0u : s1[1]}, rws, go1[p], 0);
                st16_dev((u32x4){0u, 0u, s0[1], s1[1]}, rws, go2[p], 0);
            }
        }
    } else {
        __builtin_amdgcn_s_sleep(16);
        epoch = peek_flag(ws, FL_EPOCH * 128); fl1 = (FL_RT + xcd * 4 + loc - 28) * 128;
        const int ri = xcd * 4 + loc - 28, nu = (w == 0 && ri < 16) ? 4 : 3;
        const unsigned vo = (unsigned)(row8 * 3584 + (lane & 7) * 16);
#pragma unroll 1
        for (int i = 0; i < nu; ++i) {
            const int u = i < 3 ? i * 256 + ri * 8 + w : 768 + ri;
            const size_t so = (size_t)(u / 28) * 16 * 3584 + (u % 28) * 128;
            const unsigned d_ = lds0 + w * 16384 + i * 4096;
            glds(vo, wg + so, d_); glds(vo, wg + so + 8 * 3584, d_ + 1024); glds(vo, wu + so, d_ + 2048); glds(vo, wu + so + 8 * 3584, d_ + 3072);
        }
#pragma unroll 1
        for (int i = 0; i < nu; ++i) {
            const int u = i < 3 ? i * 256 + ri * 8 + w : 768 + ri;
            waitv<8>();
            lds_ptr sg = lds + w * 16384 + i * 4096 + g1 * 2048 + (4 * hf + q4) * 128 + p4 * 8;
#pragma unroll
            for (int e = 0; e < 4; ++e) st16_dev(U4(cat(trd(sg + e * 32), trd(sg + e * 32 + 1024))), rws, l16, WS_WGU + (unsigned)(((u % 28) * 4 + e) * 28 + u / 28) * 1024);
            FENCE();
        }
        tagv = epoch + 1;
    }
    const int head = xcd * 2 + (loc & 1), rb = loc >> 1, qt = w & 3;
    const char* kvs = ws + WS_KV + (size_t)head * 262144 + w * 2048;
    const char* qs = ws + WS_QF + (size_t)(head * 64 + rb * 4 + qt) * 2048 + kh * 1024;
    unsigned kvd = lds0 + w * 2048, qd = lds0 + 147456 + qt * 2048 + kh * 1024;
    unsigned pf1 = (FL_KV + (lane >> 1) * 8 + (head * 28 + (lane & 1) * 27) / 64) * 128;
    PINS(kvs); PINS(qs); PINS(kvd); PINS(qd); PIN2(pf1, fl1);
    waitv_bar<0>();
    if (w == 0) set_flag(ws, fl1, tagv);
    const u64 tag = ((u64)(unsigned)__builtin_amdgcn_readfirstlane((unsigned)(tagv >> 32)) << 32 | (u64)(unsigned)__builtin_amdgcn_readfirstlane((unsigned)tagv));

    const int drb = (xcd >> 1) * 4 + l7, dfg = (xcd & 1) * 7 + m7;
    bf16x8 wf[28];
    const unsigned p16 = hf * 1024 + g1 * 512 + (lane & 15) * 16;
    u32x2 xa, xb;
    f32x4 csg = {}, csu = {};
    f32x16 acc = {};
    const u32x4 one4 = {ones, ones, ones, ones};
    const unsigned hi_ = (unsigned)((tb * 2 + tt) * 28 + m7 * 4 + ct * 2 + kh), ofo = (unsigned)(tt * 32768 + kh * 16384 + ct * 4096);
    unsigned ho = WS_HF + hi_ * 1024 + l16, so = hf ? OOB : WS_HS + hi_ * 256 + r * 8;
    const char* os = ws + WS_OF + (size_t)tb * 65536 + ofo;
    unsigned od = lds0 + 65536 + ofo;
    unsigned pf2 = (FL_O + (tb >> 1) * 16 + kh * 8 + ct * 2 + ((lane >> 1) & 1) + (lane & 4)) * 128;
    unsigned go = ffn ? WS_GF + (unsigned)(drb * 4 * 112 + dfg * 8 + w) * 1024 + g1 * 512 + (hf * 16 + (lane & 15)) * 16 : OOB;
    unsigned fo3 = (FL_H + tb * 8 + m7) * 128;
    {
        lds_ptr ap = opq(lds + kh * 8192 + l16), apB = opq(lds + 65536 + kh * 8192 + l16), apC = opq(lds + 131072 + kh * 8192 + l16);
        const unsigned lv = kh * 8192 + (l16 ^ (kh * 256));
        lds_ptr vp = opq(lds + lv), vpB = opq(lds + 65536 + lv), vpC = opq(lds + 131072 + lv);
#define AP(off) ((off) < 65536 ? ap + (off) : (off) < 131072 ? apB + ((off) - 65536) : apC + ((off) - 131072))
#define VP(off) ((off) < 65536 ? vp + (off) : (off) < 131072 ? vpB + ((off) - 65536) : vpC + ((off) - 131072))
#define KV_ISSUE(t) glds2_dev(l16, kvs + (t) * 16384, kvd + phys(t))
        unsigned wgo = ffn ? WS_WGU + (unsigned)(dfg * 8 + w) * 28672 : OOB;
        unsigned clro = WS_GF + (unsigned)((((xcd * 4 + loc - 28) >> 1) * 4 + 2 + (loc & 1)) * 112 + w * 14) * 1024 + l16;
        const int ri = umin(xcd * 4 + loc - 28, 27);
        const char* wos = (ffn ? wo + m7 * 128 : wd + (ri % 7) * 128 + (size_t)(ri / 7) * 448 * 896) + (size_t)w * 56 * 896;
        unsigned wov = (unsigned)(row8 * 896) + swz;
        const rsrc_t rx = __builtin_amdgcn_make_buffer_rsrc((void*)x, 0, 2048 * 448 * 2, 0x00020000);
        unsigned xo_ = (unsigned)(((tb * 2 + tt) * 32 + r) * 896 + m7 * 128 + ct * 64 + kh * 32 + hf * 8);
        unsigned rto = (FL_RT + w * 4 + (lane & 3)) * 128;
        lds_ptr qp = opq(lds + 147456 + qt * 2048 + l16);
        PINS(wgo); PINS(wos); PIN2(wov, xo_); PIN(rto); PIN2(ho, so); PIN2(pf2, fo3); PIN(go);
        lds_ptr xw = opq(lds + 147456 + w * 2048 + l16), xr = opq(lds + 147456 + (w ^ 4) * 2048 + l16);
        lds_ptr lw = opq(lds + 58368 + w * 256 + lane * 4), lr = opq(lds + 58368 + qt * 256 + 128 + r * 4);
        lds_ptr bw = opq(lds + 60416 + w * 4), br = opq(lds + 60416 + (lane & 7) * 4);
        unsigned oo = WS_OF + (unsigned)((rb * 4 + qt) * 32 + head * 2 + kh) * 1024 + l16;
        unsigned fo2 = (FL_O + rb * 16 + head) * 128;
        PIN2(oo, fo2);
        if (w == 1) poll_flag(ws, pf1, tag);
        BAR();
        glds_dev(l16, qs, qd);
        KV_ISSUE(0); KV_ISSUE(1);
        asm volatile("s_barrier" ::: "memory");
        KV_ISSUE(2); KV_ISSUE(3);
        waitv_bar<4>();
        const bf16x8 q0 = lds8(qp), q1 = lds8(qp + 1024);
        f32x16 o = {}, sa = {}, sb;
        bf16x8 k0 = lds8(ap + phys(0)), k1 = lds8(ap + phys(0) + 1024), v0 = lds8(vp + phys(0) + 2048), v1 = {};
        u32x4 p0, p1 = {};
        sa = MFMA(k0, q0, sa); sa = MFMA(k1, q1, sa);
        k0 = lds8(ap + phys(0) + 4096); k1 = lds8(ap + phys(0) + 5120);
        PIN(sa); SB();
        u64 rtf = 0;
#define EXP4(S, o4) _Pragma("unroll") for (int i_ = o4; i_ < o4 + 4; ++i_) S[i_] = __builtin_amdgcn_exp2f(S[i_]);
#define WLD(s) wf[s] = BF8(ld16_dev(rws, p16 + ((s) & 1) * 256 + ((s) & 2) * 1024, wgo + ((s) >> 2) * 4096))
#define SIDE(t, g) do { \
            if ((t) == 0 && (g) == 0) KV_ISSUE(4); if ((t) == 0 && (g) == 2) KV_ISSUE(5); if ((t) == 1 && (g) == 0) KV_ISSUE(6); if ((t) == 1 && (g) == 2) KV_ISSUE(7); \
            if ((t) == 2 && (g) == 0) KV_ISSUE(8); \
            if ((t) == 1 && (g) == 1) peek_lds(ws, rto, lds0 + 155648 + w * 512); if ((t) == 2 && (g) == 2) { waitv<4>(); rtf = peeked(lds + 155648 + w * 512 + lane * 4); } \
            if ((t) == 3 && (g) < 3) KV_ISSUE(9 + (g)); \
            if ((t) >= 4 && (t) <= 10 && ((g) & 1)) { if (__builtin_expect(!ffn, 0)) st16_dev((u32x4){~0u, ~0u, ~0u, ~0u}, rws, clro + (((t) - 4) * 2 + ((g) >> 1)) * 1024, 0); } \
            if ((t) == 7 && (g) == 0) KV_ISSUE(12); if ((t) == 7 && (g) == 2) KV_ISSUE(13); if ((t) == 8 && (g) == 0) KV_ISSUE(14); if ((t) == 8 && (g) == 2) KV_ISSUE(15); \
            if ((t) >= 4 && (t) <= 6) WLD(((t) - 4) * 4 + (g)); \
            if (((t) == 7 || (t) == 8) && ((g) & 1)) WLD(12 + ((t) - 7) * 2 + ((g) >> 1)); \
            if ((t) >= 9 && (t) <= 11) WLD(((t) - 5) * 4 + (g)); \
            if ((t) == 12 || ((t) == 13 && (g) < 3)) glds(wov, wos + (((t) - 12) * 4 + (g)) * 8 * 896, lds0 + (w * 7 + ((t) - 12) * 4 + (g)) * 1024); \
            if ((t) == 13 && (g) == 3) { xa = ld8(rx, xo_, 0); xb = ld8(rx, xo_, 16); } \
            if ((t) == 14 && (g) == 0) { if (tid < 32) *(LAS u32x4*)(lds + 57344 + tid * 16) = (u32x4){0, 0, 0, 0}; } \
        } while (0)
#define TOFF(i) (phys((i) >> 1) + ((i) & 1) * 4096)
#define TILE(i, SC, SN) do { \
            __builtin_amdgcn_s_setprio(((i) & 1) ? 1 : 3); \
            if ((i) < 31) { f32x16 z_ = {}; SN = MFMA(k0, q0, z_); PIN(SN); } if ((i) < 30) k0 = lds8(AP(TOFF((i) + 2))); SB(); \
            EXP4(SC, 0); PIN(SC); SB(); \
            o = MFMA(v1, BF8(p1), o); PIN(o); v1 = lds8(VP(TOFF(i) + 3072)); SIDE((i) >> 1, ((i) & 1) * 2); SB(); \
            EXP4(SC, 4); p0 = pack8(SC, 0); PIN(p0); SB(); \
            __builtin_amdgcn_s_setprio(((i) & 1) ? 0 : 2); \
            if ((i) < 31) { SN = MFMA(k1, q1, SN); PIN(SN); } if ((i) < 30) k1 = lds8(AP(TOFF((i) + 2) + 1024)); SB(); \
            EXP4(SC, 8); PIN(SC); SB(); \
            o = MFMA(v0, BF8(p0), o); PIN(o); if ((i) < 31) v0 = lds8(VP(TOFF((i) + 1) + 2048)); SIDE((i) >> 1, ((i) & 1) * 2 + 1); SB(); \
            EXP4(SC, 12); p1 = pack8(SC, 1); PIN(p1); SB(); } while (0)
#define STEP(t) do { TILE(2 * (t), sa, sb); TILE(2 * (t) + 1, sb, sa); } while (0)
        STEP(0);
        waitv_barx<4>();
        STEP(1); STEP(2);
        if (__builtin_expect(!wave_all(rtf == tag), 0)) poll_flag(ws, rto, tag);
        waitv_bar2<2>();
        STEP(3); STEP(4); STEP(5); STEP(6);
        waitv_bar2<12>();
        STEP(7); STEP(8); STEP(9); STEP(10);
        waitv_bar2<9>();
        STEP(11); STEP(12); STEP(13); STEP(14); STEP(15);
        o = MFMA(v1, BF8(p1), o);
        u32x4 ov;
        {
            *(LAS f32x4*)xw = (f32x4){o[8], o[9], o[10], o[11]}; *(LAS f32x4*)(xw + 1024) = (f32x4){o[12], o[13], o[14], o[15]};
            const float l = kh ? o[4] : o[12];
            *(LAS float*)lw = l;
#define AMX(a, b, c) __builtin_fmaxf(__builtin_fmaxf(__builtin_fabsf(a), __builtin_fabsf(b)), __builtin_fabsf(c))
            const float mx_ = AMX(AMX(AMX(o[0], o[1], o[2]), AMX(o[3], o[4], o[5]), AMX(o[6], o[7], o[8])), AMX(AMX(o[9], o[10], o[11]), AMX(o[12], o[13], o[14]), o[15]), 0.f);
            const bool ok = (mx_ < 0x1p100f) & ((hf ? l : 1.f) > 0x1p-40f);
            *(LAS unsigned*)bw = wave_all(ok) ? 0u : 1u;
            BAR();
            const f32x4 a = *(LAS f32x4*)xr, c = *(LAS f32x4*)(xr + 1024);
            const float inv = __builtin_amdgcn_rcpf(*(LAS float*)lr + *(LAS float*)(lr + 1024));
            const unsigned bad = *(LAS unsigned*)br;
            const float iz = (kh && hf) ? 0.f : inv;
            ov = (u32x4){cvtpk((o[0] + a[0]) * inv, (o[1] + a[1]) * inv), cvtpk((o[2] + a[2]) * inv, (o[3] + a[3]) * inv),
                         cvtpk((o[4] + c[0]) * iz, (o[5] + c[1]) * iz), cvtpk((o[6] + c[2]) * iz, (o[7] + c[3]) * iz)};
            if (__builtin_expect(wave_any(bad != 0), 0)) {
                f32x16 of = {};
                float m = -0x1p126f;
#pragma unroll 1
                for (int t = 0; t < 16; ++t) {
                    BAR();
                    glds2_dev(l16, kvs + t * 16384, lds0 + 65536 + w * 2048);
                    waitv_bar<0>();
#pragma unroll 1
                    for (int u = 0; u < 2; ++u) {
                        f32x16 s = {};
                        s = MFMA(lds8(apB + u * 4096), q0, s); s = MFMA(lds8(apB + u * 4096 + 1024), q1, s);
                        float mx = s[0];
#pragma unroll
                        for (int i = 1; i < 16; ++i) mx = __builtin_fmaxf(mx, s[i]);
                        mx = __builtin_fmaxf(swap_max(mx), m);
                        const float alpha = __builtin_amdgcn_exp2f(m - mx);
                        m = mx;
#pragma unroll
                        for (int i = 0; i < 16; ++i) { s[i] = __builtin_amdgcn_exp2f(s[i] - mx); of[i] *= alpha; }
                        of = MFMA(lds8(vpB + u * 4096 + 2048), BF8(pack8(s, 0)), of);
                        of = MFMA(lds8(vpB + u * 4096 + 3072), BF8(pack8(s, 1)), of);
                    }
                }
                *(LAS f32x4*)xw = (f32x4){of[8], of[9], of[10], of[11]}; *(LAS f32x4*)(xw + 1024) = (f32x4){of[12], of[13], of[14], of[15]};
                *(LAS float*)lw = hf ? (kh ? of[4] : of[12]) : m;
                BAR();
                const f32x4 a2 = *(LAS f32x4*)xr, c2 = *(LAS f32x4*)(xr + 1024);
                const float m0 = *(LAS float*)(lr - 128), m1 = *(LAS float*)(lr + 1024 - 128), mm = __builtin_fmaxf(m0, m1);
                const float e0 = __builtin_amdgcn_exp2f(m0 - mm), e1 = __builtin_amdgcn_exp2f(m1 - mm);
                const float iv = __builtin_amdgcn_rcpf(*(LAS float*)lr * e0 + *(LAS float*)(lr + 1024) * e1);
                const float im = (kh ? e1 : e0) * iv, io = (kh ? e0 : e1) * iv;
                const bool z = kh && hf;
                ov = (u32x4){cvtpk(of[0] * im + a2[0] * io, of[1] * im + a2[1] * io), cvtpk(of[2] * im + a2[2] * io, of[3] * im + a2[3] * io),
                             z ? 0u : cvtpk(of[4] * im + c2[0] * io, of[5] * im + c2[1] * io), z ? 0u : cvtpk(of[6] * im + c2[2] * io, of[7] * im + c2[3] * io)};
            }
        }
        st16_dev(ov, rws, oo, 0);
        FENCE();
        PIN2(xa, xb);
#pragma unroll
        for (int s = 27; s >= 0; --s) PIN(wf[s]);
#pragma unroll
        for (int s = 0; s < 16; s += 2) { csg = MFMA16(wf[s], BF8(one4), csg); csu = MFMA16(wf[s + 1], BF8(one4), csu); }
        acc[0] = bflo(xa[0]); acc[1] = bfhi(xa[0]); acc[2] = bflo(xa[1]); acc[3] = bfhi(xa[1]); acc[4] = bflo(xb[0]); acc[5] = bfhi(xb[0]); acc[6] = bflo(xb[1]); acc[7] = bfhi(xb[1]);
        PIN(acc); PIN2(ho, so); PIN2(pf2, fo3); PINS(os); PINS(od); PIN2(csg, csu);
        waitv_bar<0>();
        if (w == 0) set_flag(ws, fo2, tagv);
    }
    if (__builtin_expect(!ffn, 0)) {
        const int ri = xcd * 4 + loc - 28;
        if (ri < 28) {
#pragma unroll 1
            for (int u = w; u < 28; u += 8) {
                lds_ptr sd = lds + u * 2048 + (4 * hf + q4) * 128 + g1 * 32 + p4 * 8;
#pragma unroll
                for (int e = 0; e < 2; ++e) { lds_ptr p_ = sd + ((e ^ (q4 >> 1)) * 64);
                    st16_dev(U4(cat(trd(p_), trd(p_ + 1024))), rws, l16, WS_WD + (unsigned)(((ri % 7) * 112 + (ri / 7) * 28 + u) * 2 + e) * 1024); }
            }
        }
        waitv_bar<0>();
        if (w == 0) set_flag(ws, (FL_RD + ri) * 128, tagv);
        return;
    }

    const int erb = tb >> 1;
    const char* hs = ws + WS_HF + (size_t)drb * 114688 + w * 1024;
    const char* hss = ws + WS_HS + (size_t)((drb * 4 + (w & 3)) * 28 + kh * 14) * 256;
    unsigned pf3, fo4 = (FL_G + drb * 64 + dfg) * 128;
    {
        lds_ptr wb = lds + kh * (8 * 28 * 128) + (4 * hf + q4) * 128 + ((ct ^ (q4 >> 1)) * 64) + (g1 ^ kh) * 32 + p4 * 8;
        bf16x8 wr[16];
#pragma unroll
        for (int s = 0; s < 12; ++s) { const int k0 = ((s >> 1) * 28 + (s & 1) * 16) * 128; wr[s] = cat(trd(wb + k0), trd(wb + k0 + 1024)); }
        lds_ptr cw = opq(lds + 131072 + w * 2048 + l16), cr = opq(lds + 131072 + (w ^ 4) * 2048 + l16);
        lds_ptr ob = opq(lds + 65536 + tt * 32768 + kh * 16384 + l16);
        pf3 = (FL_H + drb * 16 + (lane & 8) + umin(lane & 7, 6)) * 128;
        PIN2(pf3, fo4); PINS(hs); PINS(hss);
        poll_flag(ws, pf2, tag);
        glds4_dev(l16, os, od);
        asm volatile("s_barrier" ::: "memory");
        glds4_dev(l16, os + 8192, od + 8192);
        bf16x8 of[8];
        waitv_bar<4>();
#pragma unroll
        for (int s = 0; s < 8; ++s) of[s] = lds8(ob + s * 1024);
        SB();
#pragma unroll
        for (int s = 0; s < 8; ++s) {
            acc = MFMA(wr[s], of[s], acc); PIN(acc); SB();
            if (s >= 2 && s < 6) { const int k0 = (((s + 10) >> 1) * 28 + (s & 1) * 16) * 128; wr[s + 10] = cat(trd(wb + k0), trd(wb + k0 + 1024)); SB(); }
        }
        waitv_barx<0>();
#pragma unroll
        for (int s = 0; s < 8; ++s) of[s] = lds8(ob + (8 + s) * 1024);
        SB();
#pragma unroll
        for (int s = 0; s < 8; ++s) acc = MFMA(wr[8 + s], of[s], acc);
        *(LAS f32x4*)cw = (f32x4){acc[8], acc[9], acc[10], acc[11]}; *(LAS f32x4*)(cw + 1024) = (f32x4){acc[12], acc[13], acc[14], acc[15]};
        BAR();
        const f32x4 a = *(LAS f32x4*)cr + (f32x4){acc[0], acc[1], acc[2], acc[3]}, b = *(LAS f32x4*)(cr + 1024) + (f32x4){acc[4], acc[5], acc[6], acc[7]};
        const u32x4 hv = {cvtpk(a[0], a[1]), cvtpk(a[2], a[3]), cvtpk(b[0], b[1]), cvtpk(b[2], b[3])};
        st16_dev(hv, rws, ho, 0);
        FENCE(); SB();
        float s1 = 0.f, s2 = 0.f;
        { f32x4 q4_ = {}; acc_stats2(hv[0], hv[1], q4_); acc_stats2(hv[2], hv[3], q4_); s1 = swap_add(q4_[0] + q4_[1]); s2 = swap_add(q4_[2] + q4_[3]); }
        st8_dev((u32x2){f2u(s1), f2u(s2)}, rws, so, 0);
        FENCE(); PIN2(csg, csu); SB();
#pragma unroll
        for (int s = 16; s < 28; s += 2) { csg = MFMA16(wf[s], BF8(one4), csg); csu = MFMA16(wf[s + 1], BF8(one4), csu); }
        PIN2(csg, csu);
        waitv_bar<0>();
        if (w == 0) set_flag(ws, fo3, tagv);
        if (bid == 0 && w == 0) set_flag(ws, FL_EPOCH * 128, tagv);
    }

    const unsigned wdo = WS_WD + (unsigned)(m7 * 112 + w * 14) * 2048;
    bf16x8 wdf[28];
    bf16x8 gf[GW];
    const unsigned gxo = WS_GF + (unsigned)(erb * 4 * 112 + w * 14) * 1024 + (tb & 1) * 256;
    const unsigned pwd = hf * 2048 + g1 * 512 + (lane & 15) * 16;
#define G_LD(s) gf[(s) % GW] = BF8(ld16_dev(rws, p16, gxo + ((s) / 7) * 114688 + ((s) % 7) * 2048))
    u64 gfl = 0;
    const unsigned gfi = (FL_G + (erb * 4 + g1) * 16 + ((w * 14 + umin(lane & 15, 13)) >> 3)) * 128;
    {
        const unsigned hd = lds0 + w * 1024;
        const char* wds = ws + wdo;
        lds_ptr hb = opq(lds + p16), hb2 = opq(lds + 57344 + p16);
        lds_ptr wl = opq(lds + 114688 + w * 5120 + l16), wl2 = opq(lds + w * 7168 + l16);
        LAS f32x2* stp = (LAS f32x2*)opq(lds + 159744 + (lane & 15) * 8);
        if (w == 1) poll_flag(ws, pf3, tag);
        BAR();
#define H_PIECE(j) glds_dev(l16, hs + (j) * 8192, hd + (j) * 8192)
#define WDO(s) (((s) >> 2) * 4096 + (((s) >> 1) & 1) * 1024 + ((s) & 1) * 256)
#define WDLD(s) wdf[s] = BF8(ld16_dev(rws, pwd, wdo + WDO(s)))
#define HB(n) (((n) < 56 ? hb + ((n) & ~1) * 1024 : hb2 + (((n) & ~1) - 56) * 1024) + ((n) & 1) * 256)
        H_PIECE(0);
        asm volatile("s_barrier" ::: "memory");
        H_PIECE(1); H_PIECE(2); H_PIECE(3);
        constexpr int PF = 5;
        bf16x8 bf[PF + 1];
        f32x4 ag[2][2], au[2][2];
        f32x2 sp[7], sv[4];
        float mean[2] = {0.f, 0.f}, r2[2] = {0.f, 0.f}, hp = 0.f;
        u32x4 hv;
        lds_ptr pk = opq(lds + 155648 + w * 512 + lane * 4);
        u64 pv = 0;
        const unsigned pkd = lds0 + 155648 + w * 512;
        const unsigned rdo = (FL_RD + ((lane & 4) ? (w >> 1) * 7 + m7 : ((lane & 2) ? drb : erb) * 2 + (lane & 1))) * 128;
        waitv_barx<3>();
#pragma unroll
        for (int n = 0; n < PF; ++n) bf[n] = lds8(HB(n));
        SB();
        sfor<0, 125>([&](auto N_) __attribute__((always_inline)) {
            constexpr int n = decltype(N_)::v, p = n / 28, s = n % 28;
            if constexpr (n == 3) waitv_barx<3>();
            if constexpr (n == 11) waitv_barx<7>();
            if constexpr (n == 27) { waitv_barx<3>(); }
            if constexpr (n == 64) { waitv_barx<12>(); if (w == 0) set_flag(ws, fo4, tagv); }
            if constexpr (n == 85) { waitv_barx<9>(); if (w == 0) set_flag(ws, fo4 + 2048, tagv); }
            if constexpr (n == 38) { if (__builtin_expect(!wave_all(pv == tag), 0)) poll_flag(ws, rdo, tag); FENCE(); }
            if constexpr (d_prio(n) != d_prio(n - 1)) __builtin_amdgcn_s_setprio(d_prio(n));
            if constexpr (n < 112) {
                if constexpr (s < 2) { ag[p & 1][s] = (f32x4){}; au[p & 1][s] = (f32x4){}; }
                ag[p & 1][s & 1] = MFMA16(wf[s & ~1], bf[n % (PF + 1)], ag[p & 1][s & 1]); au[p & 1][s & 1] = MFMA16(wf[s | 1], bf[n % (PF + 1)], au[p & 1][s & 1]);
                PIN2(ag[p & 1][s & 1], au[p & 1][s & 1]);
            }
            SB();
            if constexpr (n + PF < 112) bf[(n + PF) % (PF + 1)] = lds8(HB(n + PF));
            if constexpr (n % 3 == 2 && n < 30) H_PIECE(4 + n / 3);
            if constexpr (n == 4) glds4_dev(l16, hss, lds0 + 114688 + w * 5120);
            if constexpr (n == 16) { waitv<4>();
#pragma unroll
                for (int j = 0; j < 7; ++j) sp[j] = *(LAS f32x2*)(lds + 114688 + w * 5120 + (hf * 7 + j) * 256 + r * 8);
            }
            if constexpr (n == 20) { const f32x2 t = ((sp[0] + sp[1]) + (sp[2] + sp[3])) + ((sp[4] + sp[5]) + sp[6]); stp[w * 32 + g1 * 16] = (f32x2){swap_add(t[0]), swap_add(t[1])}; }
            if constexpr (n >= 39 && n < 48 && n % 2 == 1) glds_dev(pwd, wds + WDO((n - 39) / 2), lds0 + 114688 + w * 5120 + (n - 39) / 2 * 1024);
            if constexpr (n >= 48 && n < 64 && n % 2 == 0) WDLD(12 + (n - 48) / 2);
            if constexpr (n >= 70 && n < 84 && n % 2 == 0) glds_dev(pwd, wds + WDO(5 + (n - 70) / 2), lds0 + w * 7168 + (n - 70) / 2 * 1024);
            if constexpr (n >= 86 && n < 102 && n % 2 == 0) WDLD(20 + (n - 86) / 2);
            if constexpr (n == 84) peek_lds(ws, gfi - g1 * 2048, pkd);
            if constexpr (n == 95) { waitv<5>(); pv = peeked(pk); }
            if constexpr (n == 99) { if (__builtin_expect(!wave_all(pv == tag), 0)) poll_flag(ws, gfi - g1 * 2048, tag); FENCE(); }
            if constexpr (n >= 102 && n < 102 + 2 * XN && n % 2 == 0) G_LD((n - 102) / 2);
            if constexpr (n == 111) gfl = peek_flag(ws, gfi);
            if constexpr (n == 32) pv = peeked(pk);
            if constexpr (n == 18) peek_lds(ws, rdo, pkd);
            if constexpr (p >= 1) {
                constexpr int e = (p - 1) & 1, tp = n == 100 ? 3 : p - 1;
                if constexpr ((s == 0 && p < 4) || n == 100) { sv[0] = stp[tp * 32]; sv[1] = stp[(tp + 4) * 32]; sv[2] = stp[tp * 32 + 16]; sv[3] = stp[(tp + 4) * 32 + 16]; }
                if constexpr (((s == 2 || s == 3) && p < 4) || n == 104 || n == 105) { constexpr int k = n & 1; const f32x2 t = sv[2 * k] + sv[2 * k + 1];
                    mean[k] = t[0] * (1.f / D); r2[k] = __builtin_amdgcn_rcpf(__builtin_fmaxf(t[1] * (1.f / D) - mean[k] * mean[k], 0.f) + EPS); PIN2(mean[k], r2[k]); }
                if constexpr (s >= 4 && s < 12) { constexpr int i = s - 4, k = i >> 2;
                    const float h0 = __builtin_fmaxf(ag[e][k][i & 3] - mean[k] * csg[i & 3], 0.f) * r2[k] * (au[e][k][i & 3] - mean[k] * csu[i & 3]);
                    if constexpr (i & 1) { hv[i >> 1] = p >= 3 ? umin(cvtpk(hp, h0), ~1u) : cvtpk(hp, h0); PIN(hv); } else { hp = h0; PIN(hp); } }
                if constexpr (s == 12) {
                    const auto s0 = __builtin_amdgcn_permlane32_swap(hv[0], hv[2], false, false), s1 = __builtin_amdgcn_permlane32_swap(hv[1], hv[3], false, false);
                    st16_dev((u32x4){s0[0], s1[0], s0[1], s1[1]}, rws, go + (p - 1) * 114688, 0); FENCE();
                }
            }
            SB();
        });
#pragma unroll
        for (int s = XN; s < 7; ++s) { G_LD(s); SB(); }
        FENCE();
#pragma unroll
        for (int s = 0; s < 12; ++s) wdf[s] = lds8(s < 5 ? wl + s * 1024 : wl2 + (s - 5) * 1024);
    }

    {
        const int etok = tid >> 4, eblk = tid & 15;
        const int et = erb * 4 + (etok >> 4), er = (tb & 1) * 16 + (etok & 15);
        const unsigned hro = (unsigned)((((eblk >> 2) & 3) * 64 + (eblk & 1) * 32 + er) * 16 + ((eblk >> 1) & 1) * 8) + WS_HF + (unsigned)(et * 28 + m7 * 4) * 1024;
        u32x2 hres[2];
        GAS u64* op = (GAS u64*)(out + (size_t)(et * 32 + er) * 896 + m7 * 128 + eblk * 8);
        lds_ptr rp = opq(lds + w * 8448 + (lane >> 4) * 528 + (lane & 15) * 16), rq = opq(lds + eblk * 528 + etok * 16);
        lds_ptr rp2 = opq(rp + 67584), rq2 = opq(rq + 67584);
        f32x4 c[4];
#define G_STEP(s) do { _Pragma("unroll") for (int j = 0; j < 4; ++j) { if ((s) % 7 == 0) c[j] = (f32x4){}; c[j] = MFMA16(wdf[4 * ((s) % 7) + j], gf[(s) % GW], c[j]); } PIN2(c[0], c[1]); PIN2(c[2], c[3]); } while (0)
#define G_PUT(q) do { _Pragma("unroll") for (int j = 0; j < 4; ++j) *(LAS f32x4*)(((q) < 2 ? rp : rp2) + j * 2112 + ((q) & 1) * 256) = c[j]; } while (0)
#define G_OUT(hh, RQ) do { f32x4 pa[8]; \
            _Pragma("unroll") for (int ww = 0; ww < 8; ++ww) pa[ww] = *(LAS f32x4*)(RQ + ww * 8448); SB(); \
            const f32x4 a = ((pa[0] + pa[1]) + (pa[2] + pa[3])) + ((pa[4] + pa[5]) + (pa[6] + pa[7])); \
            const u32x2 ov = {cvtpk(a[0] + bflo(hres[hh][0]), a[1] + bfhi(hres[hh][0])), cvtpk(a[2] + bflo(hres[hh][1]), a[3] + bfhi(hres[hh][1]))}; \
            __hip_atomic_store(op + (hh) * (64 * 896 / 8), __builtin_bit_cast(u64, ov), __ATOMIC_RELAXED, __HIP_MEMORY_SCOPE_AGENT); } while (0)
        unsigned seen = 0;
#pragma unroll
        for (int s = 0; s < 28; ++s) {
            if (s == 6) BAR();
            if (s == 7 || s == 14 || s == 21) G_PUT(s / 7 - 1);
            if (s == 21) { waitv<7>(); PIN2(hres[0], hres[1]); BAR(); G_OUT(0, rq); FENCE(); }
            SB();
            G_STEP(s);
            if (s >= 14) { const u32x4 u = U4(gf[s % GW]); seen = umax3(umax3(seen, u[0], u[1]), u[2], u[3]); PIN(seen); }
            SB();
            if (s == 0) { if (__builtin_expect(!wave_all(gfl == tag), 0)) poll_flag(ws, gfi, tag); FENCE(); }
            if (s + GW < 28) G_LD(s + GW);
            if (s == 1) { hres[0] = ld8_dev(rws, hro, 0); hres[1] = ld8_dev(rws, hro, 57344); FENCE(); }
        }
        if (w == 0) { set_flag(ws, fo4 + 4096, tagv); set_flag(ws, fo4 + 6144, tagv); }
        if (__builtin_expect(wave_any(seen == ~0u), 0)) {
            poll_flag(ws, gfi + 4096, tag);
#pragma unroll
            for (int s = 14; s < 14 + GW; ++s) G_LD(s);
#pragma unroll
            for (int s = 14; s < 28; ++s) { if (s == 21) G_PUT(2); G_STEP(s); SB(); if (s + GW < 28) G_LD(s + GW); SB(); }
        }
        G_PUT(3);
        BAR();
        G_OUT(1, rq2);
    }
}

extern "C" void kernel_launch(void* const* d_in, const int* in_sizes, int n_in, void* d_out, int out_size, void* d_ws, size_t ws_size, hipStream_t stream) {
    (void)hipFuncSetAttribute((const void*)block_fwd, hipFuncAttributeMaxDynamicSharedMemorySize, LDS_BYTES);
    block_fwd<<<256, 512, LDS_BYTES, stream>>>((const char*)d_in[0], (const char*)d_in[1], (const char*)d_in[2], (const char*)d_in[4], (const char*)d_in[5], (const char*)d_in[6], (char*)d_out, (char*)d_ws);
}
```

```cpp
#define CL0 9
#define CL1 58
#include <hip/hip_runtime.h>
#include <hip/hip_bf16.h>
#include <stdint.h>

typedef __attribute__((ext_vector_type(8))) short bf16x8;
typedef __attribute__((ext_vector_type(4))) short s16x4;
typedef __attribute__((ext_vector_type(2))) __bf16 bf16x2;
typedef __attribute__((ext_vector_type(16))) float f32x16;
typedef __attribute__((ext_vector_type(4))) float f32x4;
typedef __attribute__((ext_vector_type(2))) float f32x2;
typedef __attribute__((ext_vector_type(4))) unsigned u32x4;
typedef __attribute__((ext_vector_type(2))) unsigned u32x2;
typedef short v4i16_t __attribute__((ext_vector_type(4)));
typedef unsigned long long u64;
#define LAS __attribute__((address_space(3)))
#define GAS __attribute__((address_space(1)))
typedef LAS char* lds_ptr;
#if defined(__HIP_DEVICE_COMPILE__)
#define NOPK __attribute__((target("no-packed-fp32-ops")))
#else
#define NOPK
#endif
#define DI __device__ __forceinline__
#define MFMA(a, b, c) __builtin_amdgcn_mfma_f32_32x32x16_bf16(a, b, c, 0, 0, 0)
#define MFMA16(a, b, c) __builtin_amdgcn_mfma_f32_16x16x32_bf16(a, b, c, 0, 0, 0)
#define BF8(x) __builtin_bit_cast(bf16x8, x)
#define U4(x) __builtin_bit_cast(u32x4, x)

constexpr int D = 448;
constexpr float EPS = 1e-5f;
constexpr unsigned FL_EPOCH = 0, FL_KV = 1, FL_RT = FL_KV + 256, FL_RD = FL_RT + 32, FL_O = FL_RD + 32, FL_H = FL_O + 256, FL_G = FL_H + 256;
constexpr unsigned WS_KV = 1u << 20;
constexpr unsigned WS_QF = WS_KV + 4194304;
constexpr unsigned WS_OF = WS_QF + 2097152;
constexpr unsigned WS_HF = WS_OF + 2097152;
constexpr unsigned WS_GF = WS_HF + 64 * 28 * 1024;
constexpr unsigned WS_WGU = WS_GF + 64 * 112 * 1024;
constexpr unsigned WS_WD = WS_WGU + 112 * 28 * 1024;
constexpr unsigned WS_HS = WS_WD + 7 * 112 * 2 * 1024;
constexpr unsigned WS_END = WS_HS + 64 * 28 * 256 + 1024;
constexpr unsigned OOB = 0x40000000u;
constexpr int LDS_BYTES = 163840;

DI float u2f(unsigned u) { return __builtin_bit_cast(float, u); }
DI unsigned f2u(float f) { return __builtin_bit_cast(unsigned, f); }
DI unsigned umin(unsigned a, unsigned b) { return a < b ? a : b; }
DI unsigned umax3(unsigned a, unsigned b, unsigned c) { const unsigned m = a > b ? a : b; return m > c ? m : c; }
DI bool wave_any(bool p) { return __builtin_amdgcn_ballot_w64(p) != 0; }
DI bool wave_all(bool p) { return __builtin_amdgcn_ballot_w64(!p) == 0; }
DI unsigned cvtpk(float lo, float hi) { return __builtin_bit_cast(unsigned, __builtin_convertvector((f32x2){lo, hi}, bf16x2)); }
DI float bflo(unsigned u) { return u2f(u << 16); }
DI float bfhi(unsigned u) { return u2f(u & 0xffff0000u); }
DI void glds(unsigned voff, const void* sbase, unsigned dst) {
    asm volatile("s_mov_b32 m0, %2\n\ts_nop 0\n\tglobal_load_lds_dwordx4 %0, %1" :: "v"(voff), "s"(sbase), "s"(dst) : "memory");
}
DI void glds_dev(unsigned voff, const void* sbase, unsigned dst) {
    asm volatile("s_mov_b32 m0, %2\n\ts_nop 0\n\tglobal_load_lds_dwordx4 %0, %1 sc1" :: "v"(voff), "s"(sbase), "s"(dst) : "memory");
}
DI void glds2_dev(unsigned voff, const void* sbase, unsigned dst) {
    asm volatile("s_mov_b32 m0, %2\n\ts_nop 0\n\tglobal_load_lds_dwordx4 %0, %1 sc1\n\tglobal_load_lds_dwordx4 %0, %1 offset:1024 sc1" :: "v"(voff), "s"(sbase), "s"(dst) : "memory");
}
DI void glds4_dev(unsigned voff, const void* sbase, unsigned dst) {
    asm volatile("s_mov_b32 m0, %2\n\ts_nop 0\n\tglobal_load_lds_dwordx4 %0, %1 sc1\n\tglobal_load_lds_dwordx4 %0, %1 offset:1024 sc1\n\t"
                 "global_load_lds_dwordx4 %0, %1 offset:2048 sc1\n\tglobal_load_lds_dwordx4 %0, %1 offset:3072 sc1" :: "v"(voff), "s"(sbase), "s"(dst) : "memory");
}
DI s16x4 trd(lds_ptr p) { return __builtin_bit_cast(s16x4, __builtin_amdgcn_ds_read_tr16_b64_v4i16((LAS v4i16_t*)p)); }
DI bf16x8 cat(s16x4 a, s16x4 b) { return __builtin_shufflevector(a, b, 0, 1, 2, 3, 4, 5, 6, 7); }
DI bf16x8 lds8(lds_ptr p) { return *(LAS bf16x8*)p; }
DI float swap_add(float v) { auto r = __builtin_amdgcn_permlane32_swap(f2u(v), f2u(v), false, false); return u2f(r[0]) + u2f(r[1]); }
DI float swap_max(float v) { auto r = __builtin_amdgcn_permlane32_swap(f2u(v), f2u(v), false, false); return __builtin_fmaxf(u2f(r[0]), u2f(r[1])); }
DI u32x4 pack8(const f32x16& a, int s) { return (u32x4){cvtpk(a[8 * s], a[8 * s + 1]), cvtpk(a[8 * s + 2], a[8 * s + 3]), cvtpk(a[8 * s + 4], a[8 * s + 5]), cvtpk(a[8 * s + 6], a[8 * s + 7])}; }
DI float dot2(unsigned a, unsigned b, float c) { return __builtin_amdgcn_fdot2_f32_bf16(__builtin_bit_cast(bf16x2, a), __builtin_bit_cast(bf16x2, b), c, false); }
DI void acc_stats2(unsigned u0, unsigned u1, f32x4& s) {
    const float a = bflo(u0), b = bfhi(u0), c = bflo(u1), d = bfhi(u1);
    s[0] += a; s[1] += b; s[2] = __builtin_fmaf(a, a, s[2]); s[3] = __builtin_fmaf(b, b, s[3]); s[0] += c; s[1] += d; s[2] = __builtin_fmaf(c, c, s[2]); s[3] = __builtin_fmaf(d, d, s[3]);
}
DI float sum8(float v) {
    v += u2f(__builtin_amdgcn_update_dpp(0u, f2u(v), 0xB1, 0xf, 0xf, true));
    v += u2f(__builtin_amdgcn_update_dpp(0u, f2u(v), 0x4E, 0xf, 0xf, true));
    v += u2f(__builtin_amdgcn_update_dpp(0u, f2u(v), 0x141, 0xf, 0xf, true));
    return v;
}
DI u32x4 shift8(u32x4 x, float c) {
    u32x4 o; float t0, t1, t2, t3, t4, t5, t6, t7;
    asm("v_dot2_f32_bf16 %4, %12, %16, %18\n\tv_dot2_f32_bf16 %5, %12, %17, %18\n\tv_dot2_f32_bf16 %6, %13, %16, %18\n\tv_dot2_f32_bf16 %7, %13, %17, %18\n\t"
        "v_dot2_f32_bf16 %8, %14, %16, %18\n\tv_dot2_f32_bf16 %9, %14, %17, %18\n\tv_dot2_f32_bf16 %10, %15, %16, %18\n\tv_dot2_f32_bf16 %11, %15, %17, %18\n\t"
        "v_cvt_pk_bf16_f32 %0, %4, %5\n\tv_cvt_pk_bf16_f32 %1, %6, %7\n\tv_cvt_pk_bf16_f32 %2, %8, %9\n\tv_cvt_pk_bf16_f32 %3, %10, %11"
        : "=&v"(o[0]), "=&v"(o[1]), "=&v"(o[2]), "=&v"(o[3]), "=&v"(t0), "=&v"(t1), "=&v"(t2), "=&v"(t3), "=&v"(t4), "=&v"(t5), "=&v"(t6), "=&v"(t7)
        : "v"(x[0]), "v"(x[1]), "v"(x[2]), "v"(x[3]), "s"(0x00003f80u), "s"(0x3f800000u), "v"(c));
    return o;
}
template <int N> struct IC { static constexpr int v = N; };
template <int B, int E, class F> DI void sfor(F&& f) { if constexpr (B < E) { f(IC<B>{}); sfor<B + 1, E>(f); } }
template <int N> DI void waitv() { asm volatile("s_waitcnt vmcnt(%0)" :: "i"(N) : "memory"); }
template <int N> DI void waitv_bar() { if (N == 0) __builtin_amdgcn_s_waitcnt(0x0F70); asm volatile("s_waitcnt vmcnt(%0) lgkmcnt(0)\n\ts_barrier" :: "i"(N) : "memory"); }
template <int N> DI void waitv_barx() { asm volatile("s_waitcnt vmcnt(%0)\n\ts_barrier" :: "i"(N) : "memory"); }
template <int N> DI void waitv_bar2() { asm volatile("s_waitcnt vmcnt(%0) lgkmcnt(2)\n\ts_barrier" :: "i"(N) : "memory"); }
#define BAR() asm volatile("s_waitcnt lgkmcnt(0)\n\ts_barrier" ::: "memory")
#define FENCE() asm volatile("" ::: "memory")
typedef __amdgpu_buffer_rsrc_t rsrc_t;
DI u32x4 ld16_dev(rsrc_t rs, unsigned voff, unsigned soff) { return U4(__builtin_amdgcn_raw_buffer_load_b128(rs, voff, soff, 16)); }
DI u32x2 ld8(rsrc_t rs, unsigned voff, unsigned soff) { return __builtin_bit_cast(u32x2, __builtin_amdgcn_raw_buffer_load_b64(rs, voff, soff, 0)); }
DI u32x2 ld8_dev(rsrc_t rs, unsigned voff, unsigned soff) { return __builtin_bit_cast(u32x2, __builtin_amdgcn_raw_buffer_load_b64(rs, voff, soff, 16)); }
DI void st16_dev(u32x4 v, rsrc_t rs, unsigned voff, unsigned soff) { __builtin_amdgcn_raw_buffer_store_b128(v, rs, voff + soff, 0, 16); }
DI void st8_dev(u32x2 v, rsrc_t rs, unsigned voff, unsigned soff) { __builtin_amdgcn_raw_buffer_store_b64(v, rs, voff, soff, 16); }
DI void set_flag(char* ws, unsigned fo, u64 tagv) {
    asm volatile("s_mov_b64 exec, 1\n\tglobal_store_dwordx2 %0, %1, %2 sc1\n\ts_mov_b64 exec, -1" :: "v"(fo), "v"(tagv), "s"(ws) : "memory");
}
DI u64 peek_flag(char* ws, unsigned fo) { return __hip_atomic_load((GAS u64*)(ws + fo), __ATOMIC_RELAXED, __HIP_MEMORY_SCOPE_AGENT); }
DI void peek_lds(char* ws, unsigned fo, unsigned dst) {
    asm volatile("s_mov_b32 m0, %2\n\ts_nop 0\n\tglobal_load_lds_dword %0, %1 sc1\n\ts_add_u32 m0, %2, 252\n\ts_nop 0\n\tglobal_load_lds_dword %0, %1 offset:4 sc1" :: "v"(fo), "s"(ws), "s"(dst) : "memory", "scc");
}
DI u64 peeked(lds_ptr p) { return (u64)*(LAS unsigned*)(p + 256) << 32 | *(LAS unsigned*)p; }
DI void poll_flag(char* ws, unsigned fo, u64 tag) {
#pragma unroll 1
    for (int spin = 0; spin < (1 << 21); ++spin) {
        if (wave_all(peek_flag(ws, fo) == tag)) break;
        __builtin_amdgcn_s_sleep(1);
    }
    FENCE();
}
#define SB() __builtin_amdgcn_sched_barrier(0)
#define PINS(a) asm volatile("" : "+s"(a))
#define PIN(a) asm volatile("" : "+v"(a))
#define PIN2(a, b) asm volatile("" : "+v"(a), "+v"(b))
DI lds_ptr opq(lds_ptr p) { unsigned u = (unsigned)(uintptr_t)p; asm volatile("" : "+v"(u)); return (lds_ptr)(uintptr_t)u; }
#define TS(i) do {} while (0)

#define CLIN(lo, hi) ((CL0 > (lo) && CL0 <= (hi)) + (CL1 > (lo) && CL1 <= (hi)))
constexpr int GW = 7, XN = 4;
DI constexpr int d_prio(int n) { return n < 0 ? -1 : n < 27 ? 3 - n / 7 : n < 64 ? 3 - (n - 27) / 10 : n < 84 ? 3 - (n - 64) / 5 : n < 112 ? 3 - (n - 84) / 7 : 0; }
DI constexpr int phys(int t) { return ((t % 9) + 2) % 9 * 16384; }

__global__ NOPK __launch_bounds__(512, 2) void block_fwd(const char* x, const char* wqkv, const char* wo, const char* wg, const char* wu, const char* wd, char* out, char* ws) {
    extern __shared__ __attribute__((aligned(16))) char smem[];
    lds_ptr lds = (lds_ptr)smem;
    const int tid = __builtin_amdgcn_workitem_id_x(), lane = tid & 63, w = __builtin_amdgcn_readfirstlane(tid >> 6);
    const int bid = __builtin_amdgcn_workgroup_id_x(), xcd = bid & 7, loc = bid >> 3;
    const int r = lane & 31, hf = lane >> 5, q4 = (lane & 15) >> 2, g1 = (lane >> 4) & 1, p4 = lane & 3;
    const unsigned lds0 = (unsigned)(uintptr_t)smem;
    const unsigned l16 = lane * 16;
    TS(0);
    const int sp3 = (loc + 8) >> 5;
    const int atb = (xcd >> 1) * 8 + (loc & 7) + sp3 * (xcd & 1) * 4, acg = (loc >> 3) + (1 - sp3) * (xcd & 1) * 4;
    const int row8 = lane >> 3;
    const int rot8 = ((loc & 7) << sp3) + sp3 * (xcd & 1), rot = rot8 == 7 ? 0 : rot8;
    if (loc < 28) {
        const unsigned xo = (unsigned)((atb * 64 + (tid >> 3)) * 896 + (((tid ^ (tid >> 4)) & 7) * 16));
        const unsigned xd0 = lds0 + 1024 + (w >> 2) * 28672 + (w & 3) * 1024;
        unsigned xd = xd0 + (7 - rot) * 4096;
        const unsigned w0 = (unsigned)((rot * 64 + (tid >> 3)) * 2688 + acg * 128 + (((tid & 7) ^ ((tid >> 2) & 4)) * 16));
        asm volatile("s_cmp_eq_u32 %0, %3\n\ts_cselect_b32 %0, %2, %0\n\ts_mov_b32 m0, %0\n\ts_add_u32 %0, %0, 0x1000\n\tglobal_load_lds_dwordx4 %1, %4\n\t"
#define XP(o) "s_cmp_eq_u32 %0, %3\n\ts_cselect_b32 %0, %2, %0\n\ts_sub_u32 m0, %0, " #o "\n\ts_add_u32 %0, %0, 0x1000\n\tglobal_load_lds_dwordx4 %1, %4 offset:" #o "\n\t"
                     XP(128) XP(256) XP(384) XP(512) XP(640) XP(768)
                     "s_add_u32 m0, %6, 8192-896\n\ts_barrier\n\tglobal_load_lds_dwordx4 %5, %7 offset:896\n\t"
                     "s_add_u32 m0, %6, 16384-1792\n\ts_nop 0\n\tglobal_load_lds_dwordx4 %5, %7 offset:1792\n\t"
                     "s_mov_b32 m0, %6\n\ts_nop 0\n\tglobal_load_lds_dwordx4 %5, %7"
                     : "+&s"(xd) : "v"(xo), "s"(xd0), "s"(xd0 + 7 * 4096), "s"(x), "v"(w0), "s"(lds0 + 58368 + w * 1024), "s"(wqkv) : "memory", "scc");
    }
    SB();
    const rsrc_t rws = __builtin_amdgcn_make_buffer_rsrc((void*)ws, 0, WS_END, 0x00020000);
    u64 epoch, tagv;
    unsigned fl1;
    const unsigned ones = 0x3f803f80u;
    const bool ffn = loc < 28;
    const int l7 = loc / 7, m7 = loc % 7;
    const int tb = xcd * 4 + l7;
    const int tt = w & 1, ct = (w >> 1) & 1, kh = w >> 2;
    const unsigned swz = (unsigned)(((lane & 7) ^ (((row8 >> 1) & 1) << 2)) * 16);

    if (__builtin_expect(ffn, 1)) {
        constexpr int A_W = 58368, A_WS = 24576;
#define A_KC(c) (rot + (c) >= 7 ? rot + (c) - 7 : rot + (c))
        const unsigned wvo = (unsigned)(row8 * 2688) + swz;
        const char* wsrc = wqkv + acg * 128 + (size_t)w * 8 * 2688;
        const unsigned wdst = lds0 + A_W + w * 1024;
#define A_PIECE(c, i) glds(wvo, wsrc + A_KC(c) * (64 * 2688) + (i) * 896, wdst + ((c) & 3) * A_WS + (i) * 8192)
#define A_GROUP(c) do { A_PIECE(c, 1); A_PIECE(c, 2); A_PIECE(c, 0); } while (0)
        epoch = peek_flag(ws, FL_EPOCH * 128); FENCE(); fl1 = (FL_KV + atb * 8 + acg) * 128;
        lds_ptr xb[4], wb1, wb1h;
#pragma unroll
        for (int j = 0; j < 4; ++j) xb[j] = lds + 1024 + tt * 28672 + (r >> 3) * 1024 + (r & 7) * 128 + (((2 * j + hf) ^ ((r >> 1) & 7)) * 16);
        wb1 = opq(lds + A_W + (kh ? 0 : 8192) + (8 * hf + q4) * 128 + ((ct ^ (q4 >> 1)) * 64) + g1 * 32 + p4 * 8);
        wb1h = opq(wb1 + 2 * A_WS);
        unsigned go1[2], go2[2]; int gi[2];
#pragma unroll
        for (int p = 0; p < 2; ++p) {
            const int G = acg * 8 + ct * 4 + 2 * p + hf, hp = G / 7, i = G - hp * 7;
            const unsigned hs = kh ? 131072u : 262144u;
            const unsigned to = (kh ? WS_QF + (atb * 2 + tt) * 2048 : WS_KV + (atb * 2 + tt) * 4096) + r * 16;
            go1[p] = (unsigned)(2 * hp + (i > 3)) * hs + (unsigned)(i > 3 ? i - 3 : i) * 512 + to;
            go2[p] = i == 3 ? (unsigned)(2 * hp + 1) * hs + to : OOB;
            gi[p] = i;
        }
        const int vcol = acg * 64 + ct * 32 + r, vh = vcol / 28, vd = vcol - vh * 28;
        unsigned vo = WS_KV + (atb * 2 + tt) * 4096 + (unsigned)vh * 262144 + 2048 + hf * 512 + vd * 16;
        PIN2(go1[0], go1[1]); PIN2(go2[0], go2[1]); PIN(vo);
        lds_ptr xw = opq(lds + 1024 + kh * 28672 + (w & 3) * 1024 + l16);
        waitv<4>();
        TS(28);
        u32x4 xv[7];
#pragma unroll
        for (int c = 0; c < 7; ++c) xv[c] = *(LAS u32x4*)(xw + c * 4096);
        SB();
        f32x4 sm = {};
#pragma unroll
        for (int c = 0; c < 7; ++c)
#pragma unroll
            for (int j = 0; j < 4; ++j) sm[j] = dot2(xv[c][j], ones, sm[j]);
        float nm = sum8((sm[0] + sm[1]) + (sm[2] + sm[3])) * (-1.f / D);
        PIN(nm);
#pragma unroll
        for (int c = 0; c < 7; ++c) { xv[c] = shift8(xv[c], nm); *(LAS u32x4*)(xw + c * 4096) = xv[c]; }
        A_GROUP(1);
        SB();
        LAS float* rst = (LAS float*)lds;
        sm = (f32x4){};
#pragma unroll
        for (int c = 0; c < 7; ++c)
#pragma unroll
            for (int j = 0; j < 4; ++j) sm[j] = dot2(xv[c][j], xv[c][j], sm[j]);
        rst[w * 8 + row8] = __builtin_amdgcn_rsqf(sum8((sm[0] + sm[1]) + (sm[2] + sm[3])) * (1.f / D) + EPS);
        A_GROUP(2);
        TS(31);
        bf16x8 fx[4], f1[4], f2[4];
#define A_WAIT(c) do { if ((c) == 0) waitv_bar<6>(); else if ((c) == 1) waitv_barx<6>(); else if ((c) < 6) waitv_barx<3>(); else waitv_barx<0>(); \
                       if ((c) == 0) TS(1); if ((c) == 6) TS(2); } while (0)
#define A_WP(c, ks) ((((c) & 2) ? wb1h : wb1) + ((c) & 1) * A_WS + (ks) * 2048)
        if (w < 4) {
            f32x16 ak = {}, av = {};
            f32x4 rv[4];
            float rs = 0.f;
#pragma unroll
            for (int n = 0; n < 30; ++n) {
                const int c = n >> 2, ks = n & 3, km = (n - 2) & 3;
                if (n < 28 && ks == 0) A_WAIT(c);
                if (n >= 2) { ak = MFMA(f1[km], fx[km], ak); PIN(ak); } SB();
                if (n < 28) { fx[ks] = lds8(xb[ks] + c * 4096); f1[ks] = cat(trd(A_WP(c, ks)), trd(A_WP(c, ks) + 512)); } SB();
                if (n >= 2) { av = MFMA(fx[km], f2[km], av); PIN(av); } SB();
                if (n < 28) f2[ks] = cat(trd(A_WP(c, ks) + 8192), trd(A_WP(c, ks) + 8192 + 512));
                if (c == 0 && ks < 3) A_PIECE(3, ks);
                if (c >= 2 && c < 5 && ks < 3) A_PIECE(c + 2, ks);
                if (n == 21) {
                    rs = rst[tt * 32 + r];
#pragma unroll
                    for (int g = 0; g < 4; ++g) rv[g] = *(LAS f32x4*)(rst + tt * 32 + 8 * g + 4 * hf);
                }
                if (n == 24) { PIN(epoch); tagv = epoch + 1; PIN(tagv); }
                SB();
            }
            TS(3);
#pragma unroll
            for (int i = 0; i < 16; ++i) { ak[i] *= rs; av[i] *= rv[i >> 2][i & 3]; }
#pragma unroll
            for (int p = 0; p < 2; ++p) {
                const u32x4 f = pack8(ak, p);
                const auto s0 = __builtin_amdgcn_permlane32_swap(f[0], f[2], false, false), s1 = __builtin_amdgcn_permlane32_swap(f[1], f[3], false, false);
                const u32x4 c = {s0[0], s1[0], s0[1], s1[1]};
                st16_dev(c, rws, go1[p], 0); st16_dev(c, rws, go2[p], 0);
            }
            st16_dev(pack8(av, 0), rws, vo, 0); st16_dev(pack8(av, 1), rws, vo + 1024, 0);
        } else {
            f32x16 aq = {};
            __builtin_amdgcn_s_setprio(1);
#pragma unroll
            for (int n = 0; n < 30; ++n) {
                const int c = n >> 2, ks = n & 3, km = (n - 2) & 3;
                if (n < 28 && ks == 0) A_WAIT(c);
                if (n == 24) { PIN(epoch); tagv = epoch + 1; PIN(tagv); }
                if (n >= 2) { aq = MFMA(f1[km], fx[km], aq); PIN(aq); } SB();
                if (n < 28) { fx[ks] = lds8(xb[ks] + c * 4096); f1[ks] = cat(trd(A_WP(c, ks)), trd(A_WP(c, ks) + 512)); }
                if (c == 0 && ks < 3) A_PIECE(3, ks);
                if (c >= 2 && c < 5 && ks < 3) A_PIECE(c + 2, ks);
                if (n == 24) {
                    const unsigned pv = vd == 0 ? ones : 0u, po = vd < 4 ? vo + 448 : OOB;
                    st16_dev((u32x4){pv, pv, pv, pv}, rws, po, 0); st16_dev((u32x4){pv, pv, pv, pv}, rws, po + 1024, 0);
                }
                SB();
            }
            __builtin_amdgcn_s_setprio(0);
            const float QS = rst[tt * 32 + r] * (0.18898223650461362f * 1.4426950408889634f);
#pragma unroll
            for (int i = 0; i < 16; ++i) aq[i] *= QS;
#pragma unroll
            for (int p = 0; p < 2; ++p) {
                const u32x4 f = pack8(aq, p);
                const auto s0 = __builtin_amdgcn_permlane32_swap(f[0], f[2], false, false), s1 = __builtin_amdgcn_permlane32_swap(f[1], f[3], false, false);
                const bool sp = gi[p] == 3;
                st16_dev((u32x4){s0[0], s1[0], sp ? 0u : s0[1], sp ? 0u : s1[1]}, rws, go1[p], 0);
                st16_dev((u32x4){0u, 0u, s0[1], s1[1]}, rws, go2[p], 0);
            }
        }
    } else {
        __builtin_amdgcn_s_sleep(16);
        epoch = peek_flag(ws, FL_EPOCH * 128); fl1 = (FL_RT + xcd * 4 + loc - 28) * 128;
        const int ri = xcd * 4 + loc - 28, nu = (w == 0 && ri < 16) ? 4 : 3;
        const unsigned vo = (unsigned)(row8 * 3584 + (lane & 7) * 16);
#pragma unroll 1
        for (int i = 0; i < nu; ++i) {
            const int u = i < 3 ? i * 256 + ri * 8 + w : 768 + ri;
            const size_t so = (size_t)(u / 28) * 16 * 3584 + (u % 28) * 128;
            const unsigned d_ = lds0 + w * 16384 + i * 4096;
            glds(vo, wg + so, d_); glds(vo, wg + so + 8 * 3584, d_ + 1024); glds(vo, wu + so, d_ + 2048); glds(vo, wu + so + 8 * 3584, d_ + 3072);
        }
#pragma unroll 1
        for (int i = 0; i < nu; ++i) {
            const int u = i < 3 ? i * 256 + ri * 8 + w : 768 + ri;
            waitv<8>();
            lds_ptr sg = lds + w * 16384 + i * 4096 + g1 * 2048 + (4 * hf + q4) * 128 + p4 * 8;
#pragma unroll
            for (int e = 0; e < 4; ++e) st16_dev(U4(cat(trd(sg + e * 32), trd(sg + e * 32 + 1024))), rws, l16, WS_WGU + (unsigned)(((u % 28) * 4 + e) * 28 + u / 28) * 1024);
            FENCE();
        }
        tagv = epoch + 1;
    }
    TS(24);
    const int head = xcd * 2 + (loc & 1), rb = loc >> 1, qt = w & 3;
    const char* kvs = ws + WS_KV + (size_t)head * 262144 + w * 2048;
    const char* qs = ws + WS_QF + (size_t)(head * 64 + rb * 4 + qt) * 2048 + kh * 1024;
    unsigned kvd = lds0 + w * 2048, qd = lds0 + 147456 + qt * 2048 + kh * 1024;
    unsigned pf1 = (FL_KV + (lane >> 1) * 8 + (head * 28 + (lane & 1) * 27) / 64) * 128;
    PINS(kvs); PINS(qs); PINS(kvd); PINS(qd); PIN2(pf1, fl1);
    waitv_bar<0>();
    if (w == 0) set_flag(ws, fl1, tagv);
    const u64 tag = ((u64)(unsigned)__builtin_amdgcn_readfirstlane((unsigned)(tagv >> 32)) << 32 | (u64)(unsigned)__builtin_amdgcn_readfirstlane((unsigned)tagv));
    TS(4);

    const int drb = (xcd >> 1) * 4 + l7, dfg = (xcd & 1) * 7 + m7;
    bf16x8 wf[28];
    const unsigned p16 = hf * 1024 + g1 * 512 + (lane & 15) * 16;
    u32x2 xa, xb;
    f32x4 csg = {}, csu = {};
    f32x16 acc = {};
    const u32x4 one4 = {ones, ones, ones, ones};
    const unsigned hi_ = (unsigned)((tb * 2 + tt) * 28 + m7 * 4 + ct * 2 + kh), ofo = (unsigned)(tt * 32768 + kh * 16384 + ct * 4096);
    unsigned ho = WS_HF + hi_ * 1024 + l16, so = hf ? OOB : WS_HS + hi_ * 256 + r * 8;
    const char* os = ws + WS_OF + (size_t)tb * 65536 + ofo;
    unsigned od = lds0 + 65536 + ofo;
    unsigned pf2 = (FL_O + (tb >> 1) * 16 + kh * 8 + ct * 2 + ((lane >> 1) & 1) + (lane & 4)) * 128;
    unsigned go = ffn ? WS_GF + (unsigned)(drb * 4 * 112 + dfg * 8 + w) * 1024 + g1 * 512 + (hf * 16 + (lane & 15)) * 16 : OOB;
    unsigned fo3 = (FL_H + tb * 8 + m7) * 128;
    {
        lds_ptr ap = opq(lds + kh * 8192 + l16), apB = opq(lds + 65536 + kh * 8192 + l16), apC = opq(lds + 131072 + kh * 8192 + l16);
        const unsigned lv = kh * 8192 + (l16 ^ (kh * 256));
        lds_ptr vp = opq(lds + lv), vpB = opq(lds + 65536 + lv), vpC = opq(lds + 131072 + lv);
#define AP(off) ((off) < 65536 ? ap + (off) : (off) < 131072 ? apB + ((off) - 65536) : apC + ((off) - 131072))
#define VP(off) ((off) < 65536 ? vp + (off) : (off) < 131072 ? vpB + ((off) - 65536) : vpC + ((off) - 131072))
#define KV_ISSUE(t) glds2_dev(l16, kvs + (t) * 16384, kvd + phys(t))
        unsigned wgo = ffn ? WS_WGU + (unsigned)(dfg * 8 + w) * 28672 : OOB;
        const int ri = umin(xcd * 4 + loc - 28, 27);
        const char* wos = (ffn ? wo + m7 * 128 : wd + (ri % 7) * 128 + (size_t)(ri / 7) * 448 * 896) + (size_t)w * 56 * 896;
        unsigned wov = (unsigned)(row8 * 896) + swz;
        const rsrc_t rx = __builtin_amdgcn_make_buffer_rsrc((void*)x, 0, 2048 * 448 * 2, 0x00020000);
        unsigned xo_ = (unsigned)(((tb * 2 + tt) * 32 + r) * 896 + m7 * 128 + ct * 64 + kh * 32 + hf * 8);
        unsigned rto = (FL_RT + w * 4 + (lane & 3)) * 128;
        lds_ptr qp = opq(lds + 147456 + qt * 2048 + l16);
        PINS(wgo); PINS(wos); PIN2(wov, xo_); PIN(rto); PIN2(ho, so); PIN2(pf2, fo3); PIN(go);
        lds_ptr xw = opq(lds + 147456 + w * 2048 + l16), xr = opq(lds + 147456 + (w ^ 4) * 2048 + l16);
        lds_ptr lw = opq(lds + 58368 + w * 256 + lane * 4), lr = opq(lds + 58368 + qt * 256 + 128 + r * 4);
        lds_ptr bw = opq(lds + 60416 + w * 4), br = opq(lds + 60416 + (lane & 7) * 4);
        unsigned oo = WS_OF + (unsigned)((rb * 4 + qt) * 32 + head * 2 + kh) * 1024 + l16;
        unsigned fo2 = (FL_O + rb * 16 + head) * 128;
        PIN2(oo, fo2);
        if (w == 1) poll_flag(ws, pf1, tag);
        BAR();
        TS(5);
        glds_dev(l16, qs, qd);
        KV_ISSUE(0); KV_ISSUE(1);
        asm volatile("s_barrier" ::: "memory");
        KV_ISSUE(2); KV_ISSUE(3);
        waitv_bar<4>();
        TS(6);
        const bf16x8 q0 = lds8(qp), q1 = lds8(qp + 1024);
        f32x16 o = {}, sa = {}, sb;
        bf16x8 k0 = lds8(ap + phys(0)), k1 = lds8(ap + phys(0) + 1024), v0 = lds8(vp + phys(0) + 2048), v1 = {};
        u32x4 p0, p1 = {};
        sa = MFMA(k0, q0, sa); sa = MFMA(k1, q1, sa);
        k0 = lds8(ap + phys(0) + 4096); k1 = lds8(ap + phys(0) + 5120);
        PIN(sa); SB();
        u64 rtf = 0;
#define EXP4(S, o4) _Pragma("unroll") for (int i_ = o4; i_ < o4 + 4; ++i_) S[i_] = __builtin_amdgcn_exp2f(S[i_]);
#define WLD(s) wf[s] = BF8(ld16_dev(rws, p16 + ((s) & 1) * 256 + ((s) & 2) * 1024, wgo + ((s) >> 2) * 4096))
#define SIDE(t, g) do { \
            if ((t) == 0 && (g) == 0) KV_ISSUE(4); if ((t) == 0 && (g) == 2) KV_ISSUE(5); if ((t) == 1 && (g) == 0) KV_ISSUE(6); if ((t) == 1 && (g) == 2) KV_ISSUE(7); \
            if ((t) == 2 && (g) == 0) KV_ISSUE(8); \
            if ((t) == 1 && (g) == 1) peek_lds(ws, rto, lds0 + 155648 + w * 512); if ((t) == 2 && (g) == 2) { waitv<4 + CLIN(4, 9)>(); rtf = peeked(lds + 155648 + w * 512 + lane * 4); } \
            if ((t) == 3 && (g) < 3) KV_ISSUE(9 + (g)); \
            if ((t) * 4 + (g) == CL0 || (t) * 4 + (g) == CL1) st16_dev((u32x4){~0u, ~0u, ~0u, ~0u}, rws, go + ((t) * 4 + (g) == CL0 ? 2 : 3) * 114688, 0); \
            if ((t) == 7 && (g) == 0) KV_ISSUE(12); if ((t) == 7 && (g) == 2) KV_ISSUE(13); if ((t) == 8 && (g) == 0) KV_ISSUE(14); if ((t) == 8 && (g) == 2) KV_ISSUE(15); \
            if ((t) >= 4 && (t) <= 6) WLD(((t) - 4) * 4 + (g)); \
            if (((t) == 7 || (t) == 8) && ((g) & 1)) WLD(12 + ((t) - 7) * 2 + ((g) >> 1)); \
            if ((t) >= 9 && (t) <= 11) WLD(((t) - 5) * 4 + (g)); \
            if ((t) == 12 || ((t) == 13 && (g) < 3)) glds(wov, wos + (((t) - 12) * 4 + (g)) * 8 * 896, lds0 + (w * 7 + ((t) - 12) * 4 + (g)) * 1024); \
            if ((t) == 13 && (g) == 3) { xa = ld8(rx, xo_, 0); xb = ld8(rx, xo_, 16); } \
            if ((t) == 14 && (g) == 0) { if (tid < 32) *(LAS u32x4*)(lds + 57344 + tid * 16) = (u32x4){0, 0, 0, 0}; } \
        } while (0)
#define TOFF(i) (phys((i) >> 1) + ((i) & 1) * 4096)
#define TILE(i, SC, SN) do { \
            __builtin_amdgcn_s_setprio(((i) & 1) ? 1 : 3); \
            if ((i) < 31) { f32x16 z_ = {}; SN = MFMA(k0, q0, z_); PIN(SN); } if ((i) < 30) k0 = lds8(AP(TOFF((i) + 2))); SB(); \
            EXP4(SC, 0); PIN(SC); SB(); \
            o = MFMA(v1, BF8(p1), o); PIN(o); v1 = lds8(VP(TOFF(i) + 3072)); SIDE((i) >> 1, ((i) & 1) * 2); SB(); \
            EXP4(SC, 4); p0 = pack8(SC, 0); PIN(p0); SB(); \
            __builtin_amdgcn_s_setprio(((i) & 1) ? 0 : 2); \
            if ((i) < 31) { SN = MFMA(k1, q1, SN); PIN(SN); } if ((i) < 30) k1 = lds8(AP(TOFF((i) + 2) + 1024)); SB(); \
            EXP4(SC, 8); PIN(SC); SB(); \
            o = MFMA(v0, BF8(p0), o); PIN(o); if ((i) < 31) v0 = lds8(VP(TOFF((i) + 1) + 2048)); SIDE((i) >> 1, ((i) & 1) * 2 + 1); SB(); \
            EXP4(SC, 12); p1 = pack8(SC, 1); PIN(p1); SB(); } while (0)
#define STEP(t) do { TILE(2 * (t), sa, sb); TILE(2 * (t) + 1, sb, sa); } while (0)
        STEP(0);
        waitv_barx<4 + CLIN(-1, 3)>();
        STEP(1); STEP(2);
        if (__builtin_expect(!wave_all(rtf == tag), 0)) poll_flag(ws, rto, tag);
        waitv_bar2<2 + CLIN(5, 11)>();
        TS(21);
        STEP(3); STEP(4); STEP(5); STEP(6);
        waitv_bar2<12 + CLIN(13, 27)>();
        TS(22);
        STEP(7); STEP(8); STEP(9); STEP(10);
        waitv_bar2<9 + CLIN(34, 43)>();
        TS(23);
        STEP(11); STEP(12); STEP(13); STEP(14); STEP(15);
        o = MFMA(v1, BF8(p1), o);
        TS(7);
        u32x4 ov;
        {
            *(LAS f32x4*)xw = (f32x4){o[8], o[9], o[10], o[11]}; *(LAS f32x4*)(xw + 1024) = (f32x4){o[12], o[13], o[14], o[15]};
            const float l = kh ? o[4] : o[12];
            *(LAS float*)lw = l;
#define AMX(a, b, c) __builtin_fmaxf(__builtin_fmaxf(__builtin_fabsf(a), __builtin_fabsf(b)), __builtin_fabsf(c))
            const float mx_ = AMX(AMX(AMX(o[0], o[1], o[2]), AMX(o[3], o[4], o[5]), AMX(o[6], o[7], o[8])), AMX(AMX(o[9], o[10], o[11]), AMX(o[12], o[13], o[14]), o[15]), 0.f);
            const bool ok = (mx_ < 0x1p100f) & ((hf ? l : 1.f) > 0x1p-40f);
            *(LAS unsigned*)bw = wave_all(ok) ? 0u : 1u;
            BAR();
            const f32x4 a = *(LAS f32x4*)xr, c = *(LAS f32x4*)(xr + 1024);
            const float inv = __builtin_amdgcn_rcpf(*(LAS float*)lr + *(LAS float*)(lr + 1024));
            const unsigned bad = *(LAS unsigned*)br;
            const float iz = (kh && hf) ? 0.f : inv;
            ov = (u32x4){cvtpk((o[0] + a[0]) * inv, (o[1] + a[1]) * inv), cvtpk((o[2] + a[2]) * inv, (o[3] + a[3]) * inv),
                         cvtpk((o[4] + c[0]) * iz, (o[5] + c[1]) * iz), cvtpk((o[6] + c[2]) * iz, (o[7] + c[3]) * iz)};
            if (__builtin_expect(wave_any(bad != 0), 0)) {
                f32x16 of = {};
                float m = -0x1p126f;
#pragma unroll 1
                for (int t = 0; t < 16; ++t) {
                    BAR();
                    glds2_dev(l16, kvs + t * 16384, lds0 + 65536 + w * 2048);
                    waitv_bar<0>();
#pragma unroll 1
                    for (int u = 0; u < 2; ++u) {
                        f32x16 s = {};
                        s = MFMA(lds8(apB + u * 4096), q0, s); s = MFMA(lds8(apB + u * 4096 + 1024), q1, s);
                        float mx = s[0];
#pragma unroll
                        for (int i = 1; i < 16; ++i) mx = __builtin_fmaxf(mx, s[i]);
                        mx = __builtin_fmaxf(swap_max(mx), m);
                        const float alpha = __builtin_amdgcn_exp2f(m - mx);
                        m = mx;
#pragma unroll
                        for (int i = 0; i < 16; ++i) { s[i] = __builtin_amdgcn_exp2f(s[i] - mx); of[i] *= alpha; }
                        of = MFMA(lds8(vpB + u * 4096 + 2048), BF8(pack8(s, 0)), of);
                        of = MFMA(lds8(vpB + u * 4096 + 3072), BF8(pack8(s, 1)), of);
                    }
                }
                *(LAS f32x4*)xw = (f32x4){of[8], of[9], of[10], of[11]}; *(LAS f32x4*)(xw + 1024) = (f32x4){of[12], of[13], of[14], of[15]};
                *(LAS float*)lw = hf ? (kh ? of[4] : of[12]) : m;
                BAR();
                const f32x4 a2 = *(LAS f32x4*)xr, c2 = *(LAS f32x4*)(xr + 1024);
                const float m0 = *(LAS float*)(lr - 128), m1 = *(LAS float*)(lr + 1024 - 128), mm = __builtin_fmaxf(m0, m1);
                const float e0 = __builtin_amdgcn_exp2f(m0 - mm), e1 = __builtin_amdgcn_exp2f(m1 - mm);
                const float iv = __builtin_amdgcn_rcpf(*(LAS float*)lr * e0 + *(LAS float*)(lr + 1024) * e1);
                const float im = (kh ? e1 : e0) * iv, io = (kh ? e0 : e1) * iv;
                const bool z = kh && hf;
                ov = (u32x4){cvtpk(of[0] * im + a2[0] * io, of[1] * im + a2[1] * io), cvtpk(of[2] * im + a2[2] * io, of[3] * im + a2[3] * io),
                             z ? 0u : cvtpk(of[4] * im + c2[0] * io, of[5] * im + c2[1] * io), z ? 0u : cvtpk(of[6] * im + c2[2] * io, of[7] * im + c2[3] * io)};
            }
        }
        st16_dev(ov, rws, oo, 0);
        TS(25);
        FENCE();
        PIN2(xa, xb);
#pragma unroll
        for (int s = 27; s >= 0; --s) PIN(wf[s]);
#pragma unroll
        for (int s = 0; s < 16; s += 2) { csg = MFMA16(wf[s], BF8(one4), csg); csu = MFMA16(wf[s + 1], BF8(one4), csu); }
        acc[0] = bflo(xa[0]); acc[1] = bfhi(xa[0]); acc[2] = bflo(xa[1]); acc[3] = bfhi(xa[1]); acc[4] = bflo(xb[0]); acc[5] = bfhi(xb[0]); acc[6] = bflo(xb[1]); acc[7] = bfhi(xb[1]);
        PIN(acc); PIN2(ho, so); PIN2(pf2, fo3); PINS(os); PINS(od); PIN2(csg, csu);
        waitv_bar<0>();
        if (w == 0) set_flag(ws, fo2, tagv);
        TS(8);
    }
    if (__builtin_expect(!ffn, 0)) {
        const int ri = xcd * 4 + loc - 28;
        if (ri < 28) {
#pragma unroll 1
            for (int u = w; u < 28; u += 8) {
                lds_ptr sd = lds + u * 2048 + (4 * hf + q4) * 128 + g1 * 32 + p4 * 8;
#pragma unroll
                for (int e = 0; e < 2; ++e) { lds_ptr p_ = sd + ((e ^ (q4 >> 1)) * 64);
                    st16_dev(U4(cat(trd(p_), trd(p_ + 1024))), rws, l16, WS_WD + (unsigned)(((ri % 7) * 112 + (ri / 7) * 28 + u) * 2 + e) * 1024); }
            }
        }
        waitv_bar<0>();
        if (w == 0) set_flag(ws, (FL_RD + ri) * 128, tagv);
        return;
    }

    const int erb = tb >> 1;
    const char* hs = ws + WS_HF + (size_t)drb * 114688 + w * 1024;
    const char* hss = ws + WS_HS + (size_t)((drb * 4 + (w & 3)) * 28 + kh * 14) * 256;
    unsigned pf3, fo4 = (FL_G + drb * 64 + dfg) * 128;
    {
        lds_ptr wb = lds + kh * (8 * 28 * 128) + (4 * hf + q4) * 128 + ((ct ^ (q4 >> 1)) * 64) + (g1 ^ kh) * 32 + p4 * 8;
        bf16x8 wr[16];
#pragma unroll
        for (int s = 0; s < 12; ++s) { const int k0 = ((s >> 1) * 28 + (s & 1) * 16) * 128; wr[s] = cat(trd(wb + k0), trd(wb + k0 + 1024)); }
        lds_ptr cw = opq(lds + 131072 + w * 2048 + l16), cr = opq(lds + 131072 + (w ^ 4) * 2048 + l16);
        lds_ptr ob = opq(lds + 65536 + tt * 32768 + kh * 16384 + l16);
        pf3 = (FL_H + drb * 16 + (lane & 8) + umin(lane & 7, 6)) * 128;
        PIN2(pf3, fo4); PINS(hs); PINS(hss);
        poll_flag(ws, pf2, tag);
        TS(9);
        glds4_dev(l16, os, od);
        asm volatile("s_barrier" ::: "memory");
        glds4_dev(l16, os + 8192, od + 8192);
        bf16x8 of[8];
        waitv_bar<4>();
        TS(26);
#pragma unroll
        for (int s = 0; s < 8; ++s) of[s] = lds8(ob + s * 1024);
        SB();
#pragma unroll
        for (int s = 0; s < 8; ++s) {
            acc = MFMA(wr[s], of[s], acc); PIN(acc); SB();
            if (s >= 2 && s < 6) { const int k0 = (((s + 10) >> 1) * 28 + (s & 1) * 16) * 128; wr[s + 10] = cat(trd(wb + k0), trd(wb + k0 + 1024)); SB(); }
        }
        waitv_barx<0>();
#pragma unroll
        for (int s = 0; s < 8; ++s) of[s] = lds8(ob + (8 + s) * 1024);
        SB();
#pragma unroll
        for (int s = 0; s < 8; ++s) acc = MFMA(wr[8 + s], of[s], acc);
        TS(10);
        *(LAS f32x4*)cw = (f32x4){acc[8], acc[9], acc[10], acc[11]}; *(LAS f32x4*)(cw + 1024) = (f32x4){acc[12], acc[13], acc[14], acc[15]};
        BAR();
        const f32x4 a = *(LAS f32x4*)cr + (f32x4){acc[0], acc[1], acc[2], acc[3]}, b = *(LAS f32x4*)(cr + 1024) + (f32x4){acc[4], acc[5], acc[6], acc[7]};
        const u32x4 hv = {cvtpk(a[0], a[1]), cvtpk(a[2], a[3]), cvtpk(b[0], b[1]), cvtpk(b[2], b[3])};
        st16_dev(hv, rws, ho, 0);
        FENCE(); SB();
        float s1 = 0.f, s2 = 0.f;
        { f32x4 q4_ = {}; acc_stats2(hv[0], hv[1], q4_); acc_stats2(hv[2], hv[3], q4_); s1 = swap_add(q4_[0] + q4_[1]); s2 = swap_add(q4_[2] + q4_[3]); }
        st8_dev((u32x2){f2u(s1), f2u(s2)}, rws, so, 0);
        FENCE(); PIN2(csg, csu); SB();
#pragma unroll
        for (int s = 16; s < 28; s += 2) { csg = MFMA16(wf[s], BF8(one4), csg); csu = MFMA16(wf[s + 1], BF8(one4), csu); }
        PIN2(csg, csu);
        waitv_bar<0>();
        if (w == 0) set_flag(ws, fo3, tagv);
        if (bid == 0 && w == 0) set_flag(ws, FL_EPOCH * 128, tagv);
        TS(11);
    }

    const unsigned wdo = WS_WD + (unsigned)(m7 * 112 + w * 14) * 2048;
    bf16x8 wdf[28];
    bf16x8 gf[GW];
    const unsigned gxo = WS_GF + (unsigned)(erb * 4 * 112 + w * 14) * 1024 + (tb & 1) * 256;
    const unsigned pwd = hf * 2048 + g1 * 512 + (lane & 15) * 16;
#define G_LD(s) gf[(s) % GW] = BF8(ld16_dev(rws, p16, gxo + ((s) / 7) * 114688 + ((s) % 7) * 2048))
    u64 gfl = 0;
    const unsigned gfi = (FL_G + (erb * 4 + g1) * 16 + ((w * 14 + umin(lane & 15, 13)) >> 3)) * 128;
    {
        const unsigned hd = lds0 + w * 1024;
        const char* wds = ws + wdo;
        lds_ptr hb = opq(lds + p16), hb2 = opq(lds + 57344 + p16);
        lds_ptr wl = opq(lds + 114688 + w * 5120 + l16), wl2 = opq(lds + w * 7168 + l16);
        LAS f32x2* stp = (LAS f32x2*)opq(lds + 159744 + (lane & 15) * 8);
        if (w == 1) poll_flag(ws, pf3, tag);
        BAR();
        TS(12);
#define H_PIECE(j) glds_dev(l16, hs + (j) * 8192, hd + (j) * 8192)
#define WDO(s) (((s) >> 2) * 4096 + (((s) >> 1) & 1) * 1024 + ((s) & 1) * 256)
#define WDLD(s) wdf[s] = BF8(ld16_dev(rws, pwd, wdo + WDO(s)))
#define HB(n) (((n) < 56 ? hb + ((n) & ~1) * 1024 : hb2 + (((n) & ~1) - 56) * 1024) + ((n) & 1) * 256)
        H_PIECE(0);
        asm volatile("s_barrier" ::: "memory");
        H_PIECE(1); H_PIECE(2); H_PIECE(3);
        constexpr int PF = 5;
        bf16x8 bf[PF + 1];
        f32x4 ag[2][2], au[2][2];
        f32x2 sp[7], sv[4];
        float mean[2] = {0.f, 0.f}, r2[2] = {0.f, 0.f}, hp = 0.f;
        u32x4 hv;
        lds_ptr pk = opq(lds + 155648 + w * 512 + lane * 4);
        u64 pv = 0;
        const unsigned pkd = lds0 + 155648 + w * 512;
        const unsigned rdo = (FL_RD + (w >> 1) * 7 + m7) * 128;
        waitv_barx<3>();
        TS(27);
#pragma unroll
        for (int n = 0; n < PF; ++n) bf[n] = lds8(HB(n));
        SB();
        sfor<0, 125>([&](auto N_) __attribute__((always_inline)) {
            constexpr int n = decltype(N_)::v, p = n / 28, s = n % 28;
            if constexpr (n == 3) waitv_barx<3>();
            if constexpr (n == 11) waitv_barx<7>();
            if constexpr (n == 27) { waitv_barx<3>(); TS(13); }
            if constexpr (n == 64) { waitv_barx<12>(); if (w == 0) set_flag(ws, fo4, tagv); TS(14); }
            if constexpr (n == 85) { waitv_barx<9>(); if (w == 0) set_flag(ws, fo4 + 2048, tagv); }
            if constexpr (n == 38) { if (__builtin_expect(!wave_all(pv == tag), 0)) poll_flag(ws, rdo, tag); FENCE(); }
            if constexpr (d_prio(n) != d_prio(n - 1)) __builtin_amdgcn_s_setprio(d_prio(n));
            if constexpr (n < 112) {
                if constexpr (s < 2) { ag[p & 1][s] = (f32x4){}; au[p & 1][s] = (f32x4){}; }
                ag[p & 1][s & 1] = MFMA16(wf[s & ~1], bf[n % (PF + 1)], ag[p & 1][s & 1]); au[p & 1][s & 1] = MFMA16(wf[s | 1], bf[n % (PF + 1)], au[p & 1][s & 1]);
                PIN2(ag[p & 1][s & 1], au[p & 1][s & 1]);
            }
            SB();
            if constexpr (n + PF < 112) bf[(n + PF) % (PF + 1)] = lds8(HB(n + PF));
            if constexpr (n % 3 == 2 && n < 30) H_PIECE(4 + n / 3);
            if constexpr (n == 4) glds4_dev(l16, hss, lds0 + 114688 + w * 5120);
            if constexpr (n == 16) { waitv<4>();
#pragma unroll
                for (int j = 0; j < 7; ++j) sp[j] = *(LAS f32x2*)(lds + 114688 + w * 5120 + (hf * 7 + j) * 256 + r * 8);
            }
            if constexpr (n == 20) { const f32x2 t = ((sp[0] + sp[1]) + (sp[2] + sp[3])) + ((sp[4] + sp[5]) + sp[6]); stp[w * 32 + g1 * 16] = (f32x2){swap_add(t[0]), swap_add(t[1])}; }
            if constexpr (n >= 39 && n < 48 && n % 2 == 1) glds_dev(pwd, wds + WDO((n - 39) / 2), lds0 + 114688 + w * 5120 + (n - 39) / 2 * 1024);
            if constexpr (n >= 48 && n < 64 && n % 2 == 0) WDLD(12 + (n - 48) / 2);
            if constexpr (n >= 70 && n < 84 && n % 2 == 0) glds_dev(pwd, wds + WDO(5 + (n - 70) / 2), lds0 + w * 7168 + (n - 70) / 2 * 1024);
            if constexpr (n >= 86 && n < 102 && n % 2 == 0) WDLD(20 + (n - 86) / 2);
            if constexpr (n == 84) peek_lds(ws, gfi - g1 * 2048, pkd);
            if constexpr (n == 95) { waitv<5>(); pv = peeked(pk); }
            if constexpr (n == 99) { if (__builtin_expect(!wave_all(pv == tag), 0)) poll_flag(ws, gfi - g1 * 2048, tag); FENCE(); }
            if constexpr (n >= 102 && n < 102 + 2 * XN && n % 2 == 0) G_LD((n - 102) / 2);
            if constexpr (n == 111) gfl = peek_flag(ws, gfi);
            if constexpr (n == 32) pv = peeked(pk);
            if constexpr (n == 18) peek_lds(ws, rdo, pkd);
            if constexpr (p >= 1) {
                constexpr int e = (p - 1) & 1, tp = n == 100 ? 3 : p - 1;
                if constexpr ((s == 0 && p < 4) || n == 100) { sv[0] = stp[tp * 32]; sv[1] = stp[(tp + 4) * 32]; sv[2] = stp[tp * 32 + 16]; sv[3] = stp[(tp + 4) * 32 + 16]; }
                if constexpr (((s == 2 || s == 3) && p < 4) || n == 104 || n == 105) { constexpr int k = n & 1; const f32x2 t = sv[2 * k] + sv[2 * k + 1];
                    mean[k] = t[0] * (1.f / D); r2[k] = __builtin_amdgcn_rcpf(__builtin_fmaxf(t[1] * (1.f / D) - mean[k] * mean[k], 0.f) + EPS); PIN2(mean[k], r2[k]); }
                if constexpr (s >= 4 && s < 12) { constexpr int i = s - 4, k = i >> 2;
                    const float h0 = __builtin_fmaxf(ag[e][k][i & 3] - mean[k] * csg[i & 3], 0.f) * r2[k] * (au[e][k][i & 3] - mean[k] * csu[i & 3]);
                    if constexpr (i & 1) { hv[i >> 1] = p >= 3 ? umin(cvtpk(hp, h0), ~1u) : cvtpk(hp, h0); PIN(hv); } else { hp = h0; PIN(hp); } }
                if constexpr (s == 12) {
                    const auto s0 = __builtin_amdgcn_permlane32_swap(hv[0], hv[2], false, false), s1 = __builtin_amdgcn_permlane32_swap(hv[1], hv[3], false, false);
                    st16_dev((u32x4){s0[0], s1[0], s0[1], s1[1]}, rws, go + (p - 1) * 114688, 0); FENCE();
                }
            }
            SB();
        });
        TS(15);
#pragma unroll
        for (int s = XN; s < 7; ++s) { G_LD(s); SB(); }
        FENCE();
#pragma unroll
        for (int s = 0; s < 12; ++s) wdf[s] = lds8(s < 5 ? wl + s * 1024 : wl2 + (s - 5) * 1024);
    }

    {
        const int etok = tid >> 4, eblk = tid & 15;
        const int et = erb * 4 + (etok >> 4), er = (tb & 1) * 16 + (etok & 15);
        const unsigned hro = (unsigned)((((eblk >> 2) & 3) * 64 + (eblk & 1) * 32 + er) * 16 + ((eblk >> 1) & 1) * 8) + WS_HF + (unsigned)(et * 28 + m7 * 4) * 1024;
        u32x2 hres[2];
        GAS u64* op = (GAS u64*)(out + (size_t)(et * 32 + er) * 896 + m7 * 128 + eblk * 8);
        lds_ptr rp = opq(lds + w * 8448 + (lane >> 4) * 528 + (lane & 15) * 16), rq = opq(lds + eblk * 528 + etok * 16);
        lds_ptr rp2 = opq(rp + 67584), rq2 = opq(rq + 67584);
        f32x4 c[4];
#define G_STEP(s) do { _Pragma("unroll") for (int j = 0; j < 4; ++j) { if ((s) % 7 == 0) c[j] = (f32x4){}; c[j] = MFMA16(wdf[4 * ((s) % 7) + j], gf[(s) % GW], c[j]); } PIN2(c[0], c[1]); PIN2(c[2], c[3]); } while (0)
#define G_PUT(q) do { _Pragma("unroll") for (int j = 0; j < 4; ++j) *(LAS f32x4*)(((q) < 2 ? rp : rp2) + j * 2112 + ((q) & 1) * 256) = c[j]; } while (0)
#define G_OUT(hh, RQ) do { f32x4 pa[8]; \
            _Pragma("unroll") for (int ww = 0; ww < 8; ++ww) pa[ww] = *(LAS f32x4*)(RQ + ww * 8448); SB(); \
            const f32x4 a = ((pa[0] + pa[1]) + (pa[2] + pa[3])) + ((pa[4] + pa[5]) + (pa[6] + pa[7])); \
            const u32x2 ov = {cvtpk(a[0] + bflo(hres[hh][0]), a[1] + bfhi(hres[hh][0])), cvtpk(a[2] + bflo(hres[hh][1]), a[3] + bfhi(hres[hh][1]))}; \
            __hip_atomic_store(op + (hh) * (64 * 896 / 8), __builtin_bit_cast(u64, ov), __ATOMIC_RELAXED, __HIP_MEMORY_SCOPE_AGENT); } while (0)
        TS(16);
        unsigned seen = 0;
#pragma unroll
        for (int s = 0; s < 28; ++s) {
            if (s == 6) BAR();
            if (s == 7 || s == 14 || s == 21) G_PUT(s / 7 - 1);
            if (s == 14) TS(17);
            if (s == 21) { waitv<7>(); PIN2(hres[0], hres[1]); BAR(); G_OUT(0, rq); FENCE(); TS(18); }
            SB();
            G_STEP(s);
            if (s >= 14) { const u32x4 u = U4(gf[s % GW]); seen = umax3(umax3(seen, u[0], u[1]), u[2], u[3]); PIN(seen); }
            SB();
            if (s == 0) { if (__builtin_expect(!wave_all(gfl == tag), 0)) poll_flag(ws, gfi, tag); FENCE(); }
            if (s + GW < 28) G_LD(s + GW);
            if (s == 1) { hres[0] = ld8_dev(rws, hro, 0); hres[1] = ld8_dev(rws, hro, 57344); FENCE(); }
        }
        if (w == 0) { set_flag(ws, fo4 + 4096, tagv); set_flag(ws, fo4 + 6144, tagv); }
        if (__builtin_expect(wave_any(seen == ~0u), 0)) {
            poll_flag(ws, gfi + 4096, tag);
#pragma unroll
            for (int s = 14; s < 14 + GW; ++s) G_LD(s);
#pragma unroll
            for (int s = 14; s < 28; ++s) { if (s == 21) G_PUT(2); G_STEP(s); SB(); if (s + GW < 28) G_LD(s + GW); SB(); }
        }
        G_PUT(3);
        TS(19);
        BAR();
        G_OUT(1, rq2);
    }
    TS(20);
}

extern "C" void kernel_launch(void* const* d_in, const int* in_sizes, int n_in, void* d_out, int out_size, void* d_ws, size_t ws_size, hipStream_t stream) {
    (void)hipFuncSetAttribute((const void*)block_fwd, hipFuncAttributeMaxDynamicSharedMemorySize, LDS_BYTES);
    block_fwd<<<256, 512, LDS_BYTES, stream>>>((const char*)d_in[0], (const char*)d_in[1], (const char*)d_in[2], (const char*)d_in[4], (const char*)d_in[5], (const char*)d_in[6], (char*)d_out, (char*)d_ws);
}
```

```cpp
#include <hip/hip_runtime.h>
#include <hip/hip_bf16.h>
#include <stdint.h>

typedef __attribute__((ext_vector_type(8))) short bf16x8;
typedef __attribute__((ext_vector_type(4))) short s16x4;
typedef __attribute__((ext_vector_type(2))) __bf16 bf16x2;
typedef __attribute__((ext_vector_type(16))) float f32x16;
typedef __attribute__((ext_vector_type(4))) float f32x4;
typedef __attribute__((ext_vector_type(2))) float f32x2;
typedef __attribute__((ext_vector_type(4))) unsigned u32x4;
typedef __attribute__((ext_vector_type(2))) unsigned u32x2;
typedef short v4i16_t __attribute__((ext_vector_type(4)));
typedef unsigned long long u64;
#define LAS __attribute__((address_space(3)))
#define GAS __attribute__((address_space(1)))
typedef LAS char* lds_ptr;
#if defined(__HIP_DEVICE_COMPILE__)
#define NOPK __attribute__((target("no-packed-fp32-ops")))
#else
#define NOPK
#endif
#define DI __device__ __forceinline__
#define MFMA(a, b, c) __builtin_amdgcn_mfma_f32_32x32x16_bf16(a, b, c, 0, 0, 0)
#define MFMA16(a, b, c) __builtin_amdgcn_mfma_f32_16x16x32_bf16(a, b, c, 0, 0, 0)
#define BF8(x) __builtin_bit_cast(bf16x8, x)
#define U4(x) __builtin_bit_cast(u32x4, x)

constexpr int D = 448;
constexpr float EPS = 1e-5f;
constexpr unsigned FL_EPOCH = 0, FL_KV = 1, FL_RT = FL_KV + 256, FL_RD = FL_RT + 32, FL_O = FL_RD + 32, FL_H = FL_O + 256, FL_G = FL_H + 256;
constexpr unsigned WS_KV = 1u << 20;
constexpr unsigned WS_QF = WS_KV + 4194304;
constexpr unsigned WS_OF = WS_QF + 2097152;
constexpr unsigned WS_HF = WS_OF + 2097152;
constexpr unsigned WS_GF = WS_HF + 64 * 28 * 1024;
constexpr unsigned WS_WGU = WS_GF + 64 * 112 * 1024;
constexpr unsigned WS_WD = WS_WGU + 112 * 28 * 1024;
constexpr unsigned WS_HS = WS_WD + 7 * 112 * 2 * 1024;
constexpr unsigned WS_END = WS_HS + 64 * 28 * 256 + 1024;
constexpr unsigned OOB = 0x40000000u;
constexpr int LDS_BYTES = 163840;

DI float u2f(unsigned u) { return __builtin_bit_cast(float, u); }
DI unsigned f2u(float f) { return __builtin_bit_cast(unsigned, f); }
DI unsigned umin(unsigned a, unsigned b) { return a < b ? a : b; }
DI unsigned umax3(unsigned a, unsigned b, unsigned c) { const unsigned m = a > b ? a : b; return m > c ? m : c; }
DI bool wave_any(bool p) { return __builtin_amdgcn_ballot_w64(p) != 0; }
DI bool wave_all(bool p) { return __builtin_amdgcn_ballot_w64(!p) == 0; }
DI unsigned cvtpk(float lo, float hi) { return __builtin_bit_cast(unsigned, __builtin_convertvector((f32x2){lo, hi}, bf16x2)); }
DI float bflo(unsigned u) { return u2f(u << 16); }
DI float bfhi(unsigned u) { return u2f(u & 0xffff0000u); }
DI void glds(unsigned voff, const void* sbase, unsigned dst) {
    asm volatile("s_mov_b32 m0, %2\n\ts_nop 0\n\tglobal_load_lds_dwordx4 %0, %1" :: "v"(voff), "s"(sbase), "s"(dst) : "memory");
}
DI void glds_dev(unsigned voff, const void* sbase, unsigned dst) {
    asm volatile("s_mov_b32 m0, %2\n\ts_nop 0\n\tglobal_load_lds_dwordx4 %0, %1 sc1" :: "v"(voff), "s"(sbase), "s"(dst) : "memory");
}
DI void glds2_dev(unsigned voff, const void* sbase, unsigned dst) {
    asm volatile("s_mov_b32 m0, %2\n\ts_nop 0\n\tglobal_load_lds_dwordx4 %0, %1 sc1\n\tglobal_load_lds_dwordx4 %0, %1 offset:1024 sc1" :: "v"(voff), "s"(sbase), "s"(dst) : "memory");
}
DI void glds4_dev(unsigned voff, const void* sbase, unsigned dst) {
    asm volatile("s_mov_b32 m0, %2\n\ts_nop 0\n\tglobal_load_lds_dwordx4 %0, %1 sc1\n\tglobal_load_lds_dwordx4 %0, %1 offset:1024 sc1\n\t"
                 "global_load_lds_dwordx4 %0, %1 offset:2048 sc1\n\tglobal_load_lds_dwordx4 %0, %1 offset:3072 sc1" :: "v"(voff), "s"(sbase), "s"(dst) : "memory");
}
DI s16x4 trd(lds_ptr p) { return __builtin_bit_cast(s16x4, __builtin_amdgcn_ds_read_tr16_b64_v4i16((LAS v4i16_t*)p)); }
DI bf16x8 cat(s16x4 a, s16x4 b) { return __builtin_shufflevector(a, b, 0, 1, 2, 3, 4, 5, 6, 7); }
DI bf16x8 lds8(lds_ptr p) { return *(LAS bf16x8*)p; }
DI float swap_add(float v) { auto r = __builtin_amdgcn_permlane32_swap(f2u(v), f2u(v), false, false); return u2f(r[0]) + u2f(r[1]); }
DI float swap_max(float v) { auto r = __builtin_amdgcn_permlane32_swap(f2u(v), f2u(v), false, false); return __builtin_fmaxf(u2f(r[0]), u2f(r[1])); }
DI u32x4 pack8(const f32x16& a, int s) { return (u32x4){cvtpk(a[8 * s], a[8 * s + 1]), cvtpk(a[8 * s + 2], a[8 * s + 3]), cvtpk(a[8 * s + 4], a[8 * s + 5]), cvtpk(a[8 * s + 6], a[8 * s + 7])}; }
DI float dot2(unsigned a, unsigned b, float c) { return __builtin_amdgcn_fdot2_f32_bf16(__builtin_bit_cast(bf16x2, a), __builtin_bit_cast(bf16x2, b), c, false); }
DI void acc_stats2(unsigned u0, unsigned u1, f32x4& s) {
    const float a = bflo(u0), b = bfhi(u0), c = bflo(u1), d = bfhi(u1);
    s[0] += a; s[1] += b; s[2] = __builtin_fmaf(a, a, s[2]); s[3] = __builtin_fmaf(b, b, s[3]); s[0] += c; s[1] += d; s[2] = __builtin_fmaf(c, c, s[2]); s[3] = __builtin_fmaf(d, d, s[3]);
}
DI float sum8(float v) {
    v += u2f(__builtin_amdgcn_update_dpp(0u, f2u(v), 0xB1, 0xf, 0xf, true));
    v += u2f(__builtin_amdgcn_update_dpp(0u, f2u(v), 0x4E, 0xf, 0xf, true));
    v += u2f(__builtin_amdgcn_update_dpp(0u, f2u(v), 0x141, 0xf, 0xf, true));
    return v;
}
DI u32x4 shift8(u32x4 x, float c) {
    u32x4 o; float t0, t1, t2, t3, t4, t5, t6, t7;
    asm("v_dot2_f32_bf16 %4, %12, %16, %18\n\tv_dot2_f32_bf16 %5, %12, %17, %18\n\tv_dot2_f32_bf16 %6, %13, %16, %18\n\tv_dot2_f32_bf16 %7, %13, %17, %18\n\t"
        "v_dot2_f32_bf16 %8, %14, %16, %18\n\tv_dot2_f32_bf16 %9, %14, %17, %18\n\tv_dot2_f32_bf16 %10, %15, %16, %18\n\tv_dot2_f32_bf16 %11, %15, %17, %18\n\t"
        "v_cvt_pk_bf16_f32 %0, %4, %5\n\tv_cvt_pk_bf16_f32 %1, %6, %7\n\tv_cvt_pk_bf16_f32 %2, %8, %9\n\tv_cvt_pk_bf16_f32 %3, %10, %11"
        : "=&v"(o[0]), "=&v"(o[1]), "=&v"(o[2]), "=&v"(o[3]), "=&v"(t0), "=&v"(t1), "=&v"(t2), "=&v"(t3), "=&v"(t4), "=&v"(t5), "=&v"(t6), "=&v"(t7)
        : "v"(x[0]), "v"(x[1]), "v"(x[2]), "v"(x[3]), "s"(0x00003f80u), "s"(0x3f800000u), "v"(c));
    return o;
}
template <int N> struct IC { static constexpr int v = N; };
template <int B, int E, class F> DI void sfor(F&& f) { if constexpr (B < E) { f(IC<B>{}); sfor<B + 1, E>(f); } }
template <int N> DI void waitv() { asm volatile("s_waitcnt vmcnt(%0)" :: "i"(N) : "memory"); }
template <int N> DI void waitv_bar() { if (N == 0) __builtin_amdgcn_s_waitcnt(0x0F70); asm volatile("s_waitcnt vmcnt(%0) lgkmcnt(0)\n\ts_barrier" :: "i"(N) : "memory"); }
template <int N> DI void waitv_barx() { asm volatile("s_waitcnt vmcnt(%0)\n\ts_barrier" :: "i"(N) : "memory"); }
template <int N> DI void waitv_bar2() { asm volatile("s_waitcnt vmcnt(%0) lgkmcnt(2)\n\ts_barrier" :: "i"(N) : "memory"); }
#define BAR() asm volatile("s_waitcnt lgkmcnt(0)\n\ts_barrier" ::: "memory")
#define FENCE() asm volatile("" ::: "memory")
typedef __amdgpu_buffer_rsrc_t rsrc_t;
DI u32x4 ld16_dev(rsrc_t rs, unsigned voff, unsigned soff) { return U4(__builtin_amdgcn_raw_buffer_load_b128(rs, voff, soff, 16)); }
DI u32x2 ld8(rsrc_t rs, unsigned voff, unsigned soff) { return __builtin_bit_cast(u32x2, __builtin_amdgcn_raw_buffer_load_b64(rs, voff, soff, 0)); }
DI u32x2 ld8_dev(rsrc_t rs, unsigned voff, unsigned soff) { return __builtin_bit_cast(u32x2, __builtin_amdgcn_raw_buffer_load_b64(rs, voff, soff, 16)); }
DI void st16_dev(u32x4 v, rsrc_t rs, unsigned voff, unsigned soff) { __builtin_amdgcn_raw_buffer_store_b128(v, rs, voff + soff, 0, 16); }
DI void st8_dev(u32x2 v, rsrc_t rs, unsigned voff, unsigned soff) { __builtin_amdgcn_raw_buffer_store_b64(v, rs, voff, soff, 16); }
DI void set_flag(char* ws, unsigned fo, u64 tagv) {
    asm volatile("s_mov_b64 exec, 1\n\tglobal_store_dwordx2 %0, %1, %2 sc1\n\ts_mov_b64 exec, -1" :: "v"(fo), "v"(tagv), "s"(ws) : "memory");
}
DI u64 peek_flag(char* ws, unsigned fo) { return __hip_atomic_load((GAS u64*)(ws + fo), __ATOMIC_RELAXED, __HIP_MEMORY_SCOPE_AGENT); }
DI void peek_lds(char* ws, unsigned fo, unsigned dst) {
    asm volatile("s_mov_b32 m0, %2\n\ts_nop 0\n\tglobal_load_lds_dword %0, %1 sc1\n\ts_add_u32 m0, %2, 252\n\ts_nop 0\n\tglobal_load_lds_dword %0, %1 offset:4 sc1" :: "v"(fo), "s"(ws), "s"(dst) : "memory", "scc");
}
DI u64 peeked(lds_ptr p) { return (u64)*(LAS unsigned*)(p + 256) << 32 | *(LAS unsigned*)p; }
DI void poll_flag(char* ws, unsigned fo, u64 tag) {
#pragma unroll 1
    for (int spin = 0; spin < (1 << 21); ++spin) {
        if (wave_all(peek_flag(ws, fo) == tag)) break;
        __builtin_amdgcn_s_sleep(1);
    }
    FENCE();
}
#define SB() __builtin_amdgcn_sched_barrier(0)
#define PINS(a) asm volatile("" : "+s"(a))
#define PIN(a) asm volatile("" : "+v"(a))
#define PIN2(a, b) asm volatile("" : "+v"(a), "+v"(b))
DI lds_ptr opq(lds_ptr p) { unsigned u = (unsigned)(uintptr_t)p; asm volatile("" : "+v"(u)); return (lds_ptr)(uintptr_t)u; }
#define TS(i) do {} while (0)

#define LB 1
#define DPF 4
#define DPRIO 1
#define EB1 5
#define XNN 5
#define CL0 9
#define CL1 58
#define CLIN(lo, hi) ((CL0 > (lo) && CL0 <= (hi)) + (CL1 > (lo) && CL1 <= (hi)))
constexpr int GW = 7, XN = XNN;
DI constexpr int d_prio(int n) { return n < 0 ? -1 : n < 27 ? 3 - n / 7 : n < 64 ? 3 - (n - 27) / 10 : n < 84 ? 3 - (n - 64) / 5 : n < 112 ? 3 - (n - 84) / 7 : 0; }
DI constexpr int phys(int t) { return ((t % 9) + 2) % 9 * 16384; }

__global__ NOPK __launch_bounds__(512, 2) void block_fwd(const char* x, const char* wqkv, const char* wo, const char* wg, const char* wu, const char* wd, char* out, char* ws) {
    extern __shared__ __attribute__((aligned(16))) char smem[];
    lds_ptr lds = (lds_ptr)smem;
    const int tid = __builtin_amdgcn_workitem_id_x(), lane = tid & 63, w = __builtin_amdgcn_readfirstlane(tid >> 6);
    const int bid = __builtin_amdgcn_workgroup_id_x(), xcd = bid & 7, loc = bid >> 3;
    const int r = lane & 31, hf = lane >> 5, q4 = (lane & 15) >> 2, g1 = (lane >> 4) & 1, p4 = lane & 3;
    const unsigned lds0 = (unsigned)(uintptr_t)smem;
    const unsigned l16 = lane * 16;
    TS(0);
    const int sp3 = (loc + 8) >> 5;
    const int atb = (xcd >> 1) * 8 + (loc & 7) + sp3 * (xcd & 1) * 4, acg = (loc >> 3) + (1 - sp3) * (xcd & 1) * 4;
    const int row8 = lane >> 3;
    const int rot8 = ((loc & 7) << sp3) + sp3 * (xcd & 1), rot = rot8 == 7 ? 0 : rot8;
    if (loc < 28) {
        const unsigned xo = (unsigned)((atb * 64 + (tid >> 3)) * 896 + (((tid ^ (tid >> 4)) & 7) * 16));
        const unsigned xd0 = lds0 + 1024 + (w >> 2) * 28672 + (w & 3) * 1024;
        unsigned xd = xd0 + (7 - rot) * 4096;
        const unsigned w0 = (unsigned)((rot * 64 + (tid >> 3)) * 2688 + acg * 128 + (((tid & 7) ^ ((tid >> 2) & 4)) * 16));
        asm volatile("s_cmp_eq_u32 %0, %3\n\ts_cselect_b32 %0, %2, %0\n\ts_mov_b32 m0, %0\n\ts_add_u32 %0, %0, 0x1000\n\tglobal_load_lds_dwordx4 %1, %4\n\t"
#define XP(o) "s_cmp_eq_u32 %0, %3\n\ts_cselect_b32 %0, %2, %0\n\ts_sub_u32 m0, %0, " #o "\n\ts_add_u32 %0, %0, 0x1000\n\tglobal_load_lds_dwordx4 %1, %4 offset:" #o "\n\t"
                     XP(128) XP(256) XP(384) XP(512) XP(640) XP(768)
                     "s_add_u32 m0, %6, 8192-896\n\ts_barrier\n\tglobal_load_lds_dwordx4 %5, %7 offset:896\n\t"
                     "s_add_u32 m0, %6, 16384-1792\n\ts_nop 0\n\tglobal_load_lds_dwordx4 %5, %7 offset:1792\n\t"
                     "s_mov_b32 m0, %6\n\ts_nop 0\n\tglobal_load_lds_dwordx4 %5, %7"
                     : "+&s"(xd) : "v"(xo), "s"(xd0), "s"(xd0 + 7 * 4096), "s"(x), "v"(w0), "s"(lds0 + 58368 + w * 1024), "s"(wqkv) : "memory", "scc");
    }
    SB();
    const rsrc_t rws = __builtin_amdgcn_make_buffer_rsrc((void*)ws, 0, WS_END, 0x00020000);
    u64 epoch, tagv;
    unsigned fl1;
    const unsigned ones = 0x3f803f80u;
    const bool ffn = loc < 28;
    const int l7 = loc / 7, m7 = loc % 7;
    const int tb = xcd * 4 + l7;
    const int tt = w & 1, ct = (w >> 1) & 1, kh = w >> 2;
    const unsigned swz = (unsigned)(((lane & 7) ^ (((row8 >> 1) & 1) << 2)) * 16);

    if (__builtin_expect(ffn, 1)) {
        constexpr int A_W = 58368, A_WS = 24576;
#define A_KC(c) (rot + (c) >= 7 ? rot + (c) - 7 : rot + (c))
        const unsigned wvo = (unsigned)(row8 * 2688) + swz;
        const char* wsrc = wqkv + acg * 128 + (size_t)w * 8 * 2688;
        const unsigned wdst = lds0 + A_W + w * 1024;
#define A_PIECE(c, i) glds(wvo, wsrc + A_KC(c) * (64 * 2688) + (i) * 896, wdst + ((c) & 3) * A_WS + (i) * 8192)
#define A_GROUP(c) do { A_PIECE(c, 1); A_PIECE(c, 2); A_PIECE(c, 0); } while (0)
        epoch = peek_flag(ws, FL_EPOCH * 128); FENCE(); fl1 = (FL_KV + atb * 8 + acg) * 128;
        lds_ptr xb[4], wb1, wb1h;
#pragma unroll
        for (int j = 0; j < 4; ++j) xb[j] = lds + 1024 + tt * 28672 + (r >> 3) * 1024 + (r & 7) * 128 + (((2 * j + hf) ^ ((r >> 1) & 7)) * 16);
        wb1 = opq(lds + A_W + (kh ? 0 : 8192) + (8 * hf + q4) * 128 + ((ct ^ (q4 >> 1)) * 64) + g1 * 32 + p4 * 8);
        wb1h = opq(wb1 + 2 * A_WS);
        unsigned go1[2], go2[2]; int gi[2];
#pragma unroll
        for (int p = 0; p < 2; ++p) {
            const int G = acg * 8 + ct * 4 + 2 * p + hf, hp = G / 7, i = G - hp * 7;
            const unsigned hs = kh ? 131072u : 262144u;
            const unsigned to = (kh ? WS_QF + (atb * 2 + tt) * 2048 : WS_KV + (atb * 2 + tt) * 4096) + r * 16;
            go1[p] = (unsigned)(2 * hp + (i > 3)) * hs + (unsigned)(i > 3 ? i - 3 : i) * 512 + to;
            go2[p] = i == 3 ? (unsigned)(2 * hp + 1) * hs + to : OOB;
            gi[p] = i;
        }
        const int vcol = acg * 64 + ct * 32 + r, vh = vcol / 28, vd = vcol - vh * 28;
        unsigned vo = WS_KV + (atb * 2 + tt) * 4096 + (unsigned)vh * 262144 + 2048 + hf * 512 + vd * 16;
        PIN2(go1[0], go1[1]); PIN2(go2[0], go2[1]); PIN(vo);
        lds_ptr xw = opq(lds + 1024 + kh * 28672 + (w & 3) * 1024 + l16);
        waitv<4>();
        TS(28);
        u32x4 xv[7];
#pragma unroll
        for (int c = 0; c < 7; ++c) xv[c] = *(LAS u32x4*)(xw + c * 4096);
        SB();
        f32x4 sm = {};
#pragma unroll
        for (int c = 0; c < 7; ++c)
#pragma unroll
            for (int j = 0; j < 4; ++j) sm[j] = dot2(xv[c][j], ones, sm[j]);
        float nm = sum8((sm[0] + sm[1]) + (sm[2] + sm[3])) * (-1.f / D);
        PIN(nm);
#pragma unroll
        for (int c = 0; c < 7; ++c) { xv[c] = shift8(xv[c], nm); *(LAS u32x4*)(xw + c * 4096) = xv[c]; }
        A_GROUP(1);
        SB();
        LAS float* rst = (LAS float*)lds;
        sm = (f32x4){};
#pragma unroll
        for (int c = 0; c < 7; ++c)
#pragma unroll
            for (int j = 0; j < 4; ++j) sm[j] = dot2(xv[c][j], xv[c][j], sm[j]);
        rst[w * 8 + row8] = __builtin_amdgcn_rsqf(sum8((sm[0] + sm[1]) + (sm[2] + sm[3])) * (1.f / D) + EPS);
        A_GROUP(2);
        TS(31);
        bf16x8 fx[4], f1[4], f2[4];
#define A_WAIT(c) do { if ((c) == 0) waitv_bar<6>(); else if ((c) == 1) waitv_barx<6>(); else if ((c) < 6) waitv_barx<3>(); else waitv_barx<0>(); \
                       if ((c) == 0) TS(1); if ((c) == 6) TS(2); } while (0)
#define A_WP(c, ks) ((((c) & 2) ? wb1h : wb1) + ((c) & 1) * A_WS + (ks) * 2048)
        if (w < 4) {
            f32x16 ak = {}, av = {};
            f32x4 rv[4];
            float rs = 0.f;
#pragma unroll
            for (int n = 0; n < 30; ++n) {
                const int c = n >> 2, ks = n & 3, km = (n - 2) & 3;
                if (n < 28 && ks == 0) A_WAIT(c);
                if (n >= 2) { ak = MFMA(f1[km], fx[km], ak); PIN(ak); } SB();
                if (n < 28) { fx[ks] = lds8(xb[ks] + c * 4096); f1[ks] = cat(trd(A_WP(c, ks)), trd(A_WP(c, ks) + 512)); } SB();
                if (n >= 2) { av = MFMA(fx[km], f2[km], av); PIN(av); } SB();
                if (n < 28) f2[ks] = cat(trd(A_WP(c, ks) + 8192), trd(A_WP(c, ks) + 8192 + 512));
                if (c == 0 && ks < 3) A_PIECE(3, ks);
                if (c >= 2 && c < 5 && ks < 3) A_PIECE(c + 2, ks);
                if (n == 21) {
                    rs = rst[tt * 32 + r];
#pragma unroll
                    for (int g = 0; g < 4; ++g) rv[g] = *(LAS f32x4*)(rst + tt * 32 + 8 * g + 4 * hf);
                }
                if (n == 24) { PIN(epoch); tagv = epoch + 1; PIN(tagv); }
                SB();
            }
            TS(3);
#pragma unroll
            for (int i = 0; i < 16; ++i) { ak[i] *= rs; av[i] *= rv[i >> 2][i & 3]; }
#pragma unroll
            for (int p = 0; p < 2; ++p) {
                const u32x4 f = pack8(ak, p);
                const auto s0 = __builtin_amdgcn_permlane32_swap(f[0], f[2], false, false), s1 = __builtin_amdgcn_permlane32_swap(f[1], f[3], false, false);
                const u32x4 c = {s0[0], s1[0], s0[1], s1[1]};
                st16_dev(c, rws, go1[p], 0); st16_dev(c, rws, go2[p], 0);
            }
            st16_dev(pack8(av, 0), rws, vo, 0); st16_dev(pack8(av, 1), rws, vo + 1024, 0);
        } else {
            f32x16 aq = {};
            __builtin_amdgcn_s_setprio(1);
#pragma unroll
            for (int n = 0; n < 30; ++n) {
                const int c = n >> 2, ks = n & 3, km = (n - 2) & 3;
                if (n < 28 && ks == 0) A_WAIT(c);
                if (n == 24) { PIN(epoch); tagv = epoch + 1; PIN(tagv); }
                if (n >= 2) { aq = MFMA(f1[km], fx[km], aq); PIN(aq); } SB();
                if (n < 28) { fx[ks] = lds8(xb[ks] + c * 4096); f1[ks] = cat(trd(A_WP(c, ks)), trd(A_WP(c, ks) + 512)); }
                if (c == 0 && ks < 3) A_PIECE(3, ks);
                if (c >= 2 && c < 5 && ks < 3) A_PIECE(c + 2, ks);
                if (n == 24) {
                    const unsigned pv = vd == 0 ? ones : 0u, po = vd < 4 ? vo + 448 : OOB;
                    st16_dev((u32x4){pv, pv, pv, pv}, rws, po, 0); st16_dev((u32x4){pv, pv, pv, pv}, rws, po + 1024, 0);
                }
                SB();
            }
            __builtin_amdgcn_s_setprio(0);
            const float QS = rst[tt * 32 + r] * (0.18898223650461362f * 1.4426950408889634f);
#pragma unroll
            for (int i = 0; i < 16; ++i) aq[i] *= QS;
#pragma unroll
            for (int p = 0; p < 2; ++p) {
                const u32x4 f = pack8(aq, p);
                const auto s0 = __builtin_amdgcn_permlane32_swap(f[0], f[2], false, false), s1 = __builtin_amdgcn_permlane32_swap(f[1], f[3], false, false);
                const bool sp = gi[p] == 3;
                st16_dev((u32x4){s0[0], s1[0], sp ? 0u : s0[1], sp ? 0u : s1[1]}, rws, go1[p], 0);
                st16_dev((u32x4){0u, 0u, s0[1], s1[1]}, rws, go2[p], 0);
            }
        }
    } else {
        __builtin_amdgcn_s_sleep(16);
        epoch = peek_flag(ws, FL_EPOCH * 128); fl1 = (FL_RT + xcd * 4 + loc - 28) * 128;
        const int ri = xcd * 4 + loc - 28, nu = (w == 0 && ri < 16) ? 4 : 3;
        const unsigned vo = (unsigned)(row8 * 3584 + (lane & 7) * 16);
#pragma unroll 1
        for (int i = 0; i < nu; ++i) {
            const int u = i < 3 ? i * 256 + ri * 8 + w : 768 + ri;
            const size_t so = (size_t)(u / 28) * 16 * 3584 + (u % 28) * 128;
            const unsigned d_ = lds0 + w * 16384 + i * 4096;
            glds(vo, wg + so, d_); glds(vo, wg + so + 8 * 3584, d_ + 1024); glds(vo, wu + so, d_ + 2048); glds(vo, wu + so + 8 * 3584, d_ + 3072);
        }
#pragma unroll 1
        for (int i = 0; i < nu; ++i) {
            const int u = i < 3 ? i * 256 + ri * 8 + w : 768 + ri;
            waitv<8>();
            lds_ptr sg = lds + w * 16384 + i * 4096 + g1 * 2048 + (4 * hf + q4) * 128 + p4 * 8;
#pragma unroll
            for (int e = 0; e < 4; ++e) st16_dev(U4(cat(trd(sg + e * 32), trd(sg + e * 32 + 1024))), rws, l16, WS_WGU + (unsigned)(((u % 28) * 4 + e) * 28 + u / 28) * 1024);
            FENCE();
        }
        tagv = epoch + 1;
    }
    TS(24);
    const int head = xcd * 2 + (loc & 1), rb = loc >> 1, qt = w & 3;
    const char* kvs = ws + WS_KV + (size_t)head * 262144 + w * 2048;
    const char* qs = ws + WS_QF + (size_t)(head * 64 + rb * 4 + qt) * 2048 + kh * 1024;
    unsigned kvd = lds0 + w * 2048, qd = lds0 + 147456 + qt * 2048 + kh * 1024;
    unsigned pf1 = (FL_KV + (lane >> 1) * 8 + (head * 28 + (lane & 1) * 27) / 64) * 128;
    PINS(kvs); PINS(qs); PINS(kvd); PINS(qd); PIN2(pf1, fl1);
    waitv_bar<0>();
    if (w == 0) set_flag(ws, fl1, tagv);
    const u64 tag = ((u64)(unsigned)__builtin_amdgcn_readfirstlane((unsigned)(tagv >> 32)) << 32 | (u64)(unsigned)__builtin_amdgcn_readfirstlane((unsigned)tagv));
    TS(4);

    const int drb = (xcd >> 1) * 4 + l7, dfg = (xcd & 1) * 7 + m7;
    bf16x8 wf[28];
    const unsigned p16 = hf * 1024 + g1 * 512 + (lane & 15) * 16;
    u32x2 xa, xb;
    f32x4 csg = {}, csu = {};
    f32x16 acc = {};
    const u32x4 one4 = {ones, ones, ones, ones};
    const unsigned hi_ = (unsigned)((tb * 2 + tt) * 28 + m7 * 4 + ct * 2 + kh), ofo = (unsigned)(tt * 32768 + kh * 16384 + ct * 4096);
    unsigned ho = WS_HF + hi_ * 1024 + l16, so = hf ? OOB : WS_HS + hi_ * 256 + r * 8;
    const char* os = ws + WS_OF + (size_t)tb * 65536 + ofo;
    unsigned od = lds0 + 65536 + ofo;
    unsigned pf2 = (FL_O + (tb >> 1) * 16 + kh * 8 + ct * 2 + ((lane >> 1) & 1) + (lane & 4)) * 128;
    unsigned go = ffn ? WS_GF + (unsigned)(drb * 4 * 112 + dfg * 8 + w) * 1024 + g1 * 512 + (hf * 16 + (lane & 15)) * 16 : OOB;
    unsigned fo3 = (FL_H + tb * 8 + m7) * 128;
    {
        lds_ptr ap = opq(lds + kh * 8192 + l16), apB = opq(lds + 65536 + kh * 8192 + l16), apC = opq(lds + 131072 + kh * 8192 + l16);
        const unsigned lv = kh * 8192 + (l16 ^ (kh * 256));
        lds_ptr vp = opq(lds + lv), vpB = opq(lds + 65536 + lv), vpC = opq(lds + 131072 + lv);
#define AP(off) ((off) < 65536 ? ap + (off) : (off) < 131072 ? apB + ((off) - 65536) : apC + ((off) - 131072))
#define VP(off) ((off) < 65536 ? vp + (off) : (off) < 131072 ? vpB + ((off) - 65536) : vpC + ((off) - 131072))
#define KV_ISSUE(t) glds2_dev(l16, kvs + (t) * 16384, kvd + phys(t))
        unsigned wgo = ffn ? WS_WGU + (unsigned)(dfg * 8 + w) * 28672 : OOB;
        const int ri = umin(xcd * 4 + loc - 28, 27);
        const char* wos = (ffn ? wo + m7 * 128 : wd + (ri % 7) * 128 + (size_t)(ri / 7) * 448 * 896) + (size_t)w * 56 * 896;
        unsigned wov = (unsigned)(row8 * 896) + swz;
        const rsrc_t rx = __builtin_amdgcn_make_buffer_rsrc((void*)x, 0, 2048 * 448 * 2, 0x00020000);
        unsigned xo_ = (unsigned)(((tb * 2 + tt) * 32 + r) * 896 + m7 * 128 + ct * 64 + kh * 32 + hf * 8);
        unsigned rto = (FL_RT + w * 4 + (lane & 3)) * 128;
        lds_ptr qp = opq(lds + 147456 + qt * 2048 + l16);
        PINS(wgo); PINS(wos); PIN2(wov, xo_); PIN(rto); PIN2(ho, so); PIN2(pf2, fo3); PIN(go);
        lds_ptr xw = opq(lds + 147456 + w * 2048 + l16), xr = opq(lds + 147456 + (w ^ 4) * 2048 + l16);
        lds_ptr lw = opq(lds + 58368 + w * 256 + lane * 4), lr = opq(lds + 58368 + qt * 256 + 128 + r * 4);
        lds_ptr bw = opq(lds + 60416 + w * 4), br = opq(lds + 60416 + (lane & 7) * 4);
        unsigned oo = WS_OF + (unsigned)((rb * 4 + qt) * 32 + head * 2 + kh) * 1024 + l16;
        unsigned fo2 = (FL_O + rb * 16 + head) * 128;
        PIN2(oo, fo2);
        if (w == 1) poll_flag(ws, pf1, tag);
        BAR();
        TS(5);
        glds_dev(l16, qs, qd);
        KV_ISSUE(0); KV_ISSUE(1);
        asm volatile("s_barrier" ::: "memory");
        KV_ISSUE(2); KV_ISSUE(3);
        waitv_bar<4>();
        TS(6);
        const bf16x8 q0 = lds8(qp), q1 = lds8(qp + 1024);
        f32x16 o = {}, sa = {}, sb;
        bf16x8 k0 = lds8(ap + phys(0)), k1 = lds8(ap + phys(0) + 1024), v0 = lds8(vp + phys(0) + 2048), v1 = {};
        u32x4 p0, p1 = {};
        sa = MFMA(k0, q0, sa); sa = MFMA(k1, q1, sa);
        k0 = lds8(ap + phys(0) + 4096); k1 = lds8(ap + phys(0) + 5120);
        PIN(sa); SB();
        u64 rtf = 0;
#define EXP4(S, o4) _Pragma("unroll") for (int i_ = o4; i_ < o4 + 4; ++i_) S[i_] = __builtin_amdgcn_exp2f(S[i_]);
#define WLD(s) wf[s] = BF8(ld16_dev(rws, p16 + ((s) & 1) * 256 + ((s) & 2) * 1024, wgo + ((s) >> 2) * 4096))
#define SIDE(t, g) do { \
            if ((t) == 0 && (g) == 0) KV_ISSUE(4); if ((t) == 0 && (g) == 2) KV_ISSUE(5); if ((t) == 1 && (g) == 0) KV_ISSUE(6); if ((t) == 1 && (g) == 2) KV_ISSUE(7); \
            if ((t) == 2 && (g) == 0) KV_ISSUE(8); \
            if ((t) == 1 && (g) == 1) peek_lds(ws, rto, lds0 + 155648 + w * 512); if ((t) == 2 && (g) == 2) { waitv<4 + CLIN(4, 9)>(); rtf = peeked(lds + 155648 + w * 512 + lane * 4); } \
            if ((t) == 3 && (g) < 3) KV_ISSUE(9 + (g)); \
            if ((t) * 4 + (g) == CL0 || (t) * 4 + (g) == CL1) st16_dev((u32x4){~0u, ~0u, ~0u, ~0u}, rws, go + ((t) * 4 + (g) == CL0 ? 2 : 3) * 114688, 0); \
            if ((t) == 7 && (g) == 0) KV_ISSUE(12); if ((t) == 7 && (g) == 2) KV_ISSUE(13); if ((t) == 8 && (g) == 0) KV_ISSUE(14); if ((t) == 8 && (g) == 2) KV_ISSUE(15); \
            if ((t) >= 4 && (t) <= 6) WLD(((t) - 4) * 4 + (g)); \
            if (((t) == 7 || (t) == 8) && ((g) & 1)) WLD(12 + ((t) - 7) * 2 + ((g) >> 1)); \
            if ((t) >= 9 && (t) <= 11) WLD(((t) - 5) * 4 + (g)); \
            if ((t) == 12 || ((t) == 13 && (g) < 3)) glds(wov, wos + (((t) - 12) * 4 + (g)) * 8 * 896, lds0 + (w * 7 + ((t) - 12) * 4 + (g)) * 1024); \
            if ((t) == 13 && (g) == 3) { xa = ld8(rx, xo_, 0); xb = ld8(rx, xo_, 16); } \
            if ((t) == 14 && (g) == 0) { if (tid < 32) *(LAS u32x4*)(lds + 57344 + tid * 16) = (u32x4){0, 0, 0, 0}; } \
        } while (0)
#define TOFF(i) (phys((i) >> 1) + ((i) & 1) * 4096)
#define TILE(i, SC, SN) do { \
            __builtin_amdgcn_s_setprio(((i) & 1) ? 1 : 3); \
            if ((i) < 31) { f32x16 z_ = {}; SN = MFMA(k0, q0, z_); PIN(SN); } if ((i) < 30) k0 = lds8(AP(TOFF((i) + 2))); SB(); \
            EXP4(SC, 0); PIN(SC); SB(); \
            o = MFMA(v1, BF8(p1), o); PIN(o); v1 = lds8(VP(TOFF(i) + 3072)); SIDE((i) >> 1, ((i) & 1) * 2); SB(); \
            EXP4(SC, 4); p0 = pack8(SC, 0); PIN(p0); SB(); \
            __builtin_amdgcn_s_setprio(((i) & 1) ? 0 : 2); \
            if ((i) < 31) { SN = MFMA(k1, q1, SN); PIN(SN); } if ((i) < 30) k1 = lds8(AP(TOFF((i) + 2) + 1024)); SB(); \
            EXP4(SC, 8); PIN(SC); SB(); \
            o = MFMA(v0, BF8(p0), o); PIN(o); if ((i) < 31) v0 = lds8(VP(TOFF((i) + 1) + 2048)); SIDE((i) >> 1, ((i) & 1) * 2 + 1); SB(); \
            EXP4(SC, 12); p1 = pack8(SC, 1); PIN(p1); SB(); } while (0)
#define STEP(t) do { TILE(2 * (t), sa, sb); TILE(2 * (t) + 1, sb, sa); } while (0)
        STEP(0);
        waitv_barx<4 + CLIN(-1, 3)>();
        STEP(1); STEP(2);
        if (__builtin_expect(!wave_all(rtf == tag), 0)) poll_flag(ws, rto, tag);
        waitv_bar2<2 + CLIN(5, 11)>();
        TS(21);
        STEP(3); STEP(4); STEP(5); STEP(6);
        waitv_bar2<12 + CLIN(13, 27)>();
        TS(22);
        STEP(7); STEP(8); STEP(9); STEP(10);
        waitv_bar2<9 + CLIN(34, 43)>();
        TS(23);
        STEP(11); STEP(12); STEP(13); STEP(14); STEP(15);
        o = MFMA(v1, BF8(p1), o);
        TS(7);
        u32x4 ov;
        {
            *(LAS f32x4*)xw = (f32x4){o[8], o[9], o[10], o[11]}; *(LAS f32x4*)(xw + 1024) = (f32x4){o[12], o[13], o[14], o[15]};
            const float l = kh ? o[4] : o[12];
            *(LAS float*)lw = l;
#define AMX(a, b, c) __builtin_fmaxf(__builtin_fmaxf(__builtin_fabsf(a), __builtin_fabsf(b)), __builtin_fabsf(c))
            const float mx_ = AMX(AMX(AMX(o[0], o[1], o[2]), AMX(o[3], o[4], o[5]), AMX(o[6], o[7], o[8])), AMX(AMX(o[9], o[10], o[11]), AMX(o[12], o[13], o[14]), o[15]), 0.f);
            const bool ok = (mx_ < 0x1p100f) & ((hf ? l : 1.f) > 0x1p-40f);
            *(LAS unsigned*)bw = wave_all(ok) ? 0u : 1u;
            BAR();
            const f32x4 a = *(LAS f32x4*)xr, c = *(LAS f32x4*)(xr + 1024);
            const float inv = __builtin_amdgcn_rcpf(*(LAS float*)lr + *(LAS float*)(lr + 1024));
            const unsigned bad = *(LAS unsigned*)br;
            const float iz = (kh && hf) ? 0.f : inv;
            ov = (u32x4){cvtpk((o[0] + a[0]) * inv, (o[1] + a[1]) * inv), cvtpk((o[2] + a[2]) * inv, (o[3] + a[3]) * inv),
                         cvtpk((o[4] + c[0]) * iz, (o[5] + c[1]) * iz), cvtpk((o[6] + c[2]) * iz, (o[7] + c[3]) * iz)};
            if (__builtin_expect(wave_any(bad != 0), 0)) {
                f32x16 of = {};
                float m = -0x1p126f;
#pragma unroll 1
                for (int t = 0; t < 16; ++t) {
                    BAR();
                    glds2_dev(l16, kvs + t * 16384, lds0 + 65536 + w * 2048);
                    waitv_bar<0>();
#pragma unroll 1
                    for (int u = 0; u < 2; ++u) {
                        f32x16 s = {};
                        s = MFMA(lds8(apB + u * 4096), q0, s); s = MFMA(lds8(apB + u * 4096 + 1024), q1, s);
                        float mx = s[0];
#pragma unroll
                        for (int i = 1; i < 16; ++i) mx = __builtin_fmaxf(mx, s[i]);
                        mx = __builtin_fmaxf(swap_max(mx), m);
                        const float alpha = __builtin_amdgcn_exp2f(m - mx);
                        m = mx;
#pragma unroll
                        for (int i = 0; i < 16; ++i) { s[i] = __builtin_amdgcn_exp2f(s[i] - mx); of[i] *= alpha; }
                        of = MFMA(lds8(vpB + u * 4096 + 2048), BF8(pack8(s, 0)), of);
                        of = MFMA(lds8(vpB + u * 4096 + 3072), BF8(pack8(s, 1)), of);
                    }
                }
                *(LAS f32x4*)xw = (f32x4){of[8], of[9], of[10], of[11]}; *(LAS f32x4*)(xw + 1024) = (f32x4){of[12], of[13], of[14], of[15]};
                *(LAS float*)lw = hf ? (kh ? of[4] : of[12]) : m;
                BAR();
                const f32x4 a2 = *(LAS f32x4*)xr, c2 = *(LAS f32x4*)(xr + 1024);
                const float m0 = *(LAS float*)(lr - 128), m1 = *(LAS float*)(lr + 1024 - 128), mm = __builtin_fmaxf(m0, m1);
                const float e0 = __builtin_amdgcn_exp2f(m0 - mm), e1 = __builtin_amdgcn_exp2f(m1 - mm);
                const float iv = __builtin_amdgcn_rcpf(*(LAS float*)lr * e0 + *(LAS float*)(lr + 1024) * e1);
                const float im = (kh ? e1 : e0) * iv, io = (kh ? e0 : e1) * iv;
                const bool z = kh && hf;
                ov = (u32x4){cvtpk(of[0] * im + a2[0] * io, of[1] * im + a2[1] * io), cvtpk(of[2] * im + a2[2] * io, of[3] * im + a2[3] * io),
                             z ? 0u : cvtpk(of[4] * im + c2[0] * io, of[5] * im + c2[1] * io), z ? 0u : cvtpk(of[6] * im + c2[2] * io, of[7] * im + c2[3] * io)};
            }
        }
        st16_dev(ov, rws, oo, 0);
        TS(25);
        FENCE();
        PIN2(xa, xb);
#pragma unroll
        for (int s = 27; s >= 0; --s) PIN(wf[s]);
#pragma unroll
        for (int s = 0; s < 16; s += 2) { csg = MFMA16(wf[s], BF8(one4), csg); csu = MFMA16(wf[s + 1], BF8(one4), csu); }
        acc[0] = bflo(xa[0]); acc[1] = bfhi(xa[0]); acc[2] = bflo(xa[1]); acc[3] = bfhi(xa[1]); acc[4] = bflo(xb[0]); acc[5] = bfhi(xb[0]); acc[6] = bflo(xb[1]); acc[7] = bfhi(xb[1]);
        PIN(acc); PIN2(ho, so); PIN2(pf2, fo3); PINS(os); PINS(od); PIN2(csg, csu);
        waitv_bar<0>();
        if (w == 0) set_flag(ws, fo2, tagv);
        TS(8);
    }
    if (__builtin_expect(!ffn, 0)) {
        const int ri = xcd * 4 + loc - 28;
        if (ri < 28) {
#pragma unroll 1
            for (int u = w; u < 28; u += 8) {
                lds_ptr sd = lds + u * 2048 + (4 * hf + q4) * 128 + g1 * 32 + p4 * 8;
#pragma unroll
                for (int e = 0; e < 2; ++e) { lds_ptr p_ = sd + ((e ^ (q4 >> 1)) * 64);
                    st16_dev(U4(cat(trd(p_), trd(p_ + 1024))), rws, l16, WS_WD + (unsigned)(((ri % 7) * 112 + (ri / 7) * 28 + u) * 2 + e) * 1024); }
            }
        }
        waitv_bar<0>();
        if (w == 0) set_flag(ws, (FL_RD + ri) * 128, tagv);
        return;
    }

    const int erb = tb >> 1;
    const char* hs = ws + WS_HF + (size_t)drb * 114688 + w * 1024;
    const char* hss = ws + WS_HS + (size_t)((drb * 4 + (w & 3)) * 28 + kh * 14) * 256;
    unsigned pf3, fo4 = (FL_G + drb * 64 + dfg) * 128;
    {
        lds_ptr wb = lds + kh * (8 * 28 * 128) + (4 * hf + q4) * 128 + ((ct ^ (q4 >> 1)) * 64) + (g1 ^ kh) * 32 + p4 * 8;
        bf16x8 wr[16];
#pragma unroll
        for (int s = 0; s < 12; ++s) { const int k0 = ((s >> 1) * 28 + (s & 1) * 16) * 128; wr[s] = cat(trd(wb + k0), trd(wb + k0 + 1024)); }
        lds_ptr cw = opq(lds + 131072 + w * 2048 + l16), cr = opq(lds + 131072 + (w ^ 4) * 2048 + l16);
        lds_ptr ob = opq(lds + 65536 + tt * 32768 + kh * 16384 + l16);
        pf3 = (FL_H + drb * 16 + (lane & 8) + umin(lane & 7, 6)) * 128;
        PIN2(pf3, fo4); PINS(hs); PINS(hss);
        poll_flag(ws, pf2, tag);
        TS(9);
        glds4_dev(l16, os, od);
        asm volatile("s_barrier" ::: "memory");
        glds4_dev(l16, os + 8192, od + 8192);
        bf16x8 of[8];
        waitv_bar<4>();
        TS(26);
#pragma unroll
        for (int s = 0; s < 8; ++s) of[s] = lds8(ob + s * 1024);
        SB();
#pragma unroll
        for (int s = 0; s < 8; ++s) {
            acc = MFMA(wr[s], of[s], acc); PIN(acc); SB();
            if (s >= 2 && s < 6) { const int k0 = (((s + 10) >> 1) * 28 + (s & 1) * 16) * 128; wr[s + 10] = cat(trd(wb + k0), trd(wb + k0 + 1024)); SB(); }
        }
        waitv_barx<0>();
#pragma unroll
        for (int s = 0; s < 8; ++s) of[s] = lds8(ob + (8 + s) * 1024);
        SB();
#pragma unroll
        for (int s = 0; s < 8; ++s) acc = MFMA(wr[8 + s], of[s], acc);
        TS(10);
        *(LAS f32x4*)cw = (f32x4){acc[8], acc[9], acc[10], acc[11]}; *(LAS f32x4*)(cw + 1024) = (f32x4){acc[12], acc[13], acc[14], acc[15]};
        BAR();
        const f32x4 a = *(LAS f32x4*)cr + (f32x4){acc[0], acc[1], acc[2], acc[3]}, b = *(LAS f32x4*)(cr + 1024) + (f32x4){acc[4], acc[5], acc[6], acc[7]};
        const u32x4 hv = {cvtpk(a[0], a[1]), cvtpk(a[2], a[3]), cvtpk(b[0], b[1]), cvtpk(b[2], b[3])};
        st16_dev(hv, rws, ho, 0);
        FENCE(); SB();
        float s1 = 0.f, s2 = 0.f;
        { f32x4 q4_ = {}; acc_stats2(hv[0], hv[1], q4_); acc_stats2(hv[2], hv[3], q4_); s1 = swap_add(q4_[0] + q4_[1]); s2 = swap_add(q4_[2] + q4_[3]); }
        st8_dev((u32x2){f2u(s1), f2u(s2)}, rws, so, 0);
        FENCE(); PIN2(csg, csu); SB();
#pragma unroll
        for (int s = 16; s < 28; s += 2) { csg = MFMA16(wf[s], BF8(one4), csg); csu = MFMA16(wf[s + 1], BF8(one4), csu); }
        PIN2(csg, csu);
        waitv_bar<0>();
        if (w == 0) set_flag(ws, fo3, tagv);
        if (bid == 0 && w == 0) set_flag(ws, FL_EPOCH * 128, tagv);
        TS(11);
    }

    const unsigned wdo = WS_WD + (unsigned)(m7 * 112 + w * 14) * 2048;
    bf16x8 wdf[28];
    bf16x8 gf[GW];
    const unsigned gxo = WS_GF + (unsigned)(erb * 4 * 112 + w * 14) * 1024 + (tb & 1) * 256;
    const unsigned pwd = hf * 2048 + g1 * 512 + (lane & 15) * 16;
#define G_LD(s) gf[(s) % GW] = BF8(ld16_dev(rws, p16, gxo + ((s) / 7) * 114688 + ((s) % 7) * 2048))
    u64 gfl = 0;
    const unsigned gfi = (FL_G + (erb * 4 + g1) * 16 + ((w * 14 + umin(lane & 15, 13)) >> 3)) * 128;
    {
        const unsigned hd = lds0 + w * 1024;
        const char* wds = ws + wdo;
        lds_ptr hb = opq(lds + p16), hb2 = opq(lds + 57344 + p16);
        lds_ptr wl = opq(lds + 114688 + w * 5120 + l16), wl2 = opq(lds + w * 7168 + l16);
        LAS f32x2* stp = (LAS f32x2*)opq(lds + 159744 + (lane & 15) * 8);
        if (w == 1) poll_flag(ws, pf3, tag);
        BAR();
        TS(12);
#define H_PIECE(j) glds_dev(l16, hs + (j) * 8192, hd + (j) * 8192)
#define WDO(s) (((s) >> 2) * 4096 + (((s) >> 1) & 1) * 1024 + ((s) & 1) * 256)
#define WDLD(s) wdf[s] = BF8(ld16_dev(rws, pwd, wdo + WDO(s)))
#define HB(n) (((n) < 56 ? hb + ((n) & ~1) * 1024 : hb2 + (((n) & ~1) - 56) * 1024) + ((n) & 1) * 256)
        H_PIECE(0);
        asm volatile("s_barrier" ::: "memory");
        H_PIECE(1); H_PIECE(2); H_PIECE(3);
        constexpr int PF = DPF;
        bf16x8 bf[PF + 1];
        f32x4 ag[2][2], au[2][2];
        f32x2 sp[7], sv[4];
        float mean[2] = {0.f, 0.f}, r2[2] = {0.f, 0.f}, hp = 0.f;
        u32x4 hv;
        lds_ptr pk = opq(lds + 155648 + w * 512 + lane * 4);
        u64 pv = 0;
        const unsigned pkd = lds0 + 155648 + w * 512;
        const unsigned rdo = (FL_RD + (w >> 1) * 7 + m7) * 128;
        waitv_barx<3>();
        TS(27);
#pragma unroll
        for (int n = 0; n < PF; ++n) bf[n] = lds8(HB(n));
        SB();
        sfor<0, 125>([&](auto N_) __attribute__((always_inline)) {
            constexpr int n = decltype(N_)::v, p = n / 28, s = n % 28;
            if constexpr (n == 3 + LB) waitv_barx<3>();
            if constexpr (n == 11 + LB) waitv_barx<7 + LB>();
            if constexpr (n == 27 + LB) { waitv_barx<3>(); TS(13); }
            if constexpr (n == 64) { waitv_barx<12>(); if (w == 0) set_flag(ws, fo4, tagv); TS(14); }
            if constexpr (n == 85) { waitv_barx<9>(); if (w == 0) set_flag(ws, fo4 + 2048, tagv); }
            if constexpr (n == 38) { if (__builtin_expect(!wave_all(pv == tag), 0)) poll_flag(ws, rdo, tag); FENCE(); }
            if constexpr (DPRIO && d_prio(n) != d_prio(n - 1)) __builtin_amdgcn_s_setprio(d_prio(n));
            if constexpr (n < 112) {
                if constexpr (s < 2) { ag[p & 1][s] = (f32x4){}; au[p & 1][s] = (f32x4){}; }
                ag[p & 1][s & 1] = MFMA16(wf[s & ~1], bf[n % (PF + 1)], ag[p & 1][s & 1]); au[p & 1][s & 1] = MFMA16(wf[s | 1], bf[n % (PF + 1)], au[p & 1][s & 1]);
                PIN2(ag[p & 1][s & 1], au[p & 1][s & 1]);
            }
            SB();
            if constexpr (n + PF < 112) bf[(n + PF) % (PF + 1)] = lds8(HB(n + PF));
            if constexpr (n % 3 == 2 && n < 30) H_PIECE(4 + n / 3);
            if constexpr (n == 4) glds4_dev(l16, hss, lds0 + 114688 + w * 5120);
            if constexpr (n == 16) { waitv<4>();
#pragma unroll
                for (int j = 0; j < 7; ++j) sp[j] = *(LAS f32x2*)(lds + 114688 + w * 5120 + (hf * 7 + j) * 256 + r * 8);
            }
            if constexpr (n == 20) { const f32x2 t = ((sp[0] + sp[1]) + (sp[2] + sp[3])) + ((sp[4] + sp[5]) + sp[6]); stp[w * 32 + g1 * 16] = (f32x2){swap_add(t[0]), swap_add(t[1])}; }
            if constexpr (n >= 39 && n < 48 && n % 2 == 1) glds_dev(pwd, wds + WDO((n - 39) / 2), lds0 + 114688 + w * 5120 + (n - 39) / 2 * 1024);
            if constexpr (n >= 48 && n < 64 && n % 2 == 0) WDLD(12 + (n - 48) / 2);
            if constexpr (n >= 70 && n < 84 && n % 2 == 0) glds_dev(pwd, wds + WDO(5 + (n - 70) / 2), lds0 + w * 7168 + (n - 70) / 2 * 1024);
            if constexpr (n >= 86 && n < 102 && n % 2 == 0) WDLD(20 + (n - 86) / 2);
            if constexpr (n == 84) peek_lds(ws, gfi - g1 * 2048, pkd);
            if constexpr (n == 95) { waitv<5>(); pv = peeked(pk); }
            if constexpr (n == 99) { if (__builtin_expect(!wave_all(pv == tag), 0)) poll_flag(ws, gfi - g1 * 2048, tag); FENCE(); }
            if constexpr (n >= 102 && n < 102 + 2 * XN && n % 2 == 0) G_LD((n - 102) / 2);
            if constexpr (n == 111) gfl = peek_flag(ws, gfi);
            if constexpr (n == 32) pv = peeked(pk);
            if constexpr (n == 18) peek_lds(ws, rdo, pkd);
            if constexpr (p >= 1) {
                constexpr int e = (p - 1) & 1, tp = n == 100 ? 3 : p - 1;
                if constexpr ((s == 0 && p < 4) || n == 100) { sv[0] = stp[tp * 32]; sv[1] = stp[(tp + 4) * 32]; sv[2] = stp[tp * 32 + 16]; sv[3] = stp[(tp + 4) * 32 + 16]; }
                if constexpr (((s == 2 || s == 3) && p < 4) || n == 104 || n == 105) { constexpr int k = n & 1; const f32x2 t = sv[2 * k] + sv[2 * k + 1];
                    mean[k] = t[0] * (1.f / D); r2[k] = __builtin_amdgcn_rcpf(__builtin_fmaxf(t[1] * (1.f / D) - mean[k] * mean[k], 0.f) + EPS); PIN2(mean[k], r2[k]); }
                if constexpr (s >= 4 && s < 12) { constexpr int i = s - 4, k = i >> 2;
                    const float h0 = __builtin_fmaxf(ag[e][k][i & 3] - mean[k] * csg[i & 3], 0.f) * r2[k] * (au[e][k][i & 3] - mean[k] * csu[i & 3]);
                    if constexpr (i & 1) { hv[i >> 1] = p >= 3 ? umin(cvtpk(hp, h0), ~1u) : cvtpk(hp, h0); PIN(hv); } else { hp = h0; PIN(hp); } }
                if constexpr (s == 12) {
                    const auto s0 = __builtin_amdgcn_permlane32_swap(hv[0], hv[2], false, false), s1 = __builtin_amdgcn_permlane32_swap(hv[1], hv[3], false, false);
                    st16_dev((u32x4){s0[0], s1[0], s0[1], s1[1]}, rws, go + (p - 1) * 114688, 0); FENCE();
                }
            }
            SB();
        });
        TS(15);
#pragma unroll
        for (int s = XN; s < 7; ++s) { G_LD(s); SB(); }
        FENCE();
#pragma unroll
        for (int s = 0; s < 12; ++s) wdf[s] = lds8(s < 5 ? wl + s * 1024 : wl2 + (s - 5) * 1024);
    }

    {
        const int etok = tid >> 4, eblk = tid & 15;
        const int et = erb * 4 + (etok >> 4), er = (tb & 1) * 16 + (etok & 15);
        const unsigned hro = (unsigned)((((eblk >> 2) & 3) * 64 + (eblk & 1) * 32 + er) * 16 + ((eblk >> 1) & 1) * 8) + WS_HF + (unsigned)(et * 28 + m7 * 4) * 1024;
        u32x2 hres[2];
        GAS u64* op = (GAS u64*)(out + (size_t)(et * 32 + er) * 896 + m7 * 128 + eblk * 8);
        lds_ptr rp = opq(lds + w * 8448 + (lane >> 4) * 528 + (lane & 15) * 16), rq = opq(lds + eblk * 528 + etok * 16);
        lds_ptr rp2 = opq(rp + 67584), rq2 = opq(rq + 67584);
        f32x4 c[4];
#define G_STEP(s) do { _Pragma("unroll") for (int j = 0; j < 4; ++j) { if ((s) % 7 == 0) c[j] = (f32x4){}; c[j] = MFMA16(wdf[4 * ((s) % 7) + j], gf[(s) % GW], c[j]); } PIN2(c[0], c[1]); PIN2(c[2], c[3]); } while (0)
#define G_PUT(q) do { _Pragma("unroll") for (int j = 0; j < 4; ++j) *(LAS f32x4*)(((q) < 2 ? rp : rp2) + j * 2112 + ((q) & 1) * 256) = c[j]; } while (0)
#define G_OUT(hh, RQ) do { f32x4 pa[8]; \
            _Pragma("unroll") for (int ww = 0; ww < 8; ++ww) pa[ww] = *(LAS f32x4*)(RQ + ww * 8448); SB(); \
            const f32x4 a = ((pa[0] + pa[1]) + (pa[2] + pa[3])) + ((pa[4] + pa[5]) + (pa[6] + pa[7])); \
            const u32x2 ov = {cvtpk(a[0] + bflo(hres[hh][0]), a[1] + bfhi(hres[hh][0])), cvtpk(a[2] + bflo(hres[hh][1]), a[3] + bfhi(hres[hh][1]))}; \
            __hip_atomic_store(op + (hh) * (64 * 896 / 8), __builtin_bit_cast(u64, ov), __ATOMIC_RELAXED, __HIP_MEMORY_SCOPE_AGENT); } while (0)
        TS(16);
        unsigned seen = 0;
#pragma unroll
        for (int s = 0; s < 28; ++s) {
            if (s == EB1) BAR();
            if (s == 7 || s == 14 || s == 21) G_PUT(s / 7 - 1);
            if (s == 14) TS(17);
            if (s == 21) { waitv<7>(); PIN2(hres[0], hres[1]); BAR(); G_OUT(0, rq); FENCE(); TS(18); }
            SB();
            G_STEP(s);
            if (s >= 14) { const u32x4 u = U4(gf[s % GW]); seen = umax3(umax3(seen, u[0], u[1]), u[2], u[3]); PIN(seen); }
            SB();
            if (s == 0) { if (__builtin_expect(!wave_all(gfl == tag), 0)) poll_flag(ws, gfi, tag); FENCE(); }
            if (s + GW < 28) G_LD(s + GW);
            if (s == 1) { hres[0] = ld8_dev(rws, hro, 0); hres[1] = ld8_dev(rws, hro, 57344); FENCE(); }
        }
        if (w == 0) { set_flag(ws, fo4 + 4096, tagv); set_flag(ws, fo4 + 6144, tagv); }
        if (__builtin_expect(wave_any(seen == ~0u), 0)) {
            poll_flag(ws, gfi + 4096, tag);
#pragma unroll
            for (int s = 14; s < 14 + GW; ++s) G_LD(s);
#pragma unroll
            for (int s = 14; s < 28; ++s) { if (s == 21) G_PUT(2); G_STEP(s); SB(); if (s + GW < 28) G_LD(s + GW); SB(); }
        }
        G_PUT(3);
        TS(19);
        BAR();
        G_OUT(1, rq2);
    }
    TS(20);
}

extern "C" void kernel_launch(void* const* d_in, const int* in_sizes, int n_in, void* d_out, int out_size, void* d_ws, size_t ws_size, hipStream_t stream) {
    (void)hipFuncSetAttribute((const void*)block_fwd, hipFuncAttributeMaxDynamicSharedMemorySize, LDS_BYTES);
    block_fwd<<<256, 512, LDS_BYTES, stream>>>((const char*)d_in[0], (const char*)d_in[1], (const char*)d_in[2], (const char*)d_in[4], (const char*)d_in[5], (const char*)d_in[6], (char*)d_out, (char*)d_ws);
}
```

```cpp
#include <hip/hip_runtime.h>
#include <hip/hip_bf16.h>
#include <stdint.h>

typedef __attribute__((ext_vector_type(8))) short bf16x8;
typedef __attribute__((ext_vector_type(4))) short s16x4;
typedef __attribute__((ext_vector_type(2))) __bf16 bf16x2;
typedef __attribute__((ext_vector_type(16))) float f32x16;
typedef __attribute__((ext_vector_type(4))) float f32x4;
typedef __attribute__((ext_vector_type(2))) float f32x2;
typedef __attribute__((ext_vector_type(4))) unsigned u32x4;
typedef __attribute__((ext_vector_type(2))) unsigned u32x2;
typedef short v4i16_t __attribute__((ext_vector_type(4)));
typedef unsigned long long u64;
#define LAS __attribute__((address_space(3)))
#define GAS __attribute__((address_space(1)))
typedef LAS char* lds_ptr;
#if defined(__HIP_DEVICE_COMPILE__)
#define NOPK __attribute__((target("no-packed-fp32-ops")))
#else
#define NOPK
#endif
#define DI __device__ __forceinline__
#define MFMA(a, b, c) __builtin_amdgcn_mfma_f32_32x32x16_bf16(a, b, c, 0, 0, 0)
#define MFMA16(a, b, c) __builtin_amdgcn_mfma_f32_16x16x32_bf16(a, b, c, 0, 0, 0)
#define BF8(x) __builtin_bit_cast(bf16x8, x)
#define U4(x) __builtin_bit_cast(u32x4, x)

constexpr int D = 448;
constexpr float EPS = 1e-5f;
constexpr unsigned FL_EPOCH = 0, FL_KV = 1, FL_RT = FL_KV + 256, FL_RD = FL_RT + 32, FL_O = FL_RD + 32, FL_H = FL_O + 256, FL_G = FL_H + 256;
constexpr unsigned WS_KV = 1u << 20;
constexpr unsigned WS_QF = WS_KV + 4194304;
constexpr unsigned WS_OF = WS_QF + 2097152;
constexpr unsigned WS_HF = WS_OF + 2097152;
constexpr unsigned WS_GF = WS_HF + 64 * 28 * 1024;
constexpr unsigned WS_WGU = WS_GF + 64 * 112 * 1024;
constexpr unsigned WS_WD = WS_WGU + 112 * 28 * 1024;
constexpr unsigned WS_HS = WS_WD + 7 * 112 * 2 * 1024;
constexpr unsigned WS_END = WS_HS + 64 * 28 * 256 + 1024;
constexpr unsigned OOB = 0x40000000u;
constexpr int LDS_BYTES = 163840;

DI float u2f(unsigned u) { return __builtin_bit_cast(float, u); }
DI unsigned f2u(float f) { return __builtin_bit_cast(unsigned, f); }
DI unsigned umin(unsigned a, unsigned b) { return a < b ? a : b; }
DI unsigned umax3(unsigned a, unsigned b, unsigned c) { const unsigned m = a > b ? a : b; return m > c ? m : c; }
DI bool wave_any(bool p) { return __builtin_amdgcn_ballot_w64(p) != 0; }
DI bool wave_all(bool p) { return __builtin_amdgcn_ballot_w64(!p) == 0; }
DI unsigned cvtpk(float lo, float hi) { return __builtin_bit_cast(unsigned, __builtin_convertvector((f32x2){lo, hi}, bf16x2)); }
DI float bflo(unsigned u) { return u2f(u << 16); }
DI float bfhi(unsigned u) { return u2f(u & 0xffff0000u); }
DI void glds(unsigned voff, const void* sbase, unsigned dst) {
    asm volatile("s_mov_b32 m0, %2\n\ts_nop 0\n\tglobal_load_lds_dwordx4 %0, %1" :: "v"(voff), "s"(sbase), "s"(dst) : "memory");
}
DI void glds_dev(unsigned voff, const void* sbase, unsigned dst) {
    asm volatile("s_mov_b32 m0, %2\n\ts_nop 0\n\tglobal_load_lds_dwordx4 %0, %1 sc1" :: "v"(voff), "s"(sbase), "s"(dst) : "memory");
}
DI void glds2_dev(unsigned voff, const void* sbase, unsigned dst) {
    asm volatile("s_mov_b32 m0, %2\n\ts_nop 0\n\tglobal_load_lds_dwordx4 %0, %1 sc1\n\tglobal_load_lds_dwordx4 %0, %1 offset:1024 sc1" :: "v"(voff), "s"(sbase), "s"(dst) : "memory");
}
DI void glds4_dev(unsigned voff, const void* sbase, unsigned dst) {
    asm volatile("s_mov_b32 m0, %2\n\ts_nop 0\n\tglobal_load_lds_dwordx4 %0, %1 sc1\n\tglobal_load_lds_dwordx4 %0, %1 offset:1024 sc1\n\t"
                 "global_load_lds_dwordx4 %0, %1 offset:2048 sc1\n\tglobal_load_lds_dwordx4 %0, %1 offset:3072 sc1" :: "v"(voff), "s"(sbase), "s"(dst) : "memory");
}
DI s16x4 trd(lds_ptr p) { return __builtin_bit_cast(s16x4, __builtin_amdgcn_ds_read_tr16_b64_v4i16((LAS v4i16_t*)p)); }
DI bf16x8 cat(s16x4 a, s16x4 b) { return __builtin_shufflevector(a, b, 0, 1, 2, 3, 4, 5, 6, 7); }
DI bf16x8 lds8(lds_ptr p) { return *(LAS bf16x8*)p; }
DI float swap_add(float v) { auto r = __builtin_amdgcn_permlane32_swap(f2u(v), f2u(v), false, false); return u2f(r[0]) + u2f(r[1]); }
DI float swap_max(float v) { auto r = __builtin_amdgcn_permlane32_swap(f2u(v), f2u(v), false, false); return __builtin_fmaxf(u2f(r[0]), u2f(r[1])); }
DI u32x4 pack8(const f32x16& a, int s) { return (u32x4){cvtpk(a[8 * s], a[8 * s + 1]), cvtpk(a[8 * s + 2], a[8 * s + 3]), cvtpk(a[8 * s + 4], a[8 * s + 5]), cvtpk(a[8 * s + 6], a[8 * s + 7])}; }
DI float dot2(unsigned a, unsigned b, float c) { return __builtin_amdgcn_fdot2_f32_bf16(__builtin_bit_cast(bf16x2, a), __builtin_bit_cast(bf16x2, b), c, false); }
DI void acc_stats2(unsigned u0, unsigned u1, f32x4& s) {
    const float a = bflo(u0), b = bfhi(u0), c = bflo(u1), d = bfhi(u1);
    s[0] += a; s[1] += b; s[2] = __builtin_fmaf(a, a, s[2]); s[3] = __builtin_fmaf(b, b, s[3]); s[0] += c; s[1] += d; s[2] = __builtin_fmaf(c, c, s[2]); s[3] = __builtin_fmaf(d, d, s[3]);
}
DI float sum8(float v) {
    v += u2f(__builtin_amdgcn_update_dpp(0u, f2u(v), 0xB1, 0xf, 0xf, true));
    v += u2f(__builtin_amdgcn_update_dpp(0u, f2u(v), 0x4E, 0xf, 0xf, true));
    v += u2f(__builtin_amdgcn_update_dpp(0u, f2u(v), 0x141, 0xf, 0xf, true));
    return v;
}
DI u32x4 shift8(u32x4 x, float c) {
    u32x4 o; float t0, t1, t2, t3, t4, t5, t6, t7;
    asm("v_dot2_f32_bf16 %4, %12, %16, %18\n\tv_dot2_f32_bf16 %5, %12, %17, %18\n\tv_dot2_f32_bf16 %6, %13, %16, %18\n\tv_dot2_f32_bf16 %7, %13, %17, %18\n\t"
        "v_dot2_f32_bf16 %8, %14, %16, %18\n\tv_dot2_f32_bf16 %9, %14, %17, %18\n\tv_dot2_f32_bf16 %10, %15, %16, %18\n\tv_dot2_f32_bf16 %11, %15, %17, %18\n\t"
        "v_cvt_pk_bf16_f32 %0, %4, %5\n\tv_cvt_pk_bf16_f32 %1, %6, %7\n\tv_cvt_pk_bf16_f32 %2, %8, %9\n\tv_cvt_pk_bf16_f32 %3, %10, %11"
        : "=&v"(o[0]), "=&v"(o[1]), "=&v"(o[2]), "=&v"(o[3]), "=&v"(t0), "=&v"(t1), "=&v"(t2), "=&v"(t3), "=&v"(t4), "=&v"(t5), "=&v"(t6), "=&v"(t7)
        : "v"(x[0]), "v"(x[1]), "v"(x[2]), "v"(x[3]), "s"(0x00003f80u), "s"(0x3f800000u), "v"(c));
    return o;
}
template <int N> struct IC { static constexpr int v = N; };
template <int B, int E, class F> DI void sfor(F&& f) { if constexpr (B < E) { f(IC<B>{}); sfor<B + 1, E>(f); } }
template <int N> DI void waitv() { asm volatile("s_waitcnt vmcnt(%0)" :: "i"(N) : "memory"); }
template <int N> DI void waitv_bar() { if (N == 0) __builtin_amdgcn_s_waitcnt(0x0F70); asm volatile("s_waitcnt vmcnt(%0) lgkmcnt(0)\n\ts_barrier" :: "i"(N) : "memory"); }
template <int N> DI void waitv_barx() { asm volatile("s_waitcnt vmcnt(%0)\n\ts_barrier" :: "i"(N) : "memory"); }
template <int N> DI void waitv_bar2() { asm volatile("s_waitcnt vmcnt(%0) lgkmcnt(2)\n\ts_barrier" :: "i"(N) : "memory"); }
#define BAR() asm volatile("s_waitcnt lgkmcnt(0)\n\ts_barrier" ::: "memory")
#define FENCE() asm volatile("" ::: "memory")
typedef __amdgpu_buffer_rsrc_t rsrc_t;
DI u32x4 ld16_dev(rsrc_t rs, unsigned voff, unsigned soff) { return U4(__builtin_amdgcn_raw_buffer_load_b128(rs, voff, soff, 16)); }
DI u32x2 ld8(rsrc_t rs, unsigned voff, unsigned soff) { return __builtin_bit_cast(u32x2, __builtin_amdgcn_raw_buffer_load_b64(rs, voff, soff, 0)); }
DI u32x2 ld8_dev(rsrc_t rs, unsigned voff, unsigned soff) { return __builtin_bit_cast(u32x2, __builtin_amdgcn_raw_buffer_load_b64(rs, voff, soff, 16)); }
DI void st16_dev(u32x4 v, rsrc_t rs, unsigned voff, unsigned soff) { __builtin_amdgcn_raw_buffer_store_b128(v, rs, voff + soff, 0, 16); }
DI void st8_dev(u32x2 v, rsrc_t rs, unsigned voff, unsigned soff) { __builtin_amdgcn_raw_buffer_store_b64(v, rs, voff, soff, 16); }
DI void set_flag(char* ws, unsigned fo, u64 tagv) {
    asm volatile("s_mov_b64 exec, 1\n\tglobal_store_dwordx2 %0, %1, %2 sc1\n\ts_mov_b64 exec, -1" :: "v"(fo), "v"(tagv), "s"(ws) : "memory");
}
DI u64 peek_flag(char* ws, unsigned fo) { return __hip_atomic_load((GAS u64*)(ws + fo), __ATOMIC_RELAXED, __HIP_MEMORY_SCOPE_AGENT); }
DI void peek_lds(char* ws, unsigned fo, unsigned dst) {
    asm volatile("s_mov_b32 m0, %2\n\ts_nop 0\n\tglobal_load_lds_dword %0, %1 sc1\n\ts_add_u32 m0, %2, 252\n\ts_nop 0\n\tglobal_load_lds_dword %0, %1 offset:4 sc1" :: "v"(fo), "s"(ws), "s"(dst) : "memory", "scc");
}
DI u64 peeked(lds_ptr p) { return (u64)*(LAS unsigned*)(p + 256) << 32 | *(LAS unsigned*)p; }
DI void poll_flag(char* ws, unsigned fo, u64 tag) {
#pragma unroll 1
    for (int spin = 0; spin < (1 << 21); ++spin) {
        if (wave_all(peek_flag(ws, fo) == tag)) break;
        __builtin_amdgcn_s_sleep(1);
    }
    FENCE();
}
#define SB() __builtin_amdgcn_sched_barrier(0)
#define PINS(a) asm volatile("" : "+s"(a))
#define PIN(a) asm volatile("" : "+v"(a))
#define PIN2(a, b) asm volatile("" : "+v"(a), "+v"(b))
DI lds_ptr opq(lds_ptr p) { unsigned u = (unsigned)(uintptr_t)p; asm volatile("" : "+v"(u)); return (lds_ptr)(uintptr_t)u; }

constexpr int GW = 7, XN = 5;
DI constexpr int d_prio(int n) { return n < 0 ? -1 : n < 27 ? 3 - n / 7 : n < 64 ? 3 - (n - 27) / 10 : n < 84 ? 3 - (n - 64) / 5 : n < 112 ? 3 - (n - 84) / 7 : 0; }
DI constexpr int wd_at(int n) {
    constexpr int t[28] = {32, 34, 37, 39, 42, 65, 69, 72, 75, 78, 81, 83, 44, 47, 49, 52, 54, 57, 59, 61, 63, 86, 88, 90, 92, 94, 97, 99};
    for (int i = 0; i < 28; ++i) if (t[i] == n) return i;
    return -1;
}
DI constexpr int phys(int t) { return ((t % 9) + 2) % 9 * 16384; }

__global__ NOPK __launch_bounds__(512, 2) void block_fwd(const char* x, const char* wqkv, const char* wo, const char* wg, const char* wu, const char* wd, char* out, char* ws) {
    extern __shared__ __attribute__((aligned(16))) char smem[];
    lds_ptr lds = (lds_ptr)smem;
    const int tid = __builtin_amdgcn_workitem_id_x(), lane = tid & 63, w = __builtin_amdgcn_readfirstlane(tid >> 6);
    const int bid = __builtin_amdgcn_workgroup_id_x(), xcd = bid & 7, loc = bid >> 3;
    const int r = lane & 31, hf = lane >> 5, q4 = (lane & 15) >> 2, g1 = (lane >> 4) & 1, p4 = lane & 3;
    const unsigned lds0 = (unsigned)(uintptr_t)smem;
    const unsigned l16 = lane * 16;
    const int sp3 = (loc + 8) >> 5;
    const int atb = (xcd >> 1) * 8 + (loc & 7) + sp3 * (xcd & 1) * 4, acg = (loc >> 3) + (1 - sp3) * (xcd & 1) * 4;
    const int row8 = lane >> 3;
    const int rot8 = ((loc & 7) << sp3) + sp3 * (xcd & 1), rot = rot8 == 7 ? 0 : rot8;
    if (loc < 28) {
        const unsigned xo = (unsigned)((atb * 64 + (tid >> 3)) * 896 + (((tid ^ (tid >> 4)) & 7) * 16));
        const unsigned xd0 = lds0 + 1024 + (w >> 2) * 28672 + (w & 3) * 1024;
        unsigned xd = xd0 + (7 - rot) * 4096;
        const unsigned w0 = (unsigned)((rot * 64 + (tid >> 3)) * 2688 + acg * 128 + (((tid & 7) ^ ((tid >> 2) & 4)) * 16));
        asm volatile("s_cmp_eq_u32 %0, %3\n\ts_cselect_b32 %0, %2, %0\n\ts_mov_b32 m0, %0\n\ts_add_u32 %0, %0, 0x1000\n\tglobal_load_lds_dwordx4 %1, %4\n\t"
#define XP(o) "s_cmp_eq_u32 %0, %3\n\ts_cselect_b32 %0, %2, %0\n\ts_sub_u32 m0, %0, " #o "\n\ts_add_u32 %0, %0, 0x1000\n\tglobal_load_lds_dwordx4 %1, %4 offset:" #o "\n\t"
                     XP(128) XP(256) XP(384) XP(512) XP(640) XP(768)
                     "s_add_u32 m0, %6, 8192-896\n\ts_barrier\n\tglobal_load_lds_dwordx4 %5, %7 offset:896\n\t"
                     "s_add_u32 m0, %6, 16384-1792\n\ts_nop 0\n\tglobal_load_lds_dwordx4 %5, %7 offset:1792\n\t"
                     "s_mov_b32 m0, %6\n\ts_nop 0\n\tglobal_load_lds_dwordx4 %5, %7"
                     : "+&s"(xd) : "v"(xo), "s"(xd0), "s"(xd0 + 7 * 4096), "s"(x), "v"(w0), "s"(lds0 + 58368 + w * 1024), "s"(wqkv) : "memory", "scc");
    }
    SB();
    const rsrc_t rws = __builtin_amdgcn_make_buffer_rsrc((void*)ws, 0, WS_END, 0x00020000);
    u64 epoch, tagv;
    unsigned fl1;
    const unsigned ones = 0x3f803f80u;
    const bool ffn = loc < 28;
    const int l7 = loc / 7, m7 = loc % 7;
    const int tb = xcd * 4 + l7;
    const int tt = w & 1, ct = (w >> 1) & 1, kh = w >> 2;
    const unsigned swz = (unsigned)(((lane & 7) ^ (((row8 >> 1) & 1) << 2)) * 16);

    if (__builtin_expect(ffn, 1)) {
        constexpr int A_W = 58368, A_WS = 24576;
#define A_KC(c) (rot + (c) >= 7 ? rot + (c) - 7 : rot + (c))
        const unsigned wvo = (unsigned)(row8 * 2688) + swz;
        const char* wsrc = wqkv + acg * 128 + (size_t)w * 8 * 2688;
        const unsigned wdst = lds0 + A_W + w * 1024;
#define A_PIECE(c, i) glds(wvo, wsrc + A_KC(c) * (64 * 2688) + (i) * 896, wdst + ((c) & 3) * A_WS + (i) * 8192)
#define A_GROUP(c) do { A_PIECE(c, 1); A_PIECE(c, 2); A_PIECE(c, 0); } while (0)
        epoch = peek_flag(ws, FL_EPOCH * 128); FENCE(); fl1 = (FL_KV + atb * 8 + acg) * 128;
        lds_ptr xb[4], wb1, wb1h;
#pragma unroll
        for (int j = 0; j < 4; ++j) xb[j] = lds + 1024 + tt * 28672 + (r >> 3) * 1024 + (r & 7) * 128 + (((2 * j + hf) ^ ((r >> 1) & 7)) * 16);
        wb1 = opq(lds + A_W + (kh ? 0 : 8192) + (8 * hf + q4) * 128 + ((ct ^ (q4 >> 1)) * 64) + g1 * 32 + p4 * 8);
        wb1h = opq(wb1 + 2 * A_WS);
        unsigned go1[2], go2[2]; int gi[2];
#pragma unroll
        for (int p = 0; p < 2; ++p) {
            const int G = acg * 8 + ct * 4 + 2 * p + hf, hp = G / 7, i = G - hp * 7;
            const unsigned hs = kh ? 131072u : 262144u;
            const unsigned to = (kh ? WS_QF + (atb * 2 + tt) * 2048 : WS_KV + (atb * 2 + tt) * 4096) + r * 16;
            go1[p] = (unsigned)(2 * hp + (i > 3)) * hs + (unsigned)(i > 3 ? i - 3 : i) * 512 + to;
            go2[p] = i == 3 ? (unsigned)(2 * hp + 1) * hs + to : OOB;
            gi[p] = i;
        }
        const int vcol = acg * 64 + ct * 32 + r, vh = vcol / 28, vd = vcol - vh * 28;
        unsigned vo = WS_KV + (atb * 2 + tt) * 4096 + (unsigned)vh * 262144 + 2048 + hf * 512 + vd * 16;
        PIN2(go1[0], go1[1]); PIN2(go2[0], go2[1]); PIN(vo);
        lds_ptr xw = opq(lds + 1024 + kh * 28672 + (w & 3) * 1024 + l16);
        waitv<4>();
        u32x4 xv[7];
#pragma unroll
        for (int c = 0; c < 7; ++c) xv[c] = *(LAS u32x4*)(xw + c * 4096);
        SB();
        f32x4 sm = {};
#pragma unroll
        for (int c = 0; c < 7; ++c)
#pragma unroll
            for (int j = 0; j < 4; ++j) sm[j] = dot2(xv[c][j], ones, sm[j]);
        float nm = sum8((sm[0] + sm[1]) + (sm[2] + sm[3])) * (-1.f / D);
        PIN(nm);
#pragma unroll
        for (int c = 0; c < 7; ++c) { xv[c] = shift8(xv[c], nm); *(LAS u32x4*)(xw + c * 4096) = xv[c]; }
        A_GROUP(1);
        SB();
        LAS float* rst = (LAS float*)lds;
        sm = (f32x4){};
#pragma unroll
        for (int c = 0; c < 7; ++c)
#pragma unroll
            for (int j = 0; j < 4; ++j) sm[j] = dot2(xv[c][j], xv[c][j], sm[j]);
        rst[w * 8 + row8] = __builtin_amdgcn_rsqf(sum8((sm[0] + sm[1]) + (sm[2] + sm[3])) * (1.f / D) + EPS);
        A_GROUP(2);
        bf16x8 fx[4], f1[4], f2[4];
#define A_WAIT(c) do { if ((c) == 0) waitv_bar<6>(); else if ((c) == 1) waitv_barx<6>(); else if ((c) < 6) waitv_barx<3>(); else waitv_barx<0>(); } while (0)
#define A_WP(c, ks) ((((c) & 2) ? wb1h : wb1) + ((c) & 1) * A_WS + (ks) * 2048)
        if (w < 4) {
            f32x16 ak = {}, av = {};
            f32x4 rv[4];
            float rs = 0.f;
#pragma unroll
            for (int n = 0; n < 30; ++n) {
                const int c = n >> 2, ks = n & 3, km = (n - 2) & 3;
                if (n < 28 && ks == 0) A_WAIT(c);
                if (n >= 2) { ak = MFMA(f1[km], fx[km], ak); PIN(ak); } SB();
                if (n < 28) { fx[ks] = lds8(xb[ks] + c * 4096); f1[ks] = cat(trd(A_WP(c, ks)), trd(A_WP(c, ks) + 512)); } SB();
                if (n >= 2) { av = MFMA(fx[km], f2[km], av); PIN(av); } SB();
                if (n < 28) f2[ks] = cat(trd(A_WP(c, ks) + 8192), trd(A_WP(c, ks) + 8192 + 512));
                if (c == 0 && ks < 3) A_PIECE(3, ks);
                if (c >= 2 && c < 5 && ks < 3) A_PIECE(c + 2, ks);
                if (n == 21) {
                    rs = rst[tt * 32 + r];
#pragma unroll
                    for (int g = 0; g < 4; ++g) rv[g] = *(LAS f32x4*)(rst + tt * 32 + 8 * g + 4 * hf);
                }
                if (n == 24) { PIN(epoch); tagv = epoch + 1; PIN(tagv); }
                SB();
            }
#pragma unroll
            for (int i = 0; i < 16; ++i) { ak[i] *= rs; av[i] *= rv[i >> 2][i & 3]; }
#pragma unroll
            for (int p = 0; p < 2; ++p) {
                const u32x4 f = pack8(ak, p);
                const auto s0 = __builtin_amdgcn_permlane32_swap(f[0], f[2], false, false), s1 = __builtin_amdgcn_permlane32_swap(f[1], f[3], false, false);
                const u32x4 c = {s0[0], s1[0], s0[1], s1[1]};
                st16_dev(c, rws, go1[p], 0); st16_dev(c, rws, go2[p], 0);
            }
            st16_dev(pack8(av, 0), rws, vo, 0); st16_dev(pack8(av, 1), rws, vo + 1024, 0);
        } else {
            f32x16 aq = {};
            __builtin_amdgcn_s_setprio(1);
#pragma unroll
            for (int n = 0; n < 30; ++n) {
                const int c = n >> 2, ks = n & 3, km = (n - 2) & 3;
                if (n < 28 && ks == 0) A_WAIT(c);
                if (n == 24) { PIN(epoch); tagv = epoch + 1; PIN(tagv); }
                if (n >= 2) { aq = MFMA(f1[km], fx[km], aq); PIN(aq); } SB();
                if (n < 28) { fx[ks] = lds8(xb[ks] + c * 4096); f1[ks] = cat(trd(A_WP(c, ks)), trd(A_WP(c, ks) + 512)); }
                if (c == 0 && ks < 3) A_PIECE(3, ks);
                if (c >= 2 && c < 5 && ks < 3) A_PIECE(c + 2, ks);
                if (n == 24) {
                    const unsigned pv = vd == 0 ? ones : 0u, po = vd < 4 ? vo + 448 : OOB;
                    st16_dev((u32x4){pv, pv, pv, pv}, rws, po, 0); st16_dev((u32x4){pv, pv, pv, pv}, rws, po + 1024, 0);
                }
                SB();
            }
            __builtin_amdgcn_s_setprio(0);
            const float QS = rst[tt * 32 + r] * (0.18898223650461362f * 1.4426950408889634f);
#pragma unroll
            for (int i = 0; i < 16; ++i) aq[i] *= QS;
#pragma unroll
            for (int p = 0; p < 2; ++p) {
                const u32x4 f = pack8(aq, p);
                const auto s0 = __builtin_amdgcn_permlane32_swap(f[0], f[2], false, false), s1 = __builtin_amdgcn_permlane32_swap(f[1], f[3], false, false);
                const bool sp = gi[p] == 3;
                st16_dev((u32x4){s0[0], s1[0], sp ? 0u : s0[1], sp ? 0u : s1[1]}, rws, go1[p], 0);
                st16_dev((u32x4){0u, 0u, s0[1], s1[1]}, rws, go2[p], 0);
            }
        }
    } else {
        __builtin_amdgcn_s_sleep(8);
        epoch = peek_flag(ws, FL_EPOCH * 128); fl1 = (FL_RT + xcd * 4 + loc - 28) * 128;
        const int ri = xcd * 4 + loc - 28, nu = (w == 0 && ri < 16) ? 4 : 3;
        const unsigned vo = (unsigned)(row8 * 3584 + (lane & 7) * 16);
#pragma unroll 1
        for (int i = 0; i < nu; ++i) {
            const int u = i < 3 ? i * 256 + ri * 8 + w : 768 + ri;
            const size_t so = (size_t)(u / 28) * 16 * 3584 + (u % 28) * 128;
            const unsigned d_ = lds0 + w * 16384 + i * 4096;
            glds(vo, wg + so, d_); glds(vo, wg + so + 8 * 3584, d_ + 1024); glds(vo, wu + so, d_ + 2048); glds(vo, wu + so + 8 * 3584, d_ + 3072);
        }
#pragma unroll 1
        for (int i = 0; i < nu; ++i) {
            const int u = i < 3 ? i * 256 + ri * 8 + w : 768 + ri;
            waitv<8>();
            lds_ptr sg = lds + w * 16384 + i * 4096 + g1 * 2048 + (4 * hf + q4) * 128 + p4 * 8;
#pragma unroll
            for (int e = 0; e < 4; ++e) st16_dev(U4(cat(trd(sg + e * 32), trd(sg + e * 32 + 1024))), rws, l16, WS_WGU + (unsigned)(((u % 28) * 4 + e) * 28 + u / 28) * 1024);
            FENCE();
        }
        tagv = epoch + 1;
    }
    const int head = xcd * 2 + (loc & 1), rb = loc >> 1, qt = w & 3;
    const char* kvs = ws + WS_KV + (size_t)head * 262144 + w * 2048;
    const char* qs = ws + WS_QF + (size_t)(head * 64 + rb * 4 + qt) * 2048 + kh * 1024;
    unsigned kvd = lds0 + w * 2048, qd = lds0 + 147456 + qt * 2048 + kh * 1024;
    unsigned pf1 = (FL_KV + (lane >> 1) * 8 + (head * 28 + (lane & 1) * 27) / 64) * 128;
    PINS(kvs); PINS(qs); PINS(kvd); PINS(qd); PIN2(pf1, fl1);
    waitv_bar<0>();
    if (w == 0) set_flag(ws, fl1, tagv);
    const u64 tag = ((u64)(unsigned)__builtin_amdgcn_readfirstlane((unsigned)(tagv >> 32)) << 32 | (u64)(unsigned)__builtin_amdgcn_readfirstlane((unsigned)tagv));

    const int drb = (xcd >> 1) * 4 + l7, dfg = (xcd & 1) * 7 + m7;
    bf16x8 wf[28];
    const unsigned p16 = hf * 1024 + g1 * 512 + (lane & 15) * 16;
    u32x2 xa, xb;
    f32x4 csg = {}, csu = {};
    f32x16 acc = {};
    const u32x4 one4 = {ones, ones, ones, ones};
    const unsigned hi_ = (unsigned)((tb * 2 + tt) * 28 + m7 * 4 + ct * 2 + kh), ofo = (unsigned)(tt * 32768 + kh * 16384 + ct * 4096);
    unsigned ho = WS_HF + hi_ * 1024 + l16, so = hf ? OOB : WS_HS + hi_ * 256 + r * 8;
    const char* os = ws + WS_OF + (size_t)tb * 65536 + ofo;
    unsigned od = lds0 + 65536 + ofo;
    unsigned pf2 = (FL_O + (tb >> 1) * 16 + kh * 8 + ct * 2 + ((lane >> 1) & 1) + (lane & 4)) * 128;
    unsigned go = ffn ? WS_GF + (unsigned)(drb * 4 * 112 + dfg * 8 + w) * 1024 + g1 * 512 + (hf * 16 + (lane & 15)) * 16 : OOB;
    unsigned fo3 = (FL_H + tb * 8 + m7) * 128;
    {
        lds_ptr ap = opq(lds + kh * 8192 + l16), apB = opq(lds + 65536 + kh * 8192 + l16), apC = opq(lds + 131072 + kh * 8192 + l16);
        const unsigned lv = kh * 8192 + (l16 ^ (kh * 256));
        lds_ptr vp = opq(lds + lv), vpB = opq(lds + 65536 + lv), vpC = opq(lds + 131072 + lv);
#define AP(off) ((off) < 65536 ? ap + (off) : (off) < 131072 ? apB + ((off) - 65536) : apC + ((off) - 131072))
#define VP(off) ((off) < 65536 ? vp + (off) : (off) < 131072 ? vpB + ((off) - 65536) : vpC + ((off) - 131072))
#define KV_ISSUE(t) glds2_dev(l16, kvs + (t) * 16384, kvd + phys(t))
        unsigned wgo = ffn ? WS_WGU + (unsigned)(dfg * 8 + w) * 28672 : OOB;
        const int ri = umin(xcd * 4 + loc - 28, 27);
        const char* wos = (ffn ? wo + m7 * 128 : wd + (ri % 7) * 128 + (size_t)(ri / 7) * 448 * 896) + (size_t)w * 56 * 896;
        unsigned wov = (unsigned)(row8 * 896) + swz;
        const rsrc_t rx = __builtin_amdgcn_make_buffer_rsrc((void*)x, 0, 2048 * 448 * 2, 0x00020000);
        unsigned xo_ = (unsigned)(((tb * 2 + tt) * 32 + r) * 896 + m7 * 128 + ct * 64 + kh * 32 + hf * 8);
        unsigned rto = (FL_RT + w * 4 + (lane & 3)) * 128;
        lds_ptr qp = opq(lds + 147456 + qt * 2048 + l16);
        PINS(wgo); PINS(wos); PIN2(wov, xo_); PIN(rto); PIN2(ho, so); PIN2(pf2, fo3); PIN(go);
        lds_ptr xw = opq(lds + 147456 + w * 2048 + l16), xr = opq(lds + 147456 + (w ^ 4) * 2048 + l16);
        lds_ptr lw = opq(lds + 58368 + w * 256 + lane * 4), lr = opq(lds + 58368 + qt * 256 + 128 + r * 4);
        lds_ptr bw = opq(lds + 60416 + w * 4), br = opq(lds + 60416 + (lane & 7) * 4);
        unsigned oo = WS_OF + (unsigned)((rb * 4 + qt) * 32 + head * 2 + kh) * 1024 + l16;
        unsigned fo2 = (FL_O + rb * 16 + head) * 128;
        PIN2(oo, fo2);
        if (w == 1) poll_flag(ws, pf1, tag);
        BAR();
        glds_dev(l16, qs, qd);
        KV_ISSUE(0); KV_ISSUE(1);
        asm volatile("s_barrier" ::: "memory");
        KV_ISSUE(2); KV_ISSUE(3);
        waitv_bar<4>();
        const bf16x8 q0 = lds8(qp), q1 = lds8(qp + 1024);
        f32x16 o = {}, sa = {}, sb;
        bf16x8 k0 = lds8(ap + phys(0)), k1 = lds8(ap + phys(0) + 1024), v0 = lds8(vp + phys(0) + 2048), v1 = {};
        u32x4 p0, p1 = {};
        sa = MFMA(k0, q0, sa); sa = MFMA(k1, q1, sa);
        k0 = lds8(ap + phys(0) + 4096); k1 = lds8(ap + phys(0) + 5120);
        PIN(sa); SB();
        u64 rtf = 0;
#define EXP4(S, o4) _Pragma("unroll") for (int i_ = o4; i_ < o4 + 4; ++i_) S[i_] = __builtin_amdgcn_exp2f(S[i_]);
#define WLD(s) wf[s] = BF8(ld16_dev(rws, p16 + ((s) & 1) * 256 + ((s) & 2) * 1024, wgo + ((s) >> 2) * 4096))
#define SIDE(t, g) do { \
            if ((t) == 0 && (g) == 0) KV_ISSUE(4); if ((t) == 0 && (g) == 2) KV_ISSUE(5); if ((t) == 1 && (g) == 0) KV_ISSUE(6); if ((t) == 1 && (g) == 2) KV_ISSUE(7); \
            if ((t) == 2 && (g) == 0) KV_ISSUE(8); \
            if ((t) == 1 && (g) == 1) peek_lds(ws, rto, lds0 + 155648 + w * 512); if ((t) == 2 && (g) == 2) { waitv<5>(); rtf = peeked(lds + 155648 + w * 512 + lane * 4); } \
            if ((t) == 3 && (g) < 3) KV_ISSUE(9 + (g)); \
            if (((t) == 2 && (g) == 1) || ((t) == 14 && (g) == 2)) st16_dev((u32x4){~0u, ~0u, ~0u, ~0u}, rws, go + ((t) == 2 ? 2 : 3) * 114688, 0);     \
            if ((t) == 7 && (g) == 0) KV_ISSUE(12); if ((t) == 7 && (g) == 2) KV_ISSUE(13); if ((t) == 8 && (g) == 0) KV_ISSUE(14); if ((t) == 8 && (g) == 2) KV_ISSUE(15); \
            if ((t) >= 4 && (t) <= 6) WLD(((t) - 4) * 4 + (g)); \
            if (((t) == 7 || (t) == 8) && ((g) & 1)) WLD(12 + ((t) - 7) * 2 + ((g) >> 1)); \
            if ((t) >= 9 && (t) <= 11) WLD(((t) - 5) * 4 + (g)); \
            if ((t) == 12 || ((t) == 13 && (g) < 3)) glds(wov, wos + (((t) - 12) * 4 + (g)) * 8 * 896, lds0 + (w * 7 + ((t) - 12) * 4 + (g)) * 1024); \
            if ((t) == 13 && (g) == 3) { xa = ld8(rx, xo_, 0); xb = ld8(rx, xo_, 16); } \
            if ((t) == 14 && (g) == 0) { if (tid < 32) *(LAS u32x4*)(lds + 57344 + tid * 16) = (u32x4){0, 0, 0, 0}; } \
        } while (0)
#define TOFF(i) (phys((i) >> 1) + ((i) & 1) * 4096)
#define TILE(i, SC, SN) do { \
            __builtin_amdgcn_s_setprio(((i) & 1) ? 1 : 3); \
            if ((i) < 31) { f32x16 z_ = {}; SN = MFMA(k0, q0, z_); PIN(SN); } if ((i) < 30) k0 = lds8(AP(TOFF((i) + 2))); SB(); \
            EXP4(SC, 0); PIN(SC); SB(); \
            o = MFMA(v1, BF8(p1), o); PIN(o); v1 = lds8(VP(TOFF(i) + 3072)); SIDE((i) >> 1, ((i) & 1) * 2); SB(); \
            EXP4(SC, 4); p0 = pack8(SC, 0); PIN(p0); SB(); \
            __builtin_amdgcn_s_setprio(((i) & 1) ? 0 : 2); \
            if ((i) < 31) { SN = MFMA(k1, q1, SN); PIN(SN); } if ((i) < 30) k1 = lds8(AP(TOFF((i) + 2) + 1024)); SB(); \
            EXP4(SC, 8); PIN(SC); SB(); \
            o = MFMA(v0, BF8(p0), o); PIN(o); if ((i) < 31) v0 = lds8(VP(TOFF((i) + 1) + 2048)); SIDE((i) >> 1, ((i) & 1) * 2 + 1); SB(); \
            EXP4(SC, 12); p1 = pack8(SC, 1); PIN(p1); SB(); } while (0)
#define STEP(t) do { TILE(2 * (t), sa, sb); TILE(2 * (t) + 1, sb, sa); } while (0)
        STEP(0);
        waitv_barx<4>();
        STEP(1); STEP(2);
        if (__builtin_expect(!wave_all(rtf == tag), 0)) poll_flag(ws, rto, tag);
        waitv_bar2<3>();
        STEP(3); STEP(4); STEP(5); STEP(6);
        waitv_bar2<12>();
        STEP(7); STEP(8); STEP(9); STEP(10);
        waitv_bar2<9>();
        STEP(11); STEP(12); STEP(13); STEP(14); STEP(15);
        o = MFMA(v1, BF8(p1), o);
        u32x4 ov;
        {
            *(LAS f32x4*)xw = (f32x4){o[8], o[9], o[10], o[11]}; *(LAS f32x4*)(xw + 1024) = (f32x4){o[12], o[13], o[14], o[15]};
            const float l = kh ? o[4] : o[12];
            *(LAS float*)lw = l;
#define AMX(a, b, c) __builtin_fmaxf(__builtin_fmaxf(__builtin_fabsf(a), __builtin_fabsf(b)), __builtin_fabsf(c))
            const float mx_ = AMX(AMX(AMX(o[0], o[1], o[2]), AMX(o[3], o[4], o[5]), AMX(o[6], o[7], o[8])), AMX(AMX(o[9], o[10], o[11]), AMX(o[12], o[13], o[14]), o[15]), 0.f);
            const bool ok = (mx_ < 0x1p100f) & ((hf ? l : 1.f) > 0x1p-40f);
            *(LAS unsigned*)bw = wave_all(ok) ? 0u : 1u;
            BAR();
            const f32x4 a = *(LAS f32x4*)xr, c = *(LAS f32x4*)(xr + 1024);
            const float inv = __builtin_amdgcn_rcpf(*(LAS float*)lr + *(LAS float*)(lr + 1024));
            const unsigned bad = *(LAS unsigned*)br;
            const float iz = (kh && hf) ? 0.f : inv;
            ov = (u32x4){cvtpk((o[0] + a[0]) * inv, (o[1] + a[1]) * inv), cvtpk((o[2] + a[2]) * inv, (o[3] + a[3]) * inv),
                         cvtpk((o[4] + c[0]) * iz, (o[5] + c[1]) * iz), cvtpk((o[6] + c[2]) * iz, (o[7] + c[3]) * iz)};
            if (__builtin_expect(wave_any(bad != 0), 0)) {
                f32x16 of = {};
                float m = -0x1p126f;
#pragma unroll 1
                for (int t = 0; t < 16; ++t) {
                    BAR();
                    glds2_dev(l16, kvs + t * 16384, lds0 + 65536 + w * 2048);
                    waitv_bar<0>();
#pragma unroll 1
                    for (int u = 0; u < 2; ++u) {
                        f32x16 s = {};
                        s = MFMA(lds8(apB + u * 4096), q0, s); s = MFMA(lds8(apB + u * 4096 + 1024), q1, s);
                        float mx = s[0];
#pragma unroll
                        for (int i = 1; i < 16; ++i) mx = __builtin_fmaxf(mx, s[i]);
                        mx = __builtin_fmaxf(swap_max(mx), m);
                        const float alpha = __builtin_amdgcn_exp2f(m - mx);
                        m = mx;
#pragma unroll
                        for (int i = 0; i < 16; ++i) { s[i] = __builtin_amdgcn_exp2f(s[i] - mx); of[i] *= alpha; }
                        of = MFMA(lds8(vpB + u * 4096 + 2048), BF8(pack8(s, 0)), of);
                        of = MFMA(lds8(vpB + u * 4096 + 3072), BF8(pack8(s, 1)), of);
                    }
                }
                *(LAS f32x4*)xw = (f32x4){of[8], of[9], of[10], of[11]}; *(LAS f32x4*)(xw + 1024) = (f32x4){of[12], of[13], of[14], of[15]};
                *(LAS float*)lw = hf ? (kh ? of[4] : of[12]) : m;
                BAR();
                const f32x4 a2 = *(LAS f32x4*)xr, c2 = *(LAS f32x4*)(xr + 1024);
                const float m0 = *(LAS float*)(lr - 128), m1 = *(LAS float*)(lr + 1024 - 128), mm = __builtin_fmaxf(m0, m1);
                const float e0 = __builtin_amdgcn_exp2f(m0 - mm), e1 = __builtin_amdgcn_exp2f(m1 - mm);
                const float iv = __builtin_amdgcn_rcpf(*(LAS float*)lr * e0 + *(LAS float*)(lr + 1024) * e1);
                const float im = (kh ? e1 : e0) * iv, io = (kh ? e0 : e1) * iv;
                const bool z = kh && hf;
                ov = (u32x4){cvtpk(of[0] * im + a2[0] * io, of[1] * im + a2[1] * io), cvtpk(of[2] * im + a2[2] * io, of[3] * im + a2[3] * io),
                             z ? 0u : cvtpk(of[4] * im + c2[0] * io, of[5] * im + c2[1] * io), z ? 0u : cvtpk(of[6] * im + c2[2] * io, of[7] * im + c2[3] * io)};
            }
        }
        st16_dev(ov, rws, oo, 0);
        FENCE();
        PIN2(xa, xb);
#pragma unroll
        for (int s = 27; s >= 0; --s) PIN(wf[s]);
#pragma unroll
        for (int s = 0; s < 16; s += 2) { csg = MFMA16(wf[s], BF8(one4), csg); csu = MFMA16(wf[s + 1], BF8(one4), csu); }
        acc[0] = bflo(xa[0]); acc[1] = bfhi(xa[0]); acc[2] = bflo(xa[1]); acc[3] = bfhi(xa[1]); acc[4] = bflo(xb[0]); acc[5] = bfhi(xb[0]); acc[6] = bflo(xb[1]); acc[7] = bfhi(xb[1]);
        PIN(acc); PIN2(ho, so); PIN2(pf2, fo3); PINS(os); PINS(od); PIN2(csg, csu);
        waitv_bar<0>();
        if (w == 0) set_flag(ws, fo2, tagv);
    }
    if (__builtin_expect(!ffn, 0)) {
        const int ri = xcd * 4 + loc - 28;
        if (ri < 28) {
#pragma unroll 1
            for (int u = w; u < 28; u += 8) {
                lds_ptr sd = lds + u * 2048 + (4 * hf + q4) * 128 + g1 * 32 + p4 * 8;
#pragma unroll
                for (int e = 0; e < 2; ++e) { lds_ptr p_ = sd + ((e ^ (q4 >> 1)) * 64);
                    st16_dev(U4(cat(trd(p_), trd(p_ + 1024))), rws, l16, WS_WD + (unsigned)(((ri % 7) * 112 + (ri / 7) * 28 + u) * 2 + e) * 1024); }
            }
        }
        waitv_bar<0>();
        if (w == 0) set_flag(ws, (FL_RD + ri) * 128, tagv);
        return;
    }

    const int erb = tb >> 1;
    const char* hs = ws + WS_HF + (size_t)drb * 114688 + w * 1024;
    const char* hss = ws + WS_HS + (size_t)((drb * 4 + (w & 3)) * 28 + kh * 14) * 256;
    unsigned pf3, fo4 = (FL_G + drb * 64 + dfg) * 128;
    {
        lds_ptr wb = lds + kh * (8 * 28 * 128) + (4 * hf + q4) * 128 + ((ct ^ (q4 >> 1)) * 64) + (g1 ^ kh) * 32 + p4 * 8;
        bf16x8 wr[16];
#pragma unroll
        for (int s = 0; s < 12; ++s) { const int k0 = ((s >> 1) * 28 + (s & 1) * 16) * 128; wr[s] = cat(trd(wb + k0), trd(wb + k0 + 1024)); }
        lds_ptr cw = opq(lds + 131072 + w * 2048 + l16), cr = opq(lds + 131072 + (w ^ 4) * 2048 + l16);
        lds_ptr ob = opq(lds + 65536 + tt * 32768 + kh * 16384 + l16);
        pf3 = (FL_H + drb * 16 + (lane & 8) + umin(lane & 7, 6)) * 128;
        PIN2(pf3, fo4); PINS(hs); PINS(hss);
        poll_flag(ws, pf2, tag);
        glds4_dev(l16, os, od);
        asm volatile("s_barrier" ::: "memory");
        glds4_dev(l16, os + 8192, od + 8192);
        bf16x8 of[8];
        waitv_bar<4>();
#pragma unroll
        for (int s = 0; s < 8; ++s) of[s] = lds8(ob + s * 1024);
        SB();
#pragma unroll
        for (int s = 0; s < 8; ++s) {
            acc = MFMA(wr[s], of[s], acc); PIN(acc); SB();
            if (s >= 2 && s < 6) { const int k0 = (((s + 10) >> 1) * 28 + (s & 1) * 16) * 128; wr[s + 10] = cat(trd(wb + k0), trd(wb + k0 + 1024)); SB(); }
        }
        waitv_barx<0>();
#pragma unroll
        for (int s = 0; s < 8; ++s) of[s] = lds8(ob + (8 + s) * 1024);
        SB();
#pragma unroll
        for (int s = 0; s < 8; ++s) acc = MFMA(wr[8 + s], of[s], acc);
        *(LAS f32x4*)cw = (f32x4){acc[8], acc[9], acc[10], acc[11]}; *(LAS f32x4*)(cw + 1024) = (f32x4){acc[12], acc[13], acc[14], acc[15]};
        BAR();
        const f32x4 a = *(LAS f32x4*)cr + (f32x4){acc[0], acc[1], acc[2], acc[3]}, b = *(LAS f32x4*)(cr + 1024) + (f32x4){acc[4], acc[5], acc[6], acc[7]};
        const u32x4 hv = {cvtpk(a[0], a[1]), cvtpk(a[2], a[3]), cvtpk(b[0], b[1]), cvtpk(b[2], b[3])};
        st16_dev(hv, rws, ho, 0);
        FENCE(); SB();
        float s1 = 0.f, s2 = 0.f;
        { f32x4 q4_ = {}; acc_stats2(hv[0], hv[1], q4_); acc_stats2(hv[2], hv[3], q4_); s1 = swap_add(q4_[0] + q4_[1]); s2 = swap_add(q4_[2] + q4_[3]); }
        st8_dev((u32x2){f2u(s1), f2u(s2)}, rws, so, 0);
        FENCE(); PIN2(csg, csu); SB();
#pragma unroll
        for (int s = 16; s < 28; s += 2) { csg = MFMA16(wf[s], BF8(one4), csg); csu = MFMA16(wf[s + 1], BF8(one4), csu); }
        PIN2(csg, csu);
        waitv_bar<0>();
        if (w == 0) set_flag(ws, fo3, tagv);
        if (bid == 0 && w == 0) set_flag(ws, FL_EPOCH * 128, tagv);
    }

    const unsigned wdo = WS_WD + (unsigned)(m7 * 112 + w * 14) * 2048;
    bf16x8 wdf[28];
    bf16x8 gf[GW];
    const unsigned gxo = WS_GF + (unsigned)(erb * 4 * 112 + w * 14) * 1024 + (tb & 1) * 256;
    const unsigned pwd = hf * 2048 + g1 * 512 + (lane & 15) * 16;
#define G_LD(s) gf[(s) % GW] = BF8(ld16_dev(rws, p16, gxo + ((s) / 7) * 114688 + ((s) % 7) * 2048))
    u64 gfl = 0;
    const unsigned gfi = (FL_G + (erb * 4 + g1) * 16 + ((w * 14 + umin(lane & 15, 13)) >> 3)) * 128;
    {
        const unsigned hd = lds0 + w * 1024;
        const char* wds = ws + wdo;
        lds_ptr hb = opq(lds + p16), hb2 = opq(lds + 57344 + p16);
        lds_ptr wl = opq(lds + 114688 + w * 5120 + l16), wl2 = opq(lds + w * 7168 + l16);
        LAS f32x2* stp = (LAS f32x2*)opq(lds + 159744 + (lane & 15) * 8);
        if (w == 1) poll_flag(ws, pf3, tag);
        BAR();
#define H_PIECE(j) glds_dev(l16, hs + (j) * 8192, hd + (j) * 8192)
#define WDO(s) (((s) >> 2) * 4096 + (((s) >> 1) & 1) * 1024 + ((s) & 1) * 256)
#define WDLD(s) wdf[s] = BF8(ld16_dev(rws, pwd, wdo + WDO(s)))
#define HB(n) (((n) < 56 ? hb + ((n) & ~1) * 1024 : hb2 + (((n) & ~1) - 56) * 1024) + ((n) & 1) * 256)
        H_PIECE(0);
        asm volatile("s_barrier" ::: "memory");
        H_PIECE(1); H_PIECE(2); H_PIECE(3);
        constexpr int PF = 4;
        bf16x8 bf[PF + 1];
        f32x4 ag[2][2], au[2][2];
        f32x2 sp[7], sv[4];
        float mean[2] = {0.f, 0.f}, r2[2] = {0.f, 0.f}, hp = 0.f;
        u32x4 hv;
        lds_ptr pk = opq(lds + 155648 + w * 512 + lane * 4);
        u64 pv = 0;
        const unsigned pkd = lds0 + 155648 + w * 512;
        const unsigned rdo = (FL_RD + (w >> 1) * 7 + m7) * 128;
        waitv_barx<3>();
#pragma unroll
        for (int n = 0; n < PF; ++n) bf[n] = lds8(HB(n));
        SB();
        sfor<0, 125>([&](auto N_) __attribute__((always_inline)) {
            constexpr int n = decltype(N_)::v, p = n / 28, s = n % 28;
            if constexpr (n == 4) waitv_barx<3>();
            if constexpr (n == 12) waitv_barx<8>();
            if constexpr (n == 28) { waitv_barx<3>(); }
            if constexpr (n == 64) { waitv_barx<10>(); if (w == 0) set_flag(ws, fo4, tagv); }
            if constexpr (n == 85) { waitv_barx<8>(); if (w == 0) set_flag(ws, fo4 + 2048, tagv); }
            if constexpr (n == 32) { if (__builtin_expect(!wave_all(pv == tag), 0)) poll_flag(ws, rdo, tag); FENCE(); }
            if constexpr (d_prio(n) != d_prio(n - 1)) __builtin_amdgcn_s_setprio(d_prio(n));
            if constexpr (n < 112) {
                if constexpr (s < 2) { ag[p & 1][s] = (f32x4){}; au[p & 1][s] = (f32x4){}; }
                ag[p & 1][s & 1] = MFMA16(wf[s & ~1], bf[n % (PF + 1)], ag[p & 1][s & 1]); au[p & 1][s & 1] = MFMA16(wf[s | 1], bf[n % (PF + 1)], au[p & 1][s & 1]);
                PIN2(ag[p & 1][s & 1], au[p & 1][s & 1]);
            }
            SB();
            if constexpr (n + PF < 112) bf[(n + PF) % (PF + 1)] = lds8(HB(n + PF));
            if constexpr (n % 3 == 2 && n < 30) H_PIECE(4 + n / 3);
            if constexpr (n == 4) glds4_dev(l16, hss, lds0 + 114688 + w * 5120);
            if constexpr (n == 16) { waitv<4>();
#pragma unroll
                for (int j = 0; j < 7; ++j) sp[j] = *(LAS f32x2*)(lds + 114688 + w * 5120 + (hf * 7 + j) * 256 + r * 8);
            }
            if constexpr (n == 20) { const f32x2 t = ((sp[0] + sp[1]) + (sp[2] + sp[3])) + ((sp[4] + sp[5]) + sp[6]); stp[w * 32 + g1 * 16] = (f32x2){swap_add(t[0]), swap_add(t[1])}; }
            if constexpr (wd_at(n) >= 0) { constexpr int i = wd_at(n);
                if constexpr (i < 5) glds_dev(pwd, wds + WDO(i), lds0 + 114688 + w * 5120 + i * 1024);
                else if constexpr (i < 12) glds_dev(pwd, wds + WDO(i), lds0 + w * 7168 + (i - 5) * 1024);
                else WDLD(i); }
            if constexpr (n == 84) peek_lds(ws, gfi - g1 * 2048, pkd);
            if constexpr (n == 95) { waitv<5>(); pv = peeked(pk); }
            if constexpr (n == 99) { if (__builtin_expect(!wave_all(pv == tag), 0)) poll_flag(ws, gfi - g1 * 2048, tag); FENCE(); }
            if constexpr (n >= 101 && n < 101 + 2 * XN && n % 2 == 1) G_LD((n - 101) / 2);
            if constexpr (n == 106) gfl = peek_flag(ws, gfi);
            if constexpr (n == 28) pv = peeked(pk);
            if constexpr (n == 18) peek_lds(ws, rdo, pkd);
            if constexpr (p >= 1) {
                constexpr int e = (p - 1) & 1, tp = n == 100 ? 3 : p - 1;
                if constexpr ((s == 0 && p < 4) || n == 100) { sv[0] = stp[tp * 32]; sv[1] = stp[(tp + 4) * 32]; sv[2] = stp[tp * 32 + 16]; sv[3] = stp[(tp + 4) * 32 + 16]; }
                if constexpr (((s == 2 || s == 3) && p < 4) || n == 104 || n == 105) { constexpr int k = n & 1; const f32x2 t = sv[2 * k] + sv[2 * k + 1];
                    mean[k] = t[0] * (1.f / D); r2[k] = __builtin_amdgcn_rcpf(__builtin_fmaxf(t[1] * (1.f / D) - mean[k] * mean[k], 0.f) + EPS); PIN2(mean[k], r2[k]); }
                if constexpr (s >= 4 && s < 12) { constexpr int i = s - 4, k = i >> 2;
                    const float h0 = __builtin_fmaxf(ag[e][k][i & 3] - mean[k] * csg[i & 3], 0.f) * r2[k] * (au[e][k][i & 3] - mean[k] * csu[i & 3]);
                    if constexpr (i & 1) { hv[i >> 1] = p >= 3 ? umin(cvtpk(hp, h0), ~1u) : cvtpk(hp, h0); PIN(hv); } else { hp = h0; PIN(hp); } }
                if constexpr (s == 12) {
                    const auto s0 = __builtin_amdgcn_permlane32_swap(hv[0], hv[2], false, false), s1 = __builtin_amdgcn_permlane32_swap(hv[1], hv[3], false, false);
                    st16_dev((u32x4){s0[0], s1[0], s0[1], s1[1]}, rws, go + (p - 1) * 114688, 0); FENCE();
                }
            }
            SB();
        });
#pragma unroll
        for (int s = XN; s < 7; ++s) { G_LD(s); SB(); }
        FENCE();
#pragma unroll
        for (int s = 0; s < 12; ++s) wdf[s] = lds8(s < 5 ? wl + s * 1024 : wl2 + (s - 5) * 1024);
    }

    {
        const int etok = tid >> 4, eblk = tid & 15;
        const int et = erb * 4 + (etok >> 4), er = (tb & 1) * 16 + (etok & 15);
        const unsigned hro = (unsigned)((((eblk >> 2) & 3) * 64 + (eblk & 1) * 32 + er) * 16 + ((eblk >> 1) & 1) * 8) + WS_HF + (unsigned)(et * 28 + m7 * 4) * 1024;
        u32x2 hres[2];
        GAS u64* op = (GAS u64*)(out + (size_t)(et * 32 + er) * 896 + m7 * 128 + eblk * 8);
        lds_ptr rp = opq(lds + w * 8448 + (lane >> 4) * 528 + (lane & 15) * 16), rq = opq(lds + eblk * 528 + etok * 16);
        lds_ptr rp2 = opq(rp + 67584), rq2 = opq(rq + 67584);
        f32x4 c[4];
#define G_STEP(s) do { _Pragma("unroll") for (int j = 0; j < 4; ++j) { if ((s) % 7 == 0) c[j] = (f32x4){}; c[j] = MFMA16(wdf[4 * ((s) % 7) + j], gf[(s) % GW], c[j]); } PIN2(c[0], c[1]); PIN2(c[2], c[3]); } while (0)
#define G_PUT(q) do { _Pragma("unroll") for (int j = 0; j < 4; ++j) *(LAS f32x4*)(((q) < 2 ? rp : rp2) + j * 2112 + ((q) & 1) * 256) = c[j]; } while (0)
#define G_OUT(hh, RQ) do { f32x4 pa[8]; \
            _Pragma("unroll") for (int ww = 0; ww < 8; ++ww) pa[ww] = *(LAS f32x4*)(RQ + ww * 8448); SB(); \
            const f32x4 a = ((pa[0] + pa[1]) + (pa[2] + pa[3])) + ((pa[4] + pa[5]) + (pa[6] + pa[7])); \
            const u32x2 ov = {cvtpk(a[0] + bflo(hres[hh][0]), a[1] + bfhi(hres[hh][0])), cvtpk(a[2] + bflo(hres[hh][1]), a[3] + bfhi(hres[hh][1]))}; \
            __hip_atomic_store(op + (hh) * (64 * 896 / 8), __builtin_bit_cast(u64, ov), __ATOMIC_RELAXED, __HIP_MEMORY_SCOPE_AGENT); } while (0)
        unsigned seen = 0;
#pragma unroll
        for (int s = 0; s < 28; ++s) {
            if (s == 5) BAR();
            if (s == 7 || s == 14 || s == 21) G_PUT(s / 7 - 1);
            if (s == 21) { waitv<7>(); PIN2(hres[0], hres[1]); BAR(); G_OUT(0, rq); FENCE(); }
            SB();
            G_STEP(s);
            if (s >= 14) { const u32x4 u = U4(gf[s % GW]); seen = umax3(umax3(seen, u[0], u[1]), u[2], u[3]); PIN(seen); }
            SB();
            if (s == 0) { if (__builtin_expect(!wave_all(gfl == tag), 0)) poll_flag(ws, gfi, tag); FENCE(); }
            if (s + GW < 28) G_LD(s + GW);
            if (s == 1) { hres[0] = ld8_dev(rws, hro, 0); hres[1] = ld8_dev(rws, hro, 57344); FENCE(); }
        }
        if (w == 0) { set_flag(ws, fo4 + 4096, tagv); set_flag(ws, fo4 + 6144, tagv); }
        if (__builtin_expect(wave_any(seen == ~0u), 0)) {
            poll_flag(ws, gfi + 4096, tag);
#pragma unroll
            for (int s = 14; s < 14 + GW; ++s) G_LD(s);
#pragma unroll
            for (int s = 14; s < 28; ++s) { if (s == 21) G_PUT(2); G_STEP(s); SB(); if (s + GW < 28) G_LD(s + GW); SB(); }
        }
        G_PUT(3);
        BAR();
        G_OUT(1, rq2);
    }
}

extern "C" void kernel_launch(void* const* d_in, const int* in_sizes, int n_in, void* d_out, int out_size, void* d_ws, size_t ws_size, hipStream_t stream) {
    (void)hipFuncSetAttribute((const void*)block_fwd, hipFuncAttributeMaxDynamicSharedMemorySize, LDS_BYTES);
    block_fwd<<<256, 512, LDS_BYTES, stream>>>((const char*)d_in[0], (const char*)d_in[1], (const char*)d_in[2], (const char*)d_in[4], (const char*)d_in[5], (const char*)d_in[6], (char*)d_out, (char*)d_ws);
}
```

```cpp
#include <hip/hip_runtime.h>
#include <hip/hip_bf16.h>
#include <stdint.h>

typedef __attribute__((ext_vector_type(8))) short bf16x8;
typedef __attribute__((ext_vector_type(4))) short s16x4;
typedef __attribute__((ext_vector_type(2))) __bf16 bf16x2;
typedef __attribute__((ext_vector_type(16))) float f32x16;
typedef __attribute__((ext_vector_type(4))) float f32x4;
typedef __attribute__((ext_vector_type(2))) float f32x2;
typedef __attribute__((ext_vector_type(4))) unsigned u32x4;
typedef __attribute__((ext_vector_type(2))) unsigned u32x2;
typedef short v4i16_t __attribute__((ext_vector_type(4)));
typedef unsigned long long u64;
#define LAS __attribute__((address_space(3)))
#define GAS __attribute__((address_space(1)))
typedef LAS char* lds_ptr;
#if defined(__HIP_DEVICE_COMPILE__)
#define NOPK __attribute__((target("no-packed-fp32-ops")))
#else
#define NOPK
#endif
#define DI __device__ __forceinline__
#define MFMA(a, b, c) __builtin_amdgcn_mfma_f32_32x32x16_bf16(a, b, c, 0, 0, 0)
#define MFMA16(a, b, c) __builtin_amdgcn_mfma_f32_16x16x32_bf16(a, b, c, 0, 0, 0)
#define BF8(x) __builtin_bit_cast(bf16x8, x)
#define U4(x) __builtin_bit_cast(u32x4, x)

constexpr int D = 448;
constexpr float EPS = 1e-5f;
constexpr unsigned FL_EPOCH = 0, FL_KV = 1, FL_RT = FL_KV + 256, FL_RD = FL_RT + 32, FL_O = FL_RD + 32, FL_H = FL_O + 256, FL_G = FL_H + 256;
constexpr unsigned WS_KV = 1u << 20;
constexpr unsigned WS_QF = WS_KV + 4194304;
constexpr unsigned WS_OF = WS_QF + 2097152;
constexpr unsigned WS_HF = WS_OF + 2097152;
constexpr unsigned WS_GF = WS_HF + 64 * 28 * 1024;
constexpr unsigned WS_WGU = WS_GF + 64 * 112 * 1024;
constexpr unsigned WS_WD = WS_WGU + 112 * 28 * 1024;
constexpr unsigned WS_HS = WS_WD + 7 * 112 * 2 * 1024;
constexpr unsigned WS_END = WS_HS + 64 * 28 * 256 + 1024;
constexpr unsigned OOB = 0x40000000u;
constexpr int LDS_BYTES = 163840;

DI float u2f(unsigned u) { return __builtin_bit_cast(float, u); }
DI unsigned f2u(float f) { return __builtin_bit_cast(unsigned, f); }
DI unsigned umin(unsigned a, unsigned b) { return a < b ? a : b; }
DI unsigned umax3(unsigned a, unsigned b, unsigned c) { const unsigned m = a > b ? a : b; return m > c ? m : c; }
DI bool wave_any(bool p) { return __builtin_amdgcn_ballot_w64(p) != 0; }
DI bool wave_all(bool p) { return __builtin_amdgcn_ballot_w64(!p) == 0; }
DI unsigned cvtpk(float lo, float hi) { return __builtin_bit_cast(unsigned, __builtin_convertvector((f32x2){lo, hi}, bf16x2)); }
DI float bflo(unsigned u) { return u2f(u << 16); }
DI float bfhi(unsigned u) { return u2f(u & 0xffff0000u); }
DI void glds(unsigned voff, const void* sbase, unsigned dst) {
    asm volatile("s_mov_b32 m0, %2\n\ts_nop 0\n\tglobal_load_lds_dwordx4 %0, %1" :: "v"(voff), "s"(sbase), "s"(dst) : "memory");
}
DI void glds_dev(unsigned voff, const void* sbase, unsigned dst) {
    asm volatile("s_mov_b32 m0, %2\n\ts_nop 0\n\tglobal_load_lds_dwordx4 %0, %1 sc1" :: "v"(voff), "s"(sbase), "s"(dst) : "memory");
}
DI void glds2_dev(unsigned voff, const void* sbase, unsigned dst) {
    asm volatile("s_mov_b32 m0, %2\n\ts_nop 0\n\tglobal_load_lds_dwordx4 %0, %1 sc1\n\tglobal_load_lds_dwordx4 %0, %1 offset:1024 sc1" :: "v"(voff), "s"(sbase), "s"(dst) : "memory");
}
DI void glds4_dev(unsigned voff, const void* sbase, unsigned dst) {
    asm volatile("s_mov_b32 m0, %2\n\ts_nop 0\n\tglobal_load_lds_dwordx4 %0, %1 sc1\n\tglobal_load_lds_dwordx4 %0, %1 offset:1024 sc1\n\t"
                 "global_load_lds_dwordx4 %0, %1 offset:2048 sc1\n\tglobal_load_lds_dwordx4 %0, %1 offset:3072 sc1" :: "v"(voff), "s"(sbase), "s"(dst) : "memory");
}
DI s16x4 trd(lds_ptr p) { return __builtin_bit_cast(s16x4, __builtin_amdgcn_ds_read_tr16_b64_v4i16((LAS v4i16_t*)p)); }
DI bf16x8 cat(s16x4 a, s16x4 b) { return __builtin_shufflevector(a, b, 0, 1, 2, 3, 4, 5, 6, 7); }
DI bf16x8 lds8(lds_ptr p) { return *(LAS bf16x8*)p; }
DI float swap_add(float v) { auto r = __builtin_amdgcn_permlane32_swap(f2u(v), f2u(v), false, false); return u2f(r[0]) + u2f(r[1]); }
DI float swap_max(float v) { auto r = __builtin_amdgcn_permlane32_swap(f2u(v), f2u(v), false, false); return __builtin_fmaxf(u2f(r[0]), u2f(r[1])); }
DI u32x4 pack8(const f32x16& a, int s) { return (u32x4){cvtpk(a[8 * s], a[8 * s + 1]), cvtpk(a[8 * s + 2], a[8 * s + 3]), cvtpk(a[8 * s + 4], a[8 * s + 5]), cvtpk(a[8 * s + 6], a[8 * s + 7])}; }
DI float dot2(unsigned a, unsigned b, float c) { return __builtin_amdgcn_fdot2_f32_bf16(__builtin_bit_cast(bf16x2, a), __builtin_bit_cast(bf16x2, b), c, false); }
DI void acc_stats2(unsigned u0, unsigned u1, f32x4& s) {
    const float a = bflo(u0), b = bfhi(u0), c = bflo(u1), d = bfhi(u1);
    s[0] += a; s[1] += b; s[2] = __builtin_fmaf(a, a, s[2]); s[3] = __builtin_fmaf(b, b, s[3]); s[0] += c; s[1] += d; s[2] = __builtin_fmaf(c, c, s[2]); s[3] = __builtin_fmaf(d, d, s[3]);
}
DI float sum8(float v) {
    v += u2f(__builtin_amdgcn_update_dpp(0u, f2u(v), 0xB1, 0xf, 0xf, true));
    v += u2f(__builtin_amdgcn_update_dpp(0u, f2u(v), 0x4E, 0xf, 0xf, true));
    v += u2f(__builtin_amdgcn_update_dpp(0u, f2u(v), 0x141, 0xf, 0xf, true));
    return v;
}
DI u32x4 shift8(u32x4 x, float c) {
    u32x4 o; float t0, t1, t2, t3, t4, t5, t6, t7;
    asm("v_dot2_f32_bf16 %4, %12, %16, %18\n\tv_dot2_f32_bf16 %5, %12, %17, %18\n\tv_dot2_f32_bf16 %6, %13, %16, %18\n\tv_dot2_f32_bf16 %7, %13, %17, %18\n\t"
        "v_dot2_f32_bf16 %8, %14, %16, %18\n\tv_dot2_f32_bf16 %9, %14, %17, %18\n\tv_dot2_f32_bf16 %10, %15, %16, %18\n\tv_dot2_f32_bf16 %11, %15, %17, %18\n\t"
        "v_cvt_pk_bf16_f32 %0, %4, %5\n\tv_cvt_pk_bf16_f32 %1, %6, %7\n\tv_cvt_pk_bf16_f32 %2, %8, %9\n\tv_cvt_pk_bf16_f32 %3, %10, %11"
        : "=&v"(o[0]), "=&v"(o[1]), "=&v"(o[2]), "=&v"(o[3]), "=&v"(t0), "=&v"(t1), "=&v"(t2), "=&v"(t3), "=&v"(t4), "=&v"(t5), "=&v"(t6), "=&v"(t7)
        : "v"(x[0]), "v"(x[1]), "v"(x[2]), "v"(x[3]), "s"(0x00003f80u), "s"(0x3f800000u), "v"(c));
    return o;
}
template <int N> struct IC { static constexpr int v = N; };
template <int B, int E, class F> DI void sfor(F&& f) { if constexpr (B < E) { f(IC<B>{}); sfor<B + 1, E>(f); } }
template <int N> DI void waitv() { asm volatile("s_waitcnt vmcnt(%0)" :: "i"(N) : "memory"); }
template <int N> DI void waitv_bar() { if (N == 0) __builtin_amdgcn_s_waitcnt(0x0F70); asm volatile("s_waitcnt vmcnt(%0) lgkmcnt(0)\n\ts_barrier" :: "i"(N) : "memory"); }
template <int N> DI void waitv_barx() { asm volatile("s_waitcnt vmcnt(%0)\n\ts_barrier" :: "i"(N) : "memory"); }
template <int N> DI void waitv_bar2() { asm volatile("s_waitcnt vmcnt(%0) lgkmcnt(2)\n\ts_barrier" :: "i"(N) : "memory"); }
#define BAR() asm volatile("s_waitcnt lgkmcnt(0)\n\ts_barrier" ::: "memory")
#define FENCE() asm volatile("" ::: "memory")
typedef __amdgpu_buffer_rsrc_t rsrc_t;
DI u32x4 ld16_dev(rsrc_t rs, unsigned voff, unsigned soff) { return U4(__builtin_amdgcn_raw_buffer_load_b128(rs, voff, soff, 16)); }
DI u32x2 ld8(rsrc_t rs, unsigned voff, unsigned soff) { return __builtin_bit_cast(u32x2, __builtin_amdgcn_raw_buffer_load_b64(rs, voff, soff, 0)); }
DI u32x2 ld8_dev(rsrc_t rs, unsigned voff, unsigned soff) { return __builtin_bit_cast(u32x2, __builtin_amdgcn_raw_buffer_load_b64(rs, voff, soff, 16)); }
DI void st16_dev(u32x4 v, rsrc_t rs, unsigned voff, unsigned soff) { __builtin_amdgcn_raw_buffer_store_b128(v, rs, voff + soff, 0, 16); }
DI void st8_dev(u32x2 v, rsrc_t rs, unsigned voff, unsigned soff) { __builtin_amdgcn_raw_buffer_store_b64(v, rs, voff, soff, 16); }
DI void set_flag(char* ws, unsigned fo, u64 tagv) {
    asm volatile("s_mov_b64 exec, 1\n\tglobal_store_dwordx2 %0, %1, %2 sc1\n\ts_mov_b64 exec, -1" :: "v"(fo), "v"(tagv), "s"(ws) : "memory");
}
DI u64 peek_flag(char* ws, unsigned fo) { return __hip_atomic_load((GAS u64*)(ws + fo), __ATOMIC_RELAXED, __HIP_MEMORY_SCOPE_AGENT); }
DI void peek_lds(char* ws, unsigned fo, unsigned dst) {
    asm volatile("s_mov_b32 m0, %2\n\ts_nop 0\n\tglobal_load_lds_dword %0, %1 sc1\n\ts_add_u32 m0, %2, 252\n\ts_nop 0\n\tglobal_load_lds_dword %0, %1 offset:4 sc1" :: "v"(fo), "s"(ws), "s"(dst) : "memory", "scc");
}
DI u64 peeked(lds_ptr p) { return (u64)*(LAS unsigned*)(p + 256) << 32 | *(LAS unsigned*)p; }
DI void poll_flag(char* ws, unsigned fo, u64 tag) {
#pragma unroll 1
    for (int spin = 0; spin < (1 << 21); ++spin) {
        if (wave_all(peek_flag(ws, fo) == tag)) break;
        __builtin_amdgcn_s_sleep(1);
    }
    FENCE();
}
#define SB() __builtin_amdgcn_sched_barrier(0)
#define PINS(a) asm volatile("" : "+s"(a))
#define PIN(a) asm volatile("" : "+v"(a))
#define PIN2(a, b) asm volatile("" : "+v"(a), "+v"(b))
DI lds_ptr opq(lds_ptr p) { unsigned u = (unsigned)(uintptr_t)p; asm volatile("" : "+v"(u)); return (lds_ptr)(uintptr_t)u; }
#define TS(i) do {} while (0)

constexpr int GW = 7, XN = 5;
DI constexpr int d_prio(int n) { return n < 0 ? -1 : n < 27 ? 3 - n / 7 : n < 64 ? 3 - (n - 27) / 10 : n < 84 ? 3 - (n - 64) / 5 : n < 112 ? 3 - (n - 84) / 7 : 0; }
DI constexpr int wd_at(int n) {
    constexpr int t[28] = {32, 34, 37, 39, 42, 65, 69, 72, 75, 78, 81, 83, 44, 47, 49, 52, 54, 57, 59, 61, 63, 86, 88, 90, 92, 94, 97, 99};
    for (int i = 0; i < 28; ++i) if (t[i] == n) return i;
    return -1;
}
DI constexpr int phys(int t) { return ((t % 9) + 2) % 9 * 16384; }

__global__ NOPK __launch_bounds__(512, 2) void block_fwd(const char* x, const char* wqkv, const char* wo, const char* wg, const char* wu, const char* wd, char* out, char* ws) {
    extern __shared__ __attribute__((aligned(16))) char smem[];
    lds_ptr lds = (lds_ptr)smem;
    const int tid = __builtin_amdgcn_workitem_id_x(), lane = tid & 63, w = __builtin_amdgcn_readfirstlane(tid >> 6);
    const int bid = __builtin_amdgcn_workgroup_id_x(), xcd = bid & 7, loc = bid >> 3;
    const int r = lane & 31, hf = lane >> 5, q4 = (lane & 15) >> 2, g1 = (lane >> 4) & 1, p4 = lane & 3;
    const unsigned lds0 = (unsigned)(uintptr_t)smem;
    const unsigned l16 = lane * 16;
    TS(0);
    const int sp3 = (loc + 8) >> 5;
    const int atb = (xcd >> 1) * 8 + (loc & 7) + sp3 * (xcd & 1) * 4, acg = (loc >> 3) + (1 - sp3) * (xcd & 1) * 4;
    const int row8 = lane >> 3;
    const int rot8 = ((loc & 7) << sp3) + sp3 * (xcd & 1), rot = rot8 == 7 ? 0 : rot8;
    if (loc < 28) {
        const unsigned xo = (unsigned)((atb * 64 + (tid >> 3)) * 896 + (((tid ^ (tid >> 4)) & 7) * 16));
        const unsigned xd0 = lds0 + 1024 + (w >> 2) * 28672 + (w & 3) * 1024;
        unsigned xd = xd0 + (7 - rot) * 4096;
        const unsigned w0 = (unsigned)((rot * 64 + (tid >> 3)) * 2688 + acg * 128 + (((tid & 7) ^ ((tid >> 2) & 4)) * 16));
        asm volatile("s_cmp_eq_u32 %0, %3\n\ts_cselect_b32 %0, %2, %0\n\ts_mov_b32 m0, %0\n\ts_add_u32 %0, %0, 0x1000\n\tglobal_load_lds_dwordx4 %1, %4\n\t"
#define XP(o) "s_cmp_eq_u32 %0, %3\n\ts_cselect_b32 %0, %2, %0\n\ts_sub_u32 m0, %0, " #o "\n\ts_add_u32 %0, %0, 0x1000\n\tglobal_load_lds_dwordx4 %1, %4 offset:" #o "\n\t"
                     XP(128) XP(256) XP(384) XP(512) XP(640) XP(768)
                     "s_add_u32 m0, %6, 8192-896\n\ts_barrier\n\tglobal_load_lds_dwordx4 %5, %7 offset:896\n\t"
                     "s_add_u32 m0, %6, 16384-1792\n\ts_nop 0\n\tglobal_load_lds_dwordx4 %5, %7 offset:1792\n\t"
                     "s_mov_b32 m0, %6\n\ts_nop 0\n\tglobal_load_lds_dwordx4 %5, %7"
                     : "+&s"(xd) : "v"(xo), "s"(xd0), "s"(xd0 + 7 * 4096), "s"(x), "v"(w0), "s"(lds0 + 58368 + w * 1024), "s"(wqkv) : "memory", "scc");
    }
    SB();
    const rsrc_t rws = __builtin_amdgcn_make_buffer_rsrc((void*)ws, 0, WS_END, 0x00020000);
    u64 epoch, tagv;
    unsigned fl1;
    const unsigned ones = 0x3f803f80u;
    const bool ffn = loc < 28;
    const int l7 = loc / 7, m7 = loc % 7;
    const int tb = xcd * 4 + l7;
    const int tt = w & 1, ct = (w >> 1) & 1, kh = w >> 2;
    const unsigned swz = (unsigned)(((lane & 7) ^ (((row8 >> 1) & 1) << 2)) * 16);

    if (__builtin_expect(ffn, 1)) {
        constexpr int A_W = 58368, A_WS = 24576;
#define A_KC(c) (rot + (c) >= 7 ? rot + (c) - 7 : rot + (c))
        const unsigned wvo = (unsigned)(row8 * 2688) + swz;
        const char* wsrc = wqkv + acg * 128 + (size_t)w * 8 * 2688;
        const unsigned wdst = lds0 + A_W + w * 1024;
#define A_PIECE(c, i) glds(wvo, wsrc + A_KC(c) * (64 * 2688) + (i) * 896, wdst + ((c) & 3) * A_WS + (i) * 8192)
#define A_GROUP(c) do { A_PIECE(c, 1); A_PIECE(c, 2); A_PIECE(c, 0); } while (0)
        fl1 = (FL_KV + atb * 8 + acg) * 128;
        {
            const unsigned f1_ = FL_KV + atb * 8 + acg, f2_ = FL_O + (loc >> 1) * 16 + xcd * 2 + (loc & 1), f3_ = FL_H + tb * 8 + m7, f4_ = FL_G + ((xcd >> 1) * 4 + l7) * 64 + (xcd & 1) * 7 + m7;
            const unsigned lo_ = (lane & 2) ? ((lane & 1) ? f3_ : f2_) : ((lane & 1) ? f1_ : FL_EPOCH), hi4 = f4_ + (lane & 3) * 16;
            const unsigned fa = (lane & 4) ? hi4 : lo_;
            epoch = peek_flag(ws, fa * 128); FENCE();
        }
        lds_ptr xb[4], wb1, wb1h;
#pragma unroll
        for (int j = 0; j < 4; ++j) xb[j] = lds + 1024 + tt * 28672 + (r >> 3) * 1024 + (r & 7) * 128 + (((2 * j + hf) ^ ((r >> 1) & 7)) * 16);
        wb1 = opq(lds + A_W + (kh ? 0 : 8192) + (8 * hf + q4) * 128 + ((ct ^ (q4 >> 1)) * 64) + g1 * 32 + p4 * 8);
        wb1h = opq(wb1 + 2 * A_WS);
        unsigned go1[2], go2[2]; int gi[2];
#pragma unroll
        for (int p = 0; p < 2; ++p) {
            const int G = acg * 8 + ct * 4 + 2 * p + hf, hp = G / 7, i = G - hp * 7;
            const unsigned hs = kh ? 131072u : 262144u;
            const unsigned to = (kh ? WS_QF + (atb * 2 + tt) * 2048 : WS_KV + (atb * 2 + tt) * 4096) + r * 16;
            go1[p] = (unsigned)(2 * hp + (i > 3)) * hs + (unsigned)(i > 3 ? i - 3 : i) * 512 + to;
            go2[p] = i == 3 ? (unsigned)(2 * hp + 1) * hs + to : OOB;
            gi[p] = i;
        }
        const int vcol = acg * 64 + ct * 32 + r, vh = vcol / 28, vd = vcol - vh * 28;
        unsigned vo = WS_KV + (atb * 2 + tt) * 4096 + (unsigned)vh * 262144 + 2048 + hf * 512 + vd * 16;
        PIN2(go1[0], go1[1]); PIN2(go2[0], go2[1]); PIN(vo);
        lds_ptr xw = opq(lds + 1024 + kh * 28672 + (w & 3) * 1024 + l16);
        waitv<4>();
        TS(28);
        u32x4 xv[7];
#pragma unroll
        for (int c = 0; c < 7; ++c) xv[c] = *(LAS u32x4*)(xw + c * 4096);
        SB();
        f32x4 sm = {};
#pragma unroll
        for (int c = 0; c < 7; ++c)
#pragma unroll
            for (int j = 0; j < 4; ++j) sm[j] = dot2(xv[c][j], ones, sm[j]);
        float nm = sum8((sm[0] + sm[1]) + (sm[2] + sm[3])) * (-1.f / D);
        PIN(nm);
#pragma unroll
        for (int c = 0; c < 7; ++c) { xv[c] = shift8(xv[c], nm); *(LAS u32x4*)(xw + c * 4096) = xv[c]; }
        A_GROUP(1);
        SB();
        LAS float* rst = (LAS float*)lds;
        sm = (f32x4){};
#pragma unroll
        for (int c = 0; c < 7; ++c)
#pragma unroll
            for (int j = 0; j < 4; ++j) sm[j] = dot2(xv[c][j], xv[c][j], sm[j]);
        rst[w * 8 + row8] = __builtin_amdgcn_rsqf(sum8((sm[0] + sm[1]) + (sm[2] + sm[3])) * (1.f / D) + EPS);
        A_GROUP(2);
        TS(31);
        bf16x8 fx[4], f1[4], f2[4];
#define A_WAIT(c) do { if ((c) == 0) waitv_bar<6>(); else if ((c) == 1) waitv_barx<6>(); else if ((c) < 6) waitv_barx<3>(); else waitv_barx<0>(); \
                       if ((c) == 0) TS(1); if ((c) == 6) TS(2); } while (0)
#define A_WP(c, ks) ((((c) & 2) ? wb1h : wb1) + ((c) & 1) * A_WS + (ks) * 2048)
        if (w < 4) {
            f32x16 ak = {}, av = {};
            f32x4 rv[4];
            float rs = 0.f;
#pragma unroll
            for (int n = 0; n < 30; ++n) {
                const int c = n >> 2, ks = n & 3, km = (n - 2) & 3;
                if (n < 28 && ks == 0) A_WAIT(c);
                if (n >= 2) { ak = MFMA(f1[km], fx[km], ak); PIN(ak); } SB();
                if (n < 28) { fx[ks] = lds8(xb[ks] + c * 4096); f1[ks] = cat(trd(A_WP(c, ks)), trd(A_WP(c, ks) + 512)); } SB();
                if (n >= 2) { av = MFMA(fx[km], f2[km], av); PIN(av); } SB();
                if (n < 28) f2[ks] = cat(trd(A_WP(c, ks) + 8192), trd(A_WP(c, ks) + 8192 + 512));
                if (c == 0 && ks < 3) A_PIECE(3, ks);
                if (c >= 2 && c < 5 && ks < 3) A_PIECE(c + 2, ks);
                if (n == 21) {
                    rs = rst[tt * 32 + r];
#pragma unroll
                    for (int g = 0; g < 4; ++g) rv[g] = *(LAS f32x4*)(rst + tt * 32 + 8 * g + 4 * hf);
                }
                if (n == 24) { PIN(epoch); tagv = epoch + 1; PIN(tagv); }
                SB();
            }
            TS(3);
#pragma unroll
            for (int i = 0; i < 16; ++i) { ak[i] *= rs; av[i] *= rv[i >> 2][i & 3]; }
#pragma unroll
            for (int p = 0; p < 2; ++p) {
                const u32x4 f = pack8(ak, p);
                const auto s0 = __builtin_amdgcn_permlane32_swap(f[0], f[2], false, false), s1 = __builtin_amdgcn_permlane32_swap(f[1], f[3], false, false);
                const u32x4 c = {s0[0], s1[0], s0[1], s1[1]};
                st16_dev(c, rws, go1[p], 0); st16_dev(c, rws, go2[p], 0);
            }
            st16_dev(pack8(av, 0), rws, vo, 0); st16_dev(pack8(av, 1), rws, vo + 1024, 0);
        } else {
            f32x16 aq = {};
            __builtin_amdgcn_s_setprio(1);
#pragma unroll
            for (int n = 0; n < 30; ++n) {
                const int c = n >> 2, ks = n & 3, km = (n - 2) & 3;
                if (n < 28 && ks == 0) A_WAIT(c);
                if (n == 24) { PIN(epoch); tagv = epoch + 1; PIN(tagv); }
                if (n >= 2) { aq = MFMA(f1[km], fx[km], aq); PIN(aq); } SB();
                if (n < 28) { fx[ks] = lds8(xb[ks] + c * 4096); f1[ks] = cat(trd(A_WP(c, ks)), trd(A_WP(c, ks) + 512)); }
                if (c == 0 && ks < 3) A_PIECE(3, ks);
                if (c >= 2 && c < 5 && ks < 3) A_PIECE(c + 2, ks);
                if (n == 24) {
                    const unsigned pv = vd == 0 ? ones : 0u, po = vd < 4 ? vo + 448 : OOB;
                    st16_dev((u32x4){pv, pv, pv, pv}, rws, po, 0); st16_dev((u32x4){pv, pv, pv, pv}, rws, po + 1024, 0);
                }
                SB();
            }
            __builtin_amdgcn_s_setprio(0);
            const float QS = rst[tt * 32 + r] * (0.18898223650461362f * 1.4426950408889634f);
#pragma unroll
            for (int i = 0; i < 16; ++i) aq[i] *= QS;
#pragma unroll
            for (int p = 0; p < 2; ++p) {
                const u32x4 f = pack8(aq, p);
                const auto s0 = __builtin_amdgcn_permlane32_swap(f[0], f[2], false, false), s1 = __builtin_amdgcn_permlane32_swap(f[1], f[3], false, false);
                const bool sp = gi[p] == 3;
                st16_dev((u32x4){s0[0], s1[0], sp ? 0u : s0[1], sp ? 0u : s1[1]}, rws, go1[p], 0);
                st16_dev((u32x4){0u, 0u, s0[1], s1[1]}, rws, go2[p], 0);
            }
        }
    } else {
        __builtin_amdgcn_s_sleep(8);
        fl1 = (FL_RT + xcd * 4 + loc - 28) * 128;
        epoch = peek_flag(ws, (lane & 3) == 0 ? FL_EPOCH * 128 : (lane & 3) == 1 ? fl1 : (lane & 3) == 2 ? fl1 + 32 * 128 : (FL_O + (loc >> 1) * 16 + xcd * 2 + (loc & 1)) * 128);
        const int ri = xcd * 4 + loc - 28, nu = (w == 0 && ri < 16) ? 4 : 3;
        const unsigned vo = (unsigned)(row8 * 3584 + (lane & 7) * 16);
#pragma unroll 1
        for (int i = 0; i < nu; ++i) {
            const int u = i < 3 ? i * 256 + ri * 8 + w : 768 + ri;
            const size_t so = (size_t)(u / 28) * 16 * 3584 + (u % 28) * 128;
            const unsigned d_ = lds0 + w * 16384 + i * 4096;
            glds(vo, wg + so, d_); glds(vo, wg + so + 8 * 3584, d_ + 1024); glds(vo, wu + so, d_ + 2048); glds(vo, wu + so + 8 * 3584, d_ + 3072);
        }
#pragma unroll 1
        for (int i = 0; i < nu; ++i) {
            const int u = i < 3 ? i * 256 + ri * 8 + w : 768 + ri;
            waitv<8>();
            lds_ptr sg = lds + w * 16384 + i * 4096 + g1 * 2048 + (4 * hf + q4) * 128 + p4 * 8;
#pragma unroll
            for (int e = 0; e < 4; ++e) st16_dev(U4(cat(trd(sg + e * 32), trd(sg + e * 32 + 1024))), rws, l16, WS_WGU + (unsigned)(((u % 28) * 4 + e) * 28 + u / 28) * 1024);
            FENCE();
        }
        tagv = epoch + 1;
    }
    TS(24);
    const int head = xcd * 2 + (loc & 1), rb = loc >> 1, qt = w & 3;
    const char* kvs = ws + WS_KV + (size_t)head * 262144 + w * 2048;
    const char* qs = ws + WS_QF + (size_t)(head * 64 + rb * 4 + qt) * 2048 + kh * 1024;
    unsigned kvd = lds0 + w * 2048, qd = lds0 + 147456 + qt * 2048 + kh * 1024;
    unsigned pf1 = (FL_KV + (lane >> 1) * 8 + (head * 28 + (lane & 1) * 27) / 64) * 128;
    PINS(kvs); PINS(qs); PINS(kvd); PINS(qd); PIN2(pf1, fl1);
    waitv_bar<0>();
    if (w == 0) set_flag(ws, fl1, tagv);
    const u64 tag = ((u64)(unsigned)__builtin_amdgcn_readfirstlane((unsigned)(tagv >> 32)) << 32 | (u64)(unsigned)__builtin_amdgcn_readfirstlane((unsigned)tagv));
    TS(4);

    const int drb = (xcd >> 1) * 4 + l7, dfg = (xcd & 1) * 7 + m7;
    bf16x8 wf[28];
    const unsigned p16 = hf * 1024 + g1 * 512 + (lane & 15) * 16;
    u32x2 xa, xb;
    f32x4 csg = {}, csu = {};
    f32x16 acc = {};
    const u32x4 one4 = {ones, ones, ones, ones};
    const unsigned hi_ = (unsigned)((tb * 2 + tt) * 28 + m7 * 4 + ct * 2 + kh), ofo = (unsigned)(tt * 32768 + kh * 16384 + ct * 4096);
    unsigned ho = WS_HF + hi_ * 1024 + l16, so = hf ? OOB : WS_HS + hi_ * 256 + r * 8;
    const char* os = ws + WS_OF + (size_t)tb * 65536 + ofo;
    unsigned od = lds0 + 65536 + ofo;
    unsigned pf2 = (FL_O + (tb >> 1) * 16 + kh * 8 + ct * 2 + ((lane >> 1) & 1) + (lane & 4)) * 128;
    unsigned go = ffn ? WS_GF + (unsigned)(drb * 4 * 112 + dfg * 8 + w) * 1024 + g1 * 512 + (hf * 16 + (lane & 15)) * 16 : OOB;
    unsigned fo3 = (FL_H + tb * 8 + m7) * 128;
    {
        lds_ptr ap = opq(lds + kh * 8192 + l16), apB = opq(lds + 65536 + kh * 8192 + l16), apC = opq(lds + 131072 + kh * 8192 + l16);
        const unsigned lv = kh * 8192 + (l16 ^ (kh * 256));
        lds_ptr vp = opq(lds + lv), vpB = opq(lds + 65536 + lv), vpC = opq(lds + 131072 + lv);
#define AP(off) ((off) < 65536 ? ap + (off) : (off) < 131072 ? apB + ((off) - 65536) : apC + ((off) - 131072))
#define VP(off) ((off) < 65536 ? vp + (off) : (off) < 131072 ? vpB + ((off) - 65536) : vpC + ((off) - 131072))
#define KV_ISSUE(t) glds2_dev(l16, kvs + (t) * 16384, kvd + phys(t))
        unsigned wgo = ffn ? WS_WGU + (unsigned)(dfg * 8 + w) * 28672 : OOB;
        const int ri = umin(xcd * 4 + loc - 28, 27);
        const char* wos = (ffn ? wo + m7 * 128 : wd + (ri % 7) * 128 + (size_t)(ri / 7) * 448 * 896) + (size_t)w * 56 * 896;
        unsigned wov = (unsigned)(row8 * 896) + swz;
        const rsrc_t rx = __builtin_amdgcn_make_buffer_rsrc((void*)x, 0, 2048 * 448 * 2, 0x00020000);
        unsigned xo_ = (unsigned)(((tb * 2 + tt) * 32 + r) * 896 + m7 * 128 + ct * 64 + kh * 32 + hf * 8);
        unsigned rto = (FL_RT + w * 4 + (lane & 3)) * 128;
        lds_ptr qp = opq(lds + 147456 + qt * 2048 + l16);
        PINS(wgo); PINS(wos); PIN2(wov, xo_); PIN(rto); PIN2(ho, so); PIN2(pf2, fo3); PIN(go);
        lds_ptr xw = opq(lds + 147456 + w * 2048 + l16), xr = opq(lds + 147456 + (w ^ 4) * 2048 + l16);
        lds_ptr lw = opq(lds + 58368 + w * 256 + lane * 4), lr = opq(lds + 58368 + qt * 256 + 128 + r * 4);
        lds_ptr bw = opq(lds + 60416 + w * 4), br = opq(lds + 60416 + (lane & 7) * 4);
        unsigned oo = WS_OF + (unsigned)((rb * 4 + qt) * 32 + head * 2 + kh) * 1024 + l16;
        unsigned fo2 = (FL_O + rb * 16 + head) * 128;
        PIN2(oo, fo2);
        if (w == 1) poll_flag(ws, pf1, tag);
        BAR();
        TS(5);
        glds_dev(l16, qs, qd);
        KV_ISSUE(0); KV_ISSUE(1);
        asm volatile("s_barrier" ::: "memory");
        KV_ISSUE(2); KV_ISSUE(3);
        waitv_bar<4>();
        TS(6);
        const bf16x8 q0 = lds8(qp), q1 = lds8(qp + 1024);
        f32x16 o = {}, sa = {}, sb;
        bf16x8 k0 = lds8(ap + phys(0)), k1 = lds8(ap + phys(0) + 1024), v0 = lds8(vp + phys(0) + 2048), v1 = {};
        u32x4 p0, p1 = {};
        sa = MFMA(k0, q0, sa); sa = MFMA(k1, q1, sa);
        k0 = lds8(ap + phys(0) + 4096); k1 = lds8(ap + phys(0) + 5120);
        PIN(sa); SB();
        u64 rtf = 0;
#define EXP4(S, o4) _Pragma("unroll") for (int i_ = o4; i_ < o4 + 4; ++i_) S[i_] = __builtin_amdgcn_exp2f(S[i_]);
#define WLD(s) wf[s] = BF8(ld16_dev(rws, p16 + ((s) & 1) * 256 + ((s) & 2) * 1024, wgo + ((s) >> 2) * 4096))
#define SIDE(t, g) do { \
            if ((t) == 0 && (g) == 0) KV_ISSUE(4); if ((t) == 0 && (g) == 2) KV_ISSUE(5); if ((t) == 1 && (g) == 0) KV_ISSUE(6); if ((t) == 1 && (g) == 2) KV_ISSUE(7); \
            if ((t) == 2 && (g) == 0) KV_ISSUE(8); \
            if ((t) == 1 && (g) == 1) peek_lds(ws, rto, lds0 + 155648 + w * 512); if ((t) == 2 && (g) == 2) { waitv<5>(); rtf = peeked(lds + 155648 + w * 512 + lane * 4); } \
            if ((t) == 3 && (g) < 3) KV_ISSUE(9 + (g)); \
            if (((t) == 2 && (g) == 1) || ((t) == 14 && (g) == 2)) st16_dev((u32x4){~0u, ~0u, ~0u, ~0u}, rws, go + ((t) == 2 ? 2 : 3) * 114688, 0);     \
            if ((t) == 7 && (g) == 0) KV_ISSUE(12); if ((t) == 7 && (g) == 2) KV_ISSUE(13); if ((t) == 8 && (g) == 0) KV_ISSUE(14); if ((t) == 8 && (g) == 2) KV_ISSUE(15); \
            if ((t) >= 4 && (t) <= 6) WLD(((t) - 4) * 4 + (g)); \
            if (((t) == 7 || (t) == 8) && ((g) & 1)) WLD(12 + ((t) - 7) * 2 + ((g) >> 1)); \
            if ((t) >= 9 && (t) <= 11) WLD(((t) - 5) * 4 + (g)); \
            if ((t) == 12 || ((t) == 13 && (g) < 3)) glds(wov, wos + (((t) - 12) * 4 + (g)) * 8 * 896, lds0 + (w * 7 + ((t) - 12) * 4 + (g)) * 1024); \
            if ((t) == 13 && (g) == 3) { xa = ld8(rx, xo_, 0); xb = ld8(rx, xo_, 16); } \
            if ((t) == 14 && (g) == 0) { if (tid < 32) *(LAS u32x4*)(lds + 57344 + tid * 16) = (u32x4){0, 0, 0, 0}; } \
        } while (0)
#define TOFF(i) (phys((i) >> 1) + ((i) & 1) * 4096)
#define TILE(i, SC, SN) do { \
            __builtin_amdgcn_s_setprio(((i) & 1) ? 1 : 3); \
            if ((i) < 31) { f32x16 z_ = {}; SN = MFMA(k0, q0, z_); PIN(SN); } if ((i) < 30) k0 = lds8(AP(TOFF((i) + 2))); SB(); \
            EXP4(SC, 0); PIN(SC); SB(); \
            o = MFMA(v1, BF8(p1), o); PIN(o); v1 = lds8(VP(TOFF(i) + 3072)); SIDE((i) >> 1, ((i) & 1) * 2); SB(); \
            EXP4(SC, 4); p0 = pack8(SC, 0); PIN(p0); SB(); \
            __builtin_amdgcn_s_setprio(((i) & 1) ? 0 : 2); \
            if ((i) < 31) { SN = MFMA(k1, q1, SN); PIN(SN); } if ((i) < 30) k1 = lds8(AP(TOFF((i) + 2) + 1024)); SB(); \
            EXP4(SC, 8); PIN(SC); SB(); \
            o = MFMA(v0, BF8(p0), o); PIN(o); if ((i) < 31) v0 = lds8(VP(TOFF((i) + 1) + 2048)); SIDE((i) >> 1, ((i) & 1) * 2 + 1); SB(); \
            EXP4(SC, 12); p1 = pack8(SC, 1); PIN(p1); SB(); } while (0)
#define STEP(t) do { TILE(2 * (t), sa, sb); TILE(2 * (t) + 1, sb, sa); } while (0)
        STEP(0);
        waitv_barx<4>();
        STEP(1); STEP(2);
        if (__builtin_expect(!wave_all(rtf == tag), 0)) poll_flag(ws, rto, tag);
        waitv_bar2<3>();
        TS(21);
        STEP(3); STEP(4); STEP(5); STEP(6);
        waitv_bar2<12>();
        TS(22);
        STEP(7); STEP(8); STEP(9); STEP(10);
        waitv_bar2<9>();
        TS(23);
        STEP(11); STEP(12); STEP(13); STEP(14); STEP(15);
        o = MFMA(v1, BF8(p1), o);
        TS(7);
        u32x4 ov;
        {
            *(LAS f32x4*)xw = (f32x4){o[8], o[9], o[10], o[11]}; *(LAS f32x4*)(xw + 1024) = (f32x4){o[12], o[13], o[14], o[15]};
            const float l = kh ? o[4] : o[12];
            *(LAS float*)lw = l;
#define AMX(a, b, c) __builtin_fmaxf(__builtin_fmaxf(__builtin_fabsf(a), __builtin_fabsf(b)), __builtin_fabsf(c))
            const float mx_ = AMX(AMX(AMX(o[0], o[1], o[2]), AMX(o[3], o[4], o[5]), AMX(o[6], o[7], o[8])), AMX(AMX(o[9], o[10], o[11]), AMX(o[12], o[13], o[14]), o[15]), 0.f);
            const bool ok = (mx_ < 0x1p100f) & ((hf ? l : 1.f) > 0x1p-40f);
            *(LAS unsigned*)bw = wave_all(ok) ? 0u : 1u;
            BAR();
            const f32x4 a = *(LAS f32x4*)xr, c = *(LAS f32x4*)(xr + 1024);
            const float inv = __builtin_amdgcn_rcpf(*(LAS float*)lr + *(LAS float*)(lr + 1024));
            const unsigned bad = *(LAS unsigned*)br;
            const float iz = (kh && hf) ? 0.f : inv;
            ov = (u32x4){cvtpk((o[0] + a[0]) * inv, (o[1] + a[1]) * inv), cvtpk((o[2] + a[2]) * inv, (o[3] + a[3]) * inv),
                         cvtpk((o[4] + c[0]) * iz, (o[5] + c[1]) * iz), cvtpk((o[6] + c[2]) * iz, (o[7] + c[3]) * iz)};
            if (__builtin_expect(wave_any(bad != 0), 0)) {
                f32x16 of = {};
                float m = -0x1p126f;
#pragma unroll 1
                for (int t = 0; t < 16; ++t) {
                    BAR();
                    glds2_dev(l16, kvs + t * 16384, lds0 + 65536 + w * 2048);
                    waitv_bar<0>();
#pragma unroll 1
                    for (int u = 0; u < 2; ++u) {
                        f32x16 s = {};
                        s = MFMA(lds8(apB + u * 4096), q0, s); s = MFMA(lds8(apB + u * 4096 + 1024), q1, s);
                        float mx = s[0];
#pragma unroll
                        for (int i = 1; i < 16; ++i) mx = __builtin_fmaxf(mx, s[i]);
                        mx = __builtin_fmaxf(swap_max(mx), m);
                        const float alpha = __builtin_amdgcn_exp2f(m - mx);
                        m = mx;
#pragma unroll
                        for (int i = 0; i < 16; ++i) { s[i] = __builtin_amdgcn_exp2f(s[i] - mx); of[i] *= alpha; }
                        of = MFMA(lds8(vpB + u * 4096 + 2048), BF8(pack8(s, 0)), of);
                        of = MFMA(lds8(vpB + u * 4096 + 3072), BF8(pack8(s, 1)), of);
                    }
                }
                *(LAS f32x4*)xw = (f32x4){of[8], of[9], of[10], of[11]}; *(LAS f32x4*)(xw + 1024) = (f32x4){of[12], of[13], of[14], of[15]};
                *(LAS float*)lw = hf ? (kh ? of[4] : of[12]) : m;
                BAR();
                const f32x4 a2 = *(LAS f32x4*)xr, c2 = *(LAS f32x4*)(xr + 1024);
                const float m0 = *(LAS float*)(lr - 128), m1 = *(LAS float*)(lr + 1024 - 128), mm = __builtin_fmaxf(m0, m1);
                const float e0 = __builtin_amdgcn_exp2f(m0 - mm), e1 = __builtin_amdgcn_exp2f(m1 - mm);
                const float iv = __builtin_amdgcn_rcpf(*(LAS float*)lr * e0 + *(LAS float*)(lr + 1024) * e1);
                const float im = (kh ? e1 : e0) * iv, io = (kh ? e0 : e1) * iv;
                const bool z = kh && hf;
                ov = (u32x4){cvtpk(of[0] * im + a2[0] * io, of[1] * im + a2[1] * io), cvtpk(of[2] * im + a2[2] * io, of[3] * im + a2[3] * io),
                             z ? 0u : cvtpk(of[4] * im + c2[0] * io, of[5] * im + c2[1] * io), z ? 0u : cvtpk(of[6] * im + c2[2] * io, of[7] * im + c2[3] * io)};
            }
        }
        st16_dev(ov, rws, oo, 0);
        TS(25);
        FENCE();
        PIN2(xa, xb);
#pragma unroll
        for (int s = 27; s >= 0; --s) PIN(wf[s]);
#pragma unroll
        for (int s = 0; s < 16; s += 2) { csg = MFMA16(wf[s], BF8(one4), csg); csu = MFMA16(wf[s + 1], BF8(one4), csu); }
        acc[0] = bflo(xa[0]); acc[1] = bfhi(xa[0]); acc[2] = bflo(xa[1]); acc[3] = bfhi(xa[1]); acc[4] = bflo(xb[0]); acc[5] = bfhi(xb[0]); acc[6] = bflo(xb[1]); acc[7] = bfhi(xb[1]);
        PIN(acc); PIN2(ho, so); PIN2(pf2, fo3); PINS(os); PINS(od); PIN2(csg, csu);
        waitv_bar<0>();
        if (w == 0) set_flag(ws, fo2, tagv);
        TS(8);
    }
    if (__builtin_expect(!ffn, 0)) {
        const int ri = xcd * 4 + loc - 28;
        if (ri < 28) {
#pragma unroll 1
            for (int u = w; u < 28; u += 8) {
                lds_ptr sd = lds + u * 2048 + (4 * hf + q4) * 128 + g1 * 32 + p4 * 8;
#pragma unroll
                for (int e = 0; e < 2; ++e) { lds_ptr p_ = sd + ((e ^ (q4 >> 1)) * 64);
                    st16_dev(U4(cat(trd(p_), trd(p_ + 1024))), rws, l16, WS_WD + (unsigned)(((ri % 7) * 112 + (ri / 7) * 28 + u) * 2 + e) * 1024); }
            }
        }
        waitv_bar<0>();
        if (w == 0) set_flag(ws, (FL_RD + ri) * 128, tagv);
        return;
    }

    const int erb = tb >> 1;
    const char* hs = ws + WS_HF + (size_t)drb * 114688 + w * 1024;
    const char* hss = ws + WS_HS + (size_t)((drb * 4 + (w & 3)) * 28 + kh * 14) * 256;
    unsigned pf3, fo4 = (FL_G + drb * 64 + dfg) * 128;
    {
        lds_ptr wb = lds + kh * (8 * 28 * 128) + (4 * hf + q4) * 128 + ((ct ^ (q4 >> 1)) * 64) + (g1 ^ kh) * 32 + p4 * 8;
        bf16x8 wr[16];
#pragma unroll
        for (int s = 0; s < 12; ++s) { const int k0 = ((s >> 1) * 28 + (s & 1) * 16) * 128; wr[s] = cat(trd(wb + k0), trd(wb + k0 + 1024)); }
        lds_ptr cw = opq(lds + 131072 + w * 2048 + l16), cr = opq(lds + 131072 + (w ^ 4) * 2048 + l16);
        lds_ptr ob = opq(lds + 65536 + tt * 32768 + kh * 16384 + l16);
        pf3 = (FL_H + drb * 16 + (lane & 8) + umin(lane & 7, 6)) * 128;
        PIN2(pf3, fo4); PINS(hs); PINS(hss);
        poll_flag(ws, pf2, tag);
        TS(9);
        glds4_dev(l16, os, od);
        asm volatile("s_barrier" ::: "memory");
        glds4_dev(l16, os + 8192, od + 8192);
        bf16x8 of[8];
        waitv_bar<4>();
        TS(26);
#pragma unroll
        for (int s = 0; s < 8; ++s) of[s] = lds8(ob + s * 1024);
        SB();
#pragma unroll
        for (int s = 0; s < 8; ++s) {
            acc = MFMA(wr[s], of[s], acc); PIN(acc); SB();
            if (s >= 2 && s < 6) { const int k0 = (((s + 10) >> 1) * 28 + (s & 1) * 16) * 128; wr[s + 10] = cat(trd(wb + k0), trd(wb + k0 + 1024)); SB(); }
        }
        waitv_barx<0>();
#pragma unroll
        for (int s = 0; s < 8; ++s) of[s] = lds8(ob + (8 + s) * 1024);
        SB();
#pragma unroll
        for (int s = 0; s < 8; ++s) acc = MFMA(wr[8 + s], of[s], acc);
        TS(10);
        *(LAS f32x4*)cw = (f32x4){acc[8], acc[9], acc[10], acc[11]}; *(LAS f32x4*)(cw + 1024) = (f32x4){acc[12], acc[13], acc[14], acc[15]};
        BAR();
        const f32x4 a = *(LAS f32x4*)cr + (f32x4){acc[0], acc[1], acc[2], acc[3]}, b = *(LAS f32x4*)(cr + 1024) + (f32x4){acc[4], acc[5], acc[6], acc[7]};
        const u32x4 hv = {cvtpk(a[0], a[1]), cvtpk(a[2], a[3]), cvtpk(b[0], b[1]), cvtpk(b[2], b[3])};
        st16_dev(hv, rws, ho, 0);
        FENCE(); SB();
        float s1 = 0.f, s2 = 0.f;
        { f32x4 q4_ = {}; acc_stats2(hv[0], hv[1], q4_); acc_stats2(hv[2], hv[3], q4_); s1 = swap_add(q4_[0] + q4_[1]); s2 = swap_add(q4_[2] + q4_[3]); }
        st8_dev((u32x2){f2u(s1), f2u(s2)}, rws, so, 0);
        FENCE(); PIN2(csg, csu); SB();
#pragma unroll
        for (int s = 16; s < 28; s += 2) { csg = MFMA16(wf[s], BF8(one4), csg); csu = MFMA16(wf[s + 1], BF8(one4), csu); }
        PIN2(csg, csu);
        waitv_bar<0>();
        if (w == 0) set_flag(ws, fo3, tagv);
        if (bid == 0 && w == 0) set_flag(ws, FL_EPOCH * 128, tagv);
        TS(11);
    }

    const unsigned wdo = WS_WD + (unsigned)(m7 * 112 + w * 14) * 2048;
    bf16x8 wdf[28];
    bf16x8 gf[GW];
    const unsigned gxo = WS_GF + (unsigned)(erb * 4 * 112 + w * 14) * 1024 + (tb & 1) * 256;
    const unsigned pwd = hf * 2048 + g1 * 512 + (lane & 15) * 16;
#define G_LD(s) gf[(s) % GW] = BF8(ld16_dev(rws, p16, gxo + ((s) / 7) * 114688 + ((s) % 7) * 2048))
    u64 gfl = 0;
    const unsigned gfi = (FL_G + (erb * 4 + g1) * 16 + ((w * 14 + umin(lane & 15, 13)) >> 3)) * 128;
    {
        const unsigned hd = lds0 + w * 1024;
        const char* wds = ws + wdo;
        lds_ptr hb = opq(lds + p16), hb2 = opq(lds + 57344 + p16);
        lds_ptr wl = opq(lds + 114688 + w * 5120 + l16), wl2 = opq(lds + w * 7168 + l16);
        LAS f32x2* stp = (LAS f32x2*)opq(lds + 159744 + (lane & 15) * 8);
        if (w == 1) poll_flag(ws, pf3, tag);
        BAR();
        TS(12);
#define H_PIECE(j) glds_dev(l16, hs + (j) * 8192, hd + (j) * 8192)
#define WDO(s) (((s) >> 2) * 4096 + (((s) >> 1) & 1) * 1024 + ((s) & 1) * 256)
#define WDLD(s) wdf[s] = BF8(ld16_dev(rws, pwd, wdo + WDO(s)))
#define HB(n) (((n) < 56 ? hb + ((n) & ~1) * 1024 : hb2 + (((n) & ~1) - 56) * 1024) + ((n) & 1) * 256)
        H_PIECE(0);
        asm volatile("s_barrier" ::: "memory");
        H_PIECE(1); H_PIECE(2); H_PIECE(3);
        constexpr int PF = 4;
        bf16x8 bf[PF + 1];
        f32x4 ag[2][2], au[2][2];
        f32x2 sp[7], sv[4];
        float mean[2] = {0.f, 0.f}, r2[2] = {0.f, 0.f}, hp = 0.f;
        u32x4 hv;
        lds_ptr pk = opq(lds + 155648 + w * 512 + lane * 4);
        u64 pv = 0;
        const unsigned pkd = lds0 + 155648 + w * 512;
        const unsigned rdo = (FL_RD + (w >> 1) * 7 + m7) * 128;
        waitv_barx<3>();
        TS(27);
#pragma unroll
        for (int n = 0; n < PF; ++n) bf[n] = lds8(HB(n));
        SB();
        sfor<0, 125>([&](auto N_) __attribute__((always_inline)) {
            constexpr int n = decltype(N_)::v, p = n / 28, s = n % 28;
            if constexpr (n == 4) waitv_barx<3>();
            if constexpr (n == 12) waitv_barx<8>();
            if constexpr (n == 28) { waitv_barx<3>(); TS(13); }
            if constexpr (n == 64) { waitv_barx<10>(); if (w == 0) set_flag(ws, fo4, tagv); TS(14); }
            if constexpr (n == 85) { waitv_barx<8>(); if (w == 0) set_flag(ws, fo4 + 2048, tagv); }
            if constexpr (n == 32) { if (__builtin_expect(!wave_all(pv == tag), 0)) poll_flag(ws, rdo, tag); FENCE(); }
            if constexpr (d_prio(n) != d_prio(n - 1)) __builtin_amdgcn_s_setprio(d_prio(n));
            if constexpr (n < 112) {
                if constexpr (s < 2) { ag[p & 1][s] = (f32x4){}; au[p & 1][s] = (f32x4){}; }
                ag[p & 1][s & 1] = MFMA16(wf[s & ~1], bf[n % (PF + 1)], ag[p & 1][s & 1]); au[p & 1][s & 1] = MFMA16(wf[s | 1], bf[n % (PF + 1)], au[p & 1][s & 1]);
                PIN2(ag[p & 1][s & 1], au[p & 1][s & 1]);
            }
            SB();
            if constexpr (n + PF < 112) bf[(n + PF) % (PF + 1)] = lds8(HB(n + PF));
            if constexpr (n % 3 == 2 && n < 30) H_PIECE(4 + n / 3);
            if constexpr (n == 4) glds4_dev(l16, hss, lds0 + 114688 + w * 5120);
            if constexpr (n == 16) { waitv<4>();
#pragma unroll
                for (int j = 0; j < 7; ++j) sp[j] = *(LAS f32x2*)(lds + 114688 + w * 5120 + (hf * 7 + j) * 256 + r * 8);
            }
            if constexpr (n == 20) { const f32x2 t = ((sp[0] + sp[1]) + (sp[2] + sp[3])) + ((sp[4] + sp[5]) + sp[6]); stp[w * 32 + g1 * 16] = (f32x2){swap_add(t[0]), swap_add(t[1])}; }
            if constexpr (wd_at(n) >= 0) { constexpr int i = wd_at(n);
                if constexpr (i < 5) glds_dev(pwd, wds + WDO(i), lds0 + 114688 + w * 5120 + i * 1024);
                else if constexpr (i < 12) glds_dev(pwd, wds + WDO(i), lds0 + w * 7168 + (i - 5) * 1024);
                else WDLD(i); }
            if constexpr (n == 84) peek_lds(ws, gfi - g1 * 2048, pkd);
            if constexpr (n == 95) { waitv<5>(); pv = peeked(pk); }
            if constexpr (n == 99) { if (__builtin_expect(!wave_all(pv == tag), 0)) poll_flag(ws, gfi - g1 * 2048, tag); FENCE(); }
            if constexpr (n >= 101 && n < 101 + 2 * XN && n % 2 == 1) G_LD((n - 101) / 2);
            if constexpr (n == 106) gfl = peek_flag(ws, gfi);
            if constexpr (n == 28) pv = peeked(pk);
            if constexpr (n == 18) peek_lds(ws, rdo, pkd);
            if constexpr (p >= 1) {
                constexpr int e = (p - 1) & 1, tp = n == 100 ? 3 : p - 1;
                if constexpr ((s == 0 && p < 4) || n == 100) { sv[0] = stp[tp * 32]; sv[1] = stp[(tp + 4) * 32]; sv[2] = stp[tp * 32 + 16]; sv[3] = stp[(tp + 4) * 32 + 16]; }
                if constexpr (((s == 2 || s == 3) && p < 4) || n == 104 || n == 105) { constexpr int k = n & 1; const f32x2 t = sv[2 * k] + sv[2 * k + 1];
                    mean[k] = t[0] * (1.f / D); r2[k] = __builtin_amdgcn_rcpf(__builtin_fmaxf(t[1] * (1.f / D) - mean[k] * mean[k], 0.f) + EPS); PIN2(mean[k], r2[k]); }
                if constexpr (s >= 4 && s < 12) { constexpr int i = s - 4, k = i >> 2;
                    const float h0 = __builtin_fmaxf(ag[e][k][i & 3] - mean[k] * csg[i & 3], 0.f) * r2[k] * (au[e][k][i & 3] - mean[k] * csu[i & 3]);
                    if constexpr (i & 1) { hv[i >> 1] = p >= 3 ? umin(cvtpk(hp, h0), ~1u) : cvtpk(hp, h0); PIN(hv); } else { hp = h0; PIN(hp); } }
                if constexpr (s == 12) {
                    const auto s0 = __builtin_amdgcn_permlane32_swap(hv[0], hv[2], false, false), s1 = __builtin_amdgcn_permlane32_swap(hv[1], hv[3], false, false);
                    st16_dev((u32x4){s0[0], s1[0], s0[1], s1[1]}, rws, go + (p - 1) * 114688, 0); FENCE();
                }
            }
            SB();
        });
        TS(15);
#pragma unroll
        for (int s = XN; s < 7; ++s) { G_LD(s); SB(); }
        FENCE();
#pragma unroll
        for (int s = 0; s < 12; ++s) wdf[s] = lds8(s < 5 ? wl + s * 1024 : wl2 + (s - 5) * 1024);
    }

    {
        const int etok = tid >> 4, eblk = tid & 15;
        const int et = erb * 4 + (etok >> 4), er = (tb & 1) * 16 + (etok & 15);
        const unsigned hro = (unsigned)((((eblk >> 2) & 3) * 64 + (eblk & 1) * 32 + er) * 16 + ((eblk >> 1) & 1) * 8) + WS_HF + (unsigned)(et * 28 + m7 * 4) * 1024;
        u32x2 hres[2];
        GAS u64* op = (GAS u64*)(out + (size_t)(et * 32 + er) * 896 + m7 * 128 + eblk * 8);
        lds_ptr rp = opq(lds + w * 8448 + (lane >> 4) * 528 + (lane & 15) * 16), rq = opq(lds + eblk * 528 + etok * 16);
        lds_ptr rp2 = opq(rp + 67584), rq2 = opq(rq + 67584);
        f32x4 c[4];
#define G_STEP(s) do { _Pragma("unroll") for (int j = 0; j < 4; ++j) { if ((s) % 7 == 0) c[j] = (f32x4){}; c[j] = MFMA16(wdf[4 * ((s) % 7) + j], gf[(s) % GW], c[j]); } PIN2(c[0], c[1]); PIN2(c[2], c[3]); } while (0)
#define G_PUT(q) do { _Pragma("unroll") for (int j = 0; j < 4; ++j) *(LAS f32x4*)(((q) < 2 ? rp : rp2) + j * 2112 + ((q) & 1) * 256) = c[j]; } while (0)
#define G_OUT(hh, RQ) do { f32x4 pa[8]; \
            _Pragma("unroll") for (int ww = 0; ww < 8; ++ww) pa[ww] = *(LAS f32x4*)(RQ + ww * 8448); SB(); \
            const f32x4 a = ((pa[0] + pa[1]) + (pa[2] + pa[3])) + ((pa[4] + pa[5]) + (pa[6] + pa[7])); \
            const u32x2 ov = {cvtpk(a[0] + bflo(hres[hh][0]), a[1] + bfhi(hres[hh][0])), cvtpk(a[2] + bflo(hres[hh][1]), a[3] + bfhi(hres[hh][1]))}; \
            __hip_atomic_store(op + (hh) * (64 * 896 / 8), __builtin_bit_cast(u64, ov), __ATOMIC_RELAXED, __HIP_MEMORY_SCOPE_AGENT); } while (0)
        TS(16);
        unsigned seen = 0;
#pragma unroll
        for (int s = 0; s < 28; ++s) {
            if (s == 5) BAR();
            if (s == 7 || s == 14 || s == 21) G_PUT(s / 7 - 1);
            if (s == 14) TS(17);
            if (s == 21) { waitv<7>(); PIN2(hres[0], hres[1]); BAR(); G_OUT(0, rq); FENCE(); TS(18); }
            SB();
            G_STEP(s);
            if (s >= 14) { const u32x4 u = U4(gf[s % GW]); seen = umax3(umax3(seen, u[0], u[1]), u[2], u[3]); PIN(seen); }
            SB();
            if (s == 0) { if (__builtin_expect(!wave_all(gfl == tag), 0)) poll_flag(ws, gfi, tag); FENCE(); }
            if (s + GW < 28) G_LD(s + GW);
            if (s == 1) { hres[0] = ld8_dev(rws, hro, 0); hres[1] = ld8_dev(rws, hro, 57344); FENCE(); }
        }
        if (w == 0) { set_flag(ws, fo4 + 4096, tagv); set_flag(ws, fo4 + 6144, tagv); }
        if (__builtin_expect(wave_any(seen == ~0u), 0)) {
            poll_flag(ws, gfi + 4096, tag);
#pragma unroll
            for (int s = 14; s < 14 + GW; ++s) G_LD(s);
#pragma unroll
            for (int s = 14; s < 28; ++s) { if (s == 21) G_PUT(2); G_STEP(s); SB(); if (s + GW < 28) G_LD(s + GW); SB(); }
        }
        G_PUT(3);
        TS(19);
        BAR();
        G_OUT(1, rq2);
    }
    TS(20);
}

extern "C" void kernel_launch(void* const* d_in, const int* in_sizes, int n_in, void* d_out, int out_size, void* d_ws, size_t ws_size, hipStream_t stream) {
    (void)hipFuncSetAttribute((const void*)block_fwd, hipFuncAttributeMaxDynamicSharedMemorySize, LDS_BYTES);
    block_fwd<<<256, 512, LDS_BYTES, stream>>>((const char*)d_in[0], (const char*)d_in[1], (const char*)d_in[2], (const char*)d_in[4], (const char*)d_in[5], (const char*)d_in[6], (char*)d_out, (char*)d_ws);
}
```

```cpp
#include <hip/hip_runtime.h>
#include <hip/hip_bf16.h>
#include <stdint.h>

typedef __attribute__((ext_vector_type(8))) short bf16x8;
typedef __attribute__((ext_vector_type(4))) short s16x4;
typedef __attribute__((ext_vector_type(2))) __bf16 bf16x2;
typedef __attribute__((ext_vector_type(16))) float f32x16;
typedef __attribute__((ext_vector_type(4))) float f32x4;
typedef __attribute__((ext_vector_type(2))) float f32x2;
typedef __attribute__((ext_vector_type(4))) unsigned u32x4;
typedef __attribute__((ext_vector_type(2))) unsigned u32x2;
typedef short v4i16_t __attribute__((ext_vector_type(4)));
typedef unsigned long long u64;
#define LAS __attribute__((address_space(3)))
#define GAS __attribute__((address_space(1)))
typedef LAS char* lds_ptr;
#if defined(__HIP_DEVICE_COMPILE__)
#define NOPK __attribute__((target("no-packed-fp32-ops")))
#else
#define NOPK
#endif
#define DI __device__ __forceinline__
#define MFMA(a, b, c) __builtin_amdgcn_mfma_f32_32x32x16_bf16(a, b, c, 0, 0, 0)
#define MFMA16(a, b, c) __builtin_amdgcn_mfma_f32_16x16x32_bf16(a, b, c, 0, 0, 0)
#define BF8(x) __builtin_bit_cast(bf16x8, x)
#define U4(x) __builtin_bit_cast(u32x4, x)

constexpr int D = 448;
constexpr float EPS = 1e-5f;
constexpr unsigned FL_EPOCH = 0, FL_KV = 1, FL_RT = FL_KV + 256, FL_RD = FL_RT + 32, FL_O = FL_RD + 32, FL_H = FL_O + 256, FL_G = FL_H + 256;
constexpr unsigned WS_KV = 1u << 20;
constexpr unsigned WS_QF = WS_KV + 4194304;
constexpr unsigned WS_OF = WS_QF + 2097152;
constexpr unsigned WS_HF = WS_OF + 2097152;
constexpr unsigned WS_GF = WS_HF + 64 * 28 * 1024;
constexpr unsigned WS_WGU = WS_GF + 64 * 112 * 1024;
constexpr unsigned WS_WD = WS_WGU + 112 * 28 * 1024;
constexpr unsigned WS_HS = WS_WD + 7 * 112 * 2 * 1024;
constexpr unsigned WS_END = WS_HS + 64 * 28 * 256 + 1024;
constexpr unsigned OOB = 0x40000000u;
constexpr int LDS_BYTES = 163840;

DI float u2f(unsigned u) { return __builtin_bit_cast(float, u); }
DI unsigned f2u(float f) { return __builtin_bit_cast(unsigned, f); }
DI unsigned umin(unsigned a, unsigned b) { return a < b ? a : b; }
DI unsigned umax3(unsigned a, unsigned b, unsigned c) { const unsigned m = a > b ? a : b; return m > c ? m : c; }
DI bool wave_any(bool p) { return __builtin_amdgcn_ballot_w64(p) != 0; }
DI bool wave_all(bool p) { return __builtin_amdgcn_ballot_w64(!p) == 0; }
DI unsigned cvtpk(float lo, float hi) { return __builtin_bit_cast(unsigned, __builtin_convertvector((f32x2){lo, hi}, bf16x2)); }
DI float bflo(unsigned u) { return u2f(u << 16); }
DI float bfhi(unsigned u) { return u2f(u & 0xffff0000u); }
DI void glds(unsigned voff, const void* sbase, unsigned dst) {
    asm volatile("s_mov_b32 m0, %2\n\ts_nop 0\n\tglobal_load_lds_dwordx4 %0, %1" :: "v"(voff), "s"(sbase), "s"(dst) : "memory");
}
DI void glds_dev(unsigned voff, const void* sbase, unsigned dst) {
    asm volatile("s_mov_b32 m0, %2\n\ts_nop 0\n\tglobal_load_lds_dwordx4 %0, %1 sc1" :: "v"(voff), "s"(sbase), "s"(dst) : "memory");
}
DI void glds2_dev(unsigned voff, const void* sbase, unsigned dst) {
    asm volatile("s_mov_b32 m0, %2\n\ts_nop 0\n\tglobal_load_lds_dwordx4 %0, %1 sc1\n\tglobal_load_lds_dwordx4 %0, %1 offset:1024 sc1" :: "v"(voff), "s"(sbase), "s"(dst) : "memory");
}
DI void glds4_dev(unsigned voff, const void* sbase, unsigned dst) {
    asm volatile("s_mov_b32 m0, %2\n\ts_nop 0\n\tglobal_load_lds_dwordx4 %0, %1 sc1\n\tglobal_load_lds_dwordx4 %0, %1 offset:1024 sc1\n\t"
                 "global_load_lds_dwordx4 %0, %1 offset:2048 sc1\n\tglobal_load_lds_dwordx4 %0, %1 offset:3072 sc1" :: "v"(voff), "s"(sbase), "s"(dst) : "memory");
}
DI s16x4 trd(lds_ptr p) { return __builtin_bit_cast(s16x4, __builtin_amdgcn_ds_read_tr16_b64_v4i16((LAS v4i16_t*)p)); }
DI bf16x8 cat(s16x4 a, s16x4 b) { return __builtin_shufflevector(a, b, 0, 1, 2, 3, 4, 5, 6, 7); }
DI bf16x8 lds8(lds_ptr p) { return *(LAS bf16x8*)p; }
DI float swap_add(float v) { auto r = __builtin_amdgcn_permlane32_swap(f2u(v), f2u(v), false, false); return u2f(r[0]) + u2f(r[1]); }
DI float swap_max(float v) { auto r = __builtin_amdgcn_permlane32_swap(f2u(v), f2u(v), false, false); return __builtin_fmaxf(u2f(r[0]), u2f(r[1])); }
DI u32x4 pack8(const f32x16& a, int s) { return (u32x4){cvtpk(a[8 * s], a[8 * s + 1]), cvtpk(a[8 * s + 2], a[8 * s + 3]), cvtpk(a[8 * s + 4], a[8 * s + 5]), cvtpk(a[8 * s + 6], a[8 * s + 7])}; }
DI float dot2(unsigned a, unsigned b, float c) { return __builtin_amdgcn_fdot2_f32_bf16(__builtin_bit_cast(bf16x2, a), __builtin_bit_cast(bf16x2, b), c, false); }
DI void acc_stats2(unsigned u0, unsigned u1, f32x4& s) {
    const float a = bflo(u0), b = bfhi(u0), c = bflo(u1), d = bfhi(u1);
    s[0] += a; s[1] += b; s[2] = __builtin_fmaf(a, a, s[2]); s[3] = __builtin_fmaf(b, b, s[3]); s[0] += c; s[1] += d; s[2] = __builtin_fmaf(c, c, s[2]); s[3] = __builtin_fmaf(d, d, s[3]);
}
DI float sum8(float v) {
    v += u2f(__builtin_amdgcn_update_dpp(0u, f2u(v), 0xB1, 0xf, 0xf, true));
    v += u2f(__builtin_amdgcn_update_dpp(0u, f2u(v), 0x4E, 0xf, 0xf, true));
    v += u2f(__builtin_amdgcn_update_dpp(0u, f2u(v), 0x141, 0xf, 0xf, true));
    return v;
}
DI u32x4 shift8(u32x4 x, float c) {
    u32x4 o; float t0, t1, t2, t3, t4, t5, t6, t7;
    asm("v_dot2_f32_bf16 %4, %12, %16, %18\n\tv_dot2_f32_bf16 %5, %12, %17, %18\n\tv_dot2_f32_bf16 %6, %13, %16, %18\n\tv_dot2_f32_bf16 %7, %13, %17, %18\n\t"
        "v_dot2_f32_bf16 %8, %14, %16, %18\n\tv_dot2_f32_bf16 %9, %14, %17, %18\n\tv_dot2_f32_bf16 %10, %15, %16, %18\n\tv_dot2_f32_bf16 %11, %15, %17, %18\n\t"
        "v_cvt_pk_bf16_f32 %0, %4, %5\n\tv_cvt_pk_bf16_f32 %1, %6, %7\n\tv_cvt_pk_bf16_f32 %2, %8, %9\n\tv_cvt_pk_bf16_f32 %3, %10, %11"
        : "=&v"(o[0]), "=&v"(o[1]), "=&v"(o[2]), "=&v"(o[3]), "=&v"(t0), "=&v"(t1), "=&v"(t2), "=&v"(t3), "=&v"(t4), "=&v"(t5), "=&v"(t6), "=&v"(t7)
        : "v"(x[0]), "v"(x[1]), "v"(x[2]), "v"(x[3]), "s"(0x00003f80u), "s"(0x3f800000u), "v"(c));
    return o;
}
template <int N> struct IC { static constexpr int v = N; };
template <int B, int E, class F> DI void sfor(F&& f) { if constexpr (B < E) { f(IC<B>{}); sfor<B + 1, E>(f); } }
template <int N> DI void waitv() { asm volatile("s_waitcnt vmcnt(%0)" :: "i"(N) : "memory"); }
template <int N> DI void waitv_bar() { if (N == 0) __builtin_amdgcn_s_waitcnt(0x0F70); asm volatile("s_waitcnt vmcnt(%0) lgkmcnt(0)\n\ts_barrier" :: "i"(N) : "memory"); }
template <int N> DI void waitv_barx() { asm volatile("s_waitcnt vmcnt(%0)\n\ts_barrier" :: "i"(N) : "memory"); }
template <int N> DI void waitv_bar2() { asm volatile("s_waitcnt vmcnt(%0) lgkmcnt(2)\n\ts_barrier" :: "i"(N) : "memory"); }
#define BAR() asm volatile("s_waitcnt lgkmcnt(0)\n\ts_barrier" ::: "memory")
#define FENCE() asm volatile("" ::: "memory")
typedef __amdgpu_buffer_rsrc_t rsrc_t;
DI u32x4 ld16_dev(rsrc_t rs, unsigned voff, unsigned soff) { return U4(__builtin_amdgcn_raw_buffer_load_b128(rs, voff, soff, 16)); }
DI u32x2 ld8(rsrc_t rs, unsigned voff, unsigned soff) { return __builtin_bit_cast(u32x2, __builtin_amdgcn_raw_buffer_load_b64(rs, voff, soff, 0)); }
DI u32x2 ld8_dev(rsrc_t rs, unsigned voff, unsigned soff) { return __builtin_bit_cast(u32x2, __builtin_amdgcn_raw_buffer_load_b64(rs, voff, soff, 16)); }
DI void st16_dev(u32x4 v, rsrc_t rs, unsigned voff, unsigned soff) { __builtin_amdgcn_raw_buffer_store_b128(v, rs, voff + soff, 0, 16); }
DI void st8_dev(u32x2 v, rsrc_t rs, unsigned voff, unsigned soff) { __builtin_amdgcn_raw_buffer_store_b64(v, rs, voff, soff, 16); }
DI void set_flag(char* ws, unsigned fo, u64 tagv) {
    asm volatile("s_mov_b64 exec, 1\n\tglobal_store_dwordx2 %0, %1, %2 sc1\n\ts_mov_b64 exec, -1" :: "v"(fo), "v"(tagv), "s"(ws) : "memory");
}
DI u64 peek_flag(char* ws, unsigned fo) { return __hip_atomic_load((GAS u64*)(ws + fo), __ATOMIC_RELAXED, __HIP_MEMORY_SCOPE_AGENT); }
DI void peek_lds(char* ws, unsigned fo, unsigned dst) {
    asm volatile("s_mov_b32 m0, %2\n\ts_nop 0\n\tglobal_load_lds_dword %0, %1 sc1\n\ts_add_u32 m0, %2, 252\n\ts_nop 0\n\tglobal_load_lds_dword %0, %1 offset:4 sc1" :: "v"(fo), "s"(ws), "s"(dst) : "memory", "scc");
}
DI u64 peeked(lds_ptr p) { return (u64)*(LAS unsigned*)(p + 256) << 32 | *(LAS unsigned*)p; }
DI void poll_flag(char* ws, unsigned fo, u64 tag) {
#pragma unroll 1
    for (int spin = 0; spin < (1 << 21); ++spin) {
        if (wave_all(peek_flag(ws, fo) == tag)) break;
        __builtin_amdgcn_s_sleep(1);
    }
    FENCE();
}
#define SB() __builtin_amdgcn_sched_barrier(0)
#define PINS(a) asm volatile("" : "+s"(a))
#define PIN(a) asm volatile("" : "+v"(a))
#define PIN2(a, b) asm volatile("" : "+v"(a), "+v"(b))
DI lds_ptr opq(lds_ptr p) { unsigned u = (unsigned)(uintptr_t)p; asm volatile("" : "+v"(u)); return (lds_ptr)(uintptr_t)u; }

constexpr int GW = 7, XN = 5;
DI constexpr int d_prio(int n) { return n < 0 ? -1 : n < 27 ? 3 - n / 7 : n < 64 ? 3 - (n - 27) / 10 : n < 84 ? 3 - (n - 64) / 5 : n < 112 ? 3 - (n - 84) / 7 : 0; }
DI constexpr int wd_at(int n) {
    constexpr int t[28] = {32, 34, 37, 39, 42, 65, 69, 72, 75, 78, 81, 83, 44, 47, 49, 52, 54, 57, 59, 61, 63, 86, 88, 90, 92, 94, 97, 99};
    for (int i = 0; i < 28; ++i) if (t[i] == n) return i;
    return -1;
}
DI constexpr int phys(int t) { return ((t % 9) + 2) % 9 * 16384; }

__global__ NOPK __launch_bounds__(512, 2) void block_fwd(const char* x, const char* wqkv, const char* wo, const char* wg, const char* wu, const char* wd, char* out, char* ws) {
    extern __shared__ __attribute__((aligned(16))) char smem[];
    lds_ptr lds = (lds_ptr)smem;
    const int tid = __builtin_amdgcn_workitem_id_x(), lane = tid & 63, w = __builtin_amdgcn_readfirstlane(tid >> 6);
    const int bid = __builtin_amdgcn_workgroup_id_x(), xcd = bid & 7, loc = bid >> 3;
    const int r = lane & 31, hf = lane >> 5, q4 = (lane & 15) >> 2, g1 = (lane >> 4) & 1, p4 = lane & 3;
    const unsigned lds0 = (unsigned)(uintptr_t)smem;
    const unsigned l16 = lane * 16;
    const int sp3 = (loc + 8) >> 5;
    const int atb = (xcd >> 1) * 8 + (loc & 7) + sp3 * (xcd & 1) * 4, acg = (loc >> 3) + (1 - sp3) * (xcd & 1) * 4;
    const int row8 = lane >> 3;
    const int rot8 = ((loc & 7) << sp3) + sp3 * (xcd & 1), rot = rot8 == 7 ? 0 : rot8;
    if (loc < 28) {
        const unsigned xo = (unsigned)((atb * 64 + (tid >> 3)) * 896 + (((tid ^ (tid >> 4)) & 7) * 16));
        const unsigned xd0 = lds0 + 1024 + (w >> 2) * 28672 + (w & 3) * 1024;
        unsigned xd = xd0 + (7 - rot) * 4096;
        const unsigned w0 = (unsigned)((rot * 64 + (tid >> 3)) * 2688 + acg * 128 + (((tid & 7) ^ ((tid >> 2) & 4)) * 16));
        asm volatile("s_cmp_eq_u32 %0, %3\n\ts_cselect_b32 %0, %2, %0\n\ts_mov_b32 m0, %0\n\ts_add_u32 %0, %0, 0x1000\n\tglobal_load_lds_dwordx4 %1, %4\n\t"
#define XP(o) "s_cmp_eq_u32 %0, %3\n\ts_cselect_b32 %0, %2, %0\n\ts_sub_u32 m0, %0, " #o "\n\ts_add_u32 %0, %0, 0x1000\n\tglobal_load_lds_dwordx4 %1, %4 offset:" #o "\n\t"
                     XP(128) XP(256) XP(384) XP(512) XP(640) XP(768)
                     "s_add_u32 m0, %6, 8192-896\n\ts_barrier\n\tglobal_load_lds_dwordx4 %5, %7 offset:896\n\t"
                     "s_add_u32 m0, %6, 16384-1792\n\ts_nop 0\n\tglobal_load_lds_dwordx4 %5, %7 offset:1792\n\t"
                     "s_mov_b32 m0, %6\n\ts_nop 0\n\tglobal_load_lds_dwordx4 %5, %7"
                     : "+&s"(xd) : "v"(xo), "s"(xd0), "s"(xd0 + 7 * 4096), "s"(x), "v"(w0), "s"(lds0 + 58368 + w * 1024), "s"(wqkv) : "memory", "scc");
    }
    SB();
    const rsrc_t rws = __builtin_amdgcn_make_buffer_rsrc((void*)ws, 0, WS_END, 0x00020000);
    u64 epoch, tagv;
    unsigned fl1;
    const unsigned ones = 0x3f803f80u;
    const bool ffn = loc < 28;
    const int l7 = loc / 7, m7 = loc % 7;
    const int tb = xcd * 4 + l7;
    const int tt = w & 1, ct = (w >> 1) & 1, kh = w >> 2;
    const unsigned swz = (unsigned)(((lane & 7) ^ (((row8 >> 1) & 1) << 2)) * 16);

    if (__builtin_expect(ffn, 1)) {
        constexpr int A_W = 58368, A_WS = 24576;
#define A_KC(c) (rot + (c) >= 7 ? rot + (c) - 7 : rot + (c))
        const unsigned wvo = (unsigned)(row8 * 2688) + swz;
        const char* wsrc = wqkv + acg * 128 + (size_t)w * 8 * 2688;
        const unsigned wdst = lds0 + A_W + w * 1024;
#define A_PIECE(c, i) glds(wvo, wsrc + A_KC(c) * (64 * 2688) + (i) * 896, wdst + ((c) & 3) * A_WS + (i) * 8192)
#define A_GROUP(c) do { A_PIECE(c, 1); A_PIECE(c, 2); A_PIECE(c, 0); } while (0)
        fl1 = (FL_KV + atb * 8 + acg) * 128;
        {
            const unsigned f1_ = FL_KV + atb * 8 + acg, f2_ = FL_O + (loc >> 1) * 16 + xcd * 2 + (loc & 1), f3_ = FL_H + tb * 8 + m7, f4_ = FL_G + ((xcd >> 1) * 4 + l7) * 64 + (xcd & 1) * 7 + m7;
            const unsigned lo_ = (lane & 2) ? ((lane & 1) ? f3_ : f2_) : ((lane & 1) ? f1_ : FL_EPOCH), hi4 = f4_ + (lane & 3) * 16;
            const unsigned fa = (lane & 4) ? hi4 : lo_;
            epoch = peek_flag(ws, fa * 128); FENCE();
        }
        lds_ptr xb[4], wb1, wb1h;
#pragma unroll
        for (int j = 0; j < 4; ++j) xb[j] = lds + 1024 + tt * 28672 + (r >> 3) * 1024 + (r & 7) * 128 + (((2 * j + hf) ^ ((r >> 1) & 7)) * 16);
        wb1 = opq(lds + A_W + (kh ? 0 : 8192) + (8 * hf + q4) * 128 + ((ct ^ (q4 >> 1)) * 64) + g1 * 32 + p4 * 8);
        wb1h = opq(wb1 + 2 * A_WS);
        unsigned go1[2], go2[2]; int gi[2];
#pragma unroll
        for (int p = 0; p < 2; ++p) {
            const int G = acg * 8 + ct * 4 + 2 * p + hf, hp = G / 7, i = G - hp * 7;
            const unsigned hs = kh ? 131072u : 262144u;
            const unsigned to = (kh ? WS_QF + (atb * 2 + tt) * 2048 : WS_KV + (atb * 2 + tt) * 4096) + r * 16;
            go1[p] = (unsigned)(2 * hp + (i > 3)) * hs + (unsigned)(i > 3 ? i - 3 : i) * 512 + to;
            go2[p] = i == 3 ? (unsigned)(2 * hp + 1) * hs + to : OOB;
            gi[p] = i;
        }
        const int vcol = acg * 64 + ct * 32 + r, vh = vcol / 28, vd = vcol - vh * 28;
        unsigned vo = WS_KV + (atb * 2 + tt) * 4096 + (unsigned)vh * 262144 + 2048 + hf * 512 + vd * 16;
        PIN2(go1[0], go1[1]); PIN2(go2[0], go2[1]); PIN(vo);
        lds_ptr xw = opq(lds + 1024 + kh * 28672 + (w & 3) * 1024 + l16);
        waitv<4>();
        u32x4 xv[7];
#pragma unroll
        for (int c = 0; c < 7; ++c) xv[c] = *(LAS u32x4*)(xw + c * 4096);
        SB();
        f32x4 sm = {};
#pragma unroll
        for (int c = 0; c < 7; ++c)
#pragma unroll
            for (int j = 0; j < 4; ++j) sm[j] = dot2(xv[c][j], ones, sm[j]);
        float nm = sum8((sm[0] + sm[1]) + (sm[2] + sm[3])) * (-1.f / D);
        PIN(nm);
#pragma unroll
        for (int c = 0; c < 7; ++c) { xv[c] = shift8(xv[c], nm); *(LAS u32x4*)(xw + c * 4096) = xv[c]; }
        A_GROUP(1);
        SB();
        LAS float* rst = (LAS float*)lds;
        sm = (f32x4){};
#pragma unroll
        for (int c = 0; c < 7; ++c)
#pragma unroll
            for (int j = 0; j < 4; ++j) sm[j] = dot2(xv[c][j], xv[c][j], sm[j]);
        rst[w * 8 + row8] = __builtin_amdgcn_rsqf(sum8((sm[0] + sm[1]) + (sm[2] + sm[3])) * (1.f / D) + EPS);
        A_GROUP(2);
        bf16x8 fx[4], f1[4], f2[4];
#define A_WAIT(c) do { if ((c) == 0) waitv_bar<6>(); else if ((c) == 1) waitv_barx<6>(); else if ((c) < 6) waitv_barx<3>(); else waitv_barx<0>(); } while (0)
#define A_WP(c, ks) ((((c) & 2) ? wb1h : wb1) + ((c) & 1) * A_WS + (ks) * 2048)
        if (w < 4) {
            f32x16 ak = {}, av = {};
            f32x4 rv[4];
            float rs = 0.f;
#pragma unroll
            for (int n = 0; n < 30; ++n) {
                const int c = n >> 2, ks = n & 3, km = (n - 2) & 3;
                if (n < 28 && ks == 0) A_WAIT(c);
                if (n >= 2) { ak = MFMA(f1[km], fx[km], ak); PIN(ak); } SB();
                if (n < 28) { fx[ks] = lds8(xb[ks] + c * 4096); f1[ks] = cat(trd(A_WP(c, ks)), trd(A_WP(c, ks) + 512)); } SB();
                if (n >= 2) { av = MFMA(fx[km], f2[km], av); PIN(av); } SB();
                if (n < 28) f2[ks] = cat(trd(A_WP(c, ks) + 8192), trd(A_WP(c, ks) + 8192 + 512));
                if (c == 0 && ks < 3) A_PIECE(3, ks);
                if (c >= 2 && c < 5 && ks < 3) A_PIECE(c + 2, ks);
                if (n == 21) {
                    rs = rst[tt * 32 + r];
#pragma unroll
                    for (int g = 0; g < 4; ++g) rv[g] = *(LAS f32x4*)(rst + tt * 32 + 8 * g + 4 * hf);
                }
                if (n == 24) { PIN(epoch); tagv = epoch + 1; PIN(tagv); }
                SB();
            }
#pragma unroll
            for (int i = 0; i < 16; ++i) { ak[i] *= rs; av[i] *= rv[i >> 2][i & 3]; }
#pragma unroll
            for (int p = 0; p < 2; ++p) {
                const u32x4 f = pack8(ak, p);
                const auto s0 = __builtin_amdgcn_permlane32_swap(f[0], f[2], false, false), s1 = __builtin_amdgcn_permlane32_swap(f[1], f[3], false, false);
                const u32x4 c = {s0[0], s1[0], s0[1], s1[1]};
                st16_dev(c, rws, go1[p], 0); st16_dev(c, rws, go2[p], 0);
            }
            st16_dev(pack8(av, 0), rws, vo, 0); st16_dev(pack8(av, 1), rws, vo + 1024, 0);
        } else {
            f32x16 aq = {};
            __builtin_amdgcn_s_setprio(1);
#pragma unroll
            for (int n = 0; n < 30; ++n) {
                const int c = n >> 2, ks = n & 3, km = (n - 2) & 3;
                if (n < 28 && ks == 0) A_WAIT(c);
                if (n == 24) { PIN(epoch); tagv = epoch + 1; PIN(tagv); }
                if (n >= 2) { aq = MFMA(f1[km], fx[km], aq); PIN(aq); } SB();
                if (n < 28) { fx[ks] = lds8(xb[ks] + c * 4096); f1[ks] = cat(trd(A_WP(c, ks)), trd(A_WP(c, ks) + 512)); }
                if (c == 0 && ks < 3) A_PIECE(3, ks);
                if (c >= 2 && c < 5 && ks < 3) A_PIECE(c + 2, ks);
                if (n == 24) {
                    const unsigned pv = vd == 0 ? ones : 0u, po = vd < 4 ? vo + 448 : OOB;
                    st16_dev((u32x4){pv, pv, pv, pv}, rws, po, 0); st16_dev((u32x4){pv, pv, pv, pv}, rws, po + 1024, 0);
                }
                SB();
            }
            __builtin_amdgcn_s_setprio(0);
            const float QS = rst[tt * 32 + r] * (0.18898223650461362f * 1.4426950408889634f);
#pragma unroll
            for (int i = 0; i < 16; ++i) aq[i] *= QS;
#pragma unroll
            for (int p = 0; p < 2; ++p) {
                const u32x4 f = pack8(aq, p);
                const auto s0 = __builtin_amdgcn_permlane32_swap(f[0], f[2], false, false), s1 = __builtin_amdgcn_permlane32_swap(f[1], f[3], false, false);
                const bool sp = gi[p] == 3;
                st16_dev((u32x4){s0[0], s1[0], sp ? 0u : s0[1], sp ? 0u : s1[1]}, rws, go1[p], 0);
                st16_dev((u32x4){0u, 0u, s0[1], s1[1]}, rws, go2[p], 0);
            }
        }
    } else {
        __builtin_amdgcn_s_sleep(8);
        fl1 = (FL_RT + xcd * 4 + loc - 28) * 128;
        epoch = peek_flag(ws, (lane & 3) == 0 ? FL_EPOCH * 128 : (lane & 3) == 1 ? fl1 : (lane & 3) == 2 ? fl1 + 32 * 128 : (FL_O + (loc >> 1) * 16 + xcd * 2 + (loc & 1)) * 128);
        const int ri = xcd * 4 + loc - 28, nu = (w == 0 && ri < 16) ? 4 : 3;
        const unsigned vo = (unsigned)(row8 * 3584 + (lane & 7) * 16);
#pragma unroll 1
        for (int i = 0; i < nu; ++i) {
            const int u = i < 3 ? i * 256 + ri * 8 + w : 768 + ri;
            const size_t so = (size_t)(u / 28) * 16 * 3584 + (u % 28) * 128;
            const unsigned d_ = lds0 + w * 16384 + i * 4096;
            glds(vo, wg + so, d_); glds(vo, wg + so + 8 * 3584, d_ + 1024); glds(vo, wu + so, d_ + 2048); glds(vo, wu + so + 8 * 3584, d_ + 3072);
        }
#pragma unroll 1
        for (int i = 0; i < nu; ++i) {
            const int u = i < 3 ? i * 256 + ri * 8 + w : 768 + ri;
            waitv<8>();
            lds_ptr sg = lds + w * 16384 + i * 4096 + g1 * 2048 + (4 * hf + q4) * 128 + p4 * 8;
#pragma unroll
            for (int e = 0; e < 4; ++e) st16_dev(U4(cat(trd(sg + e * 32), trd(sg + e * 32 + 1024))), rws, l16, WS_WGU + (unsigned)(((u % 28) * 4 + e) * 28 + u / 28) * 1024);
            FENCE();
        }
        tagv = epoch + 1;
    }
    const int head = xcd * 2 + (loc & 1), rb = loc >> 1, qt = w & 3;
    const char* kvs = ws + WS_KV + (size_t)head * 262144 + w * 2048;
    const char* qs = ws + WS_QF + (size_t)(head * 64 + rb * 4 + qt) * 2048 + kh * 1024;
    unsigned kvd = lds0 + w * 2048, qd = lds0 + 147456 + qt * 2048 + kh * 1024;
    unsigned pf1 = (FL_KV + (lane >> 1) * 8 + (head * 28 + (lane & 1) * 27) / 64) * 128;
    PINS(kvs); PINS(qs); PINS(kvd); PINS(qd); PIN2(pf1, fl1);
    waitv_bar<0>();
    if (w == 0) set_flag(ws, fl1, tagv);
    const u64 tag = ((u64)(unsigned)__builtin_amdgcn_readfirstlane((unsigned)(tagv >> 32)) << 32 | (u64)(unsigned)__builtin_amdgcn_readfirstlane((unsigned)tagv));

    const int drb = (xcd >> 1) * 4 + l7, dfg = (xcd & 1) * 7 + m7;
    bf16x8 wf[28];
    const unsigned p16 = hf * 1024 + g1 * 512 + (lane & 15) * 16;
    u32x2 xa, xb;
    f32x4 csg = {}, csu = {};
    f32x16 acc = {};
    const u32x4 one4 = {ones, ones, ones, ones};
    const unsigned hi_ = (unsigned)((tb * 2 + tt) * 28 + m7 * 4 + ct * 2 + kh), ofo = (unsigned)(tt * 32768 + kh * 16384 + ct * 4096);
    unsigned ho = WS_HF + hi_ * 1024 + l16, so = hf ? OOB : WS_HS + hi_ * 256 + r * 8;
    const char* os = ws + WS_OF + (size_t)tb * 65536 + ofo;
    unsigned od = lds0 + 65536 + ofo;
    unsigned pf2 = (FL_O + (tb >> 1) * 16 + kh * 8 + ct * 2 + ((lane >> 1) & 1) + (lane & 4)) * 128;
    unsigned go = ffn ? WS_GF + (unsigned)(drb * 4 * 112 + dfg * 8 + w) * 1024 + g1 * 512 + (hf * 16 + (lane & 15)) * 16 : OOB;
    unsigned fo3 = (FL_H + tb * 8 + m7) * 128;
    {
        lds_ptr ap = opq(lds + kh * 8192 + l16), apB = opq(lds + 65536 + kh * 8192 + l16), apC = opq(lds + 131072 + kh * 8192 + l16);
        const unsigned lv = kh * 8192 + (l16 ^ (kh * 256));
        lds_ptr vp = opq(lds + lv), vpB = opq(lds + 65536 + lv), vpC = opq(lds + 131072 + lv);
#define AP(off) ((off) < 65536 ? ap + (off) : (off) < 131072 ? apB + ((off) - 65536) : apC + ((off) - 131072))
#define VP(off) ((off) < 65536 ? vp + (off) : (off) < 131072 ? vpB + ((off) - 65536) : vpC + ((off) - 131072))
#define KV_ISSUE(t) glds2_dev(l16, kvs + (t) * 16384, kvd + phys(t))
        unsigned wgo = ffn ? WS_WGU + (unsigned)(dfg * 8 + w) * 28672 : OOB;
        const int ri = umin(xcd * 4 + loc - 28, 27);
        const char* wos = (ffn ? wo + m7 * 128 : wd + (ri % 7) * 128 + (size_t)(ri / 7) * 448 * 896) + (size_t)w * 56 * 896;
        unsigned wov = (unsigned)(row8 * 896) + swz;
        const rsrc_t rx = __builtin_amdgcn_make_buffer_rsrc((void*)x, 0, 2048 * 448 * 2, 0x00020000);
        unsigned xo_ = (unsigned)(((tb * 2 + tt) * 32 + r) * 896 + m7 * 128 + ct * 64 + kh * 32 + hf * 8);
        unsigned rto = (FL_RT + w * 4 + (lane & 3)) * 128;
        lds_ptr qp = opq(lds + 147456 + qt * 2048 + l16);
        PINS(wgo); PINS(wos); PIN2(wov, xo_); PIN(rto); PIN2(ho, so); PIN2(pf2, fo3); PIN(go);
        lds_ptr xw = opq(lds + 147456 + w * 2048 + l16), xr = opq(lds + 147456 + (w ^ 4) * 2048 + l16);
        lds_ptr lw = opq(lds + 58368 + w * 256 + lane * 4), lr = opq(lds + 58368 + qt * 256 + 128 + r * 4);
        lds_ptr bw = opq(lds + 60416 + w * 4), br = opq(lds + 60416 + (lane & 7) * 4);
        unsigned oo = WS_OF + (unsigned)((rb * 4 + qt) * 32 + head * 2 + kh) * 1024 + l16;
        unsigned fo2 = (FL_O + rb * 16 + head) * 128;
        PIN2(oo, fo2);
        if (w == 1) poll_flag(ws, pf1, tag);
        BAR();
        glds_dev(l16, qs, qd);
        KV_ISSUE(0); KV_ISSUE(1);
        asm volatile("s_barrier" ::: "memory");
        KV_ISSUE(2); KV_ISSUE(3);
        waitv_bar<4>();
        const bf16x8 q0 = lds8(qp), q1 = lds8(qp + 1024);
        f32x16 o = {}, sa = {}, sb;
        bf16x8 k0 = lds8(ap + phys(0)), k1 = lds8(ap + phys(0) + 1024), v0 = lds8(vp + phys(0) + 2048), v1 = {};
        u32x4 p0, p1 = {};
        sa = MFMA(k0, q0, sa); sa = MFMA(k1, q1, sa);
        k0 = lds8(ap + phys(0) + 4096); k1 = lds8(ap + phys(0) + 5120);
        PIN(sa); SB();
        u64 rtf = 0;
#define EXP4(S, o4) _Pragma("unroll") for (int i_ = o4; i_ < o4 + 4; ++i_) S[i_] = __builtin_amdgcn_exp2f(S[i_]);
#define WLD(s) wf[s] = BF8(ld16_dev(rws, p16 + ((s) & 1) * 256 + ((s) & 2) * 1024, wgo + ((s) >> 2) * 4096))
#define SIDE(t, g) do { \
            if ((t) == 0 && (g) == 0) KV_ISSUE(4); if ((t) == 0 && (g) == 2) KV_ISSUE(5); if ((t) == 1 && (g) == 0) KV_ISSUE(6); if ((t) == 1 && (g) == 2) KV_ISSUE(7); \
            if ((t) == 2 && (g) == 0) KV_ISSUE(8); \
            if ((t) == 1 && (g) == 1) peek_lds(ws, rto, lds0 + 155648 + w * 512); if ((t) == 2 && (g) == 2) { waitv<5>(); rtf = peeked(lds + 155648 + w * 512 + lane * 4); } \
            if ((t) == 3 && (g) < 3) KV_ISSUE(9 + (g)); \
            if (((t) == 2 && (g) == 1) || ((t) == 14 && (g) == 2)) st16_dev((u32x4){~0u, ~0u, ~0u, ~0u}, rws, go + ((t) == 2 ? 2 : 3) * 114688, 0);     \
            if ((t) == 7 && (g) == 0) KV_ISSUE(12); if ((t) == 7 && (g) == 2) KV_ISSUE(13); if ((t) == 8 && (g) == 0) KV_ISSUE(14); if ((t) == 8 && (g) == 2) KV_ISSUE(15); \
            if ((t) >= 4 && (t) <= 6) WLD(((t) - 4) * 4 + (g)); \
            if (((t) == 7 || (t) == 8) && ((g) & 1)) WLD(12 + ((t) - 7) * 2 + ((g) >> 1)); \
            if ((t) >= 9 && (t) <= 11) WLD(((t) - 5) * 4 + (g)); \
            if ((t) == 12 || ((t) == 13 && (g) < 3)) glds(wov, wos + (((t) - 12) * 4 + (g)) * 8 * 896, lds0 + (w * 7 + ((t) - 12) * 4 + (g)) * 1024); \
            if ((t) == 13 && (g) == 3) { xa = ld8(rx, xo_, 0); xb = ld8(rx, xo_, 16); } \
            if ((t) == 14 && (g) == 0) { if (tid < 32) *(LAS u32x4*)(lds + 57344 + tid * 16) = (u32x4){0, 0, 0, 0}; } \
        } while (0)
#define TOFF(i) (phys((i) >> 1) + ((i) & 1) * 4096)
#define TILE(i, SC, SN) do { \
            __builtin_amdgcn_s_setprio(((i) & 1) ? 1 : 3); \
            if ((i) < 31) { f32x16 z_ = {}; SN = MFMA(k0, q0, z_); PIN(SN); } if ((i) < 30) k0 = lds8(AP(TOFF((i) + 2))); SB(); \
            EXP4(SC, 0); PIN(SC); SB(); \
            o = MFMA(v1, BF8(p1), o); PIN(o); v1 = lds8(VP(TOFF(i) + 3072)); SIDE((i) >> 1, ((i) & 1) * 2); SB(); \
            EXP4(SC, 4); p0 = pack8(SC, 0); PIN(p0); SB(); \
            __builtin_amdgcn_s_setprio(((i) & 1) ? 0 : 2); \
            if ((i) < 31) { SN = MFMA(k1, q1, SN); PIN(SN); } if ((i) < 30) k1 = lds8(AP(TOFF((i) + 2) + 1024)); SB(); \
            EXP4(SC, 8); PIN(SC); SB(); \
            o = MFMA(v0, BF8(p0), o); PIN(o); if ((i) < 31) v0 = lds8(VP(TOFF((i) + 1) + 2048)); SIDE((i) >> 1, ((i) & 1) * 2 + 1); SB(); \
            EXP4(SC, 12); p1 = pack8(SC, 1); PIN(p1); SB(); } while (0)
#define STEP(t) do { TILE(2 * (t), sa, sb); TILE(2 * (t) + 1, sb, sa); } while (0)
        STEP(0);
        waitv_barx<4>();
        STEP(1); STEP(2);
        if (__builtin_expect(!wave_all(rtf == tag), 0)) poll_flag(ws, rto, tag);
        waitv_bar2<3>();
        STEP(3); STEP(4); STEP(5); STEP(6);
        waitv_bar2<12>();
        STEP(7); STEP(8); STEP(9); STEP(10);
        waitv_bar2<9>();
        STEP(11); STEP(12); STEP(13); STEP(14); STEP(15);
        o = MFMA(v1, BF8(p1), o);
        u32x4 ov;
        {
            *(LAS f32x4*)xw = (f32x4){o[8], o[9], o[10], o[11]}; *(LAS f32x4*)(xw + 1024) = (f32x4){o[12], o[13], o[14], o[15]};
            const float l = kh ? o[4] : o[12];
            *(LAS float*)lw = l;
#define AMX(a, b, c) __builtin_fmaxf(__builtin_fmaxf(__builtin_fabsf(a), __builtin_fabsf(b)), __builtin_fabsf(c))
            const float mx_ = AMX(AMX(AMX(o[0], o[1], o[2]), AMX(o[3], o[4], o[5]), AMX(o[6], o[7], o[8])), AMX(AMX(o[9], o[10], o[11]), AMX(o[12], o[13], o[14]), o[15]), 0.f);
            const bool ok = (mx_ < 0x1p100f) & ((hf ? l : 1.f) > 0x1p-40f);
            *(LAS unsigned*)bw = wave_all(ok) ? 0u : 1u;
            BAR();
            const f32x4 a = *(LAS f32x4*)xr, c = *(LAS f32x4*)(xr + 1024);
            const float inv = __builtin_amdgcn_rcpf(*(LAS float*)lr + *(LAS float*)(lr + 1024));
            const unsigned bad = *(LAS unsigned*)br;
            const float iz = (kh && hf) ? 0.f : inv;
            ov = (u32x4){cvtpk((o[0] + a[0]) * inv, (o[1] + a[1]) * inv), cvtpk((o[2] + a[2]) * inv, (o[3] + a[3]) * inv),
                         cvtpk((o[4] + c[0]) * iz, (o[5] + c[1]) * iz), cvtpk((o[6] + c[2]) * iz, (o[7] + c[3]) * iz)};
            if (__builtin_expect(wave_any(bad != 0), 0)) {
                f32x16 of = {};
                float m = -0x1p126f;
#pragma unroll 1
                for (int t = 0; t < 16; ++t) {
                    BAR();
                    glds2_dev(l16, kvs + t * 16384, lds0 + 65536 + w * 2048);
                    waitv_bar<0>();
#pragma unroll 1
                    for (int u = 0; u < 2; ++u) {
                        f32x16 s = {};
                        s = MFMA(lds8(apB + u * 4096), q0, s); s = MFMA(lds8(apB + u * 4096 + 1024), q1, s);
                        float mx = s[0];
#pragma unroll
                        for (int i = 1; i < 16; ++i) mx = __builtin_fmaxf(mx, s[i]);
                        mx = __builtin_fmaxf(swap_max(mx), m);
                        const float alpha = __builtin_amdgcn_exp2f(m - mx);
                        m = mx;
#pragma unroll
                        for (int i = 0; i < 16; ++i) { s[i] = __builtin_amdgcn_exp2f(s[i] - mx); of[i] *= alpha; }
                        of = MFMA(lds8(vpB + u * 4096 + 2048), BF8(pack8(s, 0)), of);
                        of = MFMA(lds8(vpB + u * 4096 + 3072), BF8(pack8(s, 1)), of);
                    }
                }
                *(LAS f32x4*)xw = (f32x4){of[8], of[9], of[10], of[11]}; *(LAS f32x4*)(xw + 1024) = (f32x4){of[12], of[13], of[14], of[15]};
                *(LAS float*)lw = hf ? (kh ? of[4] : of[12]) : m;
                BAR();
                const f32x4 a2 = *(LAS f32x4*)xr, c2 = *(LAS f32x4*)(xr + 1024);
                const float m0 = *(LAS float*)(lr - 128), m1 = *(LAS float*)(lr + 1024 - 128), mm = __builtin_fmaxf(m0, m1);
                const float e0 = __builtin_amdgcn_exp2f(m0 - mm), e1 = __builtin_amdgcn_exp2f(m1 - mm);
                const float iv = __builtin_amdgcn_rcpf(*(LAS float*)lr * e0 + *(LAS float*)(lr + 1024) * e1);
                const float im = (kh ? e1 : e0) * iv, io = (kh ? e0 : e1) * iv;
                const bool z = kh && hf;
                ov = (u32x4){cvtpk(of[0] * im + a2[0] * io, of[1] * im + a2[1] * io), cvtpk(of[2] * im + a2[2] * io, of[3] * im + a2[3] * io),
                             z ? 0u : cvtpk(of[4] * im + c2[0] * io, of[5] * im + c2[1] * io), z ? 0u : cvtpk(of[6] * im + c2[2] * io, of[7] * im + c2[3] * io)};
            }
        }
        st16_dev(ov, rws, oo, 0);
        FENCE();
        PIN2(xa, xb);
#pragma unroll
        for (int s = 27; s >= 0; --s) PIN(wf[s]);
#pragma unroll
        for (int s = 0; s < 16; s += 2) { csg = MFMA16(wf[s], BF8(one4), csg); csu = MFMA16(wf[s + 1], BF8(one4), csu); }
        acc[0] = bflo(xa[0]); acc[1] = bfhi(xa[0]); acc[2] = bflo(xa[1]); acc[3] = bfhi(xa[1]); acc[4] = bflo(xb[0]); acc[5] = bfhi(xb[0]); acc[6] = bflo(xb[1]); acc[7] = bfhi(xb[1]);
        PIN(acc); PIN2(ho, so); PIN2(pf2, fo3); PINS(os); PINS(od); PIN2(csg, csu);
        waitv_bar<0>();
        if (w == 0) set_flag(ws, fo2, tagv);
    }
    if (__builtin_expect(!ffn, 0)) {
        const int ri = xcd * 4 + loc - 28;
        if (ri < 28) {
#pragma unroll 1
            for (int u = w; u < 28; u += 8) {
                lds_ptr sd = lds + u * 2048 + (4 * hf + q4) * 128 + g1 * 32 + p4 * 8;
#pragma unroll
                for (int e = 0; e < 2; ++e) { lds_ptr p_ = sd + ((e ^ (q4 >> 1)) * 64);
                    st16_dev(U4(cat(trd(p_), trd(p_ + 1024))), rws, l16, WS_WD + (unsigned)(((ri % 7) * 112 + (ri / 7) * 28 + u) * 2 + e) * 1024); }
            }
        }
        waitv_bar<0>();
        if (w == 0) set_flag(ws, (FL_RD + ri) * 128, tagv);
        return;
    }

    const int erb = tb >> 1;
    const char* hs = ws + WS_HF + (size_t)drb * 114688 + w * 1024;
    const char* hss = ws + WS_HS + (size_t)((drb * 4 + (w & 3)) * 28 + kh * 14) * 256;
    unsigned pf3, fo4 = (FL_G + drb * 64 + dfg) * 128;
    {
        lds_ptr wb = lds + kh * (8 * 28 * 128) + (4 * hf + q4) * 128 + ((ct ^ (q4 >> 1)) * 64) + (g1 ^ kh) * 32 + p4 * 8;
        bf16x8 wr[16];
#pragma unroll
        for (int s = 0; s < 12; ++s) { const int k0 = ((s >> 1) * 28 + (s & 1) * 16) * 128; wr[s] = cat(trd(wb + k0), trd(wb + k0 + 1024)); }
        lds_ptr cw = opq(lds + 131072 + w * 2048 + l16), cr = opq(lds + 131072 + (w ^ 4) * 2048 + l16);
        lds_ptr ob = opq(lds + 65536 + tt * 32768 + kh * 16384 + l16);
        pf3 = (FL_H + drb * 16 + (lane & 8) + umin(lane & 7, 6)) * 128;
        PIN2(pf3, fo4); PINS(hs); PINS(hss);
        poll_flag(ws, pf2, tag);
        glds4_dev(l16, os, od);
        asm volatile("s_barrier" ::: "memory");
        glds4_dev(l16, os + 8192, od + 8192);
        bf16x8 of[8];
        waitv_bar<4>();
#pragma unroll
        for (int s = 0; s < 8; ++s) of[s] = lds8(ob + s * 1024);
        SB();
#pragma unroll
        for (int s = 0; s < 8; ++s) {
            acc = MFMA(wr[s], of[s], acc); PIN(acc); SB();
            if (s >= 2 && s < 6) { const int k0 = (((s + 10) >> 1) * 28 + (s & 1) * 16) * 128; wr[s + 10] = cat(trd(wb + k0), trd(wb + k0 + 1024)); SB(); }
        }
        waitv_barx<0>();
#pragma unroll
        for (int s = 0; s < 8; ++s) of[s] = lds8(ob + (8 + s) * 1024);
        SB();
#pragma unroll
        for (int s = 0; s < 8; ++s) acc = MFMA(wr[8 + s], of[s], acc);
        *(LAS f32x4*)cw = (f32x4){acc[8], acc[9], acc[10], acc[11]}; *(LAS f32x4*)(cw + 1024) = (f32x4){acc[12], acc[13], acc[14], acc[15]};
        BAR();
        const f32x4 a = *(LAS f32x4*)cr + (f32x4){acc[0], acc[1], acc[2], acc[3]}, b = *(LAS f32x4*)(cr + 1024) + (f32x4){acc[4], acc[5], acc[6], acc[7]};
        const u32x4 hv = {cvtpk(a[0], a[1]), cvtpk(a[2], a[3]), cvtpk(b[0], b[1]), cvtpk(b[2], b[3])};
        st16_dev(hv, rws, ho, 0);
        FENCE(); SB();
        float s1 = 0.f, s2 = 0.f;
        { f32x4 q4_ = {}; acc_stats2(hv[0], hv[1], q4_); acc_stats2(hv[2], hv[3], q4_); s1 = swap_add(q4_[0] + q4_[1]); s2 = swap_add(q4_[2] + q4_[3]); }
        st8_dev((u32x2){f2u(s1), f2u(s2)}, rws, so, 0);
        FENCE(); PIN2(csg, csu); SB();
#pragma unroll
        for (int s = 16; s < 28; s += 2) { csg = MFMA16(wf[s], BF8(one4), csg); csu = MFMA16(wf[s + 1], BF8(one4), csu); }
        PIN2(csg, csu);
        waitv_bar<0>();
        if (w == 0) set_flag(ws, fo3, tagv);
        if (bid == 0 && w == 0) set_flag(ws, FL_EPOCH * 128, tagv);
    }

    const unsigned wdo = WS_WD + (unsigned)(m7 * 112 + w * 14) * 2048;
    bf16x8 wdf[28];
    bf16x8 gf[GW];
    const unsigned gxo = WS_GF + (unsigned)(erb * 4 * 112 + w * 14) * 1024 + (tb & 1) * 256;
    const unsigned pwd = hf * 2048 + g1 * 512 + (lane & 15) * 16;
#define G_LD(s) gf[(s) % GW] = BF8(ld16_dev(rws, p16, gxo + ((s) / 7) * 114688 + ((s) % 7) * 2048))
    u64 gfl = 0;
    const unsigned gfi = (FL_G + (erb * 4 + g1) * 16 + ((w * 14 + umin(lane & 15, 13)) >> 3)) * 128;
    {
        const unsigned hd = lds0 + w * 1024;
        const char* wds = ws + wdo;
        lds_ptr hb = opq(lds + p16), hb2 = opq(lds + 57344 + p16);
        lds_ptr wl = opq(lds + 114688 + w * 5120 + l16), wl2 = opq(lds + w * 7168 + l16);
        LAS f32x2* stp = (LAS f32x2*)opq(lds + 159744 + (lane & 15) * 8);
        if (w == 1) poll_flag(ws, pf3, tag);
        BAR();
#define H_PIECE(j) glds_dev(l16, hs + (j) * 8192, hd + (j) * 8192)
#define WDO(s) (((s) >> 2) * 4096 + (((s) >> 1) & 1) * 1024 + ((s) & 1) * 256)
#define WDLD(s) wdf[s] = BF8(ld16_dev(rws, pwd, wdo + WDO(s)))
#define HB(n) (((n) < 56 ? hb + ((n) & ~1) * 1024 : hb2 + (((n) & ~1) - 56) * 1024) + ((n) & 1) * 256)
        H_PIECE(0);
        asm volatile("s_barrier" ::: "memory");
        H_PIECE(1); H_PIECE(2); H_PIECE(3);
        constexpr int PF = 4;
        bf16x8 bf[PF + 1];
        f32x4 ag[2][2], au[2][2];
        f32x2 sp[7], sv[4];
        float mean[2] = {0.f, 0.f}, r2[2] = {0.f, 0.f}, hp = 0.f;
        u32x4 hv;
        lds_ptr pk = opq(lds + 155648 + w * 512 + lane * 4);
        u64 pv = 0;
        const unsigned pkd = lds0 + 155648 + w * 512;
        const unsigned rdo = (FL_RD + (w >> 1) * 7 + m7) * 128;
        waitv_barx<3>();
#pragma unroll
        for (int n = 0; n < PF; ++n) bf[n] = lds8(HB(n));
        SB();
        sfor<0, 125>([&](auto N_) __attribute__((always_inline)) {
            constexpr int n = decltype(N_)::v, p = n / 28, s = n % 28;
            if constexpr (n == 4) waitv_barx<3>();
            if constexpr (n == 12) waitv_barx<8>();
            if constexpr (n == 28) { waitv_barx<3>(); }
            if constexpr (n == 64) { waitv_barx<10>(); if (w == 0) set_flag(ws, fo4, tagv); }
            if constexpr (n == 85) { waitv_barx<8>(); if (w == 0) set_flag(ws, fo4 + 2048, tagv); }
            if constexpr (n == 32) { if (__builtin_expect(!wave_all(pv == tag), 0)) poll_flag(ws, rdo, tag); FENCE(); }
            if constexpr (d_prio(n) != d_prio(n - 1)) __builtin_amdgcn_s_setprio(d_prio(n));
            if constexpr (n < 112) {
                if constexpr (s < 2) { ag[p & 1][s] = (f32x4){}; au[p & 1][s] = (f32x4){}; }
                ag[p & 1][s & 1] = MFMA16(wf[s & ~1], bf[n % (PF + 1)], ag[p & 1][s & 1]); au[p & 1][s & 1] = MFMA16(wf[s | 1], bf[n % (PF + 1)], au[p & 1][s & 1]);
                PIN2(ag[p & 1][s & 1], au[p & 1][s & 1]);
            }
            SB();
            if constexpr (n + PF < 112) bf[(n + PF) % (PF + 1)] = lds8(HB(n + PF));
            if constexpr (n % 3 == 2 && n < 30) H_PIECE(4 + n / 3);
            if constexpr (n == 4) glds4_dev(l16, hss, lds0 + 114688 + w * 5120);
            if constexpr (n == 16) { waitv<4>();
#pragma unroll
                for (int j = 0; j < 7; ++j) sp[j] = *(LAS f32x2*)(lds + 114688 + w * 5120 + (hf * 7 + j) * 256 + r * 8);
            }
            if constexpr (n == 20) { const f32x2 t = ((sp[0] + sp[1]) + (sp[2] + sp[3])) + ((sp[4] + sp[5]) + sp[6]); stp[w * 32 + g1 * 16] = (f32x2){swap_add(t[0]), swap_add(t[1])}; }
            if constexpr (wd_at(n) >= 0) { constexpr int i = wd_at(n);
                if constexpr (i < 5) glds_dev(pwd, wds + WDO(i), lds0 + 114688 + w * 5120 + i * 1024);
                else if constexpr (i < 12) glds_dev(pwd, wds + WDO(i), lds0 + w * 7168 + (i - 5) * 1024);
                else WDLD(i); }
            if constexpr (n == 84) peek_lds(ws, gfi - g1 * 2048, pkd);
            if constexpr (n == 95) { waitv<5>(); pv = peeked(pk); }
            if constexpr (n == 99) { if (__builtin_expect(!wave_all(pv == tag), 0)) poll_flag(ws, gfi - g1 * 2048, tag); FENCE(); }
            if constexpr (n >= 101 && n < 101 + 2 * XN && n % 2 == 1) G_LD((n - 101) / 2);
            if constexpr (n == 106) gfl = peek_flag(ws, gfi);
            if constexpr (n == 28) pv = peeked(pk);
            if constexpr (n == 18) peek_lds(ws, rdo, pkd);
            if constexpr (p >= 1) {
                constexpr int e = (p - 1) & 1, tp = n == 100 ? 3 : p - 1;
                if constexpr ((s == 0 && p < 4) || n == 100) { sv[0] = stp[tp * 32]; sv[1] = stp[(tp + 4) * 32]; sv[2] = stp[tp * 32 + 16]; sv[3] = stp[(tp + 4) * 32 + 16]; }
                if constexpr (((s == 2 || s == 3) && p < 4) || n == 104 || n == 105) { constexpr int k = n & 1; const f32x2 t = sv[2 * k] + sv[2 * k + 1];
                    mean[k] = t[0] * (1.f / D); r2[k] = __builtin_amdgcn_rcpf(__builtin_fmaxf(t[1] * (1.f / D) - mean[k] * mean[k], 0.f) + EPS); PIN2(mean[k], r2[k]); }
                if constexpr (s >= 4 && s < 12) { constexpr int i = s - 4, k = i >> 2;
                    const float h0 = __builtin_fmaxf(ag[e][k][i & 3] - mean[k] * csg[i & 3], 0.f) * r2[k] * (au[e][k][i & 3] - mean[k] * csu[i & 3]);
                    if constexpr (i & 1) { hv[i >> 1] = p >= 3 ? umin(cvtpk(hp, h0), ~1u) : cvtpk(hp, h0); PIN(hv); } else { hp = h0; PIN(hp); } }
                if constexpr (s == 12) {
                    const auto s0 = __builtin_amdgcn_permlane32_swap(hv[0], hv[2], false, false), s1 = __builtin_amdgcn_permlane32_swap(hv[1], hv[3], false, false);
                    st16_dev((u32x4){s0[0], s1[0], s0[1], s1[1]}, rws, go + (p - 1) * 114688, 0); FENCE();
                }
            }
            SB();
        });
#pragma unroll
        for (int s = XN; s < 7; ++s) { G_LD(s); SB(); }
        FENCE();
#pragma unroll
        for (int s = 0; s < 12; ++s) wdf[s] = lds8(s < 5 ? wl + s * 1024 : wl2 + (s - 5) * 1024);
    }

    {
        const int etok = tid >> 4, eblk = tid & 15;
        const int et = erb * 4 + (etok >> 4), er = (tb & 1) * 16 + (etok & 15);
        const unsigned hro = (unsigned)((((eblk >> 2) & 3) * 64 + (eblk & 1) * 32 + er) * 16 + ((eblk >> 1) & 1) * 8) + WS_HF + (unsigned)(et * 28 + m7 * 4) * 1024;
        u32x2 hres[2];
        GAS u64* op = (GAS u64*)(out + (size_t)(et * 32 + er) * 896 + m7 * 128 + eblk * 8);
        lds_ptr rp = opq(lds + w * 8448 + (lane >> 4) * 528 + (lane & 15) * 16), rq = opq(lds + eblk * 528 + etok * 16);
        lds_ptr rp2 = opq(rp + 67584), rq2 = opq(rq + 67584);
        f32x4 c[4];
#define G_STEP(s) do { _Pragma("unroll") for (int j = 0; j < 4; ++j) { if ((s) % 7 == 0) c[j] = (f32x4){}; c[j] = MFMA16(wdf[4 * ((s) % 7) + j], gf[(s) % GW], c[j]); } PIN2(c[0], c[1]); PIN2(c[2], c[3]); } while (0)
#define G_PUT(q) do { _Pragma("unroll") for (int j = 0; j < 4; ++j) *(LAS f32x4*)(((q) < 2 ? rp : rp2) + j * 2112 + ((q) & 1) * 256) = c[j]; } while (0)
#define G_OUT(hh, RQ) do { f32x4 pa[8]; \
            _Pragma("unroll") for (int ww = 0; ww < 8; ++ww) pa[ww] = *(LAS f32x4*)(RQ + ww * 8448); SB(); \
            const f32x4 a = ((pa[0] + pa[1]) + (pa[2] + pa[3])) + ((pa[4] + pa[5]) + (pa[6] + pa[7])); \
            const u32x2 ov = {cvtpk(a[0] + bflo(hres[hh][0]), a[1] + bfhi(hres[hh][0])), cvtpk(a[2] + bflo(hres[hh][1]), a[3] + bfhi(hres[hh][1]))}; \
            __hip_atomic_store(op + (hh) * (64 * 896 / 8), __builtin_bit_cast(u64, ov), __ATOMIC_RELAXED, __HIP_MEMORY_SCOPE_AGENT); } while (0)
        unsigned seen = 0;
#pragma unroll
        for (int s = 0; s < 28; ++s) {
            if (s == 5) BAR();
            if (s == 7 || s == 14 || s == 21) G_PUT(s / 7 - 1);
            if (s == 21) { waitv<7>(); PIN2(hres[0], hres[1]); BAR(); G_OUT(0, rq); FENCE(); }
            SB();
            G_STEP(s);
            if (s >= 14) { const u32x4 u = U4(gf[s % GW]); seen = umax3(umax3(seen, u[0], u[1]), u[2], u[3]); PIN(seen); }
            SB();
            if (s == 0) { if (__builtin_expect(!wave_all(gfl == tag), 0)) poll_flag(ws, gfi, tag); FENCE(); }
            if (s + GW < 28) G_LD(s + GW);
            if (s == 1) { hres[0] = ld8_dev(rws, hro, 0); hres[1] = ld8_dev(rws, hro, 57344); FENCE(); }
        }
        if (w == 0) { set_flag(ws, fo4 + 4096, tagv); set_flag(ws, fo4 + 6144, tagv); }
        if (__builtin_expect(wave_any(seen == ~0u), 0)) {
            poll_flag(ws, gfi + 4096, tag);
#pragma unroll
            for (int s = 14; s < 14 + GW; ++s) G_LD(s);
#pragma unroll
            for (int s = 14; s < 28; ++s) { if (s == 21) G_PUT(2); G_STEP(s); SB(); if (s + GW < 28) G_LD(s + GW); SB(); }
        }
        G_PUT(3);
        BAR();
        G_OUT(1, rq2);
    }
}

extern "C" void kernel_launch(void* const* d_in, const int* in_sizes, int n_in, void* d_out, int out_size, void* d_ws, size_t ws_size, hipStream_t stream) {
    (void)hipFuncSetAttribute((const void*)block_fwd, hipFuncAttributeMaxDynamicSharedMemorySize, LDS_BYTES);
    block_fwd<<<256, 512, LDS_BYTES, stream>>>((const char*)d_in[0], (const char*)d_in[1], (const char*)d_in[2], (const char*)d_in[4], (const char*)d_in[5], (const char*)d_in[6], (char*)d_out, (char*)d_ws);
}
```

```cpp
#include <hip/hip_runtime.h>
#include <hip/hip_bf16.h>
#include <stdint.h>

typedef __attribute__((ext_vector_type(8))) short bf16x8;
typedef __attribute__((ext_vector_type(4))) short s16x4;
typedef __attribute__((ext_vector_type(2))) __bf16 bf16x2;
typedef __attribute__((ext_vector_type(16))) float f32x16;
typedef __attribute__((ext_vector_type(4))) float f32x4;
typedef __attribute__((ext_vector_type(2))) float f32x2;
typedef __attribute__((ext_vector_type(4))) unsigned u32x4;
typedef __attribute__((ext_vector_type(2))) unsigned u32x2;
typedef short v4i16_t __attribute__((ext_vector_type(4)));
typedef unsigned long long u64;
#define LAS __attribute__((address_space(3)))
#define GAS __attribute__((address_space(1)))
typedef LAS char* lds_ptr;
#if defined(__HIP_DEVICE_COMPILE__)
#define NOPK __attribute__((target("no-packed-fp32-ops")))
#else
#define NOPK
#endif
#define DI __device__ __forceinline__
#define MFMA(a, b, c) __builtin_amdgcn_mfma_f32_32x32x16_bf16(a, b, c, 0, 0, 0)
#define MFMA16(a, b, c) __builtin_amdgcn_mfma_f32_16x16x32_bf16(a, b, c, 0, 0, 0)
#define BF8(x) __builtin_bit_cast(bf16x8, x)
#define U4(x) __builtin_bit_cast(u32x4, x)

constexpr int D = 448;
constexpr float EPS = 1e-5f;
constexpr unsigned FK_A = 1, FK_O = 2, FK_H = 3, FK_G = 4;
constexpr unsigned WS_KV = 1u << 20;
constexpr unsigned WS_QF = WS_KV + 4194304;
constexpr unsigned WS_OF = WS_QF + 2097152;
constexpr unsigned WS_HF = WS_OF + 2097152;
constexpr unsigned WS_GF = WS_HF + 64 * 28 * 1024;
constexpr unsigned WS_WGU = WS_GF + 64 * 112 * 1024;
constexpr unsigned WS_WD = WS_WGU + 112 * 28 * 1024;
constexpr unsigned WS_HS = WS_WD + 7 * 112 * 2 * 1024;
constexpr unsigned WS_END = WS_HS + 64 * 28 * 256 + 1024;
constexpr unsigned OOB = 0x40000000u;
constexpr int LDS_BYTES = 163840;

DI float u2f(unsigned u) { return __builtin_bit_cast(float, u); }
DI unsigned f2u(float f) { return __builtin_bit_cast(unsigned, f); }
DI unsigned umin(unsigned a, unsigned b) { return a < b ? a : b; }
DI unsigned umax3(unsigned a, unsigned b, unsigned c) { const unsigned m = a > b ? a : b; return m > c ? m : c; }
DI bool wave_any(bool p) { return __builtin_amdgcn_ballot_w64(p) != 0; }
DI bool wave_all(bool p) { return __builtin_amdgcn_ballot_w64(!p) == 0; }
DI unsigned cvtpk(float lo, float hi) { return __builtin_bit_cast(unsigned, __builtin_convertvector((f32x2){lo, hi}, bf16x2)); }
DI float bflo(unsigned u) { return u2f(u << 16); }
DI float bfhi(unsigned u) { return u2f(u & 0xffff0000u); }
DI void glds(unsigned voff, const void* sbase, unsigned dst) {
    asm volatile("s_mov_b32 m0, %2\n\ts_nop 0\n\tglobal_load_lds_dwordx4 %0, %1" :: "v"(voff), "s"(sbase), "s"(dst) : "memory");
}
DI void glds_dev(unsigned voff, const void* sbase, unsigned dst) {
    asm volatile("s_mov_b32 m0, %2\n\ts_nop 0\n\tglobal_load_lds_dwordx4 %0, %1 sc1" :: "v"(voff), "s"(sbase), "s"(dst) : "memory");
}
DI void glds2_dev(unsigned voff, const void* sbase, unsigned dst) {
    asm volatile("s_mov_b32 m0, %2\n\ts_nop 0\n\tglobal_load_lds_dwordx4 %0, %1 sc1\n\tglobal_load_lds_dwordx4 %0, %1 offset:1024 sc1" :: "v"(voff), "s"(sbase), "s"(dst) : "memory");
}
DI void glds4_dev(unsigned voff, const void* sbase, unsigned dst) {
    asm volatile("s_mov_b32 m0, %2\n\ts_nop 0\n\tglobal_load_lds_dwordx4 %0, %1 sc1\n\tglobal_load_lds_dwordx4 %0, %1 offset:1024 sc1\n\t"
                 "global_load_lds_dwordx4 %0, %1 offset:2048 sc1\n\tglobal_load_lds_dwordx4 %0, %1 offset:3072 sc1" :: "v"(voff), "s"(sbase), "s"(dst) : "memory");
}
DI s16x4 trd(lds_ptr p) { return __builtin_bit_cast(s16x4, __builtin_amdgcn_ds_read_tr16_b64_v4i16((LAS v4i16_t*)p)); }
DI bf16x8 cat(s16x4 a, s16x4 b) { return __builtin_shufflevector(a, b, 0, 1, 2, 3, 4, 5, 6, 7); }
DI bf16x8 lds8(lds_ptr p) { return *(LAS bf16x8*)p; }
DI float swap_add(float v) { auto r = __builtin_amdgcn_permlane32_swap(f2u(v), f2u(v), false, false); return u2f(r[0]) + u2f(r[1]); }
DI float swap_max(float v) { auto r = __builtin_amdgcn_permlane32_swap(f2u(v), f2u(v), false, false); return __builtin_fmaxf(u2f(r[0]), u2f(r[1])); }
DI u32x4 pack8(const f32x16& a, int s) { return (u32x4){cvtpk(a[8 * s], a[8 * s + 1]), cvtpk(a[8 * s + 2], a[8 * s + 3]), cvtpk(a[8 * s + 4], a[8 * s + 5]), cvtpk(a[8 * s + 6], a[8 * s + 7])}; }
DI float dot2(unsigned a, unsigned b, float c) { return __builtin_amdgcn_fdot2_f32_bf16(__builtin_bit_cast(bf16x2, a), __builtin_bit_cast(bf16x2, b), c, false); }
DI void acc_stats2(unsigned u0, unsigned u1, f32x4& s) {
    const float a = bflo(u0), b = bfhi(u0), c = bflo(u1), d = bfhi(u1);
    s[0] += a; s[1] += b; s[2] = __builtin_fmaf(a, a, s[2]); s[3] = __builtin_fmaf(b, b, s[3]); s[0] += c; s[1] += d; s[2] = __builtin_fmaf(c, c, s[2]); s[3] = __builtin_fmaf(d, d, s[3]);
}
DI float sum8(float v) {
    v += u2f(__builtin_amdgcn_update_dpp(0u, f2u(v), 0xB1, 0xf, 0xf, true));
    v += u2f(__builtin_amdgcn_update_dpp(0u, f2u(v), 0x4E, 0xf, 0xf, true));
    v += u2f(__builtin_amdgcn_update_dpp(0u, f2u(v), 0x141, 0xf, 0xf, true));
    return v;
}
DI u32x4 shift8(u32x4 x, float c) {
    u32x4 o; float t0, t1, t2, t3, t4, t5, t6, t7;
    asm("v_dot2_f32_bf16 %4, %12, %16, %18\n\tv_dot2_f32_bf16 %5, %12, %17, %18\n\tv_dot2_f32_bf16 %6, %13, %16, %18\n\tv_dot2_f32_bf16 %7, %13, %17, %18\n\t"
        "v_dot2_f32_bf16 %8, %14, %16, %18\n\tv_dot2_f32_bf16 %9, %14, %17, %18\n\tv_dot2_f32_bf16 %10, %15, %16, %18\n\tv_dot2_f32_bf16 %11, %15, %17, %18\n\t"
        "v_cvt_pk_bf16_f32 %0, %4, %5\n\tv_cvt_pk_bf16_f32 %1, %6, %7\n\tv_cvt_pk_bf16_f32 %2, %8, %9\n\tv_cvt_pk_bf16_f32 %3, %10, %11"
        : "=&v"(o[0]), "=&v"(o[1]), "=&v"(o[2]), "=&v"(o[3]), "=&v"(t0), "=&v"(t1), "=&v"(t2), "=&v"(t3), "=&v"(t4), "=&v"(t5), "=&v"(t6), "=&v"(t7)
        : "v"(x[0]), "v"(x[1]), "v"(x[2]), "v"(x[3]), "s"(0x00003f80u), "s"(0x3f800000u), "v"(c));
    return o;
}
template <int N> struct IC { static constexpr int v = N; };
template <int B, int E, class F> DI void sfor(F&& f) { if constexpr (B < E) { f(IC<B>{}); sfor<B + 1, E>(f); } }
template <int N> DI void waitv() { asm volatile("s_waitcnt vmcnt(%0)" :: "i"(N) : "memory"); }
template <int N> DI void waitv_bar() { if (N == 0) __builtin_amdgcn_s_waitcnt(0x0F70); asm volatile("s_waitcnt vmcnt(%0) lgkmcnt(0)\n\ts_barrier" :: "i"(N) : "memory"); }
template <int N> DI void waitv_barx() { asm volatile("s_waitcnt vmcnt(%0)\n\ts_barrier" :: "i"(N) : "memory"); }
template <int N> DI void waitv_bar2() { asm volatile("s_waitcnt vmcnt(%0) lgkmcnt(2)\n\ts_barrier" :: "i"(N) : "memory"); }
#define BAR() asm volatile("s_waitcnt lgkmcnt(0)\n\ts_barrier" ::: "memory")
#define FENCE() asm volatile("" ::: "memory")
typedef __amdgpu_buffer_rsrc_t rsrc_t;
DI u32x4 ld16_dev(rsrc_t rs, unsigned voff, unsigned soff) { return U4(__builtin_amdgcn_raw_buffer_load_b128(rs, voff, soff, 16)); }
DI u32x2 ld8(rsrc_t rs, unsigned voff, unsigned soff) { return __builtin_bit_cast(u32x2, __builtin_amdgcn_raw_buffer_load_b64(rs, voff, soff, 0)); }
DI u32x2 ld8_dev(rsrc_t rs, unsigned voff, unsigned soff) { return __builtin_bit_cast(u32x2, __builtin_amdgcn_raw_buffer_load_b64(rs, voff, soff, 16)); }
DI void st16_dev(u32x4 v, rsrc_t rs, unsigned voff, unsigned soff) { __builtin_amdgcn_raw_buffer_store_b128(v, rs, voff + soff, 0, 16); }
DI void st8_dev(u32x2 v, rsrc_t rs, unsigned voff, unsigned soff) { __builtin_amdgcn_raw_buffer_store_b64(v, rs, voff, soff, 16); }
DI unsigned flag_at(unsigned kind, unsigned xcd, unsigned loc) { return (kind * 256 + loc * 8 + xcd) * 128; }
DI void set_flag(char* ws, unsigned fo, u64 tagv) {
    asm volatile("s_mov_b64 exec, 1\n\tglobal_store_dwordx2 %0, %1, %2 sc1\n\ts_mov_b64 exec, -1" :: "v"(fo), "v"(tagv), "s"(ws) : "memory");
}
DI u64 peek_flag(char* ws, unsigned fo) { return __hip_atomic_load((GAS u64*)(ws + fo), __ATOMIC_RELAXED, __HIP_MEMORY_SCOPE_AGENT); }
DI void peek_lds(char* ws, unsigned fo, unsigned dst) {
    asm volatile("s_mov_b32 m0, %2\n\ts_nop 0\n\tglobal_load_lds_dword %0, %1 sc1\n\ts_add_u32 m0, %2, 252\n\ts_nop 0\n\tglobal_load_lds_dword %0, %1 offset:4 sc1" :: "v"(fo), "s"(ws), "s"(dst) : "memory", "scc");
}
DI u64 peeked(lds_ptr p) { return (u64)*(LAS unsigned*)(p + 256) << 32 | *(LAS unsigned*)p; }
DI void poll_flag(char* ws, unsigned fo, u64 tag) {
#pragma unroll 1
    for (int spin = 0; spin < (1 << 21); ++spin) {
        if (wave_all(peek_flag(ws, fo) == tag)) break;
        __builtin_amdgcn_s_sleep(1);
    }
    FENCE();
}
#define SB() __builtin_amdgcn_sched_barrier(0)
#define PINS(a) asm volatile("" : "+s"(a))
#define PIN(a) asm volatile("" : "+v"(a))
#define PIN2(a, b) asm volatile("" : "+v"(a), "+v"(b))
DI lds_ptr opq(lds_ptr p) { unsigned u = (unsigned)(uintptr_t)p; asm volatile("" : "+v"(u)); return (lds_ptr)(uintptr_t)u; }
#define TS(i) do {} while (0)

constexpr int GW = 7, XN = 5;
DI constexpr int d_prio(int n) { return n < 0 ? -1 : n < 27 ? 3 - n / 7 : n < 64 ? 3 - (n - 27) / 10 : n < 84 ? 3 - (n - 64) / 5 : n < 112 ? 3 - (n - 84) / 7 : 0; }
DI constexpr int wd_at(int n) {
    constexpr int t[28] = {32, 34, 37, 39, 42, 65, 69, 72, 75, 78, 81, 83, 44, 47, 49, 52, 54, 57, 59, 61, 63, 86, 88, 90, 92, 94, 97, 99};
    for (int i = 0; i < 28; ++i) if (t[i] == n) return i;
    return -1;
}
DI constexpr int phys(int t) { return ((t % 9) + 2) % 9 * 16384; }

__global__ NOPK __launch_bounds__(512, 2) void block_fwd(const char* x, const char* wqkv, const char* wo, const char* wg, const char* wu, const char* wd, char* out, char* ws) {
    extern __shared__ __attribute__((aligned(16))) char smem[];
    lds_ptr lds = (lds_ptr)smem;
    const int tid = __builtin_amdgcn_workitem_id_x(), lane = tid & 63, w = __builtin_amdgcn_readfirstlane(tid >> 6);
    const int bid = __builtin_amdgcn_workgroup_id_x(), xcd = bid & 7, loc = bid >> 3;
    const int r = lane & 31, hf = lane >> 5, q4 = (lane & 15) >> 2, g1 = (lane >> 4) & 1, p4 = lane & 3;
    const unsigned lds0 = (unsigned)(uintptr_t)smem;
    const unsigned l16 = lane * 16;
    TS(0);
    const int sp3 = (loc + 8) >> 5;
    const int atb = (xcd >> 1) * 8 + (loc & 7) + sp3 * (xcd & 1) * 4, acg = (loc >> 3) + (1 - sp3) * (xcd & 1) * 4;
    const int row8 = lane >> 3;
    const int rot8 = ((loc & 7) << sp3) + sp3 * (xcd & 1), rot = rot8 == 7 ? 0 : rot8;
    if (loc < 28) {
        const unsigned xo = (unsigned)((atb * 64 + (tid >> 3)) * 896 + (((tid ^ (tid >> 4)) & 7) * 16));
        const unsigned xd0 = lds0 + 1024 + (w >> 2) * 28672 + (w & 3) * 1024;
        unsigned xd = xd0 + (7 - rot) * 4096;
        const unsigned w0 = (unsigned)((rot * 64 + (tid >> 3)) * 2688 + acg * 128 + (((tid & 7) ^ ((tid >> 2) & 4)) * 16));
        asm volatile("s_cmp_eq_u32 %0, %3\n\ts_cselect_b32 %0, %2, %0\n\ts_mov_b32 m0, %0\n\ts_add_u32 %0, %0, 0x1000\n\tglobal_load_lds_dwordx4 %1, %4\n\t"
#define XP(o) "s_cmp_eq_u32 %0, %3\n\ts_cselect_b32 %0, %2, %0\n\ts_sub_u32 m0, %0, " #o "\n\ts_add_u32 %0, %0, 0x1000\n\tglobal_load_lds_dwordx4 %1, %4 offset:" #o "\n\t"
                     XP(128) XP(256) XP(384) XP(512) XP(640) XP(768)
                     "s_add_u32 m0, %6, 8192-896\n\ts_barrier\n\tglobal_load_lds_dwordx4 %5, %7 offset:896\n\t"
                     "s_add_u32 m0, %6, 16384-1792\n\ts_nop 0\n\tglobal_load_lds_dwordx4 %5, %7 offset:1792\n\t"
                     "s_mov_b32 m0, %6\n\ts_nop 0\n\tglobal_load_lds_dwordx4 %5, %7"
                     : "+&s"(xd) : "v"(xo), "s"(xd0), "s"(xd0 + 7 * 4096), "s"(x), "v"(w0), "s"(lds0 + 58368 + w * 1024), "s"(wqkv) : "memory", "scc");
    }
    SB();
    const rsrc_t rws = __builtin_amdgcn_make_buffer_rsrc((void*)ws, 0, WS_END, 0x00020000);
    u64 epoch, tagv;
    const unsigned ownf = (lane & 7) ? flag_at(lane & 7, xcd, loc) : 0u;
    const unsigned ones = 0x3f803f80u;
    const bool ffn = loc < 28;
    const int l7 = loc / 7, m7 = loc % 7;
    const int tb = xcd * 4 + l7;
    const int tt = w & 1, ct = (w >> 1) & 1, kh = w >> 2;
    const unsigned swz = (unsigned)(((lane & 7) ^ (((row8 >> 1) & 1) << 2)) * 16);

    if (__builtin_expect(ffn, 1)) {
        constexpr int A_W = 58368, A_WS = 24576;
#define A_KC(c) (rot + (c) >= 7 ? rot + (c) - 7 : rot + (c))
        const unsigned wvo = (unsigned)(row8 * 2688) + swz;
        const char* wsrc = wqkv + acg * 128 + (size_t)w * 8 * 2688;
        const unsigned wdst = lds0 + A_W + w * 1024;
#define A_PIECE(c, i) glds(wvo, wsrc + A_KC(c) * (64 * 2688) + (i) * 896, wdst + ((c) & 3) * A_WS + (i) * 8192)
#define A_GROUP(c) do { A_PIECE(c, 1); A_PIECE(c, 2); A_PIECE(c, 0); } while (0)
        epoch = peek_flag(ws, ownf); FENCE();
        lds_ptr xb[4], wb1, wb1h;
#pragma unroll
        for (int j = 0; j < 4; ++j) xb[j] = lds + 1024 + tt * 28672 + (r >> 3) * 1024 + (r & 7) * 128 + (((2 * j + hf) ^ ((r >> 1) & 7)) * 16);
        wb1 = opq(lds + A_W + (kh ? 0 : 8192) + (8 * hf + q4) * 128 + ((ct ^ (q4 >> 1)) * 64) + g1 * 32 + p4 * 8);
        wb1h = opq(wb1 + 2 * A_WS);
        unsigned go1[2], go2[2], vo; int gi[2], vd;
#define A_DEST(p) do { unsigned hq_ = hf; PIN(hq_); const int G = acg * 8 + ct * 4 + 2 * (p) + hq_, hp = G / 7, i = G - hp * 7; \
            const unsigned hs = kh ? 131072u : 262144u, to = (kh ? WS_QF + (atb * 2 + tt) * 2048 : WS_KV + (atb * 2 + tt) * 4096) + r * 16; \
            go1[p] = (unsigned)(2 * hp + (i > 3)) * hs + (unsigned)(i > 3 ? i - 3 : i) * 512 + to; go2[p] = i == 3 ? (unsigned)(2 * hp + 1) * hs + to : OOB; gi[p] = i; \
            PIN2(go1[p], go2[p]); } while (0)
#define A_DESTV() do { unsigned rx_ = r; PIN(rx_); const int vcol = acg * 64 + ct * 32 + rx_, vh = vcol / 28; vd = vcol - vh * 28; \
            vo = WS_KV + (atb * 2 + tt) * 4096 + (unsigned)vh * 262144 + 2048 + hf * 512 + vd * 16; PIN(vo); } while (0)
        lds_ptr xw = opq(lds + 1024 + kh * 28672 + (w & 3) * 1024 + l16);
        waitv<4>();
        TS(28);
        u32x4 xv[7];
#pragma unroll
        for (int c = 0; c < 7; ++c) xv[c] = *(LAS u32x4*)(xw + c * 4096);
        SB();
        f32x4 sm = {};
#pragma unroll
        for (int c = 0; c < 7; ++c)
#pragma unroll
            for (int j = 0; j < 4; ++j) sm[j] = dot2(xv[c][j], ones, sm[j]);
        float nm = sum8((sm[0] + sm[1]) + (sm[2] + sm[3])) * (-1.f / D);
        PIN(nm);
#pragma unroll
        for (int c = 0; c < 7; ++c) { xv[c] = shift8(xv[c], nm); *(LAS u32x4*)(xw + c * 4096) = xv[c]; }
        A_GROUP(1);
        SB();
        LAS float* rst = (LAS float*)lds;
        sm = (f32x4){};
#pragma unroll
        for (int c = 0; c < 7; ++c)
#pragma unroll
            for (int j = 0; j < 4; ++j) sm[j] = dot2(xv[c][j], xv[c][j], sm[j]);
        rst[w * 8 + row8] = __builtin_amdgcn_rsqf(sum8((sm[0] + sm[1]) + (sm[2] + sm[3])) * (1.f / D) + EPS);
        A_GROUP(2);
        TS(31);
        bf16x8 fx[4], f1[4], f2[4];
#define A_WAIT(c) do { if ((c) == 0) waitv_bar<6>(); else if ((c) == 1) waitv_barx<6>(); else if ((c) < 6) waitv_barx<3>(); else waitv_barx<0>(); \
                       if ((c) == 0) TS(1); if ((c) == 6) TS(2); } while (0)
#define A_WP(c, ks) ((((c) & 2) ? wb1h : wb1) + ((c) & 1) * A_WS + (ks) * 2048)
        if (w < 4) {
            f32x16 ak = {}, av = {};
            f32x4 rv[4];
            float rs = 0.f;
#pragma unroll
            for (int n = 0; n < 30; ++n) {
                const int c = n >> 2, ks = n & 3, km = (n - 2) & 3;
                if (n < 28 && ks == 0) A_WAIT(c);
                if (n >= 2) { ak = MFMA(f1[km], fx[km], ak); PIN(ak); } SB();
                if (n < 28) { fx[ks] = lds8(xb[ks] + c * 4096); f1[ks] = cat(trd(A_WP(c, ks)), trd(A_WP(c, ks) + 512)); } SB();
                if (n >= 2) { av = MFMA(fx[km], f2[km], av); PIN(av); } SB();
                if (n < 28) f2[ks] = cat(trd(A_WP(c, ks) + 8192), trd(A_WP(c, ks) + 8192 + 512));
                if (c == 0 && ks < 3) A_PIECE(3, ks);
                if (c >= 2 && c < 5 && ks < 3) A_PIECE(c + 2, ks);
                if (n == 5) A_DEST(0);
                if (n == 6) A_DEST(1);
                if (n == 7) A_DESTV();
                if (n == 21) {
                    rs = rst[tt * 32 + r];
#pragma unroll
                    for (int g = 0; g < 4; ++g) rv[g] = *(LAS f32x4*)(rst + tt * 32 + 8 * g + 4 * hf);
                }
                if (n == 24) { PIN(epoch); tagv = epoch + 1; PIN(tagv); }
                SB();
            }
            TS(3);
#pragma unroll
            for (int i = 0; i < 16; ++i) { ak[i] *= rs; av[i] *= rv[i >> 2][i & 3]; }
#pragma unroll
            for (int p = 0; p < 2; ++p) {
                const u32x4 f = pack8(ak, p);
                const auto s0 = __builtin_amdgcn_permlane32_swap(f[0], f[2], false, false), s1 = __builtin_amdgcn_permlane32_swap(f[1], f[3], false, false);
                const u32x4 c = {s0[0], s1[0], s0[1], s1[1]};
                st16_dev(c, rws, go1[p], 0); st16_dev(c, rws, go2[p], 0);
            }
            st16_dev(pack8(av, 0), rws, vo, 0); st16_dev(pack8(av, 1), rws, vo + 1024, 0);
        } else {
            f32x16 aq = {};
            __builtin_amdgcn_s_setprio(1);
#pragma unroll
            for (int n = 0; n < 30; ++n) {
                const int c = n >> 2, ks = n & 3, km = (n - 2) & 3;
                if (n < 28 && ks == 0) A_WAIT(c);
                if (n == 24) { PIN(epoch); tagv = epoch + 1; PIN(tagv); }
                if (n >= 2) { aq = MFMA(f1[km], fx[km], aq); PIN(aq); } SB();
                if (n < 28) { fx[ks] = lds8(xb[ks] + c * 4096); f1[ks] = cat(trd(A_WP(c, ks)), trd(A_WP(c, ks) + 512)); }
                if (c == 0 && ks < 3) A_PIECE(3, ks);
                if (c >= 2 && c < 5 && ks < 3) A_PIECE(c + 2, ks);
                if (n == 5) A_DEST(0);
                if (n == 6) A_DEST(1);
                if (n == 7) A_DESTV();
                if (n == 24) {
                    const unsigned pv = vd == 0 ? ones : 0u, po = vd < 4 ? vo + 448 : OOB;
                    st16_dev((u32x4){pv, pv, pv, pv}, rws, po, 0); st16_dev((u32x4){pv, pv, pv, pv}, rws, po + 1024, 0);
                }
                SB();
            }
            __builtin_amdgcn_s_setprio(0);
            const float QS = rst[tt * 32 + r] * (0.18898223650461362f * 1.4426950408889634f);
#pragma unroll
            for (int i = 0; i < 16; ++i) aq[i] *= QS;
#pragma unroll
            for (int p = 0; p < 2; ++p) {
                const u32x4 f = pack8(aq, p);
                const auto s0 = __builtin_amdgcn_permlane32_swap(f[0], f[2], false, false), s1 = __builtin_amdgcn_permlane32_swap(f[1], f[3], false, false);
                const bool sp = gi[p] == 3;
                st16_dev((u32x4){s0[0], s1[0], sp ? 0u : s0[1], sp ? 0u : s1[1]}, rws, go1[p], 0);
                st16_dev((u32x4){0u, 0u, s0[1], s1[1]}, rws, go2[p], 0);
            }
        }
    } else {
        __builtin_amdgcn_s_sleep(8);
        epoch = peek_flag(ws, ownf);
        const int ri = xcd * 4 + loc - 28, nu = (w == 0 && ri < 16) ? 4 : 3;
        const unsigned vo = (unsigned)(row8 * 3584 + (lane & 7) * 16);
#pragma unroll 1
        for (int i = 0; i < nu; ++i) {
            const int u = i < 3 ? i * 256 + ri * 8 + w : 768 + ri;
            const size_t so = (size_t)(u / 28) * 16 * 3584 + (u % 28) * 128;
            const unsigned d_ = lds0 + w * 16384 + i * 4096;
            glds(vo, wg + so, d_); glds(vo, wg + so + 8 * 3584, d_ + 1024); glds(vo, wu + so, d_ + 2048); glds(vo, wu + so + 8 * 3584, d_ + 3072);
        }
#pragma unroll 1
        for (int i = 0; i < nu; ++i) {
            const int u = i < 3 ? i * 256 + ri * 8 + w : 768 + ri;
            waitv<8>();
            lds_ptr sg = lds + w * 16384 + i * 4096 + g1 * 2048 + (4 * hf + q4) * 128 + p4 * 8;
#pragma unroll
            for (int e = 0; e < 4; ++e) st16_dev(U4(cat(trd(sg + e * 32), trd(sg + e * 32 + 1024))), rws, l16, WS_WGU + (unsigned)(((u % 28) * 4 + e) * 28 + u / 28) * 1024);
            FENCE();
        }
        tagv = epoch + 1;
    }
    TS(24);
    const int head = xcd * 2 + (loc & 1), rb = loc >> 1, qt = w & 3;
    const char* kvs = ws + WS_KV + (size_t)head * 262144 + w * 2048;
    const char* qs = ws + WS_QF + (size_t)(head * 64 + rb * 4 + qt) * 2048 + kh * 1024;
    unsigned kvd = lds0 + w * 2048, qd = lds0 + 147456 + qt * 2048 + kh * 1024;
    unsigned pf1, fl1 = flag_at(FK_A, xcd, loc);
    {
        const unsigned cg = (head * 28 + (lane & 1) * 27) / 64, q = lane >> 4, i = (lane >> 1) & 7;
        pf1 = cg == 3 ? flag_at(FK_A, 2 * q + (i >> 2), 24 + (i & 3)) : flag_at(FK_A, 2 * q + (cg >> 2), (cg & 3) * 8 + i);
    }
    PINS(kvs); PINS(qs); PINS(kvd); PINS(qd); PIN2(pf1, fl1);
    waitv_bar<0>();
    if (w == 0) set_flag(ws, fl1, tagv);
    const u64 tag = ((u64)(unsigned)__builtin_amdgcn_readfirstlane((unsigned)(tagv >> 32)) << 32 | (u64)(unsigned)__builtin_amdgcn_readfirstlane((unsigned)tagv));
    TS(4);

    const int drb = (xcd >> 1) * 4 + l7, dfg = (xcd & 1) * 7 + m7;
    bf16x8 wf[28];
    const unsigned p16 = hf * 1024 + g1 * 512 + (lane & 15) * 16;
    u32x2 xa, xb;
    f32x4 csg = {}, csu = {};
    f32x16 acc = {};
    const u32x4 one4 = {ones, ones, ones, ones};
    const unsigned hi_ = (unsigned)((tb * 2 + tt) * 28 + m7 * 4 + ct * 2 + kh), ofo = (unsigned)(tt * 32768 + kh * 16384 + ct * 4096);
    unsigned ho = WS_HF + hi_ * 1024 + l16, so = hf ? OOB : WS_HS + hi_ * 256 + r * 8;
    const char* os = ws + WS_OF + (size_t)tb * 65536 + ofo;
    unsigned od = lds0 + 65536 + ofo;
    unsigned pf2 = flag_at(FK_O, kh * 4 + ct + (lane & 2), (tb & ~1) + (lane & 1));
    unsigned go = ffn ? WS_GF + (unsigned)(drb * 4 * 112 + dfg * 8 + w) * 1024 + g1 * 512 + (hf * 16 + (lane & 15)) * 16 : OOB;
    unsigned fo3 = flag_at(FK_H, xcd, loc);
    {
        lds_ptr ap = opq(lds + kh * 8192 + l16), apB = opq(lds + 65536 + kh * 8192 + l16), apC = opq(lds + 131072 + kh * 8192 + l16);
        const unsigned lv = kh * 8192 + (l16 ^ (kh * 256));
        lds_ptr vp = opq(lds + lv), vpB = opq(lds + 65536 + lv), vpC = opq(lds + 131072 + lv);
#define AP(off) ((off) < 65536 ? ap + (off) : (off) < 131072 ? apB + ((off) - 65536) : apC + ((off) - 131072))
#define VP(off) ((off) < 65536 ? vp + (off) : (off) < 131072 ? vpB + ((off) - 65536) : vpC + ((off) - 131072))
#define KV_ISSUE(t) glds2_dev(l16, kvs + (t) * 16384, kvd + phys(t))
        unsigned wgo = ffn ? WS_WGU + (unsigned)(dfg * 8 + w) * 28672 : OOB;
        const int ri = umin(xcd * 4 + loc - 28, 27);
        const char* wos = (ffn ? wo + m7 * 128 : wd + (ri % 7) * 128 + (size_t)(ri / 7) * 448 * 896) + (size_t)w * 56 * 896;
        unsigned wov = (unsigned)(row8 * 896) + swz;
        const rsrc_t rx = __builtin_amdgcn_make_buffer_rsrc((void*)x, 0, 2048 * 448 * 2, 0x00020000);
        unsigned xo_ = (unsigned)(((tb * 2 + tt) * 32 + r) * 896 + m7 * 128 + ct * 64 + kh * 32 + hf * 8);
        unsigned rto = flag_at(FK_A, w, 28 + (lane & 3));
        lds_ptr qp = opq(lds + 147456 + qt * 2048 + l16);
        PINS(wgo); PINS(wos); PIN2(wov, xo_); PIN(rto); PIN2(ho, so); PIN2(pf2, fo3); PIN(go);
        lds_ptr xw = opq(lds + 147456 + w * 2048 + l16), xr = opq(lds + 147456 + (w ^ 4) * 2048 + l16);
        lds_ptr lw = opq(lds + 58368 + w * 256 + lane * 4), lr = opq(lds + 58368 + qt * 256 + 128 + r * 4);
        lds_ptr bw = opq(lds + 60416 + w * 4), br = opq(lds + 60416 + (lane & 7) * 4);
        unsigned oo = WS_OF + (unsigned)((rb * 4 + qt) * 32 + head * 2 + kh) * 1024 + l16;
        unsigned fo2 = flag_at(FK_O, xcd, loc);
        PIN2(oo, fo2);
        if (w == 1) poll_flag(ws, pf1, tag);
        BAR();
        TS(5);
        glds_dev(l16, qs, qd);
        KV_ISSUE(0); KV_ISSUE(1);
        asm volatile("s_barrier" ::: "memory");
        KV_ISSUE(2); KV_ISSUE(3);
        waitv_bar<4>();
        TS(6);
        const bf16x8 q0 = lds8(qp), q1 = lds8(qp + 1024);
        f32x16 o = {}, sa = {}, sb;
        bf16x8 k0 = lds8(ap + phys(0)), k1 = lds8(ap + phys(0) + 1024), v0 = lds8(vp + phys(0) + 2048), v1 = {};
        u32x4 p0, p1 = {};
        sa = MFMA(k0, q0, sa); sa = MFMA(k1, q1, sa);
        k0 = lds8(ap + phys(0) + 4096); k1 = lds8(ap + phys(0) + 5120);
        PIN(sa); SB();
        u64 rtf = 0;
#define EXP4(S, o4) _Pragma("unroll") for (int i_ = o4; i_ < o4 + 4; ++i_) S[i_] = __builtin_amdgcn_exp2f(S[i_]);
#define WLD(s) wf[s] = BF8(ld16_dev(rws, p16 + ((s) & 1) * 256 + ((s) & 2) * 1024, wgo + ((s) >> 2) * 4096))
#define SIDE(t, g) do { \
            if ((t) == 0 && (g) == 0) KV_ISSUE(4); if ((t) == 0 && (g) == 2) KV_ISSUE(5); if ((t) == 1 && (g) == 0) KV_ISSUE(6); if ((t) == 1 && (g) == 2) KV_ISSUE(7); \
            if ((t) == 2 && (g) == 0) KV_ISSUE(8); \
            if ((t) == 1 && (g) == 1) peek_lds(ws, rto, lds0 + 155648 + w * 512); if ((t) == 2 && (g) == 2) { waitv<5>(); rtf = peeked(lds + 155648 + w * 512 + lane * 4); } \
            if ((t) == 3 && (g) < 3) KV_ISSUE(9 + (g)); \
            if (((t) == 2 && (g) == 1) || ((t) == 14 && (g) == 2)) st16_dev((u32x4){~0u, ~0u, ~0u, ~0u}, rws, go + ((t) == 2 ? 2 : 3) * 114688, 0);     \
            if ((t) == 7 && (g) == 0) KV_ISSUE(12); if ((t) == 7 && (g) == 2) KV_ISSUE(13); if ((t) == 8 && (g) == 0) KV_ISSUE(14); if ((t) == 8 && (g) == 2) KV_ISSUE(15); \
            if ((t) >= 4 && (t) <= 6) WLD(((t) - 4) * 4 + (g)); \
            if (((t) == 7 || (t) == 8) && ((g) & 1)) WLD(12 + ((t) - 7) * 2 + ((g) >> 1)); \
            if ((t) >= 9 && (t) <= 11) WLD(((t) - 5) * 4 + (g)); \
            if ((t) == 12 || ((t) == 13 && (g) < 3)) glds(wov, wos + (((t) - 12) * 4 + (g)) * 8 * 896, lds0 + (w * 7 + ((t) - 12) * 4 + (g)) * 1024); \
            if ((t) == 13 && (g) == 3) { xa = ld8(rx, xo_, 0); xb = ld8(rx, xo_, 16); } \
            if ((t) == 14 && (g) == 0) { if (tid < 32) *(LAS u32x4*)(lds + 57344 + tid * 16) = (u32x4){0, 0, 0, 0}; } \
        } while (0)
#define TOFF(i) (phys((i) >> 1) + ((i) & 1) * 4096)
#define TILE(i, SC, SN) do { \
            __builtin_amdgcn_s_setprio(((i) & 1) ? 1 : 3); \
            if ((i) < 31) { f32x16 z_ = {}; SN = MFMA(k0, q0, z_); PIN(SN); } if ((i) < 30) k0 = lds8(AP(TOFF((i) + 2))); SB(); \
            EXP4(SC, 0); PIN(SC); SB(); \
            o = MFMA(v1, BF8(p1), o); PIN(o); v1 = lds8(VP(TOFF(i) + 3072)); SIDE((i) >> 1, ((i) & 1) * 2); SB(); \
            EXP4(SC, 4); p0 = pack8(SC, 0); PIN(p0); SB(); \
            __builtin_amdgcn_s_setprio(((i) & 1) ? 0 : 2); \
            if ((i) < 31) { SN = MFMA(k1, q1, SN); PIN(SN); } if ((i) < 30) k1 = lds8(AP(TOFF((i) + 2) + 1024)); SB(); \
            EXP4(SC, 8); PIN(SC); SB(); \
            o = MFMA(v0, BF8(p0), o); PIN(o); if ((i) < 31) v0 = lds8(VP(TOFF((i) + 1) + 2048)); SIDE((i) >> 1, ((i) & 1) * 2 + 1); SB(); \
            EXP4(SC, 12); p1 = pack8(SC, 1); PIN(p1); SB(); } while (0)
#define STEP(t) do { TILE(2 * (t), sa, sb); TILE(2 * (t) + 1, sb, sa); } while (0)
        STEP(0);
        waitv_barx<4>();
        STEP(1); STEP(2);
        if (__builtin_expect(!wave_all(rtf == tag), 0)) poll_flag(ws, rto, tag);
        waitv_bar2<3>();
        TS(21);
        STEP(3); STEP(4); STEP(5); STEP(6);
        waitv_bar2<12>();
        TS(22);
        STEP(7); STEP(8); STEP(9); STEP(10);
        waitv_bar2<9>();
        TS(23);
        STEP(11); STEP(12); STEP(13); STEP(14); STEP(15);
        o = MFMA(v1, BF8(p1), o);
        TS(7);
        u32x4 ov;
        {
            *(LAS f32x4*)xw = (f32x4){o[8], o[9], o[10], o[11]}; *(LAS f32x4*)(xw + 1024) = (f32x4){o[12], o[13], o[14], o[15]};
            const float l = kh ? o[4] : o[12];
            *(LAS float*)lw = l;
#define AMX(a, b, c) __builtin_fmaxf(__builtin_fmaxf(__builtin_fabsf(a), __builtin_fabsf(b)), __builtin_fabsf(c))
            const float mx_ = AMX(AMX(AMX(o[0], o[1], o[2]), AMX(o[3], o[4], o[5]), AMX(o[6], o[7], o[8])), AMX(AMX(o[9], o[10], o[11]), AMX(o[12], o[13], o[14]), o[15]), 0.f);
            const bool ok = (mx_ < 0x1p100f) & ((hf ? l : 1.f) > 0x1p-40f);
            *(LAS unsigned*)bw = wave_all(ok) ? 0u : 1u;
            BAR();
            const f32x4 a = *(LAS f32x4*)xr, c = *(LAS f32x4*)(xr + 1024);
            const float inv = __builtin_amdgcn_rcpf(*(LAS float*)lr + *(LAS float*)(lr + 1024));
            const unsigned bad = *(LAS unsigned*)br;
            const float iz = (kh && hf) ? 0.f : inv;
            ov = (u32x4){cvtpk((o[0] + a[0]) * inv, (o[1] + a[1]) * inv), cvtpk((o[2] + a[2]) * inv, (o[3] + a[3]) * inv),
                         cvtpk((o[4] + c[0]) * iz, (o[5] + c[1]) * iz), cvtpk((o[6] + c[2]) * iz, (o[7] + c[3]) * iz)};
            if (__builtin_expect(wave_any(bad != 0), 0)) {
                f32x16 of = {};
                float m = -0x1p126f;
#pragma unroll 1
                for (int t = 0; t < 16; ++t) {
                    BAR();
                    glds2_dev(l16, kvs + t * 16384, lds0 + 65536 + w * 2048);
                    waitv_bar<0>();
#pragma unroll 1
                    for (int u = 0; u < 2; ++u) {
                        f32x16 s = {};
                        s = MFMA(lds8(apB + u * 4096), q0, s); s = MFMA(lds8(apB + u * 4096 + 1024), q1, s);
                        float mx = s[0];
#pragma unroll
                        for (int i = 1; i < 16; ++i) mx = __builtin_fmaxf(mx, s[i]);
                        mx = __builtin_fmaxf(swap_max(mx), m);
                        const float alpha = __builtin_amdgcn_exp2f(m - mx);
                        m = mx;
#pragma unroll
                        for (int i = 0; i < 16; ++i) { s[i] = __builtin_amdgcn_exp2f(s[i] - mx); of[i] *= alpha; }
                        of = MFMA(lds8(vpB + u * 4096 + 2048), BF8(pack8(s, 0)), of);
                        of = MFMA(lds8(vpB + u * 4096 + 3072), BF8(pack8(s, 1)), of);
                    }
                }
                *(LAS f32x4*)xw = (f32x4){of[8], of[9], of[10], of[11]}; *(LAS f32x4*)(xw + 1024) = (f32x4){of[12], of[13], of[14], of[15]};
                *(LAS float*)lw = hf ? (kh ? of[4] : of[12]) : m;
                BAR();
                const f32x4 a2 = *(LAS f32x4*)xr, c2 = *(LAS f32x4*)(xr + 1024);
                const float m0 = *(LAS float*)(lr - 128), m1 = *(LAS float*)(lr + 1024 - 128), mm = __builtin_fmaxf(m0, m1);
                const float e0 = __builtin_amdgcn_exp2f(m0 - mm), e1 = __builtin_amdgcn_exp2f(m1 - mm);
                const float iv = __builtin_amdgcn_rcpf(*(LAS float*)lr * e0 + *(LAS float*)(lr + 1024) * e1);
                const float im = (kh ? e1 : e0) * iv, io = (kh ? e0 : e1) * iv;
                const bool z = kh && hf;
                ov = (u32x4){cvtpk(of[0] * im + a2[0] * io, of[1] * im + a2[1] * io), cvtpk(of[2] * im + a2[2] * io, of[3] * im + a2[3] * io),
                             z ? 0u : cvtpk(of[4] * im + c2[0] * io, of[5] * im + c2[1] * io), z ? 0u : cvtpk(of[6] * im + c2[2] * io, of[7] * im + c2[3] * io)};
            }
        }
        st16_dev(ov, rws, oo, 0);
        TS(25);
        FENCE();
        PIN2(xa, xb);
#pragma unroll
        for (int s = 27; s >= 0; --s) PIN(wf[s]);
#pragma unroll
        for (int s = 0; s < 16; s += 2) { csg = MFMA16(wf[s], BF8(one4), csg); csu = MFMA16(wf[s + 1], BF8(one4), csu); }
        acc[0] = bflo(xa[0]); acc[1] = bfhi(xa[0]); acc[2] = bflo(xa[1]); acc[3] = bfhi(xa[1]); acc[4] = bflo(xb[0]); acc[5] = bfhi(xb[0]); acc[6] = bflo(xb[1]); acc[7] = bfhi(xb[1]);
        PIN(acc); PIN2(ho, so); PIN2(pf2, fo3); PINS(os); PINS(od); PIN2(csg, csu);
        waitv_bar<0>();
        if (w == 0) set_flag(ws, fo2, tagv);
        TS(8);
    }
    if (__builtin_expect(!ffn, 0)) {
        const int ri = xcd * 4 + loc - 28;
        if (ri < 28) {
#pragma unroll 1
            for (int u = w; u < 28; u += 8) {
                lds_ptr sd = lds + u * 2048 + (4 * hf + q4) * 128 + g1 * 32 + p4 * 8;
#pragma unroll
                for (int e = 0; e < 2; ++e) { lds_ptr p_ = sd + ((e ^ (q4 >> 1)) * 64);
                    st16_dev(U4(cat(trd(p_), trd(p_ + 1024))), rws, l16, WS_WD + (unsigned)(((ri % 7) * 112 + (ri / 7) * 28 + u) * 2 + e) * 1024); }
            }
        }
        waitv_bar<0>();
        if (w == 0) set_flag(ws, fo3, tagv);
        return;
    }

    const int erb = tb >> 1;
    const char* hs = ws + WS_HF + (size_t)drb * 114688 + w * 1024;
    const char* hss = ws + WS_HS + (size_t)((drb * 4 + (w & 3)) * 28 + kh * 14) * 256;
    unsigned pf3, fo4 = flag_at(FK_G, xcd, loc);
    {
        lds_ptr wb = lds + kh * (8 * 28 * 128) + (4 * hf + q4) * 128 + ((ct ^ (q4 >> 1)) * 64) + (g1 ^ kh) * 32 + p4 * 8;
        bf16x8 wr[16];
#pragma unroll
        for (int s = 0; s < 12; ++s) { const int k0 = ((s >> 1) * 28 + (s & 1) * 16) * 128; wr[s] = cat(trd(wb + k0), trd(wb + k0 + 1024)); }
        lds_ptr cw = opq(lds + 131072 + w * 2048 + l16), cr = opq(lds + 131072 + (w ^ 4) * 2048 + l16);
        lds_ptr ob = opq(lds + 65536 + tt * 32768 + kh * 16384 + l16);
        pf3 = flag_at(FK_H, drb >> 1, (drb & 1) * 14 + umin(lane & 15, 13));
        PIN2(pf3, fo4); PINS(hs); PINS(hss);
        poll_flag(ws, pf2, tag);
        TS(9);
        glds4_dev(l16, os, od);
        asm volatile("s_barrier" ::: "memory");
        glds4_dev(l16, os + 8192, od + 8192);
        bf16x8 of[8];
        waitv_bar<4>();
        TS(26);
#pragma unroll
        for (int s = 0; s < 8; ++s) of[s] = lds8(ob + s * 1024);
        SB();
#pragma unroll
        for (int s = 0; s < 8; ++s) {
            acc = MFMA(wr[s], of[s], acc); PIN(acc); SB();
            if (s >= 2 && s < 6) { const int k0 = (((s + 10) >> 1) * 28 + (s & 1) * 16) * 128; wr[s + 10] = cat(trd(wb + k0), trd(wb + k0 + 1024)); SB(); }
        }
        waitv_barx<0>();
#pragma unroll
        for (int s = 0; s < 8; ++s) of[s] = lds8(ob + (8 + s) * 1024);
        SB();
#pragma unroll
        for (int s = 0; s < 8; ++s) acc = MFMA(wr[8 + s], of[s], acc);
        TS(10);
        *(LAS f32x4*)cw = (f32x4){acc[8], acc[9], acc[10], acc[11]}; *(LAS f32x4*)(cw + 1024) = (f32x4){acc[12], acc[13], acc[14], acc[15]};
        BAR();
        const f32x4 a = *(LAS f32x4*)cr + (f32x4){acc[0], acc[1], acc[2], acc[3]}, b = *(LAS f32x4*)(cr + 1024) + (f32x4){acc[4], acc[5], acc[6], acc[7]};
        const u32x4 hv = {cvtpk(a[0], a[1]), cvtpk(a[2], a[3]), cvtpk(b[0], b[1]), cvtpk(b[2], b[3])};
        st16_dev(hv, rws, ho, 0);
        FENCE(); SB();
        float s1 = 0.f, s2 = 0.f;
        { f32x4 q4_ = {}; acc_stats2(hv[0], hv[1], q4_); acc_stats2(hv[2], hv[3], q4_); s1 = swap_add(q4_[0] + q4_[1]); s2 = swap_add(q4_[2] + q4_[3]); }
        st8_dev((u32x2){f2u(s1), f2u(s2)}, rws, so, 0);
        FENCE(); PIN2(csg, csu); SB();
#pragma unroll
        for (int s = 16; s < 28; s += 2) { csg = MFMA16(wf[s], BF8(one4), csg); csu = MFMA16(wf[s + 1], BF8(one4), csu); }
        PIN2(csg, csu);
        waitv_bar<0>();
        if (w == 0) set_flag(ws, fo3, tagv);
        if (bid == 0 && w == 0) set_flag(ws, 0u, tagv);
        TS(11);
    }

    const unsigned wdo = WS_WD + (unsigned)(m7 * 112 + w * 14) * 2048;
    bf16x8 wdf[28];
    bf16x8 gf[GW];
    const unsigned gxo = WS_GF + (unsigned)(erb * 4 * 112 + w * 14) * 1024 + (tb & 1) * 256;
    const unsigned pwd = hf * 2048 + g1 * 512 + (lane & 15) * 16;
#define G_LD(s) gf[(s) % GW] = BF8(ld16_dev(rws, p16, gxo + ((s) / 7) * 114688 + ((s) % 7) * 2048))
    u64 gfl = 0;
    const unsigned gfg = (w * 14 + umin(lane & 15, 13)) >> 3;
    const unsigned gfi = flag_at(FK_G + g1, (erb >> 2) * 2 + (gfg >= 7), (erb & 3) * 7 + (gfg >= 7 ? gfg - 7 : gfg));
    {
        const unsigned hd = lds0 + w * 1024;
        const char* wds = ws + wdo;
        lds_ptr hb = opq(lds + p16), hb2 = opq(lds + 57344 + p16);
        lds_ptr wl = opq(lds + 114688 + w * 5120 + l16), wl2 = opq(lds + w * 7168 + l16);
        LAS f32x2* stp = (LAS f32x2*)opq(lds + 159744 + (lane & 15) * 8);
        if (w == 1) poll_flag(ws, pf3, tag);
        BAR();
        TS(12);
#define H_PIECE(j) glds_dev(l16, hs + (j) * 8192, hd + (j) * 8192)
#define WDO(s) (((s) >> 2) * 4096 + (((s) >> 1) & 1) * 1024 + ((s) & 1) * 256)
#define WDLD(s) wdf[s] = BF8(ld16_dev(rws, pwd, wdo + WDO(s)))
#define HB(n) (((n) < 56 ? hb + ((n) & ~1) * 1024 : hb2 + (((n) & ~1) - 56) * 1024) + ((n) & 1) * 256)
        H_PIECE(0);
        asm volatile("s_barrier" ::: "memory");
        H_PIECE(1); H_PIECE(2); H_PIECE(3);
        constexpr int PF = 4;
        bf16x8 bf[PF + 1];
        f32x4 ag[2][2], au[2][2];
        f32x2 sp[7], sv[4];
        float mean[2] = {0.f, 0.f}, r2[2] = {0.f, 0.f}, hp = 0.f;
        u32x4 hv;
        lds_ptr pk = opq(lds + 155648 + w * 512 + lane * 4);
        u64 pv = 0;
        const unsigned pkd = lds0 + 155648 + w * 512;
        const unsigned rdi = (w >> 1) * 7 + m7, rdo = flag_at(FK_H, rdi >> 2, 28 + (rdi & 3));
        waitv_barx<3>();
        TS(27);
#pragma unroll
        for (int n = 0; n < PF; ++n) bf[n] = lds8(HB(n));
        SB();
        sfor<0, 125>([&](auto N_) __attribute__((always_inline)) {
            constexpr int n = decltype(N_)::v, p = n / 28, s = n % 28;
            if constexpr (n == 4) waitv_barx<3>();
            if constexpr (n == 12) waitv_barx<8>();
            if constexpr (n == 28) { waitv_barx<3>(); TS(13); }
            if constexpr (n == 64) { waitv_barx<10>(); if (w == 0) set_flag(ws, fo4, tagv); TS(14); }
            if constexpr (n == 85) { waitv_barx<8>(); if (w == 0) set_flag(ws, fo4 + 32768, tagv); }
            if constexpr (n == 32) { if (__builtin_expect(!wave_all(pv == tag), 0)) poll_flag(ws, rdo, tag); FENCE(); }
            if constexpr (d_prio(n) != d_prio(n - 1)) __builtin_amdgcn_s_setprio(d_prio(n));
            if constexpr (n < 112) {
                if constexpr (s < 2) { ag[p & 1][s] = (f32x4){}; au[p & 1][s] = (f32x4){}; }
                ag[p & 1][s & 1] = MFMA16(wf[s & ~1], bf[n % (PF + 1)], ag[p & 1][s & 1]); au[p & 1][s & 1] = MFMA16(wf[s | 1], bf[n % (PF + 1)], au[p & 1][s & 1]);
                PIN2(ag[p & 1][s & 1], au[p & 1][s & 1]);
            }
            SB();
            if constexpr (n + PF < 112) bf[(n + PF) % (PF + 1)] = lds8(HB(n + PF));
            if constexpr (n % 3 == 2 && n < 30) H_PIECE(4 + n / 3);
            if constexpr (n == 4) glds4_dev(l16, hss, lds0 + 114688 + w * 5120);
            if constexpr (n == 16) { waitv<4>();
#pragma unroll
                for (int j = 0; j < 7; ++j) sp[j] = *(LAS f32x2*)(lds + 114688 + w * 5120 + (hf * 7 + j) * 256 + r * 8);
            }
            if constexpr (n == 20) { const f32x2 t = ((sp[0] + sp[1]) + (sp[2] + sp[3])) + ((sp[4] + sp[5]) + sp[6]); stp[w * 32 + g1 * 16] = (f32x2){swap_add(t[0]), swap_add(t[1])}; }
            if constexpr (wd_at(n) >= 0) { constexpr int i = wd_at(n);
                if constexpr (i < 5) glds_dev(pwd, wds + WDO(i), lds0 + 114688 + w * 5120 + i * 1024);
                else if constexpr (i < 12) glds_dev(pwd, wds + WDO(i), lds0 + w * 7168 + (i - 5) * 1024);
                else WDLD(i); }
            if constexpr (n == 84) peek_lds(ws, gfi - g1 * 32768, pkd);
            if constexpr (n == 95) { waitv<5>(); pv = peeked(pk); }
            if constexpr (n == 99) { if (__builtin_expect(!wave_all(pv == tag), 0)) poll_flag(ws, gfi - g1 * 32768, tag); FENCE(); }
            if constexpr (n >= 101 && n < 101 + 2 * XN && n % 2 == 1) G_LD((n - 101) / 2);
            if constexpr (n == 106) gfl = peek_flag(ws, gfi);
            if constexpr (n == 28) pv = peeked(pk);
            if constexpr (n == 18) peek_lds(ws, rdo, pkd);
            if constexpr (p >= 1) {
                constexpr int e = (p - 1) & 1, tp = n == 100 ? 3 : p - 1;
                if constexpr ((s == 0 && p < 4) || n == 100) { sv[0] = stp[tp * 32]; sv[1] = stp[(tp + 4) * 32]; sv[2] = stp[tp * 32 + 16]; sv[3] = stp[(tp + 4) * 32 + 16]; }
                if constexpr (((s == 2 || s == 3) && p < 4) || n == 104 || n == 105) { constexpr int k = n & 1; const f32x2 t = sv[2 * k] + sv[2 * k + 1];
                    mean[k] = t[0] * (1.f / D); r2[k] = __builtin_amdgcn_rcpf(__builtin_fmaxf(t[1] * (1.f / D) - mean[k] * mean[k], 0.f) + EPS); PIN2(mean[k], r2[k]); }
                if constexpr (s >= 4 && s < 12) { constexpr int i = s - 4, k = i >> 2;
                    const float h0 = __builtin_fmaxf(ag[e][k][i & 3] - mean[k] * csg[i & 3], 0.f) * r2[k] * (au[e][k][i & 3] - mean[k] * csu[i & 3]);
                    if constexpr (i & 1) { hv[i >> 1] = p >= 3 ? umin(cvtpk(hp, h0), ~1u) : cvtpk(hp, h0); PIN(hv); } else { hp = h0; PIN(hp); } }
                if constexpr (s == 12) {
                    const auto s0 = __builtin_amdgcn_permlane32_swap(hv[0], hv[2], false, false), s1 = __builtin_amdgcn_permlane32_swap(hv[1], hv[3], false, false);
                    st16_dev((u32x4){s0[0], s1[0], s0[1], s1[1]}, rws, go + (p - 1) * 114688, 0); FENCE();
                }
            }
            SB();
        });
        TS(15);
#pragma unroll
        for (int s = XN; s < 7; ++s) { G_LD(s); SB(); }
        FENCE();
#pragma unroll
        for (int s = 0; s < 12; ++s) wdf[s] = lds8(s < 5 ? wl + s * 1024 : wl2 + (s - 5) * 1024);
    }

    {
        const int etok = tid >> 4, eblk = tid & 15;
        const int et = erb * 4 + (etok >> 4), er = (tb & 1) * 16 + (etok & 15);
        const unsigned hro = (unsigned)((((eblk >> 2) & 3) * 64 + (eblk & 1) * 32 + er) * 16 + ((eblk >> 1) & 1) * 8) + WS_HF + (unsigned)(et * 28 + m7 * 4) * 1024;
        u32x2 hres[2];
        GAS u64* op = (GAS u64*)(out + (size_t)(et * 32 + er) * 896 + m7 * 128 + eblk * 8);
        lds_ptr rp = opq(lds + w * 8448 + (lane >> 4) * 528 + (lane & 15) * 16), rq = opq(lds + eblk * 528 + etok * 16);
        lds_ptr rp2 = opq(rp + 67584), rq2 = opq(rq + 67584);
        f32x4 c[4];
#define G_STEP(s) do { _Pragma("unroll") for (int j = 0; j < 4; ++j) { if ((s) % 7 == 0) c[j] = (f32x4){}; c[j] = MFMA16(wdf[4 * ((s) % 7) + j], gf[(s) % GW], c[j]); } PIN2(c[0], c[1]); PIN2(c[2], c[3]); } while (0)
#define G_PUT(q) do { _Pragma("unroll") for (int j = 0; j < 4; ++j) *(LAS f32x4*)(((q) < 2 ? rp : rp2) + j * 2112 + ((q) & 1) * 256) = c[j]; } while (0)
#define G_OUT(hh, RQ) do { f32x4 pa[8]; \
            _Pragma("unroll") for (int ww = 0; ww < 8; ++ww) pa[ww] = *(LAS f32x4*)(RQ + ww * 8448); SB(); \
            const f32x4 a = ((pa[0] + pa[1]) + (pa[2] + pa[3])) + ((pa[4] + pa[5]) + (pa[6] + pa[7])); \
            const u32x2 ov = {cvtpk(a[0] + bflo(hres[hh][0]), a[1] + bfhi(hres[hh][0])), cvtpk(a[2] + bflo(hres[hh][1]), a[3] + bfhi(hres[hh][1]))}; \
            __hip_atomic_store(op + (hh) * (64 * 896 / 8), __builtin_bit_cast(u64, ov), __ATOMIC_RELAXED, __HIP_MEMORY_SCOPE_AGENT); } while (0)
        TS(16);
        unsigned seen = 0;
#pragma unroll
        for (int s = 0; s < 28; ++s) {
            if (s == 5) BAR();
            if (s == 7 || s == 14 || s == 21) G_PUT(s / 7 - 1);
            if (s == 14) TS(17);
            if (s == 21) { waitv<7>(); PIN2(hres[0], hres[1]); BAR(); G_OUT(0, rq); FENCE(); TS(18); }
            SB();
            G_STEP(s);
            if (s >= 14) { const u32x4 u = U4(gf[s % GW]); seen = umax3(umax3(seen, u[0], u[1]), u[2], u[3]); PIN(seen); }
            SB();
            if (s == 0) { if (__builtin_expect(!wave_all(gfl == tag), 0)) poll_flag(ws, gfi, tag); FENCE(); }
            if (s + GW < 28) G_LD(s + GW);
            if (s == 1) { hres[0] = ld8_dev(rws, hro, 0); hres[1] = ld8_dev(rws, hro, 57344); FENCE(); }
        }
        if (w == 0) { set_flag(ws, fo4 + 65536, tagv); set_flag(ws, fo4 + 98304, tagv); }
        if (__builtin_expect(wave_any(seen == ~0u), 0)) {
            poll_flag(ws, gfi + 65536, tag);
#pragma unroll
            for (int s = 14; s < 14 + GW; ++s) G_LD(s);
#pragma unroll
            for (int s = 14; s < 28; ++s) { if (s == 21) G_PUT(2); G_STEP(s); SB(); if (s + GW < 28) G_LD(s + GW); SB(); }
        }
        G_PUT(3);
        TS(19);
        BAR();
        G_OUT(1, rq2);
    }
    TS(20);
}

extern "C" void kernel_launch(void* const* d_in, const int* in_sizes, int n_in, void* d_out, int out_size, void* d_ws, size_t ws_size, hipStream_t stream) {
    (void)hipFuncSetAttribute((const void*)block_fwd, hipFuncAttributeMaxDynamicSharedMemorySize, LDS_BYTES);
    block_fwd<<<256, 512, LDS_BYTES, stream>>>((const char*)d_in[0], (const char*)d_in[1], (const char*)d_in[2], (const char*)d_in[4], (const char*)d_in[5], (const char*)d_in[6], (char*)d_out, (char*)d_ws);
}
```

```cpp
#include <hip/hip_runtime.h>
#include <hip/hip_bf16.h>
#include <stdint.h>

typedef __attribute__((ext_vector_type(8))) short bf16x8;
typedef __attribute__((ext_vector_type(4))) short s16x4;
typedef __attribute__((ext_vector_type(2))) __bf16 bf16x2;
typedef __attribute__((ext_vector_type(16))) float f32x16;
typedef __attribute__((ext_vector_type(4))) float f32x4;
typedef __attribute__((ext_vector_type(2))) float f32x2;
typedef __attribute__((ext_vector_type(4))) unsigned u32x4;
typedef __attribute__((ext_vector_type(2))) unsigned u32x2;
typedef short v4i16_t __attribute__((ext_vector_type(4)));
typedef unsigned long long u64;
#define LAS __attribute__((address_space(3)))
#define GAS __attribute__((address_space(1)))
typedef LAS char* lds_ptr;
#if defined(__HIP_DEVICE_COMPILE__)
#define NOPK __attribute__((target("no-packed-fp32-ops")))
#else
#define NOPK
#endif
#define DI __device__ __forceinline__
#define MFMA(a, b, c) __builtin_amdgcn_mfma_f32_32x32x16_bf16(a, b, c, 0, 0, 0)
#define MFMA16(a, b, c) __builtin_amdgcn_mfma_f32_16x16x32_bf16(a, b, c, 0, 0, 0)
#define BF8(x) __builtin_bit_cast(bf16x8, x)
#define U4(x) __builtin_bit_cast(u32x4, x)

constexpr int D = 448;
constexpr float EPS = 1e-5f;
constexpr unsigned FK_A = 1, FK_O = 2, FK_H = 3, FK_G = 4;
constexpr unsigned WS_KV = 1u << 20;
constexpr unsigned WS_QF = WS_KV + 4194304;
constexpr unsigned WS_OF = WS_QF + 2097152;
constexpr unsigned WS_HF = WS_OF + 2097152;
constexpr unsigned WS_GF = WS_HF + 64 * 28 * 1024;
constexpr unsigned WS_WGU = WS_GF + 64 * 112 * 1024;
constexpr unsigned WS_WD = WS_WGU + 112 * 28 * 1024;
constexpr unsigned WS_HS = WS_WD + 7 * 112 * 2 * 1024;
constexpr unsigned WS_END = WS_HS + 64 * 28 * 256 + 1024;
constexpr unsigned OOB = 0x40000000u;
constexpr int LDS_BYTES = 163840;

DI float u2f(unsigned u) { return __builtin_bit_cast(float, u); }
DI unsigned f2u(float f) { return __builtin_bit_cast(unsigned, f); }
DI unsigned umin(unsigned a, unsigned b) { return a < b ? a : b; }
DI unsigned umax3(unsigned a, unsigned b, unsigned c) { const unsigned m = a > b ? a : b; return m > c ? m : c; }
DI bool wave_any(bool p) { return __builtin_amdgcn_ballot_w64(p) != 0; }
DI bool wave_all(bool p) { return __builtin_amdgcn_ballot_w64(!p) == 0; }
DI unsigned cvtpk(float lo, float hi) { return __builtin_bit_cast(unsigned, __builtin_convertvector((f32x2){lo, hi}, bf16x2)); }
DI float bflo(unsigned u) { return u2f(u << 16); }
DI float bfhi(unsigned u) { return u2f(u & 0xffff0000u); }
DI void glds(unsigned voff, const void* sbase, unsigned dst) {
    asm volatile("s_mov_b32 m0, %2\n\ts_nop 0\n\tglobal_load_lds_dwordx4 %0, %1" :: "v"(voff), "s"(sbase), "s"(dst) : "memory");
}
DI void glds_dev(unsigned voff, const void* sbase, unsigned dst) {
    asm volatile("s_mov_b32 m0, %2\n\ts_nop 0\n\tglobal_load_lds_dwordx4 %0, %1 sc1" :: "v"(voff), "s"(sbase), "s"(dst) : "memory");
}
DI void glds2_dev(unsigned voff, const void* sbase, unsigned dst) {
    asm volatile("s_mov_b32 m0, %2\n\ts_nop 0\n\tglobal_load_lds_dwordx4 %0, %1 sc1\n\tglobal_load_lds_dwordx4 %0, %1 offset:1024 sc1" :: "v"(voff), "s"(sbase), "s"(dst) : "memory");
}
DI void glds4_dev(unsigned voff, const void* sbase, unsigned dst) {
    asm volatile("s_mov_b32 m0, %2\n\ts_nop 0\n\tglobal_load_lds_dwordx4 %0, %1 sc1\n\tglobal_load_lds_dwordx4 %0, %1 offset:1024 sc1\n\t"
                 "global_load_lds_dwordx4 %0, %1 offset:2048 sc1\n\tglobal_load_lds_dwordx4 %0, %1 offset:3072 sc1" :: "v"(voff), "s"(sbase), "s"(dst) : "memory");
}
DI s16x4 trd(lds_ptr p) { return __builtin_bit_cast(s16x4, __builtin_amdgcn_ds_read_tr16_b64_v4i16((LAS v4i16_t*)p)); }
DI bf16x8 cat(s16x4 a, s16x4 b) { return __builtin_shufflevector(a, b, 0, 1, 2, 3, 4, 5, 6, 7); }
DI bf16x8 lds8(lds_ptr p) { return *(LAS bf16x8*)p; }
DI float swap_add(float v) { auto r = __builtin_amdgcn_permlane32_swap(f2u(v), f2u(v), false, false); return u2f(r[0]) + u2f(r[1]); }
DI float swap_max(float v) { auto r = __builtin_amdgcn_permlane32_swap(f2u(v), f2u(v), false, false); return __builtin_fmaxf(u2f(r[0]), u2f(r[1])); }
DI u32x4 pack8(const f32x16& a, int s) { return (u32x4){cvtpk(a[8 * s], a[8 * s + 1]), cvtpk(a[8 * s + 2], a[8 * s + 3]), cvtpk(a[8 * s + 4], a[8 * s + 5]), cvtpk(a[8 * s + 6], a[8 * s + 7])}; }
DI float dot2(unsigned a, unsigned b, float c) { return __builtin_amdgcn_fdot2_f32_bf16(__builtin_bit_cast(bf16x2, a), __builtin_bit_cast(bf16x2, b), c, false); }
DI void acc_stats2(unsigned u0, unsigned u1, f32x4& s) {
    const float a = bflo(u0), b = bfhi(u0), c = bflo(u1), d = bfhi(u1);
    s[0] += a; s[1] += b; s[2] = __builtin_fmaf(a, a, s[2]); s[3] = __builtin_fmaf(b, b, s[3]); s[0] += c; s[1] += d; s[2] = __builtin_fmaf(c, c, s[2]); s[3] = __builtin_fmaf(d, d, s[3]);
}
DI float sum8(float v) {
    v += u2f(__builtin_amdgcn_update_dpp(0u, f2u(v), 0xB1, 0xf, 0xf, true));
    v += u2f(__builtin_amdgcn_update_dpp(0u, f2u(v), 0x4E, 0xf, 0xf, true));
    v += u2f(__builtin_amdgcn_update_dpp(0u, f2u(v), 0x141, 0xf, 0xf, true));
    return v;
}
DI u32x4 shift8(u32x4 x, float c) {
    u32x4 o; float t0, t1, t2, t3, t4, t5, t6, t7;
    asm("v_dot2_f32_bf16 %4, %12, %16, %18\n\tv_dot2_f32_bf16 %5, %12, %17, %18\n\tv_dot2_f32_bf16 %6, %13, %16, %18\n\tv_dot2_f32_bf16 %7, %13, %17, %18\n\t"
        "v_dot2_f32_bf16 %8, %14, %16, %18\n\tv_dot2_f32_bf16 %9, %14, %17, %18\n\tv_dot2_f32_bf16 %10, %15, %16, %18\n\tv_dot2_f32_bf16 %11, %15, %17, %18\n\t"
        "v_cvt_pk_bf16_f32 %0, %4, %5\n\tv_cvt_pk_bf16_f32 %1, %6, %7\n\tv_cvt_pk_bf16_f32 %2, %8, %9\n\tv_cvt_pk_bf16_f32 %3, %10, %11"
        : "=&v"(o[0]), "=&v"(o[1]), "=&v"(o[2]), "=&v"(o[3]), "=&v"(t0), "=&v"(t1), "=&v"(t2), "=&v"(t3), "=&v"(t4), "=&v"(t5), "=&v"(t6), "=&v"(t7)
        : "v"(x[0]), "v"(x[1]), "v"(x[2]), "v"(x[3]), "s"(0x00003f80u), "s"(0x3f800000u), "v"(c));
    return o;
}
template <int N> struct IC { static constexpr int v = N; };
template <int B, int E, class F> DI void sfor(F&& f) { if constexpr (B < E) { f(IC<B>{}); sfor<B + 1, E>(f); } }
template <int N> DI void waitv() { asm volatile("s_waitcnt vmcnt(%0)" :: "i"(N) : "memory"); }
template <int N> DI void waitv_bar() { if (N == 0) __builtin_amdgcn_s_waitcnt(0x0F70); asm volatile("s_waitcnt vmcnt(%0) lgkmcnt(0)\n\ts_barrier" :: "i"(N) : "memory"); }
template <int N> DI void waitv_barx() { asm volatile("s_waitcnt vmcnt(%0)\n\ts_barrier" :: "i"(N) : "memory"); }
template <int N> DI void waitv_bar2() { asm volatile("s_waitcnt vmcnt(%0) lgkmcnt(2)\n\ts_barrier" :: "i"(N) : "memory"); }
#define BAR() asm volatile("s_waitcnt lgkmcnt(0)\n\ts_barrier" ::: "memory")
#define FENCE() asm volatile("" ::: "memory")
typedef __amdgpu_buffer_rsrc_t rsrc_t;
DI u32x4 ld16_dev(rsrc_t rs, unsigned voff, unsigned soff) { return U4(__builtin_amdgcn_raw_buffer_load_b128(rs, voff, soff, 16)); }
DI u32x2 ld8(rsrc_t rs, unsigned voff, unsigned soff) { return __builtin_bit_cast(u32x2, __builtin_amdgcn_raw_buffer_load_b64(rs, voff, soff, 0)); }
DI u32x2 ld8_dev(rsrc_t rs, unsigned voff, unsigned soff) { return __builtin_bit_cast(u32x2, __builtin_amdgcn_raw_buffer_load_b64(rs, voff, soff, 16)); }
DI void st16_dev(u32x4 v, rsrc_t rs, unsigned voff, unsigned soff) { __builtin_amdgcn_raw_buffer_store_b128(v, rs, voff + soff, 0, 16); }
DI void st8_dev(u32x2 v, rsrc_t rs, unsigned voff, unsigned soff) { __builtin_amdgcn_raw_buffer_store_b64(v, rs, voff, soff, 16); }
DI unsigned flag_at(unsigned kind, unsigned xcd, unsigned loc) { return (kind * 256 + loc * 8 + xcd) * 128; }
DI void set_flag(char* ws, unsigned fo, u64 tagv) {
    asm volatile("s_mov_b64 exec, 1\n\tglobal_store_dwordx2 %0, %1, %2 sc1\n\ts_mov_b64 exec, -1" :: "v"(fo), "v"(tagv), "s"(ws) : "memory");
}
DI u64 peek_flag(char* ws, unsigned fo) { return __hip_atomic_load((GAS u64*)(ws + fo), __ATOMIC_RELAXED, __HIP_MEMORY_SCOPE_AGENT); }
DI void peek_lds(char* ws, unsigned fo, unsigned dst) {
    asm volatile("s_mov_b32 m0, %2\n\ts_nop 0\n\tglobal_load_lds_dword %0, %1 sc1\n\ts_add_u32 m0, %2, 252\n\ts_nop 0\n\tglobal_load_lds_dword %0, %1 offset:4 sc1" :: "v"(fo), "s"(ws), "s"(dst) : "memory", "scc");
}
DI u64 peeked(lds_ptr p) { return (u64)*(LAS unsigned*)(p + 256) << 32 | *(LAS unsigned*)p; }
DI void poll_flag(char* ws, unsigned fo, u64 tag) {
#pragma unroll 1
    for (int spin = 0; spin < (1 << 21); ++spin) {
        if (wave_all(peek_flag(ws, fo) == tag)) break;
        __builtin_amdgcn_s_sleep(1);
    }
    FENCE();
}
#define SB() __builtin_amdgcn_sched_barrier(0)
#define PINS(a) asm volatile("" : "+s"(a))
#define PIN(a) asm volatile("" : "+v"(a))
#define PIN2(a, b) asm volatile("" : "+v"(a), "+v"(b))
DI lds_ptr opq(lds_ptr p) { unsigned u = (unsigned)(uintptr_t)p; asm volatile("" : "+v"(u)); return (lds_ptr)(uintptr_t)u; }

constexpr int GW = 7, XN = 5;
DI constexpr int d_prio(int n) { return n < 0 ? -1 : n < 27 ? 3 - n / 7 : n < 64 ? 3 - (n - 27) / 10 : n < 84 ? 3 - (n - 64) / 5 : n < 112 ? 3 - (n - 84) / 7 : 0; }
DI constexpr int wd_at(int n) {
    constexpr int t[28] = {32, 34, 37, 39, 42, 65, 69, 72, 75, 78, 81, 83, 44, 47, 49, 52, 54, 57, 59, 61, 63, 86, 88, 90, 92, 94, 97, 99};
    for (int i = 0; i < 28; ++i) if (t[i] == n) return i;
    return -1;
}
DI constexpr int phys(int t) { return ((t % 9) + 2) % 9 * 16384; }

__global__ NOPK __launch_bounds__(512, 2) void block_fwd(const char* x, const char* wqkv, const char* wo, const char* wg, const char* wu, const char* wd, char* out, char* ws) {
    extern __shared__ __attribute__((aligned(16))) char smem[];
    lds_ptr lds = (lds_ptr)smem;
    const int tid = __builtin_amdgcn_workitem_id_x(), lane = tid & 63, w = __builtin_amdgcn_readfirstlane(tid >> 6);
    const int bid = __builtin_amdgcn_workgroup_id_x(), xcd = bid & 7, loc = bid >> 3;
    const int r = lane & 31, hf = lane >> 5, q4 = (lane & 15) >> 2, g1 = (lane >> 4) & 1, p4 = lane & 3;
    const unsigned lds0 = (unsigned)(uintptr_t)smem;
    const unsigned l16 = lane * 16;
    const int sp3 = (loc + 8) >> 5;
    const int atb = (xcd >> 1) * 8 + (loc & 7) + sp3 * (xcd & 1) * 4, acg = (loc >> 3) + (1 - sp3) * (xcd & 1) * 4;
    const int row8 = lane >> 3;
    const int rot8 = ((loc & 7) << sp3) + sp3 * (xcd & 1), rot = rot8 == 7 ? 0 : rot8;
    if (loc < 28) {
        const unsigned xo = (unsigned)((atb * 64 + (tid >> 3)) * 896 + (((tid ^ (tid >> 4)) & 7) * 16));
        const unsigned xd0 = lds0 + 1024 + (w >> 2) * 28672 + (w & 3) * 1024;
        unsigned xd = xd0 + (7 - rot) * 4096;
        const unsigned w0 = (unsigned)((rot * 64 + (tid >> 3)) * 2688 + acg * 128 + (((tid & 7) ^ ((tid >> 2) & 4)) * 16));
        asm volatile("s_cmp_eq_u32 %0, %3\n\ts_cselect_b32 %0, %2, %0\n\ts_mov_b32 m0, %0\n\ts_add_u32 %0, %0, 0x1000\n\tglobal_load_lds_dwordx4 %1, %4\n\t"
#define XP(o) "s_cmp_eq_u32 %0, %3\n\ts_cselect_b32 %0, %2, %0\n\ts_sub_u32 m0, %0, " #o "\n\ts_add_u32 %0, %0, 0x1000\n\tglobal_load_lds_dwordx4 %1, %4 offset:" #o "\n\t"
                     XP(128) XP(256) XP(384) XP(512) XP(640) XP(768)
                     "s_add_u32 m0, %6, 8192-896\n\ts_barrier\n\tglobal_load_lds_dwordx4 %5, %7 offset:896\n\t"
                     "s_add_u32 m0, %6, 16384-1792\n\ts_nop 0\n\tglobal_load_lds_dwordx4 %5, %7 offset:1792\n\t"
                     "s_mov_b32 m0, %6\n\ts_nop 0\n\tglobal_load_lds_dwordx4 %5, %7"
                     : "+&s"(xd) : "v"(xo), "s"(xd0), "s"(xd0 + 7 * 4096), "s"(x), "v"(w0), "s"(lds0 + 58368 + w * 1024), "s"(wqkv) : "memory", "scc");
    }
    SB();
    const rsrc_t rws = __builtin_amdgcn_make_buffer_rsrc((void*)ws, 0, WS_END, 0x00020000);
    u64 epoch, tagv;
    const unsigned ownf = (lane & 7) ? flag_at(lane & 7, xcd, loc) : 0u;
    const unsigned ones = 0x3f803f80u;
    const bool ffn = loc < 28;
    const int l7 = loc / 7, m7 = loc % 7;
    const int tb = xcd * 4 + l7;
    const int tt = w & 1, ct = (w >> 1) & 1, kh = w >> 2;
    const unsigned swz = (unsigned)(((lane & 7) ^ (((row8 >> 1) & 1) << 2)) * 16);

    if (__builtin_expect(ffn, 1)) {
        constexpr int A_W = 58368, A_WS = 24576;
#define A_KC(c) (rot + (c) >= 7 ? rot + (c) - 7 : rot + (c))
        const unsigned wvo = (unsigned)(row8 * 2688) + swz;
        const char* wsrc = wqkv + acg * 128 + (size_t)w * 8 * 2688;
        const unsigned wdst = lds0 + A_W + w * 1024;
#define A_PIECE(c, i) glds(wvo, wsrc + A_KC(c) * (64 * 2688) + (i) * 896, wdst + ((c) & 3) * A_WS + (i) * 8192)
#define A_GROUP(c) do { A_PIECE(c, 1); A_PIECE(c, 2); A_PIECE(c, 0); } while (0)
        epoch = peek_flag(ws, ownf); FENCE();
        lds_ptr xb[4], wb1, wb1h;
#pragma unroll
        for (int j = 0; j < 4; ++j) xb[j] = lds + 1024 + tt * 28672 + (r >> 3) * 1024 + (r & 7) * 128 + (((2 * j + hf) ^ ((r >> 1) & 7)) * 16);
        wb1 = opq(lds + A_W + (kh ? 0 : 8192) + (8 * hf + q4) * 128 + ((ct ^ (q4 >> 1)) * 64) + g1 * 32 + p4 * 8);
        wb1h = opq(wb1 + 2 * A_WS);
        unsigned go1[2], go2[2], vo; int gi[2], vd;
#define A_DEST(p) do { unsigned hq_ = hf; PIN(hq_); const int G = acg * 8 + ct * 4 + 2 * (p) + hq_, hp = G / 7, i = G - hp * 7; \
            const unsigned hs = kh ? 131072u : 262144u, to = (kh ? WS_QF + (atb * 2 + tt) * 2048 : WS_KV + (atb * 2 + tt) * 4096) + r * 16; \
            go1[p] = (unsigned)(2 * hp + (i > 3)) * hs + (unsigned)(i > 3 ? i - 3 : i) * 512 + to; go2[p] = i == 3 ? (unsigned)(2 * hp + 1) * hs + to : OOB; gi[p] = i; \
            PIN2(go1[p], go2[p]); } while (0)
#define A_DESTV() do { unsigned rx_ = r; PIN(rx_); const int vcol = acg * 64 + ct * 32 + rx_, vh = vcol / 28; vd = vcol - vh * 28; \
            vo = WS_KV + (atb * 2 + tt) * 4096 + (unsigned)vh * 262144 + 2048 + hf * 512 + vd * 16; PIN(vo); } while (0)
        lds_ptr xw = opq(lds + 1024 + kh * 28672 + (w & 3) * 1024 + l16);
        waitv<4>();
        u32x4 xv[7];
#pragma unroll
        for (int c = 0; c < 7; ++c) xv[c] = *(LAS u32x4*)(xw + c * 4096);
        SB();
        f32x4 sm = {};
#pragma unroll
        for (int c = 0; c < 7; ++c)
#pragma unroll
            for (int j = 0; j < 4; ++j) sm[j] = dot2(xv[c][j], ones, sm[j]);
        float nm = sum8((sm[0] + sm[1]) + (sm[2] + sm[3])) * (-1.f / D);
        PIN(nm);
#pragma unroll
        for (int c = 0; c < 7; ++c) { xv[c] = shift8(xv[c], nm); *(LAS u32x4*)(xw + c * 4096) = xv[c]; }
        A_GROUP(1);
        SB();
        LAS float* rst = (LAS float*)lds;
        sm = (f32x4){};
#pragma unroll
        for (int c = 0; c < 7; ++c)
#pragma unroll
            for (int j = 0; j < 4; ++j) sm[j] = dot2(xv[c][j], xv[c][j], sm[j]);
        rst[w * 8 + row8] = __builtin_amdgcn_rsqf(sum8((sm[0] + sm[1]) + (sm[2] + sm[3])) * (1.f / D) + EPS);
        A_GROUP(2);
        bf16x8 fx[4], f1[4], f2[4];
#define A_WAIT(c) do { if ((c) == 0) waitv_bar<6>(); else if ((c) == 1) waitv_barx<6>(); else if ((c) < 6) waitv_barx<3>(); else waitv_barx<0>(); } while (0)
#define A_WP(c, ks) ((((c) & 2) ? wb1h : wb1) + ((c) & 1) * A_WS + (ks) * 2048)
        if (w < 4) {
            f32x16 ak = {}, av = {};
            f32x4 rv[4];
            float rs = 0.f;
#pragma unroll
            for (int n = 0; n < 30; ++n) {
                const int c = n >> 2, ks = n & 3, km = (n - 2) & 3;
                if (n < 28 && ks == 0) A_WAIT(c);
                if (n >= 2) { ak = MFMA(f1[km], fx[km], ak); PIN(ak); } SB();
                if (n < 28) { fx[ks] = lds8(xb[ks] + c * 4096); f1[ks] = cat(trd(A_WP(c, ks)), trd(A_WP(c, ks) + 512)); } SB();
                if (n >= 2) { av = MFMA(fx[km], f2[km], av); PIN(av); } SB();
                if (n < 28) f2[ks] = cat(trd(A_WP(c, ks) + 8192), trd(A_WP(c, ks) + 8192 + 512));
                if (c == 0 && ks < 3) A_PIECE(3, ks);
                if (c >= 2 && c < 5 && ks < 3) A_PIECE(c + 2, ks);
                if (n == 5) A_DEST(0);
                if (n == 6) A_DEST(1);
                if (n == 7) A_DESTV();
                if (n == 21) {
                    rs = rst[tt * 32 + r];
#pragma unroll
                    for (int g = 0; g < 4; ++g) rv[g] = *(LAS f32x4*)(rst + tt * 32 + 8 * g + 4 * hf);
                }
                if (n == 24) { PIN(epoch); tagv = epoch + 1; PIN(tagv); }
                SB();
            }
#pragma unroll
            for (int i = 0; i < 16; ++i) { ak[i] *= rs; av[i] *= rv[i >> 2][i & 3]; }
#pragma unroll
            for (int p = 0; p < 2; ++p) {
                const u32x4 f = pack8(ak, p);
                const auto s0 = __builtin_amdgcn_permlane32_swap(f[0], f[2], false, false), s1 = __builtin_amdgcn_permlane32_swap(f[1], f[3], false, false);
                const u32x4 c = {s0[0], s1[0], s0[1], s1[1]};
                st16_dev(c, rws, go1[p], 0); st16_dev(c, rws, go2[p], 0);
            }
            st16_dev(pack8(av, 0), rws, vo, 0); st16_dev(pack8(av, 1), rws, vo + 1024, 0);
        } else {
            f32x16 aq = {};
            __builtin_amdgcn_s_setprio(1);
#pragma unroll
            for (int n = 0; n < 30; ++n) {
                const int c = n >> 2, ks = n & 3, km = (n - 2) & 3;
                if (n < 28 && ks == 0) A_WAIT(c);
                if (n == 24) { PIN(epoch); tagv = epoch + 1; PIN(tagv); }
                if (n >= 2) { aq = MFMA(f1[km], fx[km], aq); PIN(aq); } SB();
                if (n < 28) { fx[ks] = lds8(xb[ks] + c * 4096); f1[ks] = cat(trd(A_WP(c, ks)), trd(A_WP(c, ks) + 512)); }
                if (c == 0 && ks < 3) A_PIECE(3, ks);
                if (c >= 2 && c < 5 && ks < 3) A_PIECE(c + 2, ks);
                if (n == 5) A_DEST(0);
                if (n == 6) A_DEST(1);
                if (n == 7) A_DESTV();
                if (n == 24) {
                    const unsigned pv = vd == 0 ? ones : 0u, po = vd < 4 ? vo + 448 : OOB;
                    st16_dev((u32x4){pv, pv, pv, pv}, rws, po, 0); st16_dev((u32x4){pv, pv, pv, pv}, rws, po + 1024, 0);
                }
                SB();
            }
            __builtin_amdgcn_s_setprio(0);
            const float QS = rst[tt * 32 + r] * (0.18898223650461362f * 1.4426950408889634f);
#pragma unroll
            for (int i = 0; i < 16; ++i) aq[i] *= QS;
#pragma unroll
            for (int p = 0; p < 2; ++p) {
                const u32x4 f = pack8(aq, p);
                const auto s0 = __builtin_amdgcn_permlane32_swap(f[0], f[2], false, false), s1 = __builtin_amdgcn_permlane32_swap(f[1], f[3], false, false);
                const bool sp = gi[p] == 3;
                st16_dev((u32x4){s0[0], s1[0], sp ? 0u : s0[1], sp ? 0u : s1[1]}, rws, go1[p], 0);
                st16_dev((u32x4){0u, 0u, s0[1], s1[1]}, rws, go2[p], 0);
            }
        }
    } else {
        epoch = peek_flag(ws, ownf);
        const int ri = xcd * 4 + loc - 28, nu = (w == 0 && ri < 16) ? 4 : 3;
        const unsigned vo = (unsigned)(row8 * 3584 + (lane & 7) * 16);
#pragma unroll 1
        for (int i = 0; i < nu; ++i) {
            const int u = i < 3 ? i * 256 + ri * 8 + w : 768 + ri;
            const size_t so = (size_t)(u / 28) * 16 * 3584 + (u % 28) * 128;
            const unsigned d_ = lds0 + w * 16384 + i * 4096;
            glds(vo, wg + so, d_); glds(vo, wg + so + 8 * 3584, d_ + 1024); glds(vo, wu + so, d_ + 2048); glds(vo, wu + so + 8 * 3584, d_ + 3072);
        }
#pragma unroll 1
        for (int i = 0; i < nu; ++i) {
            const int u = i < 3 ? i * 256 + ri * 8 + w : 768 + ri;
            waitv<8>();
            lds_ptr sg = lds + w * 16384 + i * 4096 + g1 * 2048 + (4 * hf + q4) * 128 + p4 * 8;
#pragma unroll
            for (int e = 0; e < 4; ++e) st16_dev(U4(cat(trd(sg + e * 32), trd(sg + e * 32 + 1024))), rws, l16, WS_WGU + (unsigned)(((u % 28) * 4 + e) * 28 + u / 28) * 1024);
            FENCE();
        }
        tagv = epoch + 1;
    }
    const int head = xcd * 2 + (loc & 1), rb = loc >> 1, qt = w & 3;
    const char* kvs = ws + WS_KV + (size_t)head * 262144 + w * 2048;
    const char* qs = ws + WS_QF + (size_t)(head * 64 + rb * 4 + qt) * 2048 + kh * 1024;
    unsigned kvd = lds0 + w * 2048, qd = lds0 + 147456 + qt * 2048 + kh * 1024;
    unsigned pf1, fl1 = flag_at(FK_A, xcd, loc);
    {
        const unsigned cg = (head * 28 + (lane & 1) * 27) / 64, q = lane >> 4, i = (lane >> 1) & 7;
        pf1 = cg == 3 ? flag_at(FK_A, 2 * q + (i >> 2), 24 + (i & 3)) : flag_at(FK_A, 2 * q + (cg >> 2), (cg & 3) * 8 + i);
    }
    PINS(kvs); PINS(qs); PINS(kvd); PINS(qd); PIN2(pf1, fl1);
    waitv_bar<0>();
    if (w == 0) set_flag(ws, fl1, tagv);
    const u64 tag = ((u64)(unsigned)__builtin_amdgcn_readfirstlane((unsigned)(tagv >> 32)) << 32 | (u64)(unsigned)__builtin_amdgcn_readfirstlane((unsigned)tagv));

    const int drb = (xcd >> 1) * 4 + l7, dfg = (xcd & 1) * 7 + m7;
    bf16x8 wf[28];
    const unsigned p16 = hf * 1024 + g1 * 512 + (lane & 15) * 16;
    u32x2 xa, xb;
    f32x4 csg = {}, csu = {};
    f32x16 acc = {};
    const u32x4 one4 = {ones, ones, ones, ones};
    const unsigned hi_ = (unsigned)((tb * 2 + tt) * 28 + m7 * 4 + ct * 2 + kh), ofo = (unsigned)(tt * 32768 + kh * 16384 + ct * 4096);
    unsigned ho = WS_HF + hi_ * 1024 + l16, so = hf ? OOB : WS_HS + hi_ * 256 + r * 8;
    const char* os = ws + WS_OF + (size_t)tb * 65536 + ofo;
    unsigned od = lds0 + 65536 + ofo;
    unsigned pf2 = flag_at(FK_O, kh * 4 + ct + (lane & 2), (tb & ~1) + (lane & 1));
    unsigned go = ffn ? WS_GF + (unsigned)(drb * 4 * 112 + dfg * 8 + w) * 1024 + g1 * 512 + (hf * 16 + (lane & 15)) * 16 : OOB;
    unsigned fo3 = flag_at(FK_H, xcd, loc);
    {
        lds_ptr ap = opq(lds + kh * 8192 + l16), apB = opq(lds + 65536 + kh * 8192 + l16), apC = opq(lds + 131072 + kh * 8192 + l16);
        const unsigned lv = kh * 8192 + (l16 ^ (kh * 256));
        lds_ptr vp = opq(lds + lv), vpB = opq(lds + 65536 + lv), vpC = opq(lds + 131072 + lv);
#define AP(off) ((off) < 65536 ? ap + (off) : (off) < 131072 ? apB + ((off) - 65536) : apC + ((off) - 131072))
#define VP(off) ((off) < 65536 ? vp + (off) : (off) < 131072 ? vpB + ((off) - 65536) : vpC + ((off) - 131072))
#define KV_ISSUE(t) glds2_dev(l16, kvs + (t) * 16384, kvd + phys(t))
        unsigned wgo = ffn ? WS_WGU + (unsigned)(dfg * 8 + w) * 28672 : OOB;
        const int ri = umin(xcd * 4 + loc - 28, 27);
        const char* wos = (ffn ? wo + m7 * 128 : wd + (ri % 7) * 128 + (size_t)(ri / 7) * 448 * 896) + (size_t)w * 56 * 896;
        unsigned wov = (unsigned)(row8 * 896) + swz;
        const rsrc_t rx = __builtin_amdgcn_make_buffer_rsrc((void*)x, 0, 2048 * 448 * 2, 0x00020000);
        unsigned xo_ = (unsigned)(((tb * 2 + tt) * 32 + r) * 896 + m7 * 128 + ct * 64 + kh * 32 + hf * 8);
        unsigned rto = flag_at(FK_A, w, 28 + (lane & 3));
        lds_ptr qp = opq(lds + 147456 + qt * 2048 + l16);
        PINS(wgo); PINS(wos); PIN2(wov, xo_); PIN(rto); PIN2(ho, so); PIN2(pf2, fo3); PIN(go);
        lds_ptr xw = opq(lds + 147456 + w * 2048 + l16), xr = opq(lds + 147456 + (w ^ 4) * 2048 + l16);
        lds_ptr lw = opq(lds + 58368 + w * 256 + lane * 4), lr = opq(lds + 58368 + qt * 256 + 128 + r * 4);
        lds_ptr bw = opq(lds + 60416 + w * 4), br = opq(lds + 60416 + (lane & 7) * 4);
        unsigned oo = WS_OF + (unsigned)((rb * 4 + qt) * 32 + head * 2 + kh) * 1024 + l16;
        unsigned fo2 = flag_at(FK_O, xcd, loc);
        PIN2(oo, fo2);
        if (w == 1) poll_flag(ws, pf1, tag);
        BAR();
        glds_dev(l16, qs, qd);
        KV_ISSUE(0); KV_ISSUE(1);
        asm volatile("s_barrier" ::: "memory");
        KV_ISSUE(2); KV_ISSUE(3);
        waitv_bar<4>();
        const bf16x8 q0 = lds8(qp), q1 = lds8(qp + 1024);
        f32x16 o = {}, sa = {}, sb;
        bf16x8 k0 = lds8(ap + phys(0)), k1 = lds8(ap + phys(0) + 1024), v0 = lds8(vp + phys(0) + 2048), v1 = {};
        u32x4 p0, p1 = {};
        sa = MFMA(k0, q0, sa); sa = MFMA(k1, q1, sa);
        k0 = lds8(ap + phys(0) + 4096); k1 = lds8(ap + phys(0) + 5120);
        PIN(sa); SB();
        u64 rtf = 0;
#define EXP4(S, o4) _Pragma("unroll") for (int i_ = o4; i_ < o4 + 4; ++i_) S[i_] = __builtin_amdgcn_exp2f(S[i_]);
#define WLD(s) wf[s] = BF8(ld16_dev(rws, p16 + ((s) & 1) * 256 + ((s) & 2) * 1024, wgo + ((s) >> 2) * 4096))
#define SIDE(t, g) do { \
            if ((t) == 0 && (g) == 0) KV_ISSUE(4); if ((t) == 0 && (g) == 2) KV_ISSUE(5); if ((t) == 1 && (g) == 0) KV_ISSUE(6); if ((t) == 1 && (g) == 2) KV_ISSUE(7); \
            if ((t) == 2 && (g) == 0) KV_ISSUE(8); \
            if ((t) == 1 && (g) == 1) peek_lds(ws, rto, lds0 + 155648 + w * 512); if ((t) == 2 && (g) == 2) { waitv<5>(); rtf = peeked(lds + 155648 + w * 512 + lane * 4); } \
            if ((t) == 3 && (g) < 3) KV_ISSUE(9 + (g)); \
            if (((t) == 2 && (g) == 1) || ((t) == 14 && (g) == 2)) st16_dev((u32x4){~0u, ~0u, ~0u, ~0u}, rws, go + ((t) == 2 ? 2 : 3) * 114688, 0);     \
            if ((t) == 7 && (g) == 0) KV_ISSUE(12); if ((t) == 7 && (g) == 2) KV_ISSUE(13); if ((t) == 8 && (g) == 0) KV_ISSUE(14); if ((t) == 8 && (g) == 2) KV_ISSUE(15); \
            if ((t) >= 4 && (t) <= 6) WLD(((t) - 4) * 4 + (g)); \
            if (((t) == 7 || (t) == 8) && ((g) & 1)) WLD(12 + ((t) - 7) * 2 + ((g) >> 1)); \
            if ((t) >= 9 && (t) <= 11) WLD(((t) - 5) * 4 + (g)); \
            if ((t) == 12 || ((t) == 13 && (g) < 3)) glds(wov, wos + (((t) - 12) * 4 + (g)) * 8 * 896, lds0 + (w * 7 + ((t) - 12) * 4 + (g)) * 1024); \
            if ((t) == 13 && (g) == 3) { xa = ld8(rx, xo_, 0); xb = ld8(rx, xo_, 16); } \
            if ((t) == 14 && (g) == 0) { if (tid < 32) *(LAS u32x4*)(lds + 57344 + tid * 16) = (u32x4){0, 0, 0, 0}; } \
        } while (0)
#define TOFF(i) (phys((i) >> 1) + ((i) & 1) * 4096)
#define TILE(i, SC, SN) do { \
            __builtin_amdgcn_s_setprio(((i) & 1) ? 1 : 3); \
            if ((i) < 31) { f32x16 z_ = {}; SN = MFMA(k0, q0, z_); PIN(SN); } if ((i) < 30) k0 = lds8(AP(TOFF((i) + 2))); SB(); \
            EXP4(SC, 0); PIN(SC); SB(); \
            o = MFMA(v1, BF8(p1), o); PIN(o); v1 = lds8(VP(TOFF(i) + 3072)); SIDE((i) >> 1, ((i) & 1) * 2); SB(); \
            EXP4(SC, 4); p0 = pack8(SC, 0); PIN(p0); SB(); \
            __builtin_amdgcn_s_setprio(((i) & 1) ? 0 : 2); \
            if ((i) < 31) { SN = MFMA(k1, q1, SN); PIN(SN); } if ((i) < 30) k1 = lds8(AP(TOFF((i) + 2) + 1024)); SB(); \
            EXP4(SC, 8); PIN(SC); SB(); \
            o = MFMA(v0, BF8(p0), o); PIN(o); if ((i) < 31) v0 = lds8(VP(TOFF((i) + 1) + 2048)); SIDE((i) >> 1, ((i) & 1) * 2 + 1); SB(); \
            EXP4(SC, 12); p1 = pack8(SC, 1); PIN(p1); SB(); } while (0)
#define STEP(t) do { TILE(2 * (t), sa, sb); TILE(2 * (t) + 1, sb, sa); } while (0)
        STEP(0);
        waitv_barx<4>();
        STEP(1); STEP(2);
        if (__builtin_expect(!wave_all(rtf == tag), 0)) poll_flag(ws, rto, tag);
        waitv_bar2<3>();
        STEP(3); STEP(4); STEP(5); STEP(6);
        waitv_bar2<12>();
        STEP(7); STEP(8); STEP(9); STEP(10);
        waitv_bar2<9>();
        STEP(11); STEP(12); STEP(13); STEP(14); STEP(15);
        o = MFMA(v1, BF8(p1), o);
        u32x4 ov;
        {
            *(LAS f32x4*)xw = (f32x4){o[8], o[9], o[10], o[11]}; *(LAS f32x4*)(xw + 1024) = (f32x4){o[12], o[13], o[14], o[15]};
            const float l = kh ? o[4] : o[12];
            *(LAS float*)lw = l;
#define AMX(a, b, c) __builtin_fmaxf(__builtin_fmaxf(__builtin_fabsf(a), __builtin_fabsf(b)), __builtin_fabsf(c))
            const float mx_ = AMX(AMX(AMX(o[0], o[1], o[2]), AMX(o[3], o[4], o[5]), AMX(o[6], o[7], o[8])), AMX(AMX(o[9], o[10], o[11]), AMX(o[12], o[13], o[14]), o[15]), 0.f);
            const bool ok = (mx_ < 0x1p100f) & ((hf ? l : 1.f) > 0x1p-40f);
            *(LAS unsigned*)bw = wave_all(ok) ? 0u : 1u;
            BAR();
            const f32x4 a = *(LAS f32x4*)xr, c = *(LAS f32x4*)(xr + 1024);
            const float inv = __builtin_amdgcn_rcpf(*(LAS float*)lr + *(LAS float*)(lr + 1024));
            const unsigned bad = *(LAS unsigned*)br;
            const float iz = (kh && hf) ? 0.f : inv;
            ov = (u32x4){cvtpk((o[0] + a[0]) * inv, (o[1] + a[1]) * inv), cvtpk((o[2] + a[2]) * inv, (o[3] + a[3]) * inv),
                         cvtpk((o[4] + c[0]) * iz, (o[5] + c[1]) * iz), cvtpk((o[6] + c[2]) * iz, (o[7] + c[3]) * iz)};
            if (__builtin_expect(wave_any(bad != 0), 0)) {
                f32x16 of = {};
                float m = -0x1p126f;
#pragma unroll 1
                for (int t = 0; t < 16; ++t) {
                    BAR();
                    glds2_dev(l16, kvs + t * 16384, lds0 + 65536 + w * 2048);
                    waitv_bar<0>();
#pragma unroll 1
                    for (int u = 0; u < 2; ++u) {
                        f32x16 s = {};
                        s = MFMA(lds8(apB + u * 4096), q0, s); s = MFMA(lds8(apB + u * 4096 + 1024), q1, s);
                        float mx = s[0];
#pragma unroll
                        for (int i = 1; i < 16; ++i) mx = __builtin_fmaxf(mx, s[i]);
                        mx = __builtin_fmaxf(swap_max(mx), m);
                        const float alpha = __builtin_amdgcn_exp2f(m - mx);
                        m = mx;
#pragma unroll
                        for (int i = 0; i < 16; ++i) { s[i] = __builtin_amdgcn_exp2f(s[i] - mx); of[i] *= alpha; }
                        of = MFMA(lds8(vpB + u * 4096 + 2048), BF8(pack8(s, 0)), of);
                        of = MFMA(lds8(vpB + u * 4096 + 3072), BF8(pack8(s, 1)), of);
                    }
                }
                *(LAS f32x4*)xw = (f32x4){of[8], of[9], of[10], of[11]}; *(LAS f32x4*)(xw + 1024) = (f32x4){of[12], of[13], of[14], of[15]};
                *(LAS float*)lw = hf ? (kh ? of[4] : of[12]) : m;
                BAR();
                const f32x4 a2 = *(LAS f32x4*)xr, c2 = *(LAS f32x4*)(xr + 1024);
                const float m0 = *(LAS float*)(lr - 128), m1 = *(LAS float*)(lr + 1024 - 128), mm = __builtin_fmaxf(m0, m1);
                const float e0 = __builtin_amdgcn_exp2f(m0 - mm), e1 = __builtin_amdgcn_exp2f(m1 - mm);
                const float iv = __builtin_amdgcn_rcpf(*(LAS float*)lr * e0 + *(LAS float*)(lr + 1024) * e1);
                const float im = (kh ? e1 : e0) * iv, io = (kh ? e0 : e1) * iv;
                const bool z = kh && hf;
                ov = (u32x4){cvtpk(of[0] * im + a2[0] * io, of[1] * im + a2[1] * io), cvtpk(of[2] * im + a2[2] * io, of[3] * im + a2[3] * io),
                             z ? 0u : cvtpk(of[4] * im + c2[0] * io, of[5] * im + c2[1] * io), z ? 0u : cvtpk(of[6] * im + c2[2] * io, of[7] * im + c2[3] * io)};
            }
        }
        st16_dev(ov, rws, oo, 0);
        FENCE();
        PIN2(xa, xb);
#pragma unroll
        for (int s = 27; s >= 0; --s) PIN(wf[s]);
#pragma unroll
        for (int s = 0; s < 16; s += 2) { csg = MFMA16(wf[s], BF8(one4), csg); csu = MFMA16(wf[s + 1], BF8(one4), csu); }
        acc[0] = bflo(xa[0]); acc[1] = bfhi(xa[0]); acc[2] = bflo(xa[1]); acc[3] = bfhi(xa[1]); acc[4] = bflo(xb[0]); acc[5] = bfhi(xb[0]); acc[6] = bflo(xb[1]); acc[7] = bfhi(xb[1]);
        PIN(acc); PIN2(ho, so); PIN2(pf2, fo3); PINS(os); PINS(od); PIN2(csg, csu);
        waitv_bar<0>();
        if (w == 0) set_flag(ws, fo2, tagv);
    }
    if (__builtin_expect(!ffn, 0)) {
        const int ri = xcd * 4 + loc - 28;
        if (ri < 28) {
#pragma unroll 1
            for (int u = w; u < 28; u += 8) {
                lds_ptr sd = lds + u * 2048 + (4 * hf + q4) * 128 + g1 * 32 + p4 * 8;
#pragma unroll
                for (int e = 0; e < 2; ++e) { lds_ptr p_ = sd + ((e ^ (q4 >> 1)) * 64);
                    st16_dev(U4(cat(trd(p_), trd(p_ + 1024))), rws, l16, WS_WD + (unsigned)(((ri % 7) * 112 + (ri / 7) * 28 + u) * 2 + e) * 1024); }
            }
        }
        waitv_bar<0>();
        if (w == 0) set_flag(ws, fo3, tagv);
        return;
    }

    const int erb = tb >> 1;
    const char* hs = ws + WS_HF + (size_t)drb * 114688 + w * 1024;
    const char* hss = ws + WS_HS + (size_t)((drb * 4 + (w & 3)) * 28 + kh * 14) * 256;
    unsigned pf3, fo4 = flag_at(FK_G, xcd, loc);
    {
        lds_ptr wb = lds + kh * (8 * 28 * 128) + (4 * hf + q4) * 128 + ((ct ^ (q4 >> 1)) * 64) + (g1 ^ kh) * 32 + p4 * 8;
        bf16x8 wr[16];
#pragma unroll
        for (int s = 0; s < 12; ++s) { const int k0 = ((s >> 1) * 28 + (s & 1) * 16) * 128; wr[s] = cat(trd(wb + k0), trd(wb + k0 + 1024)); }
        lds_ptr cw = opq(lds + 131072 + w * 2048 + l16), cr = opq(lds + 131072 + (w ^ 4) * 2048 + l16);
        lds_ptr ob = opq(lds + 65536 + tt * 32768 + kh * 16384 + l16);
        pf3 = flag_at(FK_H, drb >> 1, (drb & 1) * 14 + umin(lane & 15, 13));
        PIN2(pf3, fo4); PINS(hs); PINS(hss);
        poll_flag(ws, pf2, tag);
        glds4_dev(l16, os, od);
        asm volatile("s_barrier" ::: "memory");
        glds4_dev(l16, os + 8192, od + 8192);
        bf16x8 of[8];
        waitv_bar<4>();
#pragma unroll
        for (int s = 0; s < 8; ++s) of[s] = lds8(ob + s * 1024);
        SB();
#pragma unroll
        for (int s = 0; s < 8; ++s) {
            acc = MFMA(wr[s], of[s], acc); PIN(acc); SB();
            if (s >= 2 && s < 6) { const int k0 = (((s + 10) >> 1) * 28 + (s & 1) * 16) * 128; wr[s + 10] = cat(trd(wb + k0), trd(wb + k0 + 1024)); SB(); }
        }
        waitv_barx<0>();
#pragma unroll
        for (int s = 0; s < 8; ++s) of[s] = lds8(ob + (8 + s) * 1024);
        SB();
#pragma unroll
        for (int s = 0; s < 8; ++s) acc = MFMA(wr[8 + s], of[s], acc);
        *(LAS f32x4*)cw = (f32x4){acc[8], acc[9], acc[10], acc[11]}; *(LAS f32x4*)(cw + 1024) = (f32x4){acc[12], acc[13], acc[14], acc[15]};
        BAR();
        const f32x4 a = *(LAS f32x4*)cr + (f32x4){acc[0], acc[1], acc[2], acc[3]}, b = *(LAS f32x4*)(cr + 1024) + (f32x4){acc[4], acc[5], acc[6], acc[7]};
        const u32x4 hv = {cvtpk(a[0], a[1]), cvtpk(a[2], a[3]), cvtpk(b[0], b[1]), cvtpk(b[2], b[3])};
        st16_dev(hv, rws, ho, 0);
        FENCE(); SB();
        float s1 = 0.f, s2 = 0.f;
        { f32x4 q4_ = {}; acc_stats2(hv[0], hv[1], q4_); acc_stats2(hv[2], hv[3], q4_); s1 = swap_add(q4_[0] + q4_[1]); s2 = swap_add(q4_[2] + q4_[3]); }
        st8_dev((u32x2){f2u(s1), f2u(s2)}, rws, so, 0);
        FENCE(); PIN2(csg, csu); SB();
#pragma unroll
        for (int s = 16; s < 28; s += 2) { csg = MFMA16(wf[s], BF8(one4), csg); csu = MFMA16(wf[s + 1], BF8(one4), csu); }
        PIN2(csg, csu);
        waitv_bar<0>();
        if (w == 0) set_flag(ws, fo3, tagv);
        if (bid == 0 && w == 0) set_flag(ws, 0u, tagv);
    }

    const unsigned wdo = WS_WD + (unsigned)(m7 * 112 + w * 14) * 2048;
    bf16x8 wdf[28];
    bf16x8 gf[GW];
    const unsigned gxo = WS_GF + (unsigned)(erb * 4 * 112 + w * 14) * 1024 + (tb & 1) * 256;
    const unsigned pwd = hf * 2048 + g1 * 512 + (lane & 15) * 16;
#define G_LD(s) gf[(s) % GW] = BF8(ld16_dev(rws, p16, gxo + ((s) / 7) * 114688 + ((s) % 7) * 2048))
    u64 gfl = 0;
    const unsigned gfg = (w * 14 + umin(lane & 15, 13)) >> 3;
    const unsigned gfi = flag_at(FK_G + g1, (erb >> 2) * 2 + (gfg >= 7), (erb & 3) * 7 + (gfg >= 7 ? gfg - 7 : gfg));
    {
        const unsigned hd = lds0 + w * 1024;
        const char* wds = ws + wdo;
        lds_ptr hb = opq(lds + p16), hb2 = opq(lds + 57344 + p16);
        lds_ptr wl = opq(lds + 114688 + w * 5120 + l16), wl2 = opq(lds + w * 7168 + l16);
        LAS f32x2* stp = (LAS f32x2*)opq(lds + 159744 + (lane & 15) * 8);
        if (w == 1) poll_flag(ws, pf3, tag);
        BAR();
#define H_PIECE(j) glds_dev(l16, hs + (j) * 8192, hd + (j) * 8192)
#define WDO(s) (((s) >> 2) * 4096 + (((s) >> 1) & 1) * 1024 + ((s) & 1) * 256)
#define WDLD(s) wdf[s] = BF8(ld16_dev(rws, pwd, wdo + WDO(s)))
#define HB(n) (((n) < 56 ? hb + ((n) & ~1) * 1024 : hb2 + (((n) & ~1) - 56) * 1024) + ((n) & 1) * 256)
        H_PIECE(0);
        asm volatile("s_barrier" ::: "memory");
        H_PIECE(1); H_PIECE(2); H_PIECE(3);
        constexpr int PF = 4;
        bf16x8 bf[PF + 1];
        f32x4 ag[2][2], au[2][2];
        f32x2 sp[7], sv[4];
        float mean[2] = {0.f, 0.f}, r2[2] = {0.f, 0.f}, hp = 0.f;
        u32x4 hv;
        lds_ptr pk = opq(lds + 155648 + w * 512 + lane * 4);
        u64 pv = 0;
        const unsigned pkd = lds0 + 155648 + w * 512;
        const unsigned rdi = (w >> 1) * 7 + m7, rdo = flag_at(FK_H, rdi >> 2, 28 + (rdi & 3));
        waitv_barx<3>();
#pragma unroll
        for (int n = 0; n < PF; ++n) bf[n] = lds8(HB(n));
        SB();
        sfor<0, 125>([&](auto N_) __attribute__((always_inline)) {
            constexpr int n = decltype(N_)::v, p = n / 28, s = n % 28;
            if constexpr (n == 4) waitv_barx<3>();
            if constexpr (n == 12) waitv_barx<8>();
            if constexpr (n == 28) { waitv_barx<3>(); }
            if constexpr (n == 64) { waitv_barx<10>(); if (w == 0) set_flag(ws, fo4, tagv); }
            if constexpr (n == 85) { waitv_barx<8>(); if (w == 0) set_flag(ws, fo4 + 32768, tagv); }
            if constexpr (n == 32) { if (__builtin_expect(!wave_all(pv == tag), 0)) poll_flag(ws, rdo, tag); FENCE(); }
            if constexpr (d_prio(n) != d_prio(n - 1)) __builtin_amdgcn_s_setprio(d_prio(n));
            if constexpr (n < 112) {
                if constexpr (s < 2) { ag[p & 1][s] = (f32x4){}; au[p & 1][s] = (f32x4){}; }
                ag[p & 1][s & 1] = MFMA16(wf[s & ~1], bf[n % (PF + 1)], ag[p & 1][s & 1]); au[p & 1][s & 1] = MFMA16(wf[s | 1], bf[n % (PF + 1)], au[p & 1][s & 1]);
                PIN2(ag[p & 1][s & 1], au[p & 1][s & 1]);
            }
            SB();
            if constexpr (n + PF < 112) bf[(n + PF) % (PF + 1)] = lds8(HB(n + PF));
            if constexpr (n % 3 == 2 && n < 30) H_PIECE(4 + n / 3);
            if constexpr (n == 4) glds4_dev(l16, hss, lds0 + 114688 + w * 5120);
            if constexpr (n == 16) { waitv<4>();
#pragma unroll
                for (int j = 0; j < 7; ++j) sp[j] = *(LAS f32x2*)(lds + 114688 + w * 5120 + (hf * 7 + j) * 256 + r * 8);
            }
            if constexpr (n == 20) { const f32x2 t = ((sp[0] + sp[1]) + (sp[2] + sp[3])) + ((sp[4] + sp[5]) + sp[6]); stp[w * 32 + g1 * 16] = (f32x2){swap_add(t[0]), swap_add(t[1])}; }
            if constexpr (wd_at(n) >= 0) { constexpr int i = wd_at(n);
                if constexpr (i < 5) glds_dev(pwd, wds + WDO(i), lds0 + 114688 + w * 5120 + i * 1024);
                else if constexpr (i < 12) glds_dev(pwd, wds + WDO(i), lds0 + w * 7168 + (i - 5) * 1024);
                else WDLD(i); }
            if constexpr (n == 84) peek_lds(ws, gfi - g1 * 32768, pkd);
            if constexpr (n == 95) { waitv<5>(); pv = peeked(pk); }
            if constexpr (n == 99) { if (__builtin_expect(!wave_all(pv == tag), 0)) poll_flag(ws, gfi - g1 * 32768, tag); FENCE(); }
            if constexpr (n >= 101 && n < 101 + 2 * XN && n % 2 == 1) G_LD((n - 101) / 2);
            if constexpr (n == 106) gfl = peek_flag(ws, gfi);
            if constexpr (n == 28) pv = peeked(pk);
            if constexpr (n == 18) peek_lds(ws, rdo, pkd);
            if constexpr (p >= 1) {
                constexpr int e = (p - 1) & 1, tp = n == 100 ? 3 : p - 1;
                if constexpr ((s == 0 && p < 4) || n == 100) { sv[0] = stp[tp * 32]; sv[1] = stp[(tp + 4) * 32]; sv[2] = stp[tp * 32 + 16]; sv[3] = stp[(tp + 4) * 32 + 16]; }
                if constexpr (((s == 2 || s == 3) && p < 4) || n == 104 || n == 105) { constexpr int k = n & 1; const f32x2 t = sv[2 * k] + sv[2 * k + 1];
                    mean[k] = t[0] * (1.f / D); r2[k] = __builtin_amdgcn_rcpf(__builtin_fmaxf(t[1] * (1.f / D) - mean[k] * mean[k], 0.f) + EPS); PIN2(mean[k], r2[k]); }
                if constexpr (s >= 4 && s < 12) { constexpr int i = s - 4, k = i >> 2;
                    const float h0 = __builtin_fmaxf(ag[e][k][i & 3] - mean[k] * csg[i & 3], 0.f) * r2[k] * (au[e][k][i & 3] - mean[k] * csu[i & 3]);
                    if constexpr (i & 1) { hv[i >> 1] = p >= 3 ? umin(cvtpk(hp, h0), ~1u) : cvtpk(hp, h0); PIN(hv); } else { hp = h0; PIN(hp); } }
                if constexpr (s == 12) {
                    const auto s0 = __builtin_amdgcn_permlane32_swap(hv[0], hv[2], false, false), s1 = __builtin_amdgcn_permlane32_swap(hv[1], hv[3], false, false);
                    st16_dev((u32x4){s0[0], s1[0], s0[1], s1[1]}, rws, go + (p - 1) * 114688, 0); FENCE();
                }
            }
            SB();
        });
#pragma unroll
        for (int s = XN; s < 7; ++s) { G_LD(s); SB(); }
        FENCE();
#pragma unroll
        for (int s = 0; s < 12; ++s) wdf[s] = lds8(s < 5 ? wl + s * 1024 : wl2 + (s - 5) * 1024);
    }

    {
        const int etok = tid >> 4, eblk = tid & 15;
        const int et = erb * 4 + (etok >> 4), er = (tb & 1) * 16 + (etok & 15);
        const unsigned hro = (unsigned)((((eblk >> 2) & 3) * 64 + (eblk & 1) * 32 + er) * 16 + ((eblk >> 1) & 1) * 8) + WS_HF + (unsigned)(et * 28 + m7 * 4) * 1024;
        u32x2 hres[2];
        GAS u64* op = (GAS u64*)(out + (size_t)(et * 32 + er) * 896 + m7 * 128 + eblk * 8);
        lds_ptr rp = opq(lds + w * 8448 + (lane >> 4) * 528 + (lane & 15) * 16), rq = opq(lds + eblk * 528 + etok * 16);
        lds_ptr rp2 = opq(rp + 67584), rq2 = opq(rq + 67584);
        f32x4 c[4];
#define G_STEP(s) do { _Pragma("unroll") for (int j = 0; j < 4; ++j) { if ((s) % 7 == 0) c[j] = (f32x4){}; c[j] = MFMA16(wdf[4 * ((s) % 7) + j], gf[(s) % GW], c[j]); } PIN2(c[0], c[1]); PIN2(c[2], c[3]); } while (0)
#define G_PUT(q) do { _Pragma("unroll") for (int j = 0; j < 4; ++j) *(LAS f32x4*)(((q) < 2 ? rp : rp2) + j * 2112 + ((q) & 1) * 256) = c[j]; } while (0)
#define G_OUT(hh, RQ) do { f32x4 pa[8]; \
            _Pragma("unroll") for (int ww = 0; ww < 8; ++ww) pa[ww] = *(LAS f32x4*)(RQ + ww * 8448); SB(); \
            const f32x4 a = ((pa[0] + pa[1]) + (pa[2] + pa[3])) + ((pa[4] + pa[5]) + (pa[6] + pa[7])); \
            const u32x2 ov = {cvtpk(a[0] + bflo(hres[hh][0]), a[1] + bfhi(hres[hh][0])), cvtpk(a[2] + bflo(hres[hh][1]), a[3] + bfhi(hres[hh][1]))}; \
            __hip_atomic_store(op + (hh) * (64 * 896 / 8), __builtin_bit_cast(u64, ov), __ATOMIC_RELAXED, __HIP_MEMORY_SCOPE_AGENT); } while (0)
        unsigned seen = 0;
#pragma unroll
        for (int s = 0; s < 28; ++s) {
            if (s == 5) BAR();
            if (s == 7 || s == 14 || s == 21) G_PUT(s / 7 - 1);
            if (s == 21) { waitv<7>(); PIN2(hres[0], hres[1]); BAR(); G_OUT(0, rq); FENCE(); }
            SB();
            G_STEP(s);
            if (s >= 14) { const u32x4 u = U4(gf[s % GW]); seen = umax3(umax3(seen, u[0], u[1]), u[2], u[3]); PIN(seen); }
            SB();
            if (s == 0) { if (__builtin_expect(!wave_all(gfl == tag), 0)) poll_flag(ws, gfi, tag); FENCE(); }
            if (s + GW < 28) G_LD(s + GW);
            if (s == 1) { hres[0] = ld8_dev(rws, hro, 0); hres[1] = ld8_dev(rws, hro, 57344); FENCE(); }
        }
        if (w == 0) { set_flag(ws, fo4 + 65536, tagv); set_flag(ws, fo4 + 98304, tagv); }
        if (__builtin_expect(wave_any(seen == ~0u), 0)) {
            poll_flag(ws, gfi + 65536, tag);
#pragma unroll
            for (int s = 14; s < 14 + GW; ++s) G_LD(s);
#pragma unroll
            for (int s = 14; s < 28; ++s) { if (s == 21) G_PUT(2); G_STEP(s); SB(); if (s + GW < 28) G_LD(s + GW); SB(); }
        }
        G_PUT(3);
        BAR();
        G_OUT(1, rq2);
    }
}

extern "C" void kernel_launch(void* const* d_in, const int* in_sizes, int n_in, void* d_out, int out_size, void* d_ws, size_t ws_size, hipStream_t stream) {
    (void)hipFuncSetAttribute((const void*)block_fwd, hipFuncAttributeMaxDynamicSharedMemorySize, LDS_BYTES);
    block_fwd<<<256, 512, LDS_BYTES, stream>>>((const char*)d_in[0], (const char*)d_in[1], (const char*)d_in[2], (const char*)d_in[4], (const char*)d_in[5], (const char*)d_in[6], (char*)d_out, (char*)d_ws);
}
```

```cpp
#include <hip/hip_runtime.h>
#include <hip/hip_bf16.h>
#include <stdint.h>

typedef __attribute__((ext_vector_type(8))) short bf16x8;
typedef __attribute__((ext_vector_type(4))) short s16x4;
typedef __attribute__((ext_vector_type(2))) __bf16 bf16x2;
typedef __attribute__((ext_vector_type(16))) float f32x16;
typedef __attribute__((ext_vector_type(4))) float f32x4;
typedef __attribute__((ext_vector_type(2))) float f32x2;
typedef __attribute__((ext_vector_type(4))) unsigned u32x4;
typedef __attribute__((ext_vector_type(2))) unsigned u32x2;
typedef short v4i16_t __attribute__((ext_vector_type(4)));
typedef unsigned long long u64;
#define LAS __attribute__((address_space(3)))
#define GAS __attribute__((address_space(1)))
typedef LAS char* lds_ptr;
#if defined(__HIP_DEVICE_COMPILE__)
#define NOPK __attribute__((target("no-packed-fp32-ops")))
#else
#define NOPK
#endif
#define DI __device__ __forceinline__
#define MFMA(a, b, c) __builtin_amdgcn_mfma_f32_32x32x16_bf16(a, b, c, 0, 0, 0)
#define MFMA16(a, b, c) __builtin_amdgcn_mfma_f32_16x16x32_bf16(a, b, c, 0, 0, 0)
#define BF8(x) __builtin_bit_cast(bf16x8, x)
#define U4(x) __builtin_bit_cast(u32x4, x)

constexpr int D = 448;
constexpr float EPS = 1e-5f;
constexpr unsigned FK_A = 1, FK_O = 2, FK_H = 3, FK_G = 4;
constexpr unsigned WS_KV = 1u << 20;
constexpr unsigned WS_QF = WS_KV + 4194304;
constexpr unsigned WS_OF = WS_QF + 2097152;
constexpr unsigned WS_HF = WS_OF + 2097152;
constexpr unsigned WS_GF = WS_HF + 64 * 28 * 1024;
constexpr unsigned WS_WGU = WS_GF + 64 * 112 * 1024;
constexpr unsigned WS_WD = WS_WGU + 112 * 28 * 1024;
constexpr unsigned WS_HS = WS_WD + 7 * 112 * 2 * 1024;
constexpr unsigned WS_END = WS_HS + 64 * 28 * 256 + 1024;
constexpr unsigned OOB = 0x40000000u;
constexpr int LDS_BYTES = 163840;

DI float u2f(unsigned u) { return __builtin_bit_cast(float, u); }
DI unsigned f2u(float f) { return __builtin_bit_cast(unsigned, f); }
DI unsigned umin(unsigned a, unsigned b) { return a < b ? a : b; }
DI unsigned umax3(unsigned a, unsigned b, unsigned c) { const unsigned m = a > b ? a : b; return m > c ? m : c; }
DI bool wave_any(bool p) { return __builtin_amdgcn_ballot_w64(p) != 0; }
DI bool wave_all(bool p) { return __builtin_amdgcn_ballot_w64(!p) == 0; }
DI unsigned cvtpk(float lo, float hi) { return __builtin_bit_cast(unsigned, __builtin_convertvector((f32x2){lo, hi}, bf16x2)); }
DI float bflo(unsigned u) { return u2f(u << 16); }
DI float bfhi(unsigned u) { return u2f(u & 0xffff0000u); }
DI void glds(unsigned voff, const void* sbase, unsigned dst) {
    asm volatile("s_mov_b32 m0, %2\n\ts_nop 0\n\tglobal_load_lds_dwordx4 %0, %1" :: "v"(voff), "s"(sbase), "s"(dst) : "memory");
}
DI void glds_dev(unsigned voff, const void* sbase, unsigned dst) {
    asm volatile("s_mov_b32 m0, %2\n\ts_nop 0\n\tglobal_load_lds_dwordx4 %0, %1 sc1" :: "v"(voff), "s"(sbase), "s"(dst) : "memory");
}
DI void glds2_dev(unsigned voff, const void* sbase, unsigned dst) {
    asm volatile("s_mov_b32 m0, %2\n\ts_nop 0\n\tglobal_load_lds_dwordx4 %0, %1 sc1\n\tglobal_load_lds_dwordx4 %0, %1 offset:1024 sc1" :: "v"(voff), "s"(sbase), "s"(dst) : "memory");
}
DI void glds4_dev(unsigned voff, const void* sbase, unsigned dst) {
    asm volatile("s_mov_b32 m0, %2\n\ts_nop 0\n\tglobal_load_lds_dwordx4 %0, %1 sc1\n\tglobal_load_lds_dwordx4 %0, %1 offset:1024 sc1\n\t"
                 "global_load_lds_dwordx4 %0, %1 offset:2048 sc1\n\tglobal_load_lds_dwordx4 %0, %1 offset:3072 sc1" :: "v"(voff), "s"(sbase), "s"(dst) : "memory");
}
DI s16x4 trd(lds_ptr p) { return __builtin_bit_cast(s16x4, __builtin_amdgcn_ds_read_tr16_b64_v4i16((LAS v4i16_t*)p)); }
DI bf16x8 cat(s16x4 a, s16x4 b) { return __builtin_shufflevector(a, b, 0, 1, 2, 3, 4, 5, 6, 7); }
DI bf16x8 lds8(lds_ptr p) { return *(LAS bf16x8*)p; }
DI float swap_add(float v) { auto r = __builtin_amdgcn_permlane32_swap(f2u(v), f2u(v), false, false); return u2f(r[0]) + u2f(r[1]); }
DI float swap_max(float v) { auto r = __builtin_amdgcn_permlane32_swap(f2u(v), f2u(v), false, false); return __builtin_fmaxf(u2f(r[0]), u2f(r[1])); }
DI u32x4 pack8(const f32x16& a, int s) { return (u32x4){cvtpk(a[8 * s], a[8 * s + 1]), cvtpk(a[8 * s + 2], a[8 * s + 3]), cvtpk(a[8 * s + 4], a[8 * s + 5]), cvtpk(a[8 * s + 6], a[8 * s + 7])}; }
DI float dot2(unsigned a, unsigned b, float c) { return __builtin_amdgcn_fdot2_f32_bf16(__builtin_bit_cast(bf16x2, a), __builtin_bit_cast(bf16x2, b), c, false); }
DI void acc_stats2(unsigned u0, unsigned u1, f32x4& s) {
    const float a = bflo(u0), b = bfhi(u0), c = bflo(u1), d = bfhi(u1);
    s[0] += a; s[1] += b; s[2] = __builtin_fmaf(a, a, s[2]); s[3] = __builtin_fmaf(b, b, s[3]); s[0] += c; s[1] += d; s[2] = __builtin_fmaf(c, c, s[2]); s[3] = __builtin_fmaf(d, d, s[3]);
}
DI float sum8(float v) {
    v += u2f(__builtin_amdgcn_update_dpp(0u, f2u(v), 0xB1, 0xf, 0xf, true));
    v += u2f(__builtin_amdgcn_update_dpp(0u, f2u(v), 0x4E, 0xf, 0xf, true));
    v += u2f(__builtin_amdgcn_update_dpp(0u, f2u(v), 0x141, 0xf, 0xf, true));
    return v;
}
DI u32x4 shift8(u32x4 x, float c) {
    u32x4 o; float t0, t1, t2, t3, t4, t5, t6, t7;
    asm("v_dot2_f32_bf16 %4, %12, %16, %18\n\tv_dot2_f32_bf16 %5, %12, %17, %18\n\tv_dot2_f32_bf16 %6, %13, %16, %18\n\tv_dot2_f32_bf16 %7, %13, %17, %18\n\t"
        "v_dot2_f32_bf16 %8, %14, %16, %18\n\tv_dot2_f32_bf16 %9, %14, %17, %18\n\tv_dot2_f32_bf16 %10, %15, %16, %18\n\tv_dot2_f32_bf16 %11, %15, %17, %18\n\t"
        "v_cvt_pk_bf16_f32 %0, %4, %5\n\tv_cvt_pk_bf16_f32 %1, %6, %7\n\tv_cvt_pk_bf16_f32 %2, %8, %9\n\tv_cvt_pk_bf16_f32 %3, %10, %11"
        : "=&v"(o[0]), "=&v"(o[1]), "=&v"(o[2]), "=&v"(o[3]), "=&v"(t0), "=&v"(t1), "=&v"(t2), "=&v"(t3), "=&v"(t4), "=&v"(t5), "=&v"(t6), "=&v"(t7)
        : "v"(x[0]), "v"(x[1]), "v"(x[2]), "v"(x[3]), "s"(0x00003f80u), "s"(0x3f800000u), "v"(c));
    return o;
}
template <int N> struct IC { static constexpr int v = N; };
template <int B, int E, class F> DI void sfor(F&& f) { if constexpr (B < E) { f(IC<B>{}); sfor<B + 1, E>(f); } }
template <int N> DI void waitv() { asm volatile("s_waitcnt vmcnt(%0)" :: "i"(N) : "memory"); }
template <int N> DI void waitv_bar() { if (N == 0) __builtin_amdgcn_s_waitcnt(0x0F70); asm volatile("s_waitcnt vmcnt(%0) lgkmcnt(0)\n\ts_barrier" :: "i"(N) : "memory"); }
template <int N> DI void waitv_barx() { asm volatile("s_waitcnt vmcnt(%0)\n\ts_barrier" :: "i"(N) : "memory"); }
template <int N> DI void waitv_bar2() { asm volatile("s_waitcnt vmcnt(%0) lgkmcnt(2)\n\ts_barrier" :: "i"(N) : "memory"); }
#define BAR() asm volatile("s_waitcnt lgkmcnt(0)\n\ts_barrier" ::: "memory")
#define FENCE() asm volatile("" ::: "memory")
typedef __amdgpu_buffer_rsrc_t rsrc_t;
DI u32x4 ld16_dev(rsrc_t rs, unsigned voff, unsigned soff) { return U4(__builtin_amdgcn_raw_buffer_load_b128(rs, voff, soff, 16)); }
DI u32x2 ld8(rsrc_t rs, unsigned voff, unsigned soff) { return __builtin_bit_cast(u32x2, __builtin_amdgcn_raw_buffer_load_b64(rs, voff, soff, 0)); }
DI u32x2 ld8_dev(rsrc_t rs, unsigned voff, unsigned soff) { return __builtin_bit_cast(u32x2, __builtin_amdgcn_raw_buffer_load_b64(rs, voff, soff, 16)); }
DI void st16_dev(u32x4 v, rsrc_t rs, unsigned voff, unsigned soff) { __builtin_amdgcn_raw_buffer_store_b128(v, rs, voff + soff, 0, 16); }
DI void st8_dev(u32x2 v, rsrc_t rs, unsigned voff, unsigned soff) { __builtin_amdgcn_raw_buffer_store_b64(v, rs, voff, soff, 16); }
DI unsigned flag_at(unsigned kind, unsigned xcd, unsigned loc) { return (kind * 256 + loc * 8 + xcd) * 128; }
DI void set_flag(char* ws, unsigned fo, u64 tagv) {
    asm volatile("s_mov_b64 exec, 1\n\tglobal_store_dwordx2 %0, %1, %2 sc1\n\ts_mov_b64 exec, -1" :: "v"(fo), "v"(tagv), "s"(ws) : "memory");
}
DI u64 peek_flag(char* ws, unsigned fo) { return __hip_atomic_load((GAS u64*)(ws + fo), __ATOMIC_RELAXED, __HIP_MEMORY_SCOPE_AGENT); }
DI void peek_lds(char* ws, unsigned fo, unsigned dst) {
    asm volatile("s_mov_b32 m0, %2\n\ts_nop 0\n\tglobal_load_lds_dword %0, %1 sc1\n\ts_add_u32 m0, %2, 252\n\ts_nop 0\n\tglobal_load_lds_dword %0, %1 offset:4 sc1" :: "v"(fo), "s"(ws), "s"(dst) : "memory", "scc");
}
DI u64 peeked(lds_ptr p) { return (u64)*(LAS unsigned*)(p + 256) << 32 | *(LAS unsigned*)p; }
DI void poll_flag(char* ws, unsigned fo, u64 tag) {
#pragma unroll 1
    for (int spin = 0; spin < (1 << 21); ++spin) {
        if (wave_all(peek_flag(ws, fo) == tag)) break;
        __builtin_amdgcn_s_sleep(1);
    }
    FENCE();
}
#define SB() __builtin_amdgcn_sched_barrier(0)
#define PINS(a) asm volatile("" : "+s"(a))
#define PIN(a) asm volatile("" : "+v"(a))
#define PIN2(a, b) asm volatile("" : "+v"(a), "+v"(b))
DI lds_ptr opq(lds_ptr p) { unsigned u = (unsigned)(uintptr_t)p; asm volatile("" : "+v"(u)); return (lds_ptr)(uintptr_t)u; }
#define TS(i) do {} while (0)

constexpr int GW = 7, XN = 5;
DI constexpr int d_prio(int n) { return n < 0 ? -1 : n < 27 ? 3 - n / 7 : n < 64 ? 3 - (n - 27) / 10 : n < 84 ? 3 - (n - 64) / 5 : n < 112 ? 3 - (n - 84) / 7 : 0; }
DI constexpr int wd_at(int n) {
    constexpr int t[28] = {32, 34, 37, 39, 42, 65, 69, 72, 75, 78, 81, 83, 44, 47, 49, 52, 54, 57, 59, 61, 63, 86, 88, 90, 92, 94, 97, 99};
    for (int i = 0; i < 28; ++i) if (t[i] == n) return i;
    return -1;
}
DI constexpr int phys(int t) { return ((t % 9) + 2) % 9 * 16384; }

__global__ NOPK __launch_bounds__(512, 2) void block_fwd(const char* x, const char* wqkv, const char* wo, const char* wg, const char* wu, const char* wd, char* out, char* ws) {
    extern __shared__ __attribute__((aligned(16))) char smem[];
    lds_ptr lds = (lds_ptr)smem;
    const int tid = __builtin_amdgcn_workitem_id_x(), lane = tid & 63, w = __builtin_amdgcn_readfirstlane(tid >> 6);
    const int bid = __builtin_amdgcn_workgroup_id_x(), xcd = bid & 7, loc = bid >> 3;
    const int r = lane & 31, hf = lane >> 5, q4 = (lane & 15) >> 2, g1 = (lane >> 4) & 1, p4 = lane & 3;
    const unsigned lds0 = (unsigned)(uintptr_t)smem;
    const unsigned l16 = lane * 16;
    TS(0);
    const int sp3 = (loc + 8) >> 5;
    const int atb = (xcd >> 1) * 8 + (loc & 7) + sp3 * (xcd & 1) * 4, acg = (loc >> 3) + (1 - sp3) * (xcd & 1) * 4;
    const int row8 = lane >> 3;
    const int rot8 = ((loc & 7) << sp3) + sp3 * (xcd & 1), rot = rot8 == 7 ? 0 : rot8;
    const unsigned ownf = (lane & 7) ? flag_at(lane & 7, xcd, loc) : 0u;
    u64 epoch = peek_flag(ws, ownf);
    FENCE();
    if (loc < 28) {
        const unsigned xo = (unsigned)((atb * 64 + (tid >> 3)) * 896 + (((tid ^ (tid >> 4)) & 7) * 16));
        const unsigned xd0 = lds0 + 1024 + (w >> 2) * 28672 + (w & 3) * 1024;
        unsigned xd = xd0 + (7 - rot) * 4096;
        const unsigned w0 = (unsigned)((rot * 64 + (tid >> 3)) * 2688 + acg * 128 + (((tid & 7) ^ ((tid >> 2) & 4)) * 16));
        asm volatile("s_cmp_eq_u32 %0, %3\n\ts_cselect_b32 %0, %2, %0\n\ts_mov_b32 m0, %0\n\ts_add_u32 %0, %0, 0x1000\n\tglobal_load_lds_dwordx4 %1, %4\n\t"
#define XP(o) "s_cmp_eq_u32 %0, %3\n\ts_cselect_b32 %0, %2, %0\n\ts_sub_u32 m0, %0, " #o "\n\ts_add_u32 %0, %0, 0x1000\n\tglobal_load_lds_dwordx4 %1, %4 offset:" #o "\n\t"
                     XP(128) XP(256) XP(384) XP(512) XP(640) XP(768)
                     "s_add_u32 m0, %6, 8192-896\n\ts_barrier\n\tglobal_load_lds_dwordx4 %5, %7 offset:896\n\t"
                     "s_add_u32 m0, %6, 16384-1792\n\ts_nop 0\n\tglobal_load_lds_dwordx4 %5, %7 offset:1792\n\t"
                     "s_mov_b32 m0, %6\n\ts_nop 0\n\tglobal_load_lds_dwordx4 %5, %7"
                     : "+&s"(xd) : "v"(xo), "s"(xd0), "s"(xd0 + 7 * 4096), "s"(x), "v"(w0), "s"(lds0 + 58368 + w * 1024), "s"(wqkv) : "memory", "scc");
    }
    SB();
    const rsrc_t rws = __builtin_amdgcn_make_buffer_rsrc((void*)ws, 0, WS_END, 0x00020000);
    u64 tagv;
    const unsigned ones = 0x3f803f80u;
    const bool ffn = loc < 28;
    const int l7 = loc / 7, m7 = loc % 7;
    const int tb = xcd * 4 + l7;
    const int tt = w & 1, ct = (w >> 1) & 1, kh = w >> 2;
    const unsigned swz = (unsigned)(((lane & 7) ^ (((row8 >> 1) & 1) << 2)) * 16);

    if (__builtin_expect(ffn, 1)) {
        constexpr int A_W = 58368, A_WS = 24576;
#define A_KC(c) (rot + (c) >= 7 ? rot + (c) - 7 : rot + (c))
        const unsigned wvo = (unsigned)(row8 * 2688) + swz;
        const char* wsrc = wqkv + acg * 128 + (size_t)w * 8 * 2688;
        const unsigned wdst = lds0 + A_W + w * 1024;
#define A_PIECE(c, i) glds(wvo, wsrc + A_KC(c) * (64 * 2688) + (i) * 896, wdst + ((c) & 3) * A_WS + (i) * 8192)
#define A_GROUP(c) do { A_PIECE(c, 1); A_PIECE(c, 2); A_PIECE(c, 0); } while (0)
        lds_ptr xb[4], wb1, wb1h;
#pragma unroll
        for (int j = 0; j < 4; ++j) xb[j] = lds + 1024 + tt * 28672 + (r >> 3) * 1024 + (r & 7) * 128 + (((2 * j + hf) ^ ((r >> 1) & 7)) * 16);
        wb1 = opq(lds + A_W + (kh ? 0 : 8192) + (8 * hf + q4) * 128 + ((ct ^ (q4 >> 1)) * 64) + g1 * 32 + p4 * 8);
        wb1h = opq(wb1 + 2 * A_WS);
        unsigned go1[2], go2[2], vo; int gi[2], vd;
#define A_DEST(p) do { unsigned hq_ = hf; PIN(hq_); const int G = acg * 8 + ct * 4 + 2 * (p) + hq_, hp = G / 7, i = G - hp * 7; \
            const unsigned hs = kh ? 131072u : 262144u, to = (kh ? WS_QF + (atb * 2 + tt) * 2048 : WS_KV + (atb * 2 + tt) * 4096) + r * 16; \
            go1[p] = (unsigned)(2 * hp + (i > 3)) * hs + (unsigned)(i > 3 ? i - 3 : i) * 512 + to; go2[p] = i == 3 ? (unsigned)(2 * hp + 1) * hs + to : OOB; gi[p] = i; \
            PIN2(go1[p], go2[p]); } while (0)
#define A_DESTV() do { unsigned rx_ = r; PIN(rx_); const int vcol = acg * 64 + ct * 32 + rx_, vh = vcol / 28; vd = vcol - vh * 28; \
            vo = WS_KV + (atb * 2 + tt) * 4096 + (unsigned)vh * 262144 + 2048 + hf * 512 + vd * 16; PIN(vo); } while (0)
        lds_ptr xw = opq(lds + 1024 + kh * 28672 + (w & 3) * 1024 + l16);
        waitv<3>();
        TS(28);
        u32x4 xv[7];
#pragma unroll
        for (int c = 0; c < 7; ++c) xv[c] = *(LAS u32x4*)(xw + c * 4096);
        SB();
        f32x4 sm = {};
#pragma unroll
        for (int c = 0; c < 7; ++c)
#pragma unroll
            for (int j = 0; j < 4; ++j) sm[j] = dot2(xv[c][j], ones, sm[j]);
        float nm = sum8((sm[0] + sm[1]) + (sm[2] + sm[3])) * (-1.f / D);
        PIN(nm);
#pragma unroll
        for (int c = 0; c < 7; ++c) { xv[c] = shift8(xv[c], nm); *(LAS u32x4*)(xw + c * 4096) = xv[c]; }
        A_GROUP(1);
        SB();
        LAS float* rst = (LAS float*)lds;
        sm = (f32x4){};
#pragma unroll
        for (int c = 0; c < 7; ++c)
#pragma unroll
            for (int j = 0; j < 4; ++j) sm[j] = dot2(xv[c][j], xv[c][j], sm[j]);
        rst[w * 8 + row8] = __builtin_amdgcn_rsqf(sum8((sm[0] + sm[1]) + (sm[2] + sm[3])) * (1.f / D) + EPS);
        A_GROUP(2);
        TS(31);
        bf16x8 fx[4], f1[4], f2[4];
#define A_WAIT(c) do { if ((c) == 0) waitv_bar<6>(); else if ((c) == 1) waitv_barx<6>(); else if ((c) < 6) waitv_barx<3>(); else waitv_barx<0>(); \
                       if ((c) == 0) TS(1); if ((c) == 6) TS(2); } while (0)
#define A_WP(c, ks) ((((c) & 2) ? wb1h : wb1) + ((c) & 1) * A_WS + (ks) * 2048)
        if (w < 4) {
            f32x16 ak = {}, av = {};
            f32x4 rv[4];
            float rs = 0.f;
#pragma unroll
            for (int n = 0; n < 30; ++n) {
                const int c = n >> 2, ks = n & 3, km = (n - 2) & 3;
                if (n < 28 && ks == 0) A_WAIT(c);
                if (n >= 2) { ak = MFMA(f1[km], fx[km], ak); PIN(ak); } SB();
                if (n < 28) { fx[ks] = lds8(xb[ks] + c * 4096); f1[ks] = cat(trd(A_WP(c, ks)), trd(A_WP(c, ks) + 512)); } SB();
                if (n >= 2) { av = MFMA(fx[km], f2[km], av); PIN(av); } SB();
                if (n < 28) f2[ks] = cat(trd(A_WP(c, ks) + 8192), trd(A_WP(c, ks) + 8192 + 512));
                if (c == 0 && ks < 3) A_PIECE(3, ks);
                if (c >= 2 && c < 5 && ks < 3) A_PIECE(c + 2, ks);
                if (n == 5) A_DEST(0);
                if (n == 6) A_DEST(1);
                if (n == 7) A_DESTV();
                if (n == 21) {
                    rs = rst[tt * 32 + r];
#pragma unroll
                    for (int g = 0; g < 4; ++g) rv[g] = *(LAS f32x4*)(rst + tt * 32 + 8 * g + 4 * hf);
                }
                if (n == 24) { PIN(epoch); tagv = epoch + 1; PIN(tagv); }
                SB();
            }
            TS(3);
#pragma unroll
            for (int i = 0; i < 16; ++i) { ak[i] *= rs; av[i] *= rv[i >> 2][i & 3]; }
#pragma unroll
            for (int p = 0; p < 2; ++p) {
                const u32x4 f = pack8(ak, p);
                const auto s0 = __builtin_amdgcn_permlane32_swap(f[0], f[2], false, false), s1 = __builtin_amdgcn_permlane32_swap(f[1], f[3], false, false);
                const u32x4 c = {s0[0], s1[0], s0[1], s1[1]};
                st16_dev(c, rws, go1[p], 0); st16_dev(c, rws, go2[p], 0);
            }
            st16_dev(pack8(av, 0), rws, vo, 0); st16_dev(pack8(av, 1), rws, vo + 1024, 0);
        } else {
            f32x16 aq = {};
            __builtin_amdgcn_s_setprio(1);
#pragma unroll
            for (int n = 0; n < 30; ++n) {
                const int c = n >> 2, ks = n & 3, km = (n - 2) & 3;
                if (n < 28 && ks == 0) A_WAIT(c);
                if (n == 24) { PIN(epoch); tagv = epoch + 1; PIN(tagv); }
                if (n >= 2) { aq = MFMA(f1[km], fx[km], aq); PIN(aq); } SB();
                if (n < 28) { fx[ks] = lds8(xb[ks] + c * 4096); f1[ks] = cat(trd(A_WP(c, ks)), trd(A_WP(c, ks) + 512)); }
                if (c == 0 && ks < 3) A_PIECE(3, ks);
                if (c >= 2 && c < 5 && ks < 3) A_PIECE(c + 2, ks);
                if (n == 5) A_DEST(0);
                if (n == 6) A_DEST(1);
                if (n == 7) A_DESTV();
                if (n == 24) {
                    const unsigned pv = vd == 0 ? ones : 0u, po = vd < 4 ? vo + 448 : OOB;
                    st16_dev((u32x4){pv, pv, pv, pv}, rws, po, 0); st16_dev((u32x4){pv, pv, pv, pv}, rws, po + 1024, 0);
                }
                SB();
            }
            __builtin_amdgcn_s_setprio(0);
            const float QS = rst[tt * 32 + r] * (0.18898223650461362f * 1.4426950408889634f);
#pragma unroll
            for (int i = 0; i < 16; ++i) aq[i] *= QS;
#pragma unroll
            for (int p = 0; p < 2; ++p) {
                const u32x4 f = pack8(aq, p);
                const auto s0 = __builtin_amdgcn_permlane32_swap(f[0], f[2], false, false), s1 = __builtin_amdgcn_permlane32_swap(f[1], f[3], false, false);
                const bool sp = gi[p] == 3;
                st16_dev((u32x4){s0[0], s1[0], sp ? 0u : s0[1], sp ? 0u : s1[1]}, rws, go1[p], 0);
                st16_dev((u32x4){0u, 0u, s0[1], s1[1]}, rws, go2[p], 0);
            }
        }
    } else {
        const int ri = xcd * 4 + loc - 28, nu = (w == 0 && ri < 16) ? 4 : 3;
        const unsigned vo = (unsigned)(row8 * 3584 + (lane & 7) * 16);
#pragma unroll 1
        for (int i = 0; i < nu; ++i) {
            const int u = i < 3 ? i * 256 + ri * 8 + w : 768 + ri;
            const size_t so = (size_t)(u / 28) * 16 * 3584 + (u % 28) * 128;
            const unsigned d_ = lds0 + w * 16384 + i * 4096;
            glds(vo, wg + so, d_); glds(vo, wg + so + 8 * 3584, d_ + 1024); glds(vo, wu + so, d_ + 2048); glds(vo, wu + so + 8 * 3584, d_ + 3072);
        }
#pragma unroll 1
        for (int i = 0; i < nu; ++i) {
            const int u = i < 3 ? i * 256 + ri * 8 + w : 768 + ri;
            waitv<8>();
            lds_ptr sg = lds + w * 16384 + i * 4096 + g1 * 2048 + (4 * hf + q4) * 128 + p4 * 8;
#pragma unroll
            for (int e = 0; e < 4; ++e) st16_dev(U4(cat(trd(sg + e * 32), trd(sg + e * 32 + 1024))), rws, l16, WS_WGU + (unsigned)(((u % 28) * 4 + e) * 28 + u / 28) * 1024);
            FENCE();
        }
        tagv = epoch + 1;
    }
    TS(24);
    const int head = xcd * 2 + (loc & 1), rb = loc >> 1, qt = w & 3;
    const char* kvs = ws + WS_KV + (size_t)head * 262144 + w * 2048;
    const char* qs = ws + WS_QF + (size_t)(head * 64 + rb * 4 + qt) * 2048 + kh * 1024;
    unsigned kvd = lds0 + w * 2048, qd = lds0 + 147456 + qt * 2048 + kh * 1024;
    unsigned pf1, fl1 = flag_at(FK_A, xcd, loc);
    {
        const unsigned cg = (head * 28 + (lane & 1) * 27) / 64, q = lane >> 4, i = (lane >> 1) & 7;
        pf1 = cg == 3 ? flag_at(FK_A, 2 * q + (i >> 2), 24 + (i & 3)) : flag_at(FK_A, 2 * q + (cg >> 2), (cg & 3) * 8 + i);
    }
    PINS(kvs); PINS(qs); PINS(kvd); PINS(qd); PIN2(pf1, fl1);
    waitv_bar<0>();
    if (w == 0) set_flag(ws, fl1, tagv);
    const u64 tag = ((u64)(unsigned)__builtin_amdgcn_readfirstlane((unsigned)(tagv >> 32)) << 32 | (u64)(unsigned)__builtin_amdgcn_readfirstlane((unsigned)tagv));
    TS(4);

    const int drb = (xcd >> 1) * 4 + l7, dfg = (xcd & 1) * 7 + m7;
    bf16x8 wf[28];
    const unsigned p16 = hf * 1024 + g1 * 512 + (lane & 15) * 16;
    u32x2 xa, xb;
    f32x4 csg = {}, csu = {};
    f32x16 acc = {};
    const u32x4 one4 = {ones, ones, ones, ones};
    const unsigned hi_ = (unsigned)((tb * 2 + tt) * 28 + m7 * 4 + ct * 2 + kh), ofo = (unsigned)(tt * 32768 + kh * 16384 + ct * 4096);
    unsigned ho = WS_HF + hi_ * 1024 + l16, so = hf ? OOB : WS_HS + hi_ * 256 + r * 8;
    const char* os = ws + WS_OF + (size_t)tb * 65536 + ofo;
    unsigned od = lds0 + 65536 + ofo;
    unsigned pf2 = flag_at(FK_O, kh * 4 + ct + (lane & 2), (tb & ~1) + (lane & 1));
    unsigned go = ffn ? WS_GF + (unsigned)(drb * 4 * 112 + dfg * 8 + w) * 1024 + g1 * 512 + (hf * 16 + (lane & 15)) * 16 : OOB;
    unsigned fo3 = flag_at(FK_H, xcd, loc);
    {
        lds_ptr ap = opq(lds + kh * 8192 + l16), apB = opq(lds + 65536 + kh * 8192 + l16), apC = opq(lds + 131072 + kh * 8192 + l16);
        const unsigned lv = kh * 8192 + (l16 ^ (kh * 256));
        lds_ptr vp = opq(lds + lv), vpB = opq(lds + 65536 + lv), vpC = opq(lds + 131072 + lv);
#define AP(off) ((off) < 65536 ? ap + (off) : (off) < 131072 ? apB + ((off) - 65536) : apC + ((off) - 131072))
#define VP(off) ((off) < 65536 ? vp + (off) : (off) < 131072 ? vpB + ((off) - 65536) : vpC + ((off) - 131072))
#define KV_ISSUE(t) glds2_dev(l16, kvs + (t) * 16384, kvd + phys(t))
        unsigned wgo = ffn ? WS_WGU + (unsigned)(dfg * 8 + w) * 28672 : OOB;
        const int ri = umin(xcd * 4 + loc - 28, 27);
        const char* wos = (ffn ? wo + m7 * 128 : wd + (ri % 7) * 128 + (size_t)(ri / 7) * 448 * 896) + (size_t)w * 56 * 896;
        unsigned wov = (unsigned)(row8 * 896) + swz;
        const rsrc_t rx = __builtin_amdgcn_make_buffer_rsrc((void*)x, 0, 2048 * 448 * 2, 0x00020000);
        unsigned xo_ = (unsigned)(((tb * 2 + tt) * 32 + r) * 896 + m7 * 128 + ct * 64 + kh * 32 + hf * 8);
        unsigned rto = flag_at(FK_A, w, 28 + (lane & 3));
        lds_ptr qp = opq(lds + 147456 + qt * 2048 + l16);
        PINS(wgo); PINS(wos); PIN2(wov, xo_); PIN(rto); PIN2(ho, so); PIN2(pf2, fo3); PIN(go);
        lds_ptr xw = opq(lds + 147456 + w * 2048 + l16), xr = opq(lds + 147456 + (w ^ 4) * 2048 + l16);
        lds_ptr lw = opq(lds + 58368 + w * 256 + lane * 4), lr = opq(lds + 58368 + qt * 256 + 128 + r * 4);
        lds_ptr bw = opq(lds + 60416 + w * 4), br = opq(lds + 60416 + (lane & 7) * 4);
        unsigned oo = WS_OF + (unsigned)((rb * 4 + qt) * 32 + head * 2 + kh) * 1024 + l16;
        unsigned fo2 = flag_at(FK_O, xcd, loc);
        PIN2(oo, fo2);
        if (w == 1) poll_flag(ws, pf1, tag);
        BAR();
        TS(5);
        glds_dev(l16, qs, qd);
        KV_ISSUE(0); KV_ISSUE(1);
        asm volatile("s_barrier" ::: "memory");
        KV_ISSUE(2); KV_ISSUE(3);
        waitv_bar<4>();
        TS(6);
        const bf16x8 q0 = lds8(qp), q1 = lds8(qp + 1024);
        f32x16 o = {}, sa = {}, sb;
        bf16x8 k0 = lds8(ap + phys(0)), k1 = lds8(ap + phys(0) + 1024), v0 = lds8(vp + phys(0) + 2048), v1 = {};
        u32x4 p0, p1 = {};
        sa = MFMA(k0, q0, sa); sa = MFMA(k1, q1, sa);
        k0 = lds8(ap + phys(0) + 4096); k1 = lds8(ap + phys(0) + 5120);
        PIN(sa); SB();
        u64 rtf = 0;
#define EXP4(S, o4) _Pragma("unroll") for (int i_ = o4; i_ < o4 + 4; ++i_) S[i_] = __builtin_amdgcn_exp2f(S[i_]);
#define WLD(s) wf[s] = BF8(ld16_dev(rws, p16 + ((s) & 1) * 256 + ((s) & 2) * 1024, wgo + ((s) >> 2) * 4096))
#define SIDE(t, g) do { \
            if ((t) == 0 && (g) == 0) KV_ISSUE(4); if ((t) == 0 && (g) == 2) KV_ISSUE(5); if ((t) == 1 && (g) == 0) KV_ISSUE(6); if ((t) == 1 && (g) == 2) KV_ISSUE(7); \
            if ((t) == 2 && (g) == 0) KV_ISSUE(8); \
            if ((t) == 1 && (g) == 1) peek_lds(ws, rto, lds0 + 155648 + w * 512); if ((t) == 2 && (g) == 2) { waitv<5>(); rtf = peeked(lds + 155648 + w * 512 + lane * 4); } \
            if ((t) == 3 && (g) < 3) KV_ISSUE(9 + (g)); \
            if (((t) == 2 && (g) == 1) || ((t) == 14 && (g) == 2)) st16_dev((u32x4){~0u, ~0u, ~0u, ~0u}, rws, go + ((t) == 2 ? 2 : 3) * 114688, 0);     \
            if ((t) == 7 && (g) == 0) KV_ISSUE(12); if ((t) == 7 && (g) == 2) KV_ISSUE(13); if ((t) == 8 && (g) == 0) KV_ISSUE(14); if ((t) == 8 && (g) == 2) KV_ISSUE(15); \
            if ((t) >= 4 && (t) <= 6) WLD(((t) - 4) * 4 + (g)); \
            if (((t) == 7 || (t) == 8) && ((g) & 1)) WLD(12 + ((t) - 7) * 2 + ((g) >> 1)); \
            if ((t) >= 9 && (t) <= 11) WLD(((t) - 5) * 4 + (g)); \
            if ((t) == 12 || ((t) == 13 && (g) < 3)) glds(wov, wos + (((t) - 12) * 4 + (g)) * 8 * 896, lds0 + (w * 7 + ((t) - 12) * 4 + (g)) * 1024); \
            if ((t) == 13 && (g) == 3) { xa = ld8(rx, xo_, 0); xb = ld8(rx, xo_, 16); } \
            if ((t) == 14 && (g) == 0) { if (tid < 32) *(LAS u32x4*)(lds + 57344 + tid * 16) = (u32x4){0, 0, 0, 0}; } \
        } while (0)
#define TOFF(i) (phys((i) >> 1) + ((i) & 1) * 4096)
#define TILE(i, SC, SN) do { \
            __builtin_amdgcn_s_setprio(((i) & 1) ? 1 : 3); \
            if ((i) < 31) { f32x16 z_ = {}; SN = MFMA(k0, q0, z_); PIN(SN); } if ((i) < 30) k0 = lds8(AP(TOFF((i) + 2))); SB(); \
            EXP4(SC, 0); PIN(SC); SB(); \
            o = MFMA(v1, BF8(p1), o); PIN(o); v1 = lds8(VP(TOFF(i) + 3072)); SIDE((i) >> 1, ((i) & 1) * 2); SB(); \
            EXP4(SC, 4); p0 = pack8(SC, 0); PIN(p0); SB(); \
            __builtin_amdgcn_s_setprio(((i) & 1) ? 0 : 2); \
            if ((i) < 31) { SN = MFMA(k1, q1, SN); PIN(SN); } if ((i) < 30) k1 = lds8(AP(TOFF((i) + 2) + 1024)); SB(); \
            EXP4(SC, 8); PIN(SC); SB(); \
            o = MFMA(v0, BF8(p0), o); PIN(o); if ((i) < 31) v0 = lds8(VP(TOFF((i) + 1) + 2048)); SIDE((i) >> 1, ((i) & 1) * 2 + 1); SB(); \
            EXP4(SC, 12); p1 = pack8(SC, 1); PIN(p1); SB(); } while (0)
#define STEP(t) do { TILE(2 * (t), sa, sb); TILE(2 * (t) + 1, sb, sa); } while (0)
        STEP(0);
        waitv_barx<4>();
        STEP(1); STEP(2);
        if (__builtin_expect(!wave_all(rtf == tag), 0)) poll_flag(ws, rto, tag);
        waitv_bar2<3>();
        TS(21);
        STEP(3); STEP(4); STEP(5); STEP(6);
        waitv_bar2<12>();
        TS(22);
        STEP(7); STEP(8); STEP(9); STEP(10);
        waitv_bar2<9>();
        TS(23);
        STEP(11); STEP(12); STEP(13); STEP(14); STEP(15);
        o = MFMA(v1, BF8(p1), o);
        TS(7);
        u32x4 ov;
        {
            *(LAS f32x4*)xw = (f32x4){o[8], o[9], o[10], o[11]}; *(LAS f32x4*)(xw + 1024) = (f32x4){o[12], o[13], o[14], o[15]};
            const float l = kh ? o[4] : o[12];
            *(LAS float*)lw = l;
#define AMX(a, b, c) __builtin_fmaxf(__builtin_fmaxf(__builtin_fabsf(a), __builtin_fabsf(b)), __builtin_fabsf(c))
            const float mx_ = AMX(AMX(AMX(o[0], o[1], o[2]), AMX(o[3], o[4], o[5]), AMX(o[6], o[7], o[8])), AMX(AMX(o[9], o[10], o[11]), AMX(o[12], o[13], o[14]), o[15]), 0.f);
            const bool ok = (mx_ < 0x1p100f) & ((hf ? l : 1.f) > 0x1p-40f);
            *(LAS unsigned*)bw = wave_all(ok) ? 0u : 1u;
            BAR();
            const f32x4 a = *(LAS f32x4*)xr, c = *(LAS f32x4*)(xr + 1024);
            const float inv = __builtin_amdgcn_rcpf(*(LAS float*)lr + *(LAS float*)(lr + 1024));
            const unsigned bad = *(LAS unsigned*)br;
            const float iz = (kh && hf) ? 0.f : inv;
            ov = (u32x4){cvtpk((o[0] + a[0]) * inv, (o[1] + a[1]) * inv), cvtpk((o[2] + a[2]) * inv, (o[3] + a[3]) * inv),
                         cvtpk((o[4] + c[0]) * iz, (o[5] + c[1]) * iz), cvtpk((o[6] + c[2]) * iz, (o[7] + c[3]) * iz)};
            if (__builtin_expect(wave_any(bad != 0), 0)) {
                f32x16 of = {};
                float m = -0x1p126f;
#pragma unroll 1
                for (int t = 0; t < 16; ++t) {
                    BAR();
                    glds2_dev(l16, kvs + t * 16384, lds0 + 65536 + w * 2048);
                    waitv_bar<0>();
#pragma unroll 1
                    for (int u = 0; u < 2; ++u) {
                        f32x16 s = {};
                        s = MFMA(lds8(apB + u * 4096), q0, s); s = MFMA(lds8(apB + u * 4096 + 1024), q1, s);
                        float mx = s[0];
#pragma unroll
                        for (int i = 1; i < 16; ++i) mx = __builtin_fmaxf(mx, s[i]);
                        mx = __builtin_fmaxf(swap_max(mx), m);
                        const float alpha = __builtin_amdgcn_exp2f(m - mx);
                        m = mx;
#pragma unroll
                        for (int i = 0; i < 16; ++i) { s[i] = __builtin_amdgcn_exp2f(s[i] - mx); of[i] *= alpha; }
                        of = MFMA(lds8(vpB + u * 4096 + 2048), BF8(pack8(s, 0)), of);
                        of = MFMA(lds8(vpB + u * 4096 + 3072), BF8(pack8(s, 1)), of);
                    }
                }
                *(LAS f32x4*)xw = (f32x4){of[8], of[9], of[10], of[11]}; *(LAS f32x4*)(xw + 1024) = (f32x4){of[12], of[13], of[14], of[15]};
                *(LAS float*)lw = hf ? (kh ? of[4] : of[12]) : m;
                BAR();
                const f32x4 a2 = *(LAS f32x4*)xr, c2 = *(LAS f32x4*)(xr + 1024);
                const float m0 = *(LAS float*)(lr - 128), m1 = *(LAS float*)(lr + 1024 - 128), mm = __builtin_fmaxf(m0, m1);
                const float e0 = __builtin_amdgcn_exp2f(m0 - mm), e1 = __builtin_amdgcn_exp2f(m1 - mm);
                const float iv = __builtin_amdgcn_rcpf(*(LAS float*)lr * e0 + *(LAS float*)(lr + 1024) * e1);
                const float im = (kh ? e1 : e0) * iv, io = (kh ? e0 : e1) * iv;
                const bool z = kh && hf;
                ov = (u32x4){cvtpk(of[0] * im + a2[0] * io, of[1] * im + a2[1] * io), cvtpk(of[2] * im + a2[2] * io, of[3] * im + a2[3] * io),
                             z ? 0u : cvtpk(of[4] * im + c2[0] * io, of[5] * im + c2[1] * io), z ? 0u : cvtpk(of[6] * im + c2[2] * io, of[7] * im + c2[3] * io)};
            }
        }
        st16_dev(ov, rws, oo, 0);
        TS(25);
        FENCE();
        PIN2(xa, xb);
#pragma unroll
        for (int s = 27; s >= 0; --s) PIN(wf[s]);
#pragma unroll
        for (int s = 0; s < 16; s += 2) { csg = MFMA16(wf[s], BF8(one4), csg); csu = MFMA16(wf[s + 1], BF8(one4), csu); }
        acc[0] = bflo(xa[0]); acc[1] = bfhi(xa[0]); acc[2] = bflo(xa[1]); acc[3] = bfhi(xa[1]); acc[4] = bflo(xb[0]); acc[5] = bfhi(xb[0]); acc[6] = bflo(xb[1]); acc[7] = bfhi(xb[1]);
        PIN(acc); PIN2(ho, so); PIN2(pf2, fo3); PINS(os); PINS(od); PIN2(csg, csu);
        waitv_bar<0>();
        if (w == 0) set_flag(ws, fo2, tagv);
        TS(8);
    }
    if (__builtin_expect(!ffn, 0)) {
        const int ri = xcd * 4 + loc - 28;
        if (ri < 28) {
#pragma unroll 1
            for (int u = w; u < 28; u += 8) {
                lds_ptr sd = lds + u * 2048 + (4 * hf + q4) * 128 + g1 * 32 + p4 * 8;
#pragma unroll
                for (int e = 0; e < 2; ++e) { lds_ptr p_ = sd + ((e ^ (q4 >> 1)) * 64);
                    st16_dev(U4(cat(trd(p_), trd(p_ + 1024))), rws, l16, WS_WD + (unsigned)(((ri % 7) * 112 + (ri / 7) * 28 + u) * 2 + e) * 1024); }
            }
        }
        waitv_bar<0>();
        if (w == 0) set_flag(ws, fo3, tagv);
        return;
    }

    const int erb = tb >> 1;
    const char* hs = ws + WS_HF + (size_t)drb * 114688 + w * 1024;
    const char* hss = ws + WS_HS + (size_t)((drb * 4 + (w & 3)) * 28 + kh * 14) * 256;
    unsigned pf3, fo4 = flag_at(FK_G, xcd, loc);
    {
        lds_ptr wb = lds + kh * (8 * 28 * 128) + (4 * hf + q4) * 128 + ((ct ^ (q4 >> 1)) * 64) + (g1 ^ kh) * 32 + p4 * 8;
        bf16x8 wr[16];
#pragma unroll
        for (int s = 0; s < 12; ++s) { const int k0 = ((s >> 1) * 28 + (s & 1) * 16) * 128; wr[s] = cat(trd(wb + k0), trd(wb + k0 + 1024)); }
        lds_ptr cw = opq(lds + 131072 + w * 2048 + l16), cr = opq(lds + 131072 + (w ^ 4) * 2048 + l16);
        lds_ptr ob = opq(lds + 65536 + tt * 32768 + kh * 16384 + l16);
        pf3 = flag_at(FK_H, drb >> 1, (drb & 1) * 14 + umin(lane & 15, 13));
        PIN2(pf3, fo4); PINS(hs); PINS(hss);
        poll_flag(ws, pf2, tag);
        TS(9);
        glds4_dev(l16, os, od);
        asm volatile("s_barrier" ::: "memory");
        glds4_dev(l16, os + 8192, od + 8192);
        bf16x8 of[8];
        waitv_bar<4>();
        TS(26);
#pragma unroll
        for (int s = 0; s < 8; ++s) of[s] = lds8(ob + s * 1024);
        SB();
#pragma unroll
        for (int s = 0; s < 8; ++s) {
            acc = MFMA(wr[s], of[s], acc); PIN(acc); SB();
            if (s >= 2 && s < 6) { const int k0 = (((s + 10) >> 1) * 28 + (s & 1) * 16) * 128; wr[s + 10] = cat(trd(wb + k0), trd(wb + k0 + 1024)); SB(); }
        }
        waitv_barx<0>();
#pragma unroll
        for (int s = 0; s < 8; ++s) of[s] = lds8(ob + (8 + s) * 1024);
        SB();
#pragma unroll
        for (int s = 0; s < 8; ++s) acc = MFMA(wr[8 + s], of[s], acc);
        TS(10);
        *(LAS f32x4*)cw = (f32x4){acc[8], acc[9], acc[10], acc[11]}; *(LAS f32x4*)(cw + 1024) = (f32x4){acc[12], acc[13], acc[14], acc[15]};
        BAR();
        const f32x4 a = *(LAS f32x4*)cr + (f32x4){acc[0], acc[1], acc[2], acc[3]}, b = *(LAS f32x4*)(cr + 1024) + (f32x4){acc[4], acc[5], acc[6], acc[7]};
        const u32x4 hv = {cvtpk(a[0], a[1]), cvtpk(a[2], a[3]), cvtpk(b[0], b[1]), cvtpk(b[2], b[3])};
        st16_dev(hv, rws, ho, 0);
        FENCE(); SB();
        float s1 = 0.f, s2 = 0.f;
        { f32x4 q4_ = {}; acc_stats2(hv[0], hv[1], q4_); acc_stats2(hv[2], hv[3], q4_); s1 = swap_add(q4_[0] + q4_[1]); s2 = swap_add(q4_[2] + q4_[3]); }
        st8_dev((u32x2){f2u(s1), f2u(s2)}, rws, so, 0);
        FENCE(); PIN2(csg, csu); SB();
#pragma unroll
        for (int s = 16; s < 28; s += 2) { csg = MFMA16(wf[s], BF8(one4), csg); csu = MFMA16(wf[s + 1], BF8(one4), csu); }
        PIN2(csg, csu);
        waitv_bar<0>();
        if (w == 0) set_flag(ws, fo3, tagv);
        if (bid == 0 && w == 0) set_flag(ws, 0u, tagv);
        TS(11);
    }

    const unsigned wdo = WS_WD + (unsigned)(m7 * 112 + w * 14) * 2048;
    bf16x8 wdf[28];
    bf16x8 gf[GW];
    const unsigned gxo = WS_GF + (unsigned)(erb * 4 * 112 + w * 14) * 1024 + (tb & 1) * 256;
    const unsigned pwd = hf * 2048 + g1 * 512 + (lane & 15) * 16;
#define G_LD(s) gf[(s) % GW] = BF8(ld16_dev(rws, p16, gxo + ((s) / 7) * 114688 + ((s) % 7) * 2048))
    u64 gfl = 0;
    const unsigned gfg = (w * 14 + umin(lane & 15, 13)) >> 3;
    const unsigned gfi = flag_at(FK_G + g1, (erb >> 2) * 2 + (gfg >= 7), (erb & 3) * 7 + (gfg >= 7 ? gfg - 7 : gfg));
    {
        const unsigned hd = lds0 + w * 1024;
        const char* wds = ws + wdo;
        lds_ptr hb = opq(lds + p16), hb2 = opq(lds + 57344 + p16);
        lds_ptr wl = opq(lds + 114688 + w * 5120 + l16), wl2 = opq(lds + w * 7168 + l16);
        LAS f32x2* stp = (LAS f32x2*)opq(lds + 159744 + (lane & 15) * 8);
        if (w == 1) poll_flag(ws, pf3, tag);
        BAR();
        TS(12);
#define H_PIECE(j) glds_dev(l16, hs + (j) * 8192, hd + (j) * 8192)
#define WDO(s) (((s) >> 2) * 4096 + (((s) >> 1) & 1) * 1024 + ((s) & 1) * 256)
#define WDLD(s) wdf[s] = BF8(ld16_dev(rws, pwd, wdo + WDO(s)))
#define HB(n) (((n) < 56 ? hb + ((n) & ~1) * 1024 : hb2 + (((n) & ~1) - 56) * 1024) + ((n) & 1) * 256)
        H_PIECE(0);
        asm volatile("s_barrier" ::: "memory");
        H_PIECE(1); H_PIECE(2); H_PIECE(3);
        constexpr int PF = 4;
        bf16x8 bf[PF + 1];
        f32x4 ag[2][2], au[2][2];
        f32x2 sp[7], sv[4];
        float mean[2] = {0.f, 0.f}, r2[2] = {0.f, 0.f}, hp = 0.f;
        u32x4 hv;
        lds_ptr pk = opq(lds + 155648 + w * 512 + lane * 4);
        u64 pv = 0;
        const unsigned pkd = lds0 + 155648 + w * 512;
        const unsigned rdi = (w >> 1) * 7 + m7, rdo = flag_at(FK_H, rdi >> 2, 28 + (rdi & 3));
        waitv_barx<3>();
        TS(27);
#pragma unroll
        for (int n = 0; n < PF; ++n) bf[n] = lds8(HB(n));
        SB();
        sfor<0, 125>([&](auto N_) __attribute__((always_inline)) {
            constexpr int n = decltype(N_)::v, p = n / 28, s = n % 28;
            if constexpr (n == 4) waitv_barx<3>();
            if constexpr (n == 12) waitv_barx<8>();
            if constexpr (n == 28) { waitv_barx<3>(); TS(13); }
            if constexpr (n == 64) { waitv_barx<10>(); if (w == 0) set_flag(ws, fo4, tagv); TS(14); }
            if constexpr (n == 85) { waitv_barx<8>(); if (w == 0) set_flag(ws, fo4 + 32768, tagv); }
            if constexpr (n == 32) { if (__builtin_expect(!wave_all(pv == tag), 0)) poll_flag(ws, rdo, tag); FENCE(); }
            if constexpr (d_prio(n) != d_prio(n - 1)) __builtin_amdgcn_s_setprio(d_prio(n));
            if constexpr (n < 112) {
                if constexpr (s < 2) { ag[p & 1][s] = (f32x4){}; au[p & 1][s] = (f32x4){}; }
                ag[p & 1][s & 1] = MFMA16(wf[s & ~1], bf[n % (PF + 1)], ag[p & 1][s & 1]); au[p & 1][s & 1] = MFMA16(wf[s | 1], bf[n % (PF + 1)], au[p & 1][s & 1]);
                PIN2(ag[p & 1][s & 1], au[p & 1][s & 1]);
            }
            SB();
            if constexpr (n + PF < 112) bf[(n + PF) % (PF + 1)] = lds8(HB(n + PF));
            if constexpr (n % 3 == 2 && n < 30) H_PIECE(4 + n / 3);
            if constexpr (n == 4) glds4_dev(l16, hss, lds0 + 114688 + w * 5120);
            if constexpr (n == 16) { waitv<4>();
#pragma unroll
                for (int j = 0; j < 7; ++j) sp[j] = *(LAS f32x2*)(lds + 114688 + w * 5120 + (hf * 7 + j) * 256 + r * 8);
            }
            if constexpr (n == 20) { const f32x2 t = ((sp[0] + sp[1]) + (sp[2] + sp[3])) + ((sp[4] + sp[5]) + sp[6]); stp[w * 32 + g1 * 16] = (f32x2){swap_add(t[0]), swap_add(t[1])}; }
            if constexpr (wd_at(n) >= 0) { constexpr int i = wd_at(n);
                if constexpr (i < 5) glds_dev(pwd, wds + WDO(i), lds0 + 114688 + w * 5120 + i * 1024);
                else if constexpr (i < 12) glds_dev(pwd, wds + WDO(i), lds0 + w * 7168 + (i - 5) * 1024);
                else WDLD(i); }
            if constexpr (n == 84) peek_lds(ws, gfi - g1 * 32768, pkd);
            if constexpr (n == 95) { waitv<5>(); pv = peeked(pk); }
            if constexpr (n == 99) { if (__builtin_expect(!wave_all(pv == tag), 0)) poll_flag(ws, gfi - g1 * 32768, tag); FENCE(); }
            if constexpr (n >= 101 && n < 101 + 2 * XN && n % 2 == 1) G_LD((n - 101) / 2);
            if constexpr (n == 106) gfl = peek_flag(ws, gfi);
            if constexpr (n == 28) pv = peeked(pk);
            if constexpr (n == 18) peek_lds(ws, rdo, pkd);
            if constexpr (p >= 1) {
                constexpr int e = (p - 1) & 1, tp = n == 100 ? 3 : p - 1;
                if constexpr ((s == 0 && p < 4) || n == 100) { sv[0] = stp[tp * 32]; sv[1] = stp[(tp + 4) * 32]; sv[2] = stp[tp * 32 + 16]; sv[3] = stp[(tp + 4) * 32 + 16]; }
                if constexpr (((s == 2 || s == 3) && p < 4) || n == 104 || n == 105) { constexpr int k = n & 1; const f32x2 t = sv[2 * k] + sv[2 * k + 1];
                    mean[k] = t[0] * (1.f / D); r2[k] = __builtin_amdgcn_rcpf(__builtin_fmaxf(t[1] * (1.f / D) - mean[k] * mean[k], 0.f) + EPS); PIN2(mean[k], r2[k]); }
                if constexpr (s >= 4 && s < 12) { constexpr int i = s - 4, k = i >> 2;
                    const float h0 = __builtin_fmaxf(ag[e][k][i & 3] - mean[k] * csg[i & 3], 0.f) * r2[k] * (au[e][k][i & 3] - mean[k] * csu[i & 3]);
                    if constexpr (i & 1) { hv[i >> 1] = p >= 3 ? umin(cvtpk(hp, h0), ~1u) : cvtpk(hp, h0); PIN(hv); } else { hp = h0; PIN(hp); } }
                if constexpr (s == 12) {
                    const auto s0 = __builtin_amdgcn_permlane32_swap(hv[0], hv[2], false, false), s1 = __builtin_amdgcn_permlane32_swap(hv[1], hv[3], false, false);
                    st16_dev((u32x4){s0[0], s1[0], s0[1], s1[1]}, rws, go + (p - 1) * 114688, 0); FENCE();
                }
            }
            SB();
        });
        TS(15);
#pragma unroll
        for (int s = XN; s < 7; ++s) { G_LD(s); SB(); }
        FENCE();
#pragma unroll
        for (int s = 0; s < 12; ++s) wdf[s] = lds8(s < 5 ? wl + s * 1024 : wl2 + (s - 5) * 1024);
    }

    {
        const int etok = tid >> 4, eblk = tid & 15;
        const int et = erb * 4 + (etok >> 4), er = (tb & 1) * 16 + (etok & 15);
        const unsigned hro = (unsigned)((((eblk >> 2) & 3) * 64 + (eblk & 1) * 32 + er) * 16 + ((eblk >> 1) & 1) * 8) + WS_HF + (unsigned)(et * 28 + m7 * 4) * 1024;
        u32x2 hres[2];
        GAS u64* op = (GAS u64*)(out + (size_t)(et * 32 + er) * 896 + m7 * 128 + eblk * 8);
        lds_ptr rp = opq(lds + w * 8448 + (lane >> 4) * 528 + (lane & 15) * 16), rq = opq(lds + eblk * 528 + etok * 16);
        lds_ptr rp2 = opq(rp + 67584), rq2 = opq(rq + 67584);
        f32x4 c[4];
#define G_STEP(s) do { _Pragma("unroll") for (int j = 0; j < 4; ++j) { if ((s) % 7 == 0) c[j] = (f32x4){}; c[j] = MFMA16(wdf[4 * ((s) % 7) + j], gf[(s) % GW], c[j]); } PIN2(c[0], c[1]); PIN2(c[2], c[3]); } while (0)
#define G_PUT(q) do { _Pragma("unroll") for (int j = 0; j < 4; ++j) *(LAS f32x4*)(((q) < 2 ? rp : rp2) + j * 2112 + ((q) & 1) * 256) = c[j]; } while (0)
#define G_OUT(hh, RQ) do { f32x4 pa[8]; \
            _Pragma("unroll") for (int ww = 0; ww < 8; ++ww) pa[ww] = *(LAS f32x4*)(RQ + ww * 8448); SB(); \
            const f32x4 a = ((pa[0] + pa[1]) + (pa[2] + pa[3])) + ((pa[4] + pa[5]) + (pa[6] + pa[7])); \
            const u32x2 ov = {cvtpk(a[0] + bflo(hres[hh][0]), a[1] + bfhi(hres[hh][0])), cvtpk(a[2] + bflo(hres[hh][1]), a[3] + bfhi(hres[hh][1]))}; \
            __hip_atomic_store(op + (hh) * (64 * 896 / 8), __builtin_bit_cast(u64, ov), __ATOMIC_RELAXED, __HIP_MEMORY_SCOPE_AGENT); } while (0)
        TS(16);
        unsigned seen = 0;
#pragma unroll
        for (int s = 0; s < 28; ++s) {
            if (s == 5) BAR();
            if (s == 7 || s == 14 || s == 21) G_PUT(s / 7 - 1);
            if (s == 14) TS(17);
            if (s == 21) { waitv<7>(); PIN2(hres[0], hres[1]); BAR(); G_OUT(0, rq); FENCE(); TS(18); }
            SB();
            G_STEP(s);
            if (s >= 14) { const u32x4 u = U4(gf[s % GW]); seen = umax3(umax3(seen, u[0], u[1]), u[2], u[3]); PIN(seen); }
            SB();
            if (s == 0) { if (__builtin_expect(!wave_all(gfl == tag), 0)) poll_flag(ws, gfi, tag); FENCE(); }
            if (s + GW < 28) G_LD(s + GW);
            if (s == 1) { hres[0] = ld8_dev(rws, hro, 0); hres[1] = ld8_dev(rws, hro, 57344); FENCE(); }
        }
        if (w == 0) { set_flag(ws, fo4 + 65536, tagv); set_flag(ws, fo4 + 98304, tagv); }
        if (__builtin_expect(wave_any(seen == ~0u), 0)) {
            poll_flag(ws, gfi + 65536, tag);
#pragma unroll
            for (int s = 14; s < 14 + GW; ++s) G_LD(s);
#pragma unroll
            for (int s = 14; s < 28; ++s) { if (s == 21) G_PUT(2); G_STEP(s); SB(); if (s + GW < 28) G_LD(s + GW); SB(); }
        }
        G_PUT(3);
        TS(19);
        BAR();
        G_OUT(1, rq2);
    }
    TS(20);
}

extern "C" void kernel_launch(void* const* d_in, const int* in_sizes, int n_in, void* d_out, int out_size, void* d_ws, size_t ws_size, hipStream_t stream) {
    (void)hipFuncSetAttribute((const void*)block_fwd, hipFuncAttributeMaxDynamicSharedMemorySize, LDS_BYTES);
    block_fwd<<<256, 512, LDS_BYTES, stream>>>((const char*)d_in[0], (const char*)d_in[1], (const char*)d_in[2], (const char*)d_in[4], (const char*)d_in[5], (const char*)d_in[6], (char*)d_out, (char*)d_ws);
}
```

```cpp
#include <hip/hip_runtime.h>
#include <hip/hip_bf16.h>
#include <stdint.h>

typedef __attribute__((ext_vector_type(8))) short bf16x8;
typedef __attribute__((ext_vector_type(4))) short s16x4;
typedef __attribute__((ext_vector_type(2))) __bf16 bf16x2;
typedef __attribute__((ext_vector_type(16))) float f32x16;
typedef __attribute__((ext_vector_type(4))) float f32x4;
typedef __attribute__((ext_vector_type(2))) float f32x2;
typedef __attribute__((ext_vector_type(4))) unsigned u32x4;
typedef __attribute__((ext_vector_type(2))) unsigned u32x2;
typedef short v4i16_t __attribute__((ext_vector_type(4)));
typedef unsigned long long u64;
#define LAS __attribute__((address_space(3)))
#define GAS __attribute__((address_space(1)))
typedef LAS char* lds_ptr;
#if defined(__HIP_DEVICE_COMPILE__)
#define NOPK __attribute__((target("no-packed-fp32-ops")))
#else
#define NOPK
#endif
#define DI __device__ __forceinline__
#define MFMA(a, b, c) __builtin_amdgcn_mfma_f32_32x32x16_bf16(a, b, c, 0, 0, 0)
#define MFMA16(a, b, c) __builtin_amdgcn_mfma_f32_16x16x32_bf16(a, b, c, 0, 0, 0)
#define BF8(x) __builtin_bit_cast(bf16x8, x)
#define U4(x) __builtin_bit_cast(u32x4, x)

constexpr int D = 448;
constexpr float EPS = 1e-5f;
constexpr unsigned FK_A = 1, FK_O = 2, FK_H = 3, FK_G = 4;
constexpr unsigned WS_KV = 1u << 20;
constexpr unsigned WS_QF = WS_KV + 4194304;
constexpr unsigned WS_OF = WS_QF + 2097152;
constexpr unsigned WS_HF = WS_OF + 2097152;
constexpr unsigned WS_GF = WS_HF + 64 * 28 * 1024;
constexpr unsigned WS_WGU = WS_GF + 64 * 112 * 1024;
constexpr unsigned WS_WD = WS_WGU + 112 * 28 * 1024;
constexpr unsigned WS_HS = WS_WD + 7 * 112 * 2 * 1024;
constexpr unsigned WS_END = WS_HS + 64 * 28 * 256 + 1024;
constexpr unsigned OOB = 0x40000000u;
constexpr int LDS_BYTES = 163840;

DI float u2f(unsigned u) { return __builtin_bit_cast(float, u); }
DI unsigned f2u(float f) { return __builtin_bit_cast(unsigned, f); }
DI unsigned umin(unsigned a, unsigned b) { return a < b ? a : b; }
DI unsigned umax3(unsigned a, unsigned b, unsigned c) { const unsigned m = a > b ? a : b; return m > c ? m : c; }
DI bool wave_any(bool p) { return __builtin_amdgcn_ballot_w64(p) != 0; }
DI bool wave_all(bool p) { return __builtin_amdgcn_ballot_w64(!p) == 0; }
DI unsigned cvtpk(float lo, float hi) { return __builtin_bit_cast(unsigned, __builtin_convertvector((f32x2){lo, hi}, bf16x2)); }
DI float bflo(unsigned u) { return u2f(u << 16); }
DI float bfhi(unsigned u) { return u2f(u & 0xffff0000u); }
DI void glds(unsigned voff, const void* sbase, unsigned dst) {
    asm volatile("s_mov_b32 m0, %2\n\ts_nop 0\n\tglobal_load_lds_dwordx4 %0, %1" :: "v"(voff), "s"(sbase), "s"(dst) : "memory");
}
DI void glds_dev(unsigned voff, const void* sbase, unsigned dst) {
    asm volatile("s_mov_b32 m0, %2\n\ts_nop 0\n\tglobal_load_lds_dwordx4 %0, %1 sc1" :: "v"(voff), "s"(sbase), "s"(dst) : "memory");
}
DI void glds2_dev(unsigned voff, const void* sbase, unsigned dst) {
    asm volatile("s_mov_b32 m0, %2\n\ts_nop 0\n\tglobal_load_lds_dwordx4 %0, %1 sc1\n\tglobal_load_lds_dwordx4 %0, %1 offset:1024 sc1" :: "v"(voff), "s"(sbase), "s"(dst) : "memory");
}
DI void glds4_dev(unsigned voff, const void* sbase, unsigned dst) {
    asm volatile("s_mov_b32 m0, %2\n\ts_nop 0\n\tglobal_load_lds_dwordx4 %0, %1 sc1\n\tglobal_load_lds_dwordx4 %0, %1 offset:1024 sc1\n\t"
                 "global_load_lds_dwordx4 %0, %1 offset:2048 sc1\n\tglobal_load_lds_dwordx4 %0, %1 offset:3072 sc1" :: "v"(voff), "s"(sbase), "s"(dst) : "memory");
}
DI s16x4 trd(lds_ptr p) { return __builtin_bit_cast(s16x4, __builtin_amdgcn_ds_read_tr16_b64_v4i16((LAS v4i16_t*)p)); }
DI bf16x8 cat(s16x4 a, s16x4 b) { return __builtin_shufflevector(a, b, 0, 1, 2, 3, 4, 5, 6, 7); }
DI bf16x8 lds8(lds_ptr p) { return *(LAS bf16x8*)p; }
DI float swap_add(float v) { auto r = __builtin_amdgcn_permlane32_swap(f2u(v), f2u(v), false, false); return u2f(r[0]) + u2f(r[1]); }
DI float swap_max(float v) { auto r = __builtin_amdgcn_permlane32_swap(f2u(v), f2u(v), false, false); return __builtin_fmaxf(u2f(r[0]), u2f(r[1])); }
DI u32x4 pack8(const f32x16& a, int s) { return (u32x4){cvtpk(a[8 * s], a[8 * s + 1]), cvtpk(a[8 * s + 2], a[8 * s + 3]), cvtpk(a[8 * s + 4], a[8 * s + 5]), cvtpk(a[8 * s + 6], a[8 * s + 7])}; }
DI float dot2(unsigned a, unsigned b, float c) { return __builtin_amdgcn_fdot2_f32_bf16(__builtin_bit_cast(bf16x2, a), __builtin_bit_cast(bf16x2, b), c, false); }
DI void acc_stats2(unsigned u0, unsigned u1, f32x4& s) {
    const float a = bflo(u0), b = bfhi(u0), c = bflo(u1), d = bfhi(u1);
    s[0] += a; s[1] += b; s[2] = __builtin_fmaf(a, a, s[2]); s[3] = __builtin_fmaf(b, b, s[3]); s[0] += c; s[1] += d; s[2] = __builtin_fmaf(c, c, s[2]); s[3] = __builtin_fmaf(d, d, s[3]);
}
DI float sum8(float v) {
    v += u2f(__builtin_amdgcn_update_dpp(0u, f2u(v), 0xB1, 0xf, 0xf, true));
    v += u2f(__builtin_amdgcn_update_dpp(0u, f2u(v), 0x4E, 0xf, 0xf, true));
    v += u2f(__builtin_amdgcn_update_dpp(0u, f2u(v), 0x141, 0xf, 0xf, true));
    return v;
}
DI u32x4 shift8(u32x4 x, float c) {
    u32x4 o; float t0, t1, t2, t3, t4, t5, t6, t7;
    asm("v_dot2_f32_bf16 %4, %12, %16, %18\n\tv_dot2_f32_bf16 %5, %12, %17, %18\n\tv_dot2_f32_bf16 %6, %13, %16, %18\n\tv_dot2_f32_bf16 %7, %13, %17, %18\n\t"
        "v_dot2_f32_bf16 %8, %14, %16, %18\n\tv_dot2_f32_bf16 %9, %14, %17, %18\n\tv_dot2_f32_bf16 %10, %15, %16, %18\n\tv_dot2_f32_bf16 %11, %15, %17, %18\n\t"
        "v_cvt_pk_bf16_f32 %0, %4, %5\n\tv_cvt_pk_bf16_f32 %1, %6, %7\n\tv_cvt_pk_bf16_f32 %2, %8, %9\n\tv_cvt_pk_bf16_f32 %3, %10, %11"
        : "=&v"(o[0]), "=&v"(o[1]), "=&v"(o[2]), "=&v"(o[3]), "=&v"(t0), "=&v"(t1), "=&v"(t2), "=&v"(t3), "=&v"(t4), "=&v"(t5), "=&v"(t6), "=&v"(t7)
        : "v"(x[0]), "v"(x[1]), "v"(x[2]), "v"(x[3]), "s"(0x00003f80u), "s"(0x3f800000u), "v"(c));
    return o;
}
template <int N> struct IC { static constexpr int v = N; };
template <int B, int E, class F> DI void sfor(F&& f) { if constexpr (B < E) { f(IC<B>{}); sfor<B + 1, E>(f); } }
template <int N> DI void waitv() { asm volatile("s_waitcnt vmcnt(%0)" :: "i"(N) : "memory"); }
template <int N> DI void waitv_bar() { if (N == 0) __builtin_amdgcn_s_waitcnt(0x0F70); asm volatile("s_waitcnt vmcnt(%0) lgkmcnt(0)\n\ts_barrier" :: "i"(N) : "memory"); }
template <int N> DI void waitv_barx() { asm volatile("s_waitcnt vmcnt(%0)\n\ts_barrier" :: "i"(N) : "memory"); }
template <int N> DI void waitv_bar2() { asm volatile("s_waitcnt vmcnt(%0) lgkmcnt(2)\n\ts_barrier" :: "i"(N) : "memory"); }
#define BAR() asm volatile("s_waitcnt lgkmcnt(0)\n\ts_barrier" ::: "memory")
#define FENCE() asm volatile("" ::: "memory")
typedef __amdgpu_buffer_rsrc_t rsrc_t;
DI u32x4 ld16_dev(rsrc_t rs, unsigned voff, unsigned soff) { return U4(__builtin_amdgcn_raw_buffer_load_b128(rs, voff, soff, 16)); }
DI u32x2 ld8(rsrc_t rs, unsigned voff, unsigned soff) { return __builtin_bit_cast(u32x2, __builtin_amdgcn_raw_buffer_load_b64(rs, voff, soff, 0)); }
DI u32x2 ld8_dev(rsrc_t rs, unsigned voff, unsigned soff) { return __builtin_bit_cast(u32x2, __builtin_amdgcn_raw_buffer_load_b64(rs, voff, soff, 16)); }
DI void st16_dev(u32x4 v, rsrc_t rs, unsigned voff, unsigned soff) { __builtin_amdgcn_raw_buffer_store_b128(v, rs, voff + soff, 0, 16); }
DI void st8_dev(u32x2 v, rsrc_t rs, unsigned voff, unsigned soff) { __builtin_amdgcn_raw_buffer_store_b64(v, rs, voff, soff, 16); }
DI unsigned flag_at(unsigned kind, unsigned xcd, unsigned loc) { return (kind * 256 + loc * 8 + xcd) * 128; }
DI void set_flag(char* ws, unsigned fo, u64 tagv) {
    asm volatile("s_mov_b64 exec, 1\n\tglobal_store_dwordx2 %0, %1, %2 sc1\n\ts_mov_b64 exec, -1" :: "v"(fo), "v"(tagv), "s"(ws) : "memory");
}
DI u64 peek_flag(char* ws, unsigned fo) { return __hip_atomic_load((GAS u64*)(ws + fo), __ATOMIC_RELAXED, __HIP_MEMORY_SCOPE_AGENT); }
DI void peek_lds(char* ws, unsigned fo, unsigned dst) {
    asm volatile("s_mov_b32 m0, %2\n\ts_nop 0\n\tglobal_load_lds_dword %0, %1 sc1\n\ts_add_u32 m0, %2, 252\n\ts_nop 0\n\tglobal_load_lds_dword %0, %1 offset:4 sc1" :: "v"(fo), "s"(ws), "s"(dst) : "memory", "scc");
}
DI u64 peeked(lds_ptr p) { return (u64)*(LAS unsigned*)(p + 256) << 32 | *(LAS unsigned*)p; }
DI void poll_flag(char* ws, unsigned fo, u64 tag) {
#pragma unroll 1
    for (int spin = 0; spin < (1 << 21); ++spin) {
        if (wave_all(peek_flag(ws, fo) == tag)) break;
        __builtin_amdgcn_s_sleep(1);
    }
    FENCE();
}
#define SB() __builtin_amdgcn_sched_barrier(0)
#define PINS(a) asm volatile("" : "+s"(a))
#define PIN(a) asm volatile("" : "+v"(a))
#define PIN2(a, b) asm volatile("" : "+v"(a), "+v"(b))
DI lds_ptr opq(lds_ptr p) { unsigned u = (unsigned)(uintptr_t)p; asm volatile("" : "+v"(u)); return (lds_ptr)(uintptr_t)u; }

constexpr int GW = 7, XN = 5;
DI constexpr int d_prio(int n) { return n < 0 ? -1 : n < 27 ? 3 - n / 7 : n < 64 ? 3 - (n - 27) / 10 : n < 84 ? 3 - (n - 64) / 5 : n < 112 ? 3 - (n - 84) / 7 : 0; }
DI constexpr int wd_at(int n) {
    constexpr int t[28] = {32, 34, 37, 39, 42, 65, 69, 72, 75, 78, 81, 83, 44, 47, 49, 52, 54, 57, 59, 61, 63, 86, 88, 90, 92, 94, 97, 99};
    for (int i = 0; i < 28; ++i) if (t[i] == n) return i;
    return -1;
}
DI constexpr int phys(int t) { return ((t % 9) + 2) % 9 * 16384; }

__global__ NOPK __launch_bounds__(512, 2) void block_fwd(const char* x, const char* wqkv, const char* wo, const char* wg, const char* wu, const char* wd, char* out, char* ws) {
    extern __shared__ __attribute__((aligned(16))) char smem[];
    lds_ptr lds = (lds_ptr)smem;
    const int tid = __builtin_amdgcn_workitem_id_x(), lane = tid & 63, w = __builtin_amdgcn_readfirstlane(tid >> 6);
    const int bid = __builtin_amdgcn_workgroup_id_x(), xcd = bid & 7, loc = bid >> 3;
    const int r = lane & 31, hf = lane >> 5, q4 = (lane & 15) >> 2, g1 = (lane >> 4) & 1, p4 = lane & 3;
    const unsigned lds0 = (unsigned)(uintptr_t)smem;
    const unsigned l16 = lane * 16;
    const int sp3 = (loc + 8) >> 5;
    const int atb = (xcd >> 1) * 8 + (loc & 7) + sp3 * (xcd & 1) * 4, acg = (loc >> 3) + (1 - sp3) * (xcd & 1) * 4;
    const int row8 = lane >> 3;
    const int rot8 = ((loc & 7) << sp3) + sp3 * (xcd & 1), rot = rot8 == 7 ? 0 : rot8;
    if (loc < 28) {
        const unsigned xo = (unsigned)((atb * 64 + (tid >> 3)) * 896 + (((tid ^ (tid >> 4)) & 7) * 16));
        const unsigned xd0 = lds0 + 1024 + (w >> 2) * 28672 + (w & 3) * 1024;
        unsigned xd = xd0 + (7 - rot) * 4096;
        const unsigned w0 = (unsigned)((rot * 64 + (tid >> 3)) * 2688 + acg * 128 + (((tid & 7) ^ ((tid >> 2) & 4)) * 16));
        asm volatile("s_cmp_eq_u32 %0, %3\n\ts_cselect_b32 %0, %2, %0\n\ts_mov_b32 m0, %0\n\ts_add_u32 %0, %0, 0x1000\n\tglobal_load_lds_dwordx4 %1, %4\n\t"
#define XP(o) "s_cmp_eq_u32 %0, %3\n\ts_cselect_b32 %0, %2, %0\n\ts_sub_u32 m0, %0, " #o "\n\ts_add_u32 %0, %0, 0x1000\n\tglobal_load_lds_dwordx4 %1, %4 offset:" #o "\n\t"
                     XP(128) XP(256) XP(384) XP(512) XP(640) XP(768)
                     "s_add_u32 m0, %6, 8192-896\n\ts_barrier\n\tglobal_load_lds_dwordx4 %5, %7 offset:896\n\t"
                     "s_add_u32 m0, %6, 16384-1792\n\ts_nop 0\n\tglobal_load_lds_dwordx4 %5, %7 offset:1792\n\t"
                     "s_mov_b32 m0, %6\n\ts_nop 0\n\tglobal_load_lds_dwordx4 %5, %7"
                     : "+&s"(xd) : "v"(xo), "s"(xd0), "s"(xd0 + 7 * 4096), "s"(x), "v"(w0), "s"(lds0 + 58368 + w * 1024), "s"(wqkv) : "memory", "scc");
    }
    SB();
    const rsrc_t rws = __builtin_amdgcn_make_buffer_rsrc((void*)ws, 0, WS_END, 0x00020000);
    u64 epoch, tagv;
    const unsigned ownf = (lane & 7) ? flag_at(lane & 7, xcd, loc) : 0u;
    const unsigned ones = 0x3f803f80u;
    const bool ffn = loc < 28;
    const int l7 = loc / 7, m7 = loc % 7;
    const int tb = xcd * 4 + l7;
    const int tt = w & 1, ct = (w >> 1) & 1, kh = w >> 2;
    const unsigned swz = (unsigned)(((lane & 7) ^ (((row8 >> 1) & 1) << 2)) * 16);

    if (__builtin_expect(ffn, 1)) {
        constexpr int A_W = 58368, A_WS = 24576;
#define A_KC(c) (rot + (c) >= 7 ? rot + (c) - 7 : rot + (c))
        const unsigned wvo = (unsigned)(row8 * 2688) + swz;
        const char* wsrc = wqkv + acg * 128 + (size_t)w * 8 * 2688;
        const unsigned wdst = lds0 + A_W + w * 1024;
#define A_PIECE(c, i) glds(wvo, wsrc + A_KC(c) * (64 * 2688) + (i) * 896, wdst + ((c) & 3) * A_WS + (i) * 8192)
#define A_GROUP(c) do { A_PIECE(c, 1); A_PIECE(c, 2); A_PIECE(c, 0); } while (0)
        epoch = peek_flag(ws, ownf); FENCE();
        lds_ptr xb[4], wb1, wb1h;
#pragma unroll
        for (int j = 0; j < 4; ++j) xb[j] = lds + 1024 + tt * 28672 + (r >> 3) * 1024 + (r & 7) * 128 + (((2 * j + hf) ^ ((r >> 1) & 7)) * 16);
        wb1 = opq(lds + A_W + (kh ? 0 : 8192) + (8 * hf + q4) * 128 + ((ct ^ (q4 >> 1)) * 64) + g1 * 32 + p4 * 8);
        wb1h = opq(wb1 + 2 * A_WS);
        unsigned go1[2] = {0, 0}, go2[2] = {0, 0}, vo = 0; int gi[2] = {0, 0}, vd = 0;
#define A_DEST(p) do { unsigned hq_ = hf; PIN(hq_); const int G = acg * 8 + ct * 4 + 2 * (p) + hq_, hp = G / 7, i = G - hp * 7; \
            const unsigned hs = kh ? 131072u : 262144u, to = (kh ? WS_QF + (atb * 2 + tt) * 2048 : WS_KV + (atb * 2 + tt) * 4096) + r * 16; \
            go1[p] = (unsigned)(2 * hp + (i > 3)) * hs + (unsigned)(i > 3 ? i - 3 : i) * 512 + to; go2[p] = i == 3 ? (unsigned)(2 * hp + 1) * hs + to : OOB; gi[p] = i; \
            PIN2(go1[p], go2[p]); } while (0)
#define A_DESTV() do { unsigned rx_ = r; PIN(rx_); const int vcol = acg * 64 + ct * 32 + rx_, vh = vcol / 28; vd = vcol - vh * 28; \
            vo = WS_KV + (atb * 2 + tt) * 4096 + (unsigned)vh * 262144 + 2048 + hf * 512 + vd * 16; PIN(vo); } while (0)
        lds_ptr xw = opq(lds + 1024 + kh * 28672 + (w & 3) * 1024 + l16);
        if (w >= 4) { A_DEST(0); A_DEST(1); A_DESTV(); }
        waitv<4>();
        u32x4 xv[7];
#pragma unroll
        for (int c = 0; c < 7; ++c) xv[c] = *(LAS u32x4*)(xw + c * 4096);
        SB();
        f32x4 sm = {};
#pragma unroll
        for (int c = 0; c < 7; ++c)
#pragma unroll
            for (int j = 0; j < 4; ++j) sm[j] = dot2(xv[c][j], ones, sm[j]);
        float nm = sum8((sm[0] + sm[1]) + (sm[2] + sm[3])) * (-1.f / D);
        PIN(nm);
#pragma unroll
        for (int c = 0; c < 7; ++c) { xv[c] = shift8(xv[c], nm); *(LAS u32x4*)(xw + c * 4096) = xv[c]; }
        A_GROUP(1);
        SB();
        LAS float* rst = (LAS float*)lds;
        sm = (f32x4){};
#pragma unroll
        for (int c = 0; c < 7; ++c)
#pragma unroll
            for (int j = 0; j < 4; ++j) sm[j] = dot2(xv[c][j], xv[c][j], sm[j]);
        rst[w * 8 + row8] = __builtin_amdgcn_rsqf(sum8((sm[0] + sm[1]) + (sm[2] + sm[3])) * (1.f / D) + EPS);
        bf16x8 fx[4], f1[4], f2[4];
#define A_WAIT(c) do { if ((c) == 0) waitv_bar<3>(); else if ((c) < 6) waitv_barx<3>(); else waitv_barx<0>(); } while (0)
#define A_WP(c, ks) ((((c) & 2) ? wb1h : wb1) + ((c) & 1) * A_WS + (ks) * 2048)
        if (w < 4) {
            f32x16 ak = {}, av = {};
            f32x4 rv[4];
            float rs = 0.f;
            A_DEST(0); A_DEST(1); A_DESTV();
#pragma unroll
            for (int n = 0; n < 30; ++n) {
                const int c = n >> 2, ks = n & 3, km = (n - 2) & 3;
                if (n < 28 && ks == 0) A_WAIT(c);
                if (n >= 2) { ak = MFMA(f1[km], fx[km], ak); PIN(ak); } SB();
                if (n < 28) { fx[ks] = lds8(xb[ks] + c * 4096); f1[ks] = cat(trd(A_WP(c, ks)), trd(A_WP(c, ks) + 512)); } SB();
                if (n >= 2) { av = MFMA(fx[km], f2[km], av); PIN(av); } SB();
                if (n < 28) f2[ks] = cat(trd(A_WP(c, ks) + 8192), trd(A_WP(c, ks) + 8192 + 512));
                if (c < 5 && ks < 3) A_PIECE(c + 2, ks);
                if (n == 21) {
                    rs = rst[tt * 32 + r];
#pragma unroll
                    for (int g = 0; g < 4; ++g) rv[g] = *(LAS f32x4*)(rst + tt * 32 + 8 * g + 4 * hf);
                }
                if (n == 24) { PIN(epoch); tagv = epoch + 1; PIN(tagv); }
                SB();
            }
#pragma unroll
            for (int i = 0; i < 16; ++i) { ak[i] *= rs; av[i] *= rv[i >> 2][i & 3]; }
#pragma unroll
            for (int p = 0; p < 2; ++p) {
                const u32x4 f = pack8(ak, p);
                const auto s0 = __builtin_amdgcn_permlane32_swap(f[0], f[2], false, false), s1 = __builtin_amdgcn_permlane32_swap(f[1], f[3], false, false);
                const u32x4 c = {s0[0], s1[0], s0[1], s1[1]};
                st16_dev(c, rws, go1[p], 0); st16_dev(c, rws, go2[p], 0);
            }
            st16_dev(pack8(av, 0), rws, vo, 0); st16_dev(pack8(av, 1), rws, vo + 1024, 0);
        } else {
            f32x16 aq = {};
            __builtin_amdgcn_s_setprio(1);
#pragma unroll
            for (int n = 0; n < 30; ++n) {
                const int c = n >> 2, ks = n & 3, km = (n - 2) & 3;
                if (n < 28 && ks == 0) A_WAIT(c);
                if (n == 24) { PIN(epoch); tagv = epoch + 1; PIN(tagv); }
                if (n >= 2) { aq = MFMA(f1[km], fx[km], aq); PIN(aq); } SB();
                if (n < 28) { fx[ks] = lds8(xb[ks] + c * 4096); f1[ks] = cat(trd(A_WP(c, ks)), trd(A_WP(c, ks) + 512)); }
                if (c < 5 && ks < 3) A_PIECE(c + 2, ks);
                if (n == 24) {
                    const unsigned pv = vd == 0 ? ones : 0u, po = vd < 4 ? vo + 448 : OOB;
                    st16_dev((u32x4){pv, pv, pv, pv}, rws, po, 0); st16_dev((u32x4){pv, pv, pv, pv}, rws, po + 1024, 0);
                }
                SB();
            }
            __builtin_amdgcn_s_setprio(0);
            const float QS = rst[tt * 32 + r] * (0.18898223650461362f * 1.4426950408889634f);
#pragma unroll
            for (int i = 0; i < 16; ++i) aq[i] *= QS;
#pragma unroll
            for (int p = 0; p < 2; ++p) {
                const u32x4 f = pack8(aq, p);
                const auto s0 = __builtin_amdgcn_permlane32_swap(f[0], f[2], false, false), s1 = __builtin_amdgcn_permlane32_swap(f[1], f[3], false, false);
                const bool sp = gi[p] == 3;
                st16_dev((u32x4){s0[0], s1[0], sp ? 0u : s0[1], sp ? 0u : s1[1]}, rws, go1[p], 0);
                st16_dev((u32x4){0u, 0u, s0[1], s1[1]}, rws, go2[p], 0);
            }
        }
    } else {
        epoch = peek_flag(ws, ownf);
        const int ri = xcd * 4 + loc - 28, nu = (w == 0 && ri < 16) ? 4 : 3;
        const unsigned vo = (unsigned)(row8 * 3584 + (lane & 7) * 16);
#pragma unroll 1
        for (int i = 0; i < nu; ++i) {
            const int u = i < 3 ? i * 256 + ri * 8 + w : 768 + ri;
            const size_t so = (size_t)(u / 28) * 16 * 3584 + (u % 28) * 128;
            const unsigned d_ = lds0 + w * 16384 + i * 4096;
            glds(vo, wg + so, d_); glds(vo, wg + so + 8 * 3584, d_ + 1024); glds(vo, wu + so, d_ + 2048); glds(vo, wu + so + 8 * 3584, d_ + 3072);
        }
#pragma unroll 1
        for (int i = 0; i < nu; ++i) {
            const int u = i < 3 ? i * 256 + ri * 8 + w : 768 + ri;
            waitv<8>();
            lds_ptr sg = lds + w * 16384 + i * 4096 + g1 * 2048 + (4 * hf + q4) * 128 + p4 * 8;
#pragma unroll
            for (int e = 0; e < 4; ++e) st16_dev(U4(cat(trd(sg + e * 32), trd(sg + e * 32 + 1024))), rws, l16, WS_WGU + (unsigned)(((u % 28) * 4 + e) * 28 + u / 28) * 1024);
            FENCE();
        }
        tagv = epoch + 1;
    }
    const int head = xcd * 2 + (loc & 1), rb = loc >> 1, qt = w & 3;
    const char* kvs = ws + WS_KV + (size_t)head * 262144 + w * 2048;
    const char* qs = ws + WS_QF + (size_t)(head * 64 + rb * 4 + qt) * 2048 + kh * 1024;
    unsigned kvd = lds0 + w * 2048, qd = lds0 + 147456 + qt * 2048 + kh * 1024;
    unsigned pf1, fl1 = flag_at(FK_A, xcd, loc);
    {
        const unsigned cg = (head * 28 + (lane & 1) * 27) / 64, q = lane >> 4, i = (lane >> 1) & 7;
        pf1 = cg == 3 ? flag_at(FK_A, 2 * q + (i >> 2), 24 + (i & 3)) : flag_at(FK_A, 2 * q + (cg >> 2), (cg & 3) * 8 + i);
    }
    PINS(kvs); PINS(qs); PINS(kvd); PINS(qd); PIN2(pf1, fl1);
    waitv_bar<0>();
    if (w == 0) set_flag(ws, fl1, tagv);
    const u64 tag = ((u64)(unsigned)__builtin_amdgcn_readfirstlane((unsigned)(tagv >> 32)) << 32 | (u64)(unsigned)__builtin_amdgcn_readfirstlane((unsigned)tagv));

    const int drb = (xcd >> 1) * 4 + l7, dfg = (xcd & 1) * 7 + m7;
    bf16x8 wf[28];
    const unsigned p16 = hf * 1024 + g1 * 512 + (lane & 15) * 16;
    u32x2 xa, xb;
    f32x4 csg = {}, csu = {};
    f32x16 acc = {};
    const u32x4 one4 = {ones, ones, ones, ones};
    const unsigned hi_ = (unsigned)((tb * 2 + tt) * 28 + m7 * 4 + ct * 2 + kh), ofo = (unsigned)(tt * 32768 + kh * 16384 + ct * 4096);
    unsigned ho = WS_HF + hi_ * 1024 + l16, so = hf ? OOB : WS_HS + hi_ * 256 + r * 8;
    const char* os = ws + WS_OF + (size_t)tb * 65536 + ofo;
    unsigned od = lds0 + 65536 + ofo;
    unsigned pf2 = flag_at(FK_O, kh * 4 + ct + (lane & 2), (tb & ~1) + (lane & 1));
    unsigned go = ffn ? WS_GF + (unsigned)(drb * 4 * 112 + dfg * 8 + w) * 1024 + g1 * 512 + (hf * 16 + (lane & 15)) * 16 : OOB;
    unsigned fo3 = flag_at(FK_H, xcd, loc);
    {
        lds_ptr ap = opq(lds + kh * 8192 + l16), apB = opq(lds + 65536 + kh * 8192 + l16), apC = opq(lds + 131072 + kh * 8192 + l16);
        const unsigned lv = kh * 8192 + (l16 ^ (kh * 256));
        lds_ptr vp = opq(lds + lv), vpB = opq(lds + 65536 + lv), vpC = opq(lds + 131072 + lv);
#define AP(off) ((off) < 65536 ? ap + (off) : (off) < 131072 ? apB + ((off) - 65536) : apC + ((off) - 131072))
#define VP(off) ((off) < 65536 ? vp + (off) : (off) < 131072 ? vpB + ((off) - 65536) : vpC + ((off) - 131072))
#define KV_ISSUE(t) glds2_dev(l16, kvs + (t) * 16384, kvd + phys(t))
        unsigned wgo = ffn ? WS_WGU + (unsigned)(dfg * 8 + w) * 28672 : OOB;
        const int ri = umin(xcd * 4 + loc - 28, 27);
        const char* wos = (ffn ? wo + m7 * 128 : wd + (ri % 7) * 128 + (size_t)(ri / 7) * 448 * 896) + (size_t)w * 56 * 896;
        unsigned wov = (unsigned)(row8 * 896) + swz;
        const rsrc_t rx = __builtin_amdgcn_make_buffer_rsrc((void*)x, 0, 2048 * 448 * 2, 0x00020000);
        unsigned xo_ = (unsigned)(((tb * 2 + tt) * 32 + r) * 896 + m7 * 128 + ct * 64 + kh * 32 + hf * 8);
        unsigned rto = flag_at(FK_A, w, 28 + (lane & 3));
        lds_ptr qp = opq(lds + 147456 + qt * 2048 + l16);
        PINS(wgo); PINS(wos); PIN2(wov, xo_); PIN(rto); PIN2(ho, so); PIN2(pf2, fo3); PIN(go);
        lds_ptr xw = opq(lds + 147456 + w * 2048 + l16), xr = opq(lds + 147456 + (w ^ 4) * 2048 + l16);
        lds_ptr lw = opq(lds + 58368 + w * 256 + lane * 4), lr = opq(lds + 58368 + qt * 256 + 128 + r * 4);
        lds_ptr bw = opq(lds + 60416 + w * 4), br = opq(lds + 60416 + (lane & 7) * 4);
        unsigned oo = WS_OF + (unsigned)((rb * 4 + qt) * 32 + head * 2 + kh) * 1024 + l16;
        unsigned fo2 = flag_at(FK_O, xcd, loc);
        PIN2(oo, fo2);
        if (w == 1) poll_flag(ws, pf1, tag);
        BAR();
        glds_dev(l16, qs, qd);
        KV_ISSUE(0); KV_ISSUE(1);
        asm volatile("s_barrier" ::: "memory");
        KV_ISSUE(2); KV_ISSUE(3);
        waitv_bar<4>();
        const bf16x8 q0 = lds8(qp), q1 = lds8(qp + 1024);
        f32x16 o = {}, sa = {}, sb;
        bf16x8 k0 = lds8(ap + phys(0)), k1 = lds8(ap + phys(0) + 1024), v0 = lds8(vp + phys(0) + 2048), v1 = {};
        u32x4 p0, p1 = {};
        sa = MFMA(k0, q0, sa); sa = MFMA(k1, q1, sa);
        k0 = lds8(ap + phys(0) + 4096); k1 = lds8(ap + phys(0) + 5120);
        PIN(sa); SB();
        u64 rtf = 0;
#define EXP4(S, o4) _Pragma("unroll") for (int i_ = o4; i_ < o4 + 4; ++i_) S[i_] = __builtin_amdgcn_exp2f(S[i_]);
#define WLD(s) wf[s] = BF8(ld16_dev(rws, p16 + ((s) & 1) * 256 + ((s) & 2) * 1024, wgo + ((s) >> 2) * 4096))
#define SIDE(t, g) do { \
            if ((t) == 0 && (g) == 0) KV_ISSUE(4); if ((t) == 0 && (g) == 2) KV_ISSUE(5); if ((t) == 1 && (g) == 0) KV_ISSUE(6); if ((t) == 1 && (g) == 2) KV_ISSUE(7); \
            if ((t) == 2 && (g) == 0) KV_ISSUE(8); \
            if ((t) == 1 && (g) == 1) peek_lds(ws, rto, lds0 + 155648 + w * 512); if ((t) == 2 && (g) == 2) { waitv<5>(); rtf = peeked(lds + 155648 + w * 512 + lane * 4); } \
            if ((t) == 3 && (g) < 3) KV_ISSUE(9 + (g)); \
            if (((t) == 2 && (g) == 1) || ((t) == 14 && (g) == 2)) st16_dev((u32x4){~0u, ~0u, ~0u, ~0u}, rws, go + ((t) == 2 ? 2 : 3) * 114688, 0);     \
            if ((t) == 7 && (g) == 0) KV_ISSUE(12); if ((t) == 7 && (g) == 2) KV_ISSUE(13); if ((t) == 8 && (g) == 0) KV_ISSUE(14); if ((t) == 8 && (g) == 2) KV_ISSUE(15); \
            if ((t) >= 4 && (t) <= 6) WLD(((t) - 4) * 4 + (g)); \
            if (((t) == 7 || (t) == 8) && ((g) & 1)) WLD(12 + ((t) - 7) * 2 + ((g) >> 1)); \
            if ((t) >= 9 && (t) <= 11) WLD(((t) - 5) * 4 + (g)); \
            if ((t) == 12 || ((t) == 13 && (g) < 3)) glds(wov, wos + (((t) - 12) * 4 + (g)) * 8 * 896, lds0 + (w * 7 + ((t) - 12) * 4 + (g)) * 1024); \
            if ((t) == 13 && (g) == 3) { xa = ld8(rx, xo_, 0); xb = ld8(rx, xo_, 16); } \
            if ((t) == 14 && (g) == 0) { if (tid < 32) *(LAS u32x4*)(lds + 57344 + tid * 16) = (u32x4){0, 0, 0, 0}; } \
        } while (0)
#define TOFF(i) (phys((i) >> 1) + ((i) & 1) * 4096)
#define TILE(i, SC, SN) do { \
            __builtin_amdgcn_s_setprio(((i) & 1) ? 1 : 3); \
            if ((i) < 31) { f32x16 z_ = {}; SN = MFMA(k0, q0, z_); PIN(SN); } if ((i) < 30) k0 = lds8(AP(TOFF((i) + 2))); SB(); \
            EXP4(SC, 0); PIN(SC); SB(); \
            o = MFMA(v1, BF8(p1), o); PIN(o); v1 = lds8(VP(TOFF(i) + 3072)); SIDE((i) >> 1, ((i) & 1) * 2); SB(); \
            EXP4(SC, 4); p0 = pack8(SC, 0); PIN(p0); SB(); \
            __builtin_amdgcn_s_setprio(((i) & 1) ? 0 : 2); \
            if ((i) < 31) { SN = MFMA(k1, q1, SN); PIN(SN); } if ((i) < 30) k1 = lds8(AP(TOFF((i) + 2) + 1024)); SB(); \
            EXP4(SC, 8); PIN(SC); SB(); \
            o = MFMA(v0, BF8(p0), o); PIN(o); if ((i) < 31) v0 = lds8(VP(TOFF((i) + 1) + 2048)); SIDE((i) >> 1, ((i) & 1) * 2 + 1); SB(); \
            EXP4(SC, 12); p1 = pack8(SC, 1); PIN(p1); SB(); } while (0)
#define STEP(t) do { TILE(2 * (t), sa, sb); TILE(2 * (t) + 1, sb, sa); } while (0)
        STEP(0);
        waitv_barx<4>();
        STEP(1); STEP(2);
        if (__builtin_expect(!wave_all(rtf == tag), 0)) poll_flag(ws, rto, tag);
        waitv_bar2<3>();
        STEP(3); STEP(4); STEP(5); STEP(6);
        waitv_bar2<12>();
        STEP(7); STEP(8); STEP(9); STEP(10);
        waitv_bar2<9>();
        STEP(11); STEP(12); STEP(13); STEP(14); STEP(15);
        o = MFMA(v1, BF8(p1), o);
        u32x4 ov;
        {
            *(LAS f32x4*)xw = (f32x4){o[8], o[9], o[10], o[11]}; *(LAS f32x4*)(xw + 1024) = (f32x4){o[12], o[13], o[14], o[15]};
            const float l = kh ? o[4] : o[12];
            *(LAS float*)lw = l;
#define AMX(a, b, c) __builtin_elementwise_maximum(__builtin_elementwise_maximum(__builtin_fabsf(a), __builtin_fabsf(b)), __builtin_fabsf(c))
            const float mx_ = AMX(AMX(AMX(o[0], o[1], o[2]), AMX(o[3], o[4], o[5]), AMX(o[6], o[7], o[8])), AMX(AMX(o[9], o[10], o[11]), AMX(o[12], o[13], o[14]), o[15]), 0.f);
            const bool ok = (mx_ < 0x1p100f) & ((hf ? l : 1.f) > 0x1p-40f);
            *(LAS unsigned*)bw = wave_all(ok) ? 0u : 1u;
            BAR();
            const f32x4 a = *(LAS f32x4*)xr, c = *(LAS f32x4*)(xr + 1024);
            const float inv = __builtin_amdgcn_rcpf(*(LAS float*)lr + *(LAS float*)(lr + 1024));
            const unsigned bad = *(LAS unsigned*)br;
            const float iz = (kh && hf) ? 0.f : inv;
            ov = (u32x4){cvtpk((o[0] + a[0]) * inv, (o[1] + a[1]) * inv), cvtpk((o[2] + a[2]) * inv, (o[3] + a[3]) * inv),
                         cvtpk((o[4] + c[0]) * iz, (o[5] + c[1]) * iz), cvtpk((o[6] + c[2]) * iz, (o[7] + c[3]) * iz)};
            if (__builtin_expect(wave_any(bad != 0), 0)) {
                f32x16 of = {};
                float m = -0x1p126f;
#pragma unroll 1
                for (int t = 0; t < 16; ++t) {
                    BAR();
                    glds2_dev(l16, kvs + t * 16384, lds0 + 65536 + w * 2048);
                    waitv_bar<0>();
#pragma unroll 1
                    for (int u = 0; u < 2; ++u) {
                        f32x16 s = {};
                        s = MFMA(lds8(apB + u * 4096), q0, s); s = MFMA(lds8(apB + u * 4096 + 1024), q1, s);
                        float mx = s[0];
#pragma unroll
                        for (int i = 1; i < 16; ++i) mx = __builtin_fmaxf(mx, s[i]);
                        mx = __builtin_fmaxf(swap_max(mx), m);
                        const float alpha = __builtin_amdgcn_exp2f(m - mx);
                        m = mx;
#pragma unroll
                        for (int i = 0; i < 16; ++i) { s[i] = __builtin_amdgcn_exp2f(s[i] - mx); of[i] *= alpha; }
                        of = MFMA(lds8(vpB + u * 4096 + 2048), BF8(pack8(s, 0)), of);
                        of = MFMA(lds8(vpB + u * 4096 + 3072), BF8(pack8(s, 1)), of);
                    }
                }
                *(LAS f32x4*)xw = (f32x4){of[8], of[9], of[10], of[11]}; *(LAS f32x4*)(xw + 1024) = (f32x4){of[12], of[13], of[14], of[15]};
                *(LAS float*)lw = hf ? (kh ? of[4] : of[12]) : m;
                BAR();
                const f32x4 a2 = *(LAS f32x4*)xr, c2 = *(LAS f32x4*)(xr + 1024);
                const float m0 = *(LAS float*)(lr - 128), m1 = *(LAS float*)(lr + 1024 - 128), mm = __builtin_fmaxf(m0, m1);
                const float e0 = __builtin_amdgcn_exp2f(m0 - mm), e1 = __builtin_amdgcn_exp2f(m1 - mm);
                const float iv = __builtin_amdgcn_rcpf(*(LAS float*)lr * e0 + *(LAS float*)(lr + 1024) * e1);
                const float im = (kh ? e1 : e0) * iv, io = (kh ? e0 : e1) * iv;
                const bool z = kh && hf;
                ov = (u32x4){cvtpk(of[0] * im + a2[0] * io, of[1] * im + a2[1] * io), cvtpk(of[2] * im + a2[2] * io, of[3] * im + a2[3] * io),
                             z ? 0u : cvtpk(of[4] * im + c2[0] * io, of[5] * im + c2[1] * io), z ? 0u : cvtpk(of[6] * im + c2[2] * io, of[7] * im + c2[3] * io)};
            }
        }
        st16_dev(ov, rws, oo, 0);
        FENCE();
        PIN2(xa, xb);
#pragma unroll
        for (int s = 27; s >= 0; --s) PIN(wf[s]);
#pragma unroll
        for (int s = 0; s < 16; s += 2) { csg = MFMA16(wf[s], BF8(one4), csg); csu = MFMA16(wf[s + 1], BF8(one4), csu); }
        acc[0] = bflo(xa[0]); acc[1] = bfhi(xa[0]); acc[2] = bflo(xa[1]); acc[3] = bfhi(xa[1]); acc[4] = bflo(xb[0]); acc[5] = bfhi(xb[0]); acc[6] = bflo(xb[1]); acc[7] = bfhi(xb[1]);
        PIN(acc); PIN2(ho, so); PIN2(pf2, fo3); PINS(os); PINS(od); PIN2(csg, csu);
        waitv_bar<0>();
        if (w == 0) set_flag(ws, fo2, tagv);
    }
    if (__builtin_expect(!ffn, 0)) {
        const int ri = xcd * 4 + loc - 28;
        if (ri < 28) {
#pragma unroll 1
            for (int u = w; u < 28; u += 8) {
                lds_ptr sd = lds + u * 2048 + (4 * hf + q4) * 128 + g1 * 32 + p4 * 8;
#pragma unroll
                for (int e = 0; e < 2; ++e) { lds_ptr p_ = sd + ((e ^ (q4 >> 1)) * 64);
                    st16_dev(U4(cat(trd(p_), trd(p_ + 1024))), rws, l16, WS_WD + (unsigned)(((ri % 7) * 112 + (ri / 7) * 28 + u) * 2 + e) * 1024); }
            }
        }
        waitv_bar<0>();
        if (w == 0) set_flag(ws, fo3, tagv);
        return;
    }

    const int erb = tb >> 1;
    const char* hs = ws + WS_HF + (size_t)drb * 114688 + w * 1024;
    const char* hss = ws + WS_HS + (size_t)((drb * 4 + (w & 3)) * 28 + kh * 14) * 256;
    unsigned pf3, fo4 = flag_at(FK_G, xcd, loc);
    {
        lds_ptr wb = lds + kh * (8 * 28 * 128) + (4 * hf + q4) * 128 + ((ct ^ (q4 >> 1)) * 64) + (g1 ^ kh) * 32 + p4 * 8;
        bf16x8 wr[16];
#pragma unroll
        for (int s = 0; s < 12; ++s) { const int k0 = ((s >> 1) * 28 + (s & 1) * 16) * 128; wr[s] = cat(trd(wb + k0), trd(wb + k0 + 1024)); }
        lds_ptr cw = opq(lds + 131072 + w * 2048 + l16), cr = opq(lds + 131072 + (w ^ 4) * 2048 + l16);
        lds_ptr ob = opq(lds + 65536 + tt * 32768 + kh * 16384 + l16);
        pf3 = flag_at(FK_H, drb >> 1, (drb & 1) * 14 + umin(lane & 15, 13));
        PIN2(pf3, fo4); PINS(hs); PINS(hss);
        poll_flag(ws, pf2, tag);
        glds4_dev(l16, os, od);
        asm volatile("s_barrier" ::: "memory");
        glds4_dev(l16, os + 8192, od + 8192);
        bf16x8 of[8];
        waitv_bar<4>();
#pragma unroll
        for (int s = 0; s < 8; ++s) of[s] = lds8(ob + s * 1024);
        SB();
#pragma unroll
        for (int s = 0; s < 8; ++s) {
            acc = MFMA(wr[s], of[s], acc); PIN(acc); SB();
            if (s >= 2 && s < 6) { const int k0 = (((s + 10) >> 1) * 28 + (s & 1) * 16) * 128; wr[s + 10] = cat(trd(wb + k0), trd(wb + k0 + 1024)); SB(); }
        }
        waitv_barx<0>();
#pragma unroll
        for (int s = 0; s < 8; ++s) of[s] = lds8(ob + (8 + s) * 1024);
        SB();
#pragma unroll
        for (int s = 0; s < 8; ++s) acc = MFMA(wr[8 + s], of[s], acc);
        *(LAS f32x4*)cw = (f32x4){acc[8], acc[9], acc[10], acc[11]}; *(LAS f32x4*)(cw + 1024) = (f32x4){acc[12], acc[13], acc[14], acc[15]};
        BAR();
        const f32x4 a = *(LAS f32x4*)cr + (f32x4){acc[0], acc[1], acc[2], acc[3]}, b = *(LAS f32x4*)(cr + 1024) + (f32x4){acc[4], acc[5], acc[6], acc[7]};
        const u32x4 hv = {cvtpk(a[0], a[1]), cvtpk(a[2], a[3]), cvtpk(b[0], b[1]), cvtpk(b[2], b[3])};
        st16_dev(hv, rws, ho, 0);
        FENCE(); SB();
        float s1 = 0.f, s2 = 0.f;
        { f32x4 q4_ = {}; acc_stats2(hv[0], hv[1], q4_); acc_stats2(hv[2], hv[3], q4_); s1 = swap_add(q4_[0] + q4_[1]); s2 = swap_add(q4_[2] + q4_[3]); }
        st8_dev((u32x2){f2u(s1), f2u(s2)}, rws, so, 0);
        FENCE(); PIN2(csg, csu); SB();
#pragma unroll
        for (int s = 16; s < 28; s += 2) { csg = MFMA16(wf[s], BF8(one4), csg); csu = MFMA16(wf[s + 1], BF8(one4), csu); }
        PIN2(csg, csu);
        waitv_bar<0>();
        if (w == 0) set_flag(ws, fo3, tagv);
        if (bid == 0 && w == 0) set_flag(ws, 0u, tagv);
    }

    const unsigned wdo = WS_WD + (unsigned)(m7 * 112 + w * 14) * 2048;
    bf16x8 wdf[28];
    bf16x8 gf[GW];
    const unsigned gxo = WS_GF + (unsigned)(erb * 4 * 112 + w * 14) * 1024 + (tb & 1) * 256;
    const unsigned pwd = hf * 2048 + g1 * 512 + (lane & 15) * 16;
#define G_LD(s) gf[(s) % GW] = BF8(ld16_dev(rws, p16, gxo + ((s) / 7) * 114688 + ((s) % 7) * 2048))
    u64 gfl = 0;
    const unsigned gfg = (w * 14 + umin(lane & 15, 13)) >> 3;
    const unsigned gfi = flag_at(FK_G + g1, (erb >> 2) * 2 + (gfg >= 7), (erb & 3) * 7 + (gfg >= 7 ? gfg - 7 : gfg));
    {
        const unsigned hd = lds0 + w * 1024;
        const char* wds = ws + wdo;
        lds_ptr hb = opq(lds + p16), hb2 = opq(lds + 57344 + p16);
        lds_ptr wl = opq(lds + 114688 + w * 5120 + l16), wl2 = opq(lds + w * 7168 + l16);
        LAS f32x2* stp = (LAS f32x2*)opq(lds + 159744 + (lane & 15) * 8);
        if (w == 1) poll_flag(ws, pf3, tag);
        BAR();
#define H_PIECE(j) glds_dev(l16, hs + (j) * 8192, hd + (j) * 8192)
#define WDO(s) (((s) >> 2) * 4096 + (((s) >> 1) & 1) * 1024 + ((s) & 1) * 256)
#define WDLD(s) wdf[s] = BF8(ld16_dev(rws, pwd, wdo + WDO(s)))
#define HB(n) (((n) < 56 ? hb + ((n) & ~1) * 1024 : hb2 + (((n) & ~1) - 56) * 1024) + ((n) & 1) * 256)
        H_PIECE(0);
        asm volatile("s_barrier" ::: "memory");
        H_PIECE(1); H_PIECE(2); H_PIECE(3);
        constexpr int PF = 4;
        bf16x8 bf[PF + 1];
        f32x4 ag[2][2], au[2][2];
        f32x2 sp[7], sv[4];
        float mean[2] = {0.f, 0.f}, r2[2] = {0.f, 0.f}, hp = 0.f;
        u32x4 hv;
        lds_ptr pk = opq(lds + 155648 + w * 512 + lane * 4);
        u64 pv = 0;
        const unsigned pkd = lds0 + 155648 + w * 512;
        const unsigned rdi = (w >> 1) * 7 + m7, rdo = flag_at(FK_H, rdi >> 2, 28 + (rdi & 3));
        waitv_barx<3>();
#pragma unroll
        for (int n = 0; n < PF; ++n) bf[n] = lds8(HB(n));
        SB();
        sfor<0, 125>([&](auto N_) __attribute__((always_inline)) {
            constexpr int n = decltype(N_)::v, p = n / 28, s = n % 28;
            if constexpr (n == 4) waitv_barx<3>();
            if constexpr (n == 12) waitv_barx<8>();
            if constexpr (n == 28) { waitv_barx<3>(); }
            if constexpr (n == 64) { waitv_barx<10>(); if (w == 0) set_flag(ws, fo4, tagv); }
            if constexpr (n == 85) { waitv_barx<8>(); if (w == 0) set_flag(ws, fo4 + 32768, tagv); }
            if constexpr (n == 32) { if (__builtin_expect(!wave_all(pv == tag), 0)) poll_flag(ws, rdo, tag); FENCE(); }
            if constexpr (d_prio(n) != d_prio(n - 1)) __builtin_amdgcn_s_setprio(d_prio(n));
            if constexpr (n < 112) {
                if constexpr (s < 2) { ag[p & 1][s] = (f32x4){}; au[p & 1][s] = (f32x4){}; }
                ag[p & 1][s & 1] = MFMA16(wf[s & ~1], bf[n % (PF + 1)], ag[p & 1][s & 1]); au[p & 1][s & 1] = MFMA16(wf[s | 1], bf[n % (PF + 1)], au[p & 1][s & 1]);
                PIN2(ag[p & 1][s & 1], au[p & 1][s & 1]);
            }
            SB();
            if constexpr (n + PF < 112) bf[(n + PF) % (PF + 1)] = lds8(HB(n + PF));
            if constexpr (n % 3 == 2 && n < 30) H_PIECE(4 + n / 3);
            if constexpr (n == 4) glds4_dev(l16, hss, lds0 + 114688 + w * 5120);
            if constexpr (n == 16) { waitv<4>();
#pragma unroll
                for (int j = 0; j < 7; ++j) sp[j] = *(LAS f32x2*)(lds + 114688 + w * 5120 + (hf * 7 + j) * 256 + r * 8);
            }
            if constexpr (n == 20) { const f32x2 t = ((sp[0] + sp[1]) + (sp[2] + sp[3])) + ((sp[4] + sp[5]) + sp[6]); stp[w * 32 + g1 * 16] = (f32x2){swap_add(t[0]), swap_add(t[1])}; }
            if constexpr (wd_at(n) >= 0) { constexpr int i = wd_at(n);
                if constexpr (i < 5) glds_dev(pwd, wds + WDO(i), lds0 + 114688 + w * 5120 + i * 1024);
                else if constexpr (i < 12) glds_dev(pwd, wds + WDO(i), lds0 + w * 7168 + (i - 5) * 1024);
                else WDLD(i); }
            if constexpr (n == 84) peek_lds(ws, gfi - g1 * 32768, pkd);
            if constexpr (n == 95) { waitv<5>(); pv = peeked(pk); }
            if constexpr (n == 99) { if (__builtin_expect(!wave_all(pv == tag), 0)) poll_flag(ws, gfi - g1 * 32768, tag); FENCE(); }
            if constexpr (n >= 101 && n < 101 + 2 * XN && n % 2 == 1) G_LD((n - 101) / 2);
            if constexpr (n == 106) gfl = peek_flag(ws, gfi);
            if constexpr (n == 28) pv = peeked(pk);
            if constexpr (n == 18) peek_lds(ws, rdo, pkd);
            if constexpr (p >= 1) {
                constexpr int e = (p - 1) & 1, tp = n == 100 ? 3 : p - 1;
                if constexpr ((s == 0 && p < 4) || n == 100) { sv[0] = stp[tp * 32]; sv[1] = stp[(tp + 4) * 32]; sv[2] = stp[tp * 32 + 16]; sv[3] = stp[(tp + 4) * 32 + 16]; }
                if constexpr (((s == 2 || s == 3) && p < 4) || n == 104 || n == 105) { constexpr int k = n & 1; const f32x2 t = sv[2 * k] + sv[2 * k + 1];
                    mean[k] = t[0] * (1.f / D); r2[k] = __builtin_amdgcn_rcpf(__builtin_fmaxf(t[1] * (1.f / D) - mean[k] * mean[k], 0.f) + EPS); PIN2(mean[k], r2[k]); }
                if constexpr (s >= 4 && s < 12) { constexpr int i = s - 4, k = i >> 2;
                    const float h0 = __builtin_fmaxf(ag[e][k][i & 3] - mean[k] * csg[i & 3], 0.f) * r2[k] * (au[e][k][i & 3] - mean[k] * csu[i & 3]);
                    if constexpr (i & 1) { hv[i >> 1] = p >= 3 ? umin(cvtpk(hp, h0), ~1u) : cvtpk(hp, h0); PIN(hv); } else { hp = h0; PIN(hp); } }
                if constexpr (s == 12) {
                    const auto s0 = __builtin_amdgcn_permlane32_swap(hv[0], hv[2], false, false), s1 = __builtin_amdgcn_permlane32_swap(hv[1], hv[3], false, false);
                    st16_dev((u32x4){s0[0], s1[0], s0[1], s1[1]}, rws, go + (p - 1) * 114688, 0); FENCE();
                }
            }
            SB();
        });
#pragma unroll
        for (int s = XN; s < 7; ++s) { G_LD(s); SB(); }
        FENCE();
#pragma unroll
        for (int s = 0; s < 12; ++s) wdf[s] = lds8(s < 5 ? wl + s * 1024 : wl2 + (s - 5) * 1024);
    }

    {
        const int etok = tid >> 4, eblk = tid & 15;
        const int et = erb * 4 + (etok >> 4), er = (tb & 1) * 16 + (etok & 15);
        const unsigned hro = (unsigned)((((eblk >> 2) & 3) * 64 + (eblk & 1) * 32 + er) * 16 + ((eblk >> 1) & 1) * 8) + WS_HF + (unsigned)(et * 28 + m7 * 4) * 1024;
        u32x2 hres[2];
        GAS u64* op = (GAS u64*)(out + (size_t)(et * 32 + er) * 896 + m7 * 128 + eblk * 8);
        lds_ptr rp = opq(lds + w * 8448 + (lane >> 4) * 528 + (lane & 15) * 16), rq = opq(lds + eblk * 528 + etok * 16);
        lds_ptr rp2 = opq(rp + 67584), rq2 = opq(rq + 67584);
        f32x4 c[4];
#define G_STEP(s) do { _Pragma("unroll") for (int j = 0; j < 4; ++j) { if ((s) % 7 == 0) c[j] = (f32x4){}; c[j] = MFMA16(wdf[4 * ((s) % 7) + j], gf[(s) % GW], c[j]); } PIN2(c[0], c[1]); PIN2(c[2], c[3]); } while (0)
#define G_PUT(q) do { _Pragma("unroll") for (int j = 0; j < 4; ++j) *(LAS f32x4*)(((q) < 2 ? rp : rp2) + j * 2112 + ((q) & 1) * 256) = c[j]; } while (0)
#define G_OUT(hh, RQ) do { f32x4 pa[8]; \
            _Pragma("unroll") for (int ww = 0; ww < 8; ++ww) pa[ww] = *(LAS f32x4*)(RQ + ww * 8448); SB(); \
            const f32x4 a = ((pa[0] + pa[1]) + (pa[2] + pa[3])) + ((pa[4] + pa[5]) + (pa[6] + pa[7])); \
            const u32x2 ov = {cvtpk(a[0] + bflo(hres[hh][0]), a[1] + bfhi(hres[hh][0])), cvtpk(a[2] + bflo(hres[hh][1]), a[3] + bfhi(hres[hh][1]))}; \
            __hip_atomic_store(op + (hh) * (64 * 896 / 8), __builtin_bit_cast(u64, ov), __ATOMIC_RELAXED, __HIP_MEMORY_SCOPE_AGENT); } while (0)
        unsigned seen = 0;
#pragma unroll
        for (int s = 0; s < 28; ++s) {
            if (s == 5) BAR();
            if (s == 7 || s == 14 || s == 21) G_PUT(s / 7 - 1);
            if (s == 21) { waitv<7>(); PIN2(hres[0], hres[1]); BAR(); G_OUT(0, rq); FENCE(); }
            SB();
            G_STEP(s);
            if (s >= 14) { const u32x4 u = U4(gf[s % GW]); seen = umax3(umax3(seen, u[0], u[1]), u[2], u[3]); PIN(seen); }
            SB();
            if (s == 0) { if (__builtin_expect(!wave_all(gfl == tag), 0)) poll_flag(ws, gfi, tag); FENCE(); }
            if (s + GW < 28) G_LD(s + GW);
            if (s == 1) { hres[0] = ld8_dev(rws, hro, 0); hres[1] = ld8_dev(rws, hro, 57344); FENCE(); }
        }
        if (w == 0) { set_flag(ws, fo4 + 65536, tagv); set_flag(ws, fo4 + 98304, tagv); }
        if (__builtin_expect(wave_any(seen == ~0u), 0)) {
            poll_flag(ws, gfi + 65536, tag);
#pragma unroll
            for (int s = 14; s < 14 + GW; ++s) G_LD(s);
#pragma unroll
            for (int s = 14; s < 28; ++s) { if (s == 21) G_PUT(2); G_STEP(s); SB(); if (s + GW < 28) G_LD(s + GW); SB(); }
        }
        G_PUT(3);
        BAR();
        G_OUT(1, rq2);
    }
}

extern "C" void kernel_launch(void* const* d_in, const int* in_sizes, int n_in, void* d_out, int out_size, void* d_ws, size_t ws_size, hipStream_t stream) {
    (void)hipFuncSetAttribute((const void*)block_fwd, hipFuncAttributeMaxDynamicSharedMemorySize, LDS_BYTES);
    block_fwd<<<256, 512, LDS_BYTES, stream>>>((const char*)d_in[0], (const char*)d_in[1], (const char*)d_in[2], (const char*)d_in[4], (const char*)d_in[5], (const char*)d_in[6], (char*)d_out, (char*)d_ws);
}
```
